```python
import torch

def _softmax(x, axis=-1):
    return torch.softmax(x, dim=axis)

def _gelu(x):
    return torch.nn.functional.gelu(x, approximate='tanh')

def _silu(x):
    return torch.nn.functional.silu(x)

def _sigmoid(x):
    return torch.sigmoid(x)

def _layernorm(x, axis=-1, eps=1e-05):
    m = x.mean(dim=axis, keepdim=True)
    v = x.var(dim=axis, keepdim=True, unbiased=False)
    return (x - m) / torch.sqrt(v + eps)

def _scatter_add(tgt, idx, src, axis=0):
    return tgt.clone().index_add_(axis, idx, src)

def _segment_sum(src, seg, n):
    out = torch.zeros((n,) + src.shape[1:], dtype=src.dtype)
    return out.index_add_(0, seg, src)

def _pad(x, pw):
    flat = tuple((v for pair in reversed(pw) for v in pair))
    return torch.nn.functional.pad(x, flat)
kernel_name = 'adaLN_cond__ssm_conv_s4__glu_gated_swiglu__prompt'

def setup_inputs():

    _w = lambda a: (lambda t: t.to(torch.bfloat16) if t.is_floating_point() else t)(torch.as_tensor(a))

    def _A_setup():
        B, L, D, N = (1, 2048, 512, 128)
        A_re = -(lambda _x: torch.exp(torch.as_tensor(_x)))((lambda *s: torch.randn(*s, dtype=torch.bfloat16) * 3.2)(D, N))
        A_im = torch.pi * (lambda *s: torch.randn(*s, dtype=torch.bfloat16) * 0.05)(D, N)
        B_re = (lambda *s: torch.randn(*s, dtype=torch.bfloat16) * 0.1)(D, N)
        B_im = (lambda *s: torch.randn(*s, dtype=torch.bfloat16) * 0.1)(D, N)
        C_re = (lambda *s: torch.randn(*s, dtype=torch.bfloat16) * 0.1)(D, N)
        C_im = (lambda *s: torch.randn(*s, dtype=torch.bfloat16) * 0.1)(D, N)
        log_dt = (lambda *s: torch.randn(*s, dtype=torch.bfloat16) * 0.05)(D) * 0.5 - 4.0
        Dskip = (lambda *s: torch.randn(*s, dtype=torch.bfloat16) * 0.05)(D)
        return {'u': _w((lambda *s: torch.randn(*s, dtype=torch.bfloat16) * 0.05)(B, L, D)), 'A_re': _w(A_re), 'A_im': _w(A_im), 'B_re': _w(B_re), 'B_im': _w(B_im), 'C_re': _w(C_re), 'C_im': _w(C_im), 'log_dt': _w(log_dt), 'Dskip': _w(Dskip)}
    _A_in = _A_setup()

    def _B_setup():
        B, S, D, F = (1, 2048, 512, 1792)
        return {'x': _w((lambda *s: torch.randn(*s, dtype=torch.bfloat16) * 0.05)(B, S, D)), 'w1': _w((lambda *s: torch.randn(*s, dtype=torch.bfloat16) * 0.4)(D, F) * (2.0 / D) ** 0.5), 'w3': _w((lambda *s: torch.randn(*s, dtype=torch.bfloat16) * 0.4)(D, F) * (2.0 / D) ** 0.5), 'w2': _w((lambda *s: torch.randn(*s, dtype=torch.bfloat16) * 0.4)(F, D) * (2.0 / F) ** 0.5)}
    _B_in = _B_setup()
    return {'A_u': _A_in['u'], 'A_A_re': _A_in['A_re'], 'A_A_im': _A_in['A_im'], 'A_B_re': _A_in['B_re'], 'A_B_im': _A_in['B_im'], 'A_C_re': _A_in['C_re'], 'A_C_im': _A_in['C_im'], 'A_log_dt': _A_in['log_dt'], 'A_Dskip': _A_in['Dskip'], 'B_x': _B_in['x'], 'B_w1': _B_in['w1'], 'B_w3': _B_in['w3'], 'B_w2': _B_in['w2'], 'c': _w((lambda *s: torch.randn(*s, dtype=torch.bfloat16) * 0.2)(1, 512) * 8.0), 'w_mod': _w((lambda *s: torch.randn(*s, dtype=torch.bfloat16) * 0.2)(512, 6 * 512) * 512 ** (-0.5) * 8.0)}

def reference(A_u, A_A_re, A_A_im, A_B_re, A_B_im, A_C_re, A_C_im, A_log_dt, A_Dskip, B_x, B_w1, B_w3, B_w2, c, w_mod):

    def _A_ref(u, A_re, A_im, B_re, B_im, C_re, C_im, log_dt, Dskip):
        Bsz, L, D = u.shape
        dt = torch.exp(log_dt)[:, None]
        A = A_re + 1j * A_im
        Bc = B_re + 1j * B_im
        Cc = C_re + 1j * C_im
        logdA = dt * A
        dB = dt * Bc
        l = (lambda x, dt: x.to(dt))(torch.arange(L), A_re.dtype)
        P = torch.exp(logdA[:, :, None] * l[None, None, :])
        K = torch.einsum('dn,dnl,dn->dl', Cc, P, dB).real
        L2 = 2 * L
        Kp = _pad(K, ((0, 0), (0, L)))
        up = (lambda x, *d: x.permute(*d))(u, 0, 2, 1)
        up = _pad(up, ((0, 0), (0, 0), (0, L)))
        Kf = (lambda x, axes=(-2, -1): torch.fft.rfft2(x.float(), dim=axes))(Kp[:, None, :], axes=(-2, -1))
        Uf = (lambda x, axes=(-2, -1): torch.fft.rfft2(x.float(), dim=axes))(up[:, :, None, :], axes=(-2, -1))
        Yf = Uf * Kf[None, :, :, :]
        y = (lambda x, s, axes=(-2, -1): torch.fft.irfft2(x, s=s, dim=axes).to(torch.bfloat16))(Yf, (1, L2), axes=(-2, -1))[:, :, 0, :L]
        y = (lambda x, *d: x.permute(*d))(y, 0, 2, 1) + u * Dskip[None, None, :]
        return y

    def _B_ref(x, w1, w3, w2):
        gate = torch.einsum('bsd,df->bsf', x, w1)
        up = torch.einsum('bsd,df->bsf', x, w3)
        h = _silu(gate) * up
        out = torch.einsum('bsf,fd->bsd', h, w2)
        return out
    x = A_u
    mod = (_silu(c) @ w_mod).reshape(c.shape[0], 6, 1, -1)
    sa, ca, ga, sb, cb, gb = (mod[:, 0].clone(), mod[:, 1].clone(), mod[:, 2].clone(), mod[:, 3].clone(), mod[:, 4].clone(), mod[:, 5].clone())
    h = _layernorm(x, axis=-1) * (1 + ca) + sa
    x = x + ga * _A_ref(h, A_A_re, A_A_im, A_B_re, A_B_im, A_C_re, A_C_im, A_log_dt, A_Dskip)
    h = _layernorm(x, axis=-1) * (1 + cb) + sb
    x = x + gb * _B_ref(h, B_w1, B_w3, B_w2)
    return x
```

```cpp
#include <hip/hip_runtime.h>
#include <hip/hip_bf16.h>
typedef __hip_bfloat16 bf16;
static __device__ __forceinline__ float b2f(bf16 x){return __bfloat162float(x);}
static __device__ __forceinline__ float rb(float x){return __bfloat162float(__float2bfloat16(x));}
constexpr int L=2048,D=512,N=128,F=1792;
__global__ void k_mod(const bf16* c,const bf16* w,float* mod){
  int j=blockIdx.x*blockDim.x+threadIdx.x; float s=0;
  for(int k=0;k<D;k++){float x=b2f(c[k]); float sl=rb(x/(1.f+expf(-x))); s+=sl*b2f(w[k*3072+j]);}
  mod[j]=rb(s);
}
__global__ void k_ln(const bf16* x,const float* sh,const float* sc,float* h){
  __shared__ float red[16];
  int t=blockIdx.x,d=threadIdx.x; float v=b2f(x[t*D+d]);
  float s=v; for(int o=32;o;o>>=1)s+=__shfl_xor(s,o); if((d&63)==0)red[d>>6]=s; __syncthreads();
  float m=0; for(int i=0;i<8;i++)m+=red[i]; m/=D; __syncthreads();
  float q=(v-m)*(v-m); for(int o=32;o;o>>=1)q+=__shfl_xor(q,o); if((d&63)==0)red[d>>6]=q; __syncthreads();
  float var=0; for(int i=0;i<8;i++)var+=red[i]; var/=D;
  h[t*D+d]=rb((v-m)*rsqrtf(var+1e-5f)*rb(1.f+sc[d])+sh[d]);
}
__global__ void k_kgen(const bf16*are,const bf16*aim,const bf16*bre,const bf16*bim,const bf16*cre,const bf16*cim,const bf16*ldt,float*K){
  int d=blockIdx.y,l=blockIdx.x*blockDim.x+threadIdx.x; float dt=rb(expf(b2f(ldt[d]))); float lf=rb((float)l); float s=0;
  for(int n=0;n<N;n++){int i=d*N+n; float lr=dt*b2f(are[i]),li=dt*b2f(aim[i]); float e=expf(lr*lf); float sn,cs; sincosf(li*lf,&sn,&cs);
    float br=dt*b2f(bre[i]),bi=dt*b2f(bim[i]),cr=b2f(cre[i]),ci=b2f(cim[i]); float wr=cr*br-ci*bi,wi=cr*bi+ci*br;
    s+=e*(wr*cs-wi*sn);}
  K[d*L+l]=s;
}
__global__ void k_conv(const float*K,const float*h,const bf16*x,const bf16*dsk,const float*ga,bf16*x1){
  int t=blockIdx.x,d=threadIdx.x; float s=0;
  for(int j=0;j<=t;j++) s+=K[d*L+j]*h[(t-j)*D+d];
  float y=rb(rb(s)+rb(h[t*D+d]*b2f(dsk[d])));
  x1[t*D+d]=__float2bfloat16(b2f(x[t*D+d])+rb(ga[d]*y));
}
__global__ void k_g1(const float*h,const bf16*w1,const bf16*w3,float*hh){
  int t=blockIdx.y,f=blockIdx.x*blockDim.x+threadIdx.x; float g=0,u=0;
  for(int k=0;k<D;k++){float a=h[t*D+k]; g+=a*b2f(w1[k*F+f]); u+=a*b2f(w3[k*F+f]);}
  g=rb(g);u=rb(u); hh[t*F+f]=rb(rb(g/(1.f+expf(-g)))*u);
}
__global__ void k_g2(const float*hh,const bf16*w2,const bf16*x1,const float*gb,bf16*out){
  int t=blockIdx.x,d=threadIdx.x; float s=0;
  for(int k=0;k<F;k++) s+=hh[t*F+k]*b2f(w2[k*D+d]);
  out[t*D+d]=__float2bfloat16(b2f(x1[t*D+d])+rb(gb[d]*rb(s)));
}
extern "C" void kernel_launch(void* const* d_in, const int* in_sizes, int n_in, void* d_out, int out_size, void* d_ws, size_t ws_size, hipStream_t stream) {
  const bf16* const* in=(const bf16* const*)d_in;
  float* ws=(float*)d_ws; float* mod=ws; float* h=ws+4096; float* K=h+L*D; float* hh=K+L*D; bf16* x1=(bf16*)(hh+L*F);
  k_mod<<<12,256,0,stream>>>(in[13],in[14],mod);
  k_ln<<<L,512,0,stream>>>(in[0],mod,mod+512,h);
  k_kgen<<<dim3(8,D),256,0,stream>>>(in[1],in[2],in[3],in[4],in[5],in[6],in[7],K);
  k_conv<<<L,512,0,stream>>>(K,h,in[0],in[8],mod+1024,x1);
  k_ln<<<L,512,0,stream>>>(x1,mod+1536,mod+2048,h);
  k_g1<<<dim3(7,L),256,0,stream>>>(h,in[10],in[11],hh);
  k_g2<<<L,512,0,stream>>>(hh,in[12],x1,mod+2560,(bf16*)d_out);
}
```

```cpp
#include <hip/hip_runtime.h>
#include <stdint.h>

typedef unsigned int u32;
typedef unsigned short u16;
typedef __attribute__((ext_vector_type(2))) float f32x2;
typedef __attribute__((ext_vector_type(4))) float f32x4;
typedef __attribute__((ext_vector_type(16))) float f32x16;
typedef __attribute__((ext_vector_type(2))) u32 u32x2;
typedef __attribute__((ext_vector_type(4))) u32 u32x4;
typedef __attribute__((ext_vector_type(2))) __bf16 bf16x2;
typedef __attribute__((ext_vector_type(8))) __bf16 bf16x8;
typedef __attribute__((ext_vector_type(4))) short s16x4;
#define LDS_AS __attribute__((address_space(3)))
#if defined(__HIP_DEVICE_COMPILE__)
#define NOPK __attribute__((target("no-packed-fp32-ops")))
#else
#define NOPK
#endif
#define DEVINL __device__ __forceinline__ NOPK

constexpr int L = 2048, D = 512, NS = 128, F = 1792;

constexpr size_t WS_XP = 0;
constexpr size_t WS_R = WS_XP + (2u << 20);
constexpr size_t WS_X1T = WS_R + (2u << 20) + 4096;
constexpr size_t WS_W1F = WS_X1T + (2u << 20);
constexpr size_t WS_W2F = WS_W1F + 112 * 32 * 1024;
constexpr size_t WS_HF = WS_W2F + 16 * 112 * 1024;
constexpr size_t WS_STATS = WS_HF + 64 * 112 * 1024;
constexpr size_t WS_MODP = WS_STATS + 16384;
constexpr size_t WS_CD = WS_MODP + 8 * 3072 * 4;
constexpr size_t WS_GB = WS_CD + 8 * 112 * 64 * 4;

#define u2f(x) __builtin_bit_cast(float, (u32)(x))
#define i2f(x) __builtin_bit_cast(float, (int)(x))
#define f2u(x) __builtin_bit_cast(u32, (float)(x))
#define f2i(x) __builtin_bit_cast(int, (float)(x))
DEVINL void block_sync() {
  __builtin_amdgcn_fence(__ATOMIC_RELEASE, "workgroup");
  __builtin_amdgcn_s_barrier();
  __builtin_amdgcn_fence(__ATOMIC_ACQUIRE, "workgroup");
}
DEVINL float bflo(u32 u) { return u2f(u << 16); }
DEVINL float bfhi(u32 u) { return u2f(u & 0xffff0000u); }
DEVINL u32 pk(float a, float b) {
  f32x2 v = {a, b};
  return __builtin_bit_cast(u32, __builtin_convertvector(v, bf16x2));
}
DEVINL float rbf(float a) { return bflo(pk(a, a)); }
template <int CTRL> DEVINL float dpp_add(float v) {
  return v + i2f(__builtin_amdgcn_update_dpp(0, f2i(v), CTRL, 0xf, 0xf, true));
}
DEVINL float wave_sum(float v) {
  v = dpp_add<0xB1>(v);
  v = dpp_add<0x4E>(v);
  v = dpp_add<0x141>(v);
  v = dpp_add<0x140>(v);
  auto a = __builtin_amdgcn_permlane16_swap(f2u(v), f2u(v), false, false);
  v = u2f(a[0]) + u2f(a[1]);
  auto b = __builtin_amdgcn_permlane32_swap(f2u(v), f2u(v), false, false);
  return u2f(b[0]) + u2f(b[1]);
}
DEVINL f32x4 mfma16(u32x4 a, u32x4 b, f32x4 c) {
  return __builtin_amdgcn_mfma_f32_16x16x32_bf16(__builtin_bit_cast(bf16x8, a), __builtin_bit_cast(bf16x8, b), c, 0, 0, 0);
}
DEVINL f32x16 mfma32(u32x4 a, u32x4 b, f32x16 c) {
  return __builtin_amdgcn_mfma_f32_32x32x16_bf16(__builtin_bit_cast(bf16x8, a), __builtin_bit_cast(bf16x8, b), c, 0, 0, 0);
}
DEVINL float dot2(u32 a, u32 b, float c) {
  return __builtin_amdgcn_fdot2_f32_bf16(__builtin_bit_cast(bf16x2, a), __builtin_bit_cast(bf16x2, b), c, false);
}
DEVINL void raw_barrier() {
  asm volatile("s_waitcnt lgkmcnt(0)" ::: "memory");
  __builtin_amdgcn_s_barrier();
  asm volatile("" ::: "memory");
}

__global__ NOPK __launch_bounds__(256) void k1(const u16* __restrict__ x, const u16* __restrict__ Are, const u16* __restrict__ Aim,
                                          const u16* __restrict__ Bre, const u16* __restrict__ Bim, const u16* __restrict__ Cre,
                                          const u16* __restrict__ Cim, const u16* __restrict__ logdt, const u16* __restrict__ w2,
                                          const u16* __restrict__ cvec, const u16* __restrict__ wmod, char* __restrict__ ws) {
  __shared__ __attribute__((aligned(16))) char smem[17408];
  const int b = blockIdx.x, tid = threadIdx.x, lane = tid & 63, w = tid >> 6;
  if (b < 128) {
    const u32x4* xr = (const u32x4*)x + (size_t)(b * 16 + w * 4) * 64 + lane;
    u32x4 v[4];
#pragma unroll
    for (int i = 0; i < 4; i++) v[i] = xr[i * 64];
#pragma unroll
    for (int i = 0; i < 4; i++) {
      u32x4 t = {v[0][i], v[1][i], v[2][i], v[3][i]};
      *(u32x4*)(smem + lane * 272 + i * 64 + w * 16) = t;
    }
    block_sync();
    u32x4* xp = (u32x4*)(ws + WS_XP) + (size_t)b * 1024;
#pragma unroll
    for (int q = 0; q < 4; q++) {
      int idx = q * 256 + tid;
      xp[idx] = *(const u32x4*)(smem + (idx >> 4) * 272 + (idx & 15) * 16);
    }
    float f[4][8], m[4], var[4];
#pragma unroll
    for (int i = 0; i < 4; i++) {
#pragma unroll
      for (int j = 0; j < 4; j++) { f[i][2 * j] = bflo(v[i][j]); f[i][2 * j + 1] = bfhi(v[i][j]); }
      float s = ((f[i][0] + f[i][1]) + (f[i][2] + f[i][3])) + ((f[i][4] + f[i][5]) + (f[i][6] + f[i][7]));
      m[i] = wave_sum(s) * (1.f / D);
    }
#pragma unroll
    for (int i = 0; i < 4; i++) {
      float s = 0.f;
#pragma unroll
      for (int j = 0; j < 8; j++) { float t = f[i][j] - m[i]; s = fmaf(t, t, s); }
      var[i] = wave_sum(s) * (1.f / D);
    }
    if (lane < 4) {
      float mm = lane == 0 ? m[0] : lane == 1 ? m[1] : lane == 2 ? m[2] : m[3];
      float vv = lane == 0 ? var[0] : lane == 1 ? var[1] : lane == 2 ? var[2] : var[3];
      f32x2 o = {mm, 1.f / sqrtf(vv + 1e-5f)};
      ((f32x2*)(ws + WS_STATS))[b * 16 + w * 4 + lane] = o;
    }
  } else if (b < 256) {
    const int bb = b - 128, cg = bb & 15, kg = bb >> 4;
    float* red = (float*)smem;
    if (tid < 192) {
      const int rg = tid / 24, cc = tid - rg * 24;
      const int row0 = kg * 64 + rg * 8;
      const u32x4* wp = (const u32x4*)(wmod + (size_t)row0 * 3072 + cg * 192 + cc * 8);
      u32x4 wv[8];
#pragma unroll
      for (int i = 0; i < 8; i++) wv[i] = wp[i * 384];
      u32x4 cv = *(const u32x4*)(cvec + row0);
      float acc[8] = {0.f, 0.f, 0.f, 0.f, 0.f, 0.f, 0.f, 0.f};
#pragma unroll
      for (int i = 0; i < 8; i++) {
        float cx = (i & 1) ? bfhi(cv[i >> 1]) : bflo(cv[i >> 1]);
        float sl = rbf(cx / (1.f + expf(-cx)));
#pragma unroll
        for (int j = 0; j < 4; j++) {
          acc[2 * j] = fmaf(sl, bflo(wv[i][j]), acc[2 * j]);
          acc[2 * j + 1] = fmaf(sl, bfhi(wv[i][j]), acc[2 * j + 1]);
        }
      }
      f32x4 a0 = {acc[0], acc[1], acc[2], acc[3]}, a1 = {acc[4], acc[5], acc[6], acc[7]};
      *(f32x4*)(red + rg * 192 + cc * 8) = a0;
      *(f32x4*)(red + rg * 192 + cc * 8 + 4) = a1;
    }
    block_sync();
    if (tid < 192) {
      float s = 0.f;
#pragma unroll
      for (int i = 0; i < 8; i++) s += red[i * 192 + tid];
      ((float*)(ws + WS_MODP))[kg * 3072 + cg * 192 + tid] = s;
    }
  } else if (b < 768) {
    const int d = b - 256, r = lane & 15, g = lane >> 4;
    const int off = d * NS + w * 32 + g * 8;
    u32x4 are = *(const u32x4*)(Are + off), aim = *(const u32x4*)(Aim + off);
    u32x4 bre = *(const u32x4*)(Bre + off), bim = *(const u32x4*)(Bim + off);
    u32x4 cre = *(const u32x4*)(Cre + off), cim = *(const u32x4*)(Cim + off);
    const float dt = rbf(expf(bflo(logdt[d])));
    const float kre = 1.4426950408889634f * (float)r, kim = 0.15915494309189535f * (float)r;
    f32x4 acc[4];
#pragma unroll
    for (int s = 0; s < 4; s++) acc[s] = f32x4{0.f, 0.f, 0.f, 0.f};
    float a2048 = 0.f;
#pragma unroll
    for (int ks = 0; ks < 2; ks++) {
      u32x4 Ff[4], Gf[4];
#pragma unroll
      for (int i = 0; i < 4; i++) {
        const int q = ks * 2 + (i >> 1);
        const bool hi = i & 1;
        float ar = dt * (hi ? bfhi(are[q]) : bflo(are[q])), ai = dt * (hi ? bfhi(aim[q]) : bflo(aim[q]));
        float br = dt * (hi ? bfhi(bre[q]) : bflo(bre[q])), bi = dt * (hi ? bfhi(bim[q]) : bflo(bim[q]));
        float cr = hi ? bfhi(cre[q]) : bflo(cre[q]), ci = hi ? bfhi(cim[q]) : bflo(cim[q]);
        float wr = cr * br - ci * bi, wi = cr * bi + ci * br;
        float mag = __builtin_amdgcn_exp2f(ar * kre);
        float ph = __builtin_amdgcn_fractf(ai * kim);
        float zx = mag * __builtin_amdgcn_cosf(ph), zy = mag * __builtin_amdgcn_sinf(ph);
#pragma unroll
        for (int s = 0; s < 4; s++) {
          Ff[s][i] = pk(zx, zy);
          float nx = zx * zx - zy * zy, ny = (zx + zx) * zy;
          zx = nx; zy = ny;
        }
#pragma unroll
        for (int s = 0; s < 4; s++) {
          float gr = wr * zx - wi * zy, gi = -(wr * zy + wi * zx);
          Gf[s][i] = pk(gr, gi);
          if (s == 3) {
            a2048 = fmaf(gr, zx, a2048);
            a2048 = fmaf(gi, zy, a2048);
          } else {
            float nx = zx * zx - zy * zy, ny = (zx + zx) * zy;
            zx = nx; zy = ny;
          }
        }
      }
#pragma unroll
      for (int s = 0; s < 4; s++) acc[s] = mfma16(Ff[s], Gf[s], acc[s]);
    }
    float* P = (float*)smem;
    float* P2 = P + 4 * 640;
    u16* Kc = (u16*)(smem + 4 * 640 * 4 + 64);
    *(f32x4*)(P + w * 640 + r * 16 + g * 4) = acc[0];
    if (r >= 8) {
#pragma unroll
      for (int s = 1; s < 4; s++) *(f32x4*)(P + w * 640 + s * 128 + r * 16 + g * 4) = acc[s];
    }
    if (r == 8) P2[w * 4 + g] = a2048;
    block_sync();
    if (tid < 160) {
      f32x4 s = (*(const f32x4*)(P + tid * 4) + *(const f32x4*)(P + 640 + tid * 4)) +
                (*(const f32x4*)(P + 1280 + tid * 4) + *(const f32x4*)(P + 1920 + tid * 4));
      u32x2 o = {pk(s[0], s[1]), pk(s[2], s[3])};
      *(u32x2*)(Kc + tid * 4) = o;
    } else if (tid == 192) {
      f32x4 s = (*(const f32x4*)(P2) + *(const f32x4*)(P2 + 4)) + (*(const f32x4*)(P2 + 8) + *(const f32x4*)(P2 + 12));
      float t = (s[0] + s[1]) + (s[2] + s[3]);
      Kc[640] = (u16)(pk(t, t) & 0xffff);
    }
    block_sync();
    u32x4 o;
#pragma unroll
    for (int j = 0; j < 4; j++) {
      int l0 = 2047 - 8 * tid - 2 * j, l1 = l0 - 1;
      u32 bits = pk((float)l0, (float)l1);
      int c0 = l0 < 128 ? l0 : (int)(bits & 0xffff) - 17024;
      int c1 = l1 < 128 ? l1 : (int)(bits >> 16) - 17024;
      o[j] = (u32)Kc[c0] | ((u32)Kc[c1] << 16);
    }
    ((u32x4*)(ws + WS_R))[d * 256 + tid] = o;
  } else {
    const int it = (b - 768) * 256 + tid;
    const int fg = it >> 7, dq = it & 127;
    const u32x2* wp = (const u32x2*)(w2 + (size_t)fg * 8 * D + dq * 4);
    u32x2 v[8];
#pragma unroll
    for (int i = 0; i < 8; i++) v[i] = wp[i * 128];
    const int dtile = dq >> 3, dl = (dq & 7) * 4;
    const int row = 8 * ((dl >> 2) & 3) + 4 * (dl >> 4);
    u32x4* dst = (u32x4*)(ws + WS_W2F) + ((size_t)(dtile * 112 + (fg >> 1)) * 64 + (fg & 1) * 32 + row);
#pragma unroll
    for (int e = 0; e < 4; e++) {
      u32x4 o;
#pragma unroll
      for (int j = 0; j < 4; j++)
        o[j] = __builtin_amdgcn_perm(v[2 * j + 1][e >> 1], v[2 * j][e >> 1], (e & 1) ? 0x07060302u : 0x05040100u);
      dst[e] = o;
    }
  }
}

template <int TG>
DEVINL void conv_body(const char* pa, const char* pb, f32x4 (&acc)[4]) {
  constexpr int T0 = TG ? 6 : 7, T1 = TG ? 1 : 0, T2 = TG ? 5 : 4, T3 = TG ? 2 : 3;
#pragma unroll
  for (int nu = 0; nu < 4; nu++) {
    u32x4 A[8], B[8];
#pragma unroll
    for (int i = 0; i <= T0; i++) {
      A[i] = *(const u32x4*)(pa + 512 * (7 - i) + 64 * (3 - nu));
      B[i] = *(const u32x4*)(pb + 512 * i + 64 * (3 - nu));
    }
#pragma unroll
    for (int mu = 0; mu <= T0; mu++) {
      if (mu <= T1) acc[1] = mfma16(A[mu], B[T1 - mu], acc[1]);
      if (mu <= T3) acc[3] = mfma16(A[mu], B[T3 - mu], acc[3]);
      if (mu <= T2) acc[2] = mfma16(A[mu], B[T2 - mu], acc[2]);
      acc[0] = mfma16(A[mu], B[T0 - mu], acc[0]);
    }
  }
}

constexpr int K2_KC = 0, K2_KCS = 4160, K2_KCH = 4 * 4160, K2_U = 2 * K2_KCH, K2_US = 4640, K2_UCH = 2 * 4640;
constexpr int K2_LDS = K2_U + 2 * K2_UCH;

__global__ NOPK __launch_bounds__(512) void k2(const u16* __restrict__ dskip, const u16* __restrict__ w1, const u16* __restrict__ w3,
                                          char* __restrict__ ws) {
  __shared__ __attribute__((aligned(16))) char smem[K2_LDS];
  const int b = blockIdx.x, tid = threadIdx.x, lane = tid & 63, w = tid >> 6;
  const float* modp = (const float*)(ws + WS_MODP);
  if (b < 256) {
    const int p = b;
    const int kch = tid >> 8, ki = tid & 255;
    const u16* Rrow = (const u16*)(ws + WS_R) + (size_t)(2 * p + kch) * L + ki * 8;
    u32x4 r0 = *(const u32x4*)Rrow;
    u32x2 r1 = *(const u32x2*)(Rrow + 8);
    u32x4 xv = ((const u32x4*)(ws + WS_XP))[((size_t)(tid >> 2) * 256 + p) * 4 + (tid & 3)];
    f32x4 st0 = ((const f32x4*)(ws + WS_STATS))[tid * 2], st1 = ((const f32x4*)(ws + WS_STATS))[tid * 2 + 1];
    float md[4][2];
#pragma unroll
    for (int k = 0; k < 4; k++) {
      const int ch = (k == 3 ? 5 : k) * 512 + 2 * p;
      float s0 = 0.f, s1 = 0.f;
#pragma unroll
      for (int q = 0; q < 8; q++) { s0 += modp[q * 3072 + ch]; s1 += modp[q * 3072 + ch + 1]; }
      md[k][0] = rbf(s0); md[k][1] = rbf(s1);
    }
    const u32 dsk = *(const u32*)(dskip + 2 * p);
    if (tid < 32) *(u32x4*)(smem + K2_KC + (tid >> 2) * K2_KCS + 4096 + (tid & 3) * 16) = u32x4{0, 0, 0, 0};
    else if (tid >= 64 && tid < 64 + 132) {
      int i = tid - 64, cp = i / 33, e = i - cp * 33;
      if (e < 32 || (cp & 1)) *(u32x2*)(smem + K2_U + cp * K2_US + e * 16) = u32x2{0, 0};
      if (e < 32) *(u32x2*)(smem + K2_U + cp * K2_US + e * 16 + 8) = u32x2{0, 0};
    }
    if (ki == 255) r1 = u32x2{0, 0};
    {
      u32 a0 = __builtin_amdgcn_alignbit(r0[1], r0[0], 16), a1 = __builtin_amdgcn_alignbit(r0[2], r0[1], 16);
      u32 a2 = __builtin_amdgcn_alignbit(r0[3], r0[2], 16), a3 = __builtin_amdgcn_alignbit(r1[0], r0[3], 16);
      u32 a4 = __builtin_amdgcn_alignbit(r1[1], r1[0], 16);
      char* kc = smem + K2_KC + kch * K2_KCH + ki * 16;
      *(u32x4*)(kc) = r0;
      *(u32x4*)(kc + K2_KCS) = u32x4{a0, a1, a2, a3};
      *(u32x4*)(kc + 2 * K2_KCS) = u32x4{r0[1], r0[2], r0[3], r1[0]};
      *(u32x4*)(kc + 3 * K2_KCS) = u32x4{a1, a2, a3, a4};
    }
    u32x2 hp[2];
    {
      const float mean[4] = {st0[0], st0[2], st1[0], st1[2]}, rs[4] = {st0[1], st0[3], st1[1], st1[3]};
#pragma unroll
      for (int c = 0; c < 2; c++) {
        const float sc = rbf(1.f + md[1][c]), sh = md[0][c];
        float h[4];
#pragma unroll
        for (int i = 0; i < 4; i++) h[i] = fmaf(((c ? bfhi(xv[i]) : bflo(xv[i])) - mean[i]) * rs[i], sc, sh);
        hp[c] = u32x2{pk(h[0], h[1]), pk(h[2], h[3])};
        char* u = smem + K2_U + c * K2_UCH + 512 + tid * 8;
        *(u32x2*)(u) = hp[c];
        *(u32x2*)(u + K2_US + 8) = hp[c];
      }
    }
    if (tid == 0) *(f32x2*)(ws + WS_GB + p * 8) = f32x2{md[3][0], md[3][1]};
    block_sync();
    const int ch = w & 1, vh = (w >> 1) & 1, tg = w >> 2;
    const int r = lane & 15, g = lane >> 4;
    const char* pa = smem + K2_KC + ch * K2_KCH + (3 - (r & 3)) * K2_KCS + 2 * (2040 - 8 * (r >> 2) + 8 * g) - 512 * 7 - 64 * (4 * vh + 3);
    const char* pb = smem + K2_U + ch * K2_UCH + ((r & 1) ? 0 : K2_US) + 2 * (256 + 32 * (r >> 1) + 8 * g) - 64 * (4 * vh + 3);
    f32x4 acc[4];
#pragma unroll
    for (int i = 0; i < 4; i++) acc[i] = f32x4{0.f, 0.f, 0.f, 0.f};
    if (tg == 0) conv_body<0>(pa, pb, acc); else conv_body<1>(pa, pb, acc);
    block_sync();
    {
      float* Y = (float*)smem + (vh * 2 + ch) * 2560 + 40 * (r >> 1) + 4 * (r & 1) + 8 * g;
      const int t0 = tg ? 6 : 7, t1 = tg ? 1 : 0, t2 = tg ? 5 : 4, t3 = tg ? 2 : 3;
      *(f32x4*)(Y + 320 * t0) = acc[0];
      *(f32x4*)(Y + 320 * t1) = acc[1];
      *(f32x4*)(Y + 320 * t2) = acc[2];
      *(f32x4*)(Y + 320 * t3) = acc[3];
    }
    block_sync();
#pragma unroll
    for (int c = 0; c < 2; c++) {
      const float* Y = (const float*)smem + c * 2560 + 4 * tid + 8 * (tid >> 3);
      f32x4 y = *(const f32x4*)Y + *(const f32x4*)(Y + 5120);
      const float dk = c ? bfhi(dsk) : bflo(dsk), ga = md[2][c];
      float o[4];
#pragma unroll
      for (int i = 0; i < 4; i++) {
        float h = (i & 1) ? bfhi(hp[c][i >> 1]) : bflo(hp[c][i >> 1]);
        o[i] = fmaf(ga, fmaf(h, dk, y[i]), c ? bfhi(xv[i]) : bflo(xv[i]));
      }
      *(u32x2*)(ws + WS_X1T + (size_t)(2 * p + c) * 4096 + tid * 8) = u32x2{pk(o[0], o[1]), pk(o[2], o[3])};
    }
  } else {
    const int bb = b - 256;
    const int dq = bb & 7, fb = (bb >> 3) % 14, mat = bb / 112;
    const int d0 = dq * 64 + w * 8, f0 = fb * 128 + lane * 2;
    const u32* wp = (const u32*)((mat ? w3 : w1) + (size_t)d0 * F + f0);
    u32 v[8];
#pragma unroll
    for (int i = 0; i < 8; i++) v[i] = wp[i * (F / 2)];
    float* sc = (float*)smem;
    const float kf = mat ? 0.6931471805599453f : 1.4426950408889634f;
    if (tid < 128) {
      const int ch = (tid < 64 ? 4 * 512 : 3 * 512) + dq * 64 + (tid & 63);
      float s = 0.f;
#pragma unroll
      for (int q = 0; q < 8; q++) s += modp[q * 3072 + ch];
      s = rbf(s);
      if (tid < 64) s = rbf(1.f + s);
      sc[tid] = s * kf;
    }
    block_sync();
    f32x4 c0 = *(const f32x4*)(sc + w * 8), c1 = *(const f32x4*)(sc + w * 8 + 4);
    f32x4 s0 = *(const f32x4*)(sc + 64 + w * 8), s1 = *(const f32x4*)(sc + 64 + w * 8 + 4);
    const float cs[8] = {c0[0], c0[1], c0[2], c0[3], c1[0], c1[1], c1[2], c1[3]};
    const float ss[8] = {s0[0], s0[1], s0[2], s0[3], s1[0], s1[1], s1[2], s1[3]};
    u32x4 o0, o1;
    f32x4 sums = {0.f, 0.f, 0.f, 0.f};
#pragma unroll
    for (int j = 0; j < 4; j++) {
      float a0 = bflo(v[2 * j]), a1 = bflo(v[2 * j + 1]), b0 = bfhi(v[2 * j]), b1 = bfhi(v[2 * j + 1]);
      o0[j] = pk(a0 * cs[2 * j], a1 * cs[2 * j + 1]);
      o1[j] = pk(b0 * cs[2 * j], b1 * cs[2 * j + 1]);
      sums[0] = dot2(o0[j], 0x3f803f80u, sums[0]);
      sums[1] = dot2(o1[j], 0x3f803f80u, sums[1]);
      sums[2] = fmaf(a0, ss[2 * j], fmaf(a1, ss[2 * j + 1], sums[2]));
      sums[3] = fmaf(b0, ss[2 * j], fmaf(b1, ss[2 * j + 1], sums[3]));
    }
    {
      const int ft = f0 >> 4, fl = f0 & 15;
      const int row = (fl & 3) + 4 * (fl >> 3) + 16 * ((fl >> 2) & 1) + 8 * mat;
      u32x4* dst = (u32x4*)(ws + WS_W1F) + ((size_t)(ft * 32 + (d0 >> 4)) * 64 + ((d0 >> 3) & 1) * 32 + row);
      dst[0] = o0;
      dst[1] = o1;
    }
    float* red = (float*)(smem + 1024);
    *(f32x4*)(red + (w * 64 + lane) * 4) = sums;
    block_sync();
    if (tid < 256) {
      float s = 0.f;
#pragma unroll
      for (int q = 0; q < 8; q++) s += red[q * 256 + tid];
      const int f = fb * 128 + (tid >> 2) * 2 + (tid & 1), kind = (tid >> 1) & 1;
      const int ft = f >> 4, fl = f & 15;
      const int row = (fl & 3) + 4 * (fl >> 3) + 16 * ((fl >> 2) & 1) + 8 * mat;
      ((float*)(ws + WS_CD))[((dq * 112 + ft) * 2 + kind) * 32 + row] = s;
    }
  }
}

DEVINL void glds16(const void* sbase, u32 voff, u32 ldsaddr) {
  u32 keep;
  asm volatile("s_mov_b32 %0, m0\n\ts_mov_b32 m0, %3\n\ts_nop 0\n\tglobal_load_lds_dwordx4 %1, %2\n\ts_mov_b32 m0, %0"
               : "=&s"(keep) : "v"(voff), "s"(sbase), "s"(ldsaddr) : "memory");
}
DEVINL u32x2 tr_read(const char* p) {
  return __builtin_bit_cast(u32x2, __builtin_amdgcn_ds_read_tr16_b64_v4i16((LDS_AS s16x4*)p));
}
template <int N> DEVINL void wait_vm() { asm volatile("s_waitcnt vmcnt(%0)" ::"n"(N) : "memory"); }

__global__ NOPK __launch_bounds__(512) void k3(char* __restrict__ ws) {
  __shared__ __attribute__((aligned(16))) char smem[131072 + 2048];
  const int b = blockIdx.x, tid = threadIdx.x, lane = tid & 63;
  const int w = __builtin_amdgcn_readfirstlane(tid >> 6);
  const int xcd = b & 7, idx = b >> 3;
  const int tt = (xcd & 1) * 8 + (idx & 7), fs = (xcd >> 1) * 4 + (idx >> 3);
  const int h = lane >> 5, c = lane & 31;
  const char* rb = smem + h * 2048 + ((lane >> 2) & 3) * 64 + ((lane >> 4) & 1) * 32 + (lane & 3) * 8;
  u32* bext = (u32*)(smem + 131072);
  f32x2* rv = (f32x2*)(smem + 131072 + 1024);
  if (w == 7) {
    const char* xb = ws + WS_X1T + tt * 256;
    const u32 voff = ((lane >> 2) & 3) * 4096 + (lane >> 4) * 64 + (lane & 3) * 16;
    const u32 lbase = (u32)(size_t)(LDS_AS char*)smem;
#pragma unroll
    for (int i = 0; i < 48; i++) glds16(xb + i * 16384, voff, lbase + i * 1024);
    float s1[4] = {0.f, 0.f, 0.f, 0.f}, s2[4] = {0.f, 0.f, 0.f, 0.f};
#pragma unroll
    for (int ck = 0; ck < 8; ck++) {
      if (ck <= 5) wait_vm<32>(); else if (ck == 6) wait_vm<16>(); else wait_vm<0>();
      raw_barrier();
      if (ck + 3 < 8) {
#pragma unroll
        for (int i = 0; i < 16; i++) glds16(xb + ((ck + 3) * 16 + i) * 16384, voff, lbase + ((ck + 3) * 16 + i) * 1024);
      }
#pragma unroll
      for (int k = 0; k < 4; k++) {
        const int ks = ck * 4 + k;
#pragma unroll
        for (int T = 0; T < 4; T++) {
          u32x2 lo = tr_read(rb + ks * 4096 + T * 256), hi = tr_read(rb + ks * 4096 + 1024 + T * 256);
          s1[T] = dot2(lo[0], 0x3f803f80u, s1[T]); s2[T] = dot2(lo[0], lo[0], s2[T]);
          s1[T] = dot2(lo[1], 0x3f803f80u, s1[T]); s2[T] = dot2(lo[1], lo[1], s2[T]);
          s1[T] = dot2(hi[0], 0x3f803f80u, s1[T]); s2[T] = dot2(hi[0], hi[0], s2[T]);
          s1[T] = dot2(hi[1], 0x3f803f80u, s1[T]); s2[T] = dot2(hi[1], hi[1], s2[T]);
        }
        asm volatile("" : "+v"(s1[0]), "+v"(s1[1]), "+v"(s1[2]), "+v"(s1[3]), "+v"(s2[0]), "+v"(s2[1]), "+v"(s2[2]), "+v"(s2[3]));
      }
    }
#pragma unroll
    for (int T = 0; T < 4; T++) {
      auto a = __builtin_amdgcn_permlane32_swap(f2u(s1[T]), f2u(s1[T]), false, false);
      auto q = __builtin_amdgcn_permlane32_swap(f2u(s2[T]), f2u(s2[T]), false, false);
      float m = (u2f(a[0]) + u2f(a[1])) * (1.f / D);
      float e2 = (u2f(q[0]) + u2f(q[1])) * (1.f / D);
      float v = fmaxf(e2 - m * m, 0.f) + 1e-5f;
      float sd = sqrtf(v);
      float val = h ? sd : -m;
      float vhi = rbf(val);
      bext[h * 128 + T * 32 + c] = pk(vhi, val - vhi);
      if (h == 0) rv[T * 32 + c] = f32x2{-1.f / sd, v};
    }
    raw_barrier();
  } else {
    const int ft = fs * 7 + w;
    const u32x4* wp = (const u32x4*)(ws + WS_W1F) + (size_t)ft * 32 * 64 + lane;
    u32x4 A[32];
#pragma unroll
    for (int i = 0; i < 12; i++) A[i] = wp[i * 64];
    float cd = 0.f;
    f32x16 acc[4];
#pragma unroll
    for (int T = 0; T < 4; T++)
#pragma unroll
      for (int i = 0; i < 16; i++) acc[T][i] = 0.f;
#pragma unroll
    for (int ck = 0; ck < 8; ck++) {
      raw_barrier();
      if (ck + 3 < 8) {
#pragma unroll
        for (int i = 0; i < 4; i++) A[(ck + 3) * 4 + i] = wp[((ck + 3) * 4 + i) * 64];
      }
      if (ck == 5) {
        const float* cdp = (const float*)(ws + WS_CD) + ft * 64 + lane;
#pragma unroll
        for (int q = 0; q < 8; q++) cd += cdp[q * 112 * 64];
      }
#pragma unroll
      for (int k = 0; k < 4; k++) {
        const int ks = ck * 4 + k;
#pragma unroll
        for (int T = 0; T < 4; T++) {
          u32x2 lo = tr_read(rb + ks * 4096 + T * 256), hi = tr_read(rb + ks * 4096 + 1024 + T * 256);
          acc[T] = mfma32(A[ks], u32x4{lo[0], lo[1], hi[0], hi[1]}, acc[T]);
        }
      }
    }
    const float chi = rbf(cd);
    const u32x4 Ax = {pk(chi, chi), pk(cd - chi, cd - chi), 0, 0};
    raw_barrier();
    u32x4* hp = (u32x4*)(ws + WS_HF) + ((size_t)(tt * 4) * 112 + ft) * 64 + lane;
#pragma unroll
    for (int T = 0; T < 4; T++) {
      const u32 be = bext[h * 128 + T * 32 + c];
      const f32x2 q = rv[T * 32 + c];
      acc[T] = mfma32(Ax, u32x4{be, be, 0, 0}, acc[T]);
      float o[8];
#pragma unroll
      for (int i = 0; i < 8; i++) {
        const float G = acc[T][(i & 3) + 8 * (i >> 2)], U = acc[T][(i & 3) + 8 * (i >> 2) + 4];
        const float e = __builtin_amdgcn_exp2f(G * q[0]);
        o[i] = (G * U) * __builtin_amdgcn_rcpf(fmaf(e, q[1], q[1]));
      }
      hp[(size_t)T * 112 * 64] = u32x4{pk(o[0], o[1]), pk(o[2], o[3]), pk(o[4], o[5]), pk(o[6], o[7])};
    }
  }
}

__global__ NOPK __launch_bounds__(256) void k4(const char* __restrict__ ws, u16* __restrict__ out) {
  __shared__ __attribute__((aligned(16))) char smem[4 * 4 * 4096];
  const int b = blockIdx.x, tid = threadIdx.x, lane = tid & 63;
  const int w = __builtin_amdgcn_readfirstlane(tid >> 6);
  const int xcd = b & 7, idx = b >> 3;
  const int tt = (xcd & 1) * 16 + (xcd >> 1) * 4 + (idx & 3), dt = idx >> 2;
  const int h = lane >> 5, c = lane & 31;
  const u32x4* ap = (const u32x4*)(ws + WS_W2F) + ((size_t)(dt * 2) * 112 + w * 28) * 64 + lane;
  const u32x4* bp = (const u32x4*)(ws + WS_HF) + ((size_t)(tt * 2) * 112 + w * 28) * 64 + lane;
  constexpr int PF = 6;
  u32x4 A0[28], A1[28], B0[28], B1[28];
#pragma unroll
  for (int i = 0; i < PF; i++) {
    A0[i] = ap[i * 64]; B0[i] = bp[i * 64]; A1[i] = ap[(112 + i) * 64]; B1[i] = bp[(112 + i) * 64];
  }
  const int dbase = dt * 64 + (w & 1) * 32 + h * 16, tok = tt * 64 + (w >> 1) * 32 + c;
  u16 xr[16];
  f32x4 gb[4];
  f32x16 acc[4];
#pragma unroll
  for (int T = 0; T < 4; T++)
#pragma unroll
    for (int i = 0; i < 16; i++) acc[T][i] = 0.f;
#pragma unroll
  for (int i = 0; i < 28; i++) {
    __builtin_amdgcn_sched_barrier(0);
    if (i + PF < 28) {
      const int j = i + PF;
      A0[j] = ap[j * 64]; B0[j] = bp[j * 64]; A1[j] = ap[(112 + j) * 64]; B1[j] = bp[(112 + j) * 64];
    }
    if (i == 22) {
      const u16* xp = (const u16*)(ws + WS_X1T) + (size_t)dbase * L + tok;
#pragma unroll
      for (int q = 0; q < 16; q++) xr[q] = xp[q * L];
#pragma unroll
      for (int q = 0; q < 4; q++) gb[q] = *(const f32x4*)(ws + WS_GB + (dbase + q * 4) * 4);
    }
    __builtin_amdgcn_sched_barrier(0);
    acc[0] = mfma32(A0[i], B0[i], acc[0]);
    acc[1] = mfma32(A1[i], B0[i], acc[1]);
    acc[2] = mfma32(A0[i], B1[i], acc[2]);
    acc[3] = mfma32(A1[i], B1[i], acc[3]);
  }
#pragma unroll
  for (int T = 0; T < 4; T++) {
    if (T != w) {
#pragma unroll
      for (int q = 0; q < 4; q++)
        *(f32x4*)(smem + ((T * 4 + w) * 4 + q) * 1024 + lane * 16) = f32x4{acc[T][4 * q], acc[T][4 * q + 1], acc[T][4 * q + 2], acc[T][4 * q + 3]};
    }
  }
  f32x16 mine = w == 0 ? acc[0] : w == 1 ? acc[1] : w == 2 ? acc[2] : acc[3];
  block_sync();
#pragma unroll
  for (int s = 1; s < 4; s++) {
    const int src = (w + s) & 3;
#pragma unroll
    for (int q = 0; q < 4; q++) {
      f32x4 v = *(const f32x4*)(smem + ((w * 4 + src) * 4 + q) * 1024 + lane * 16);
#pragma unroll
      for (int j = 0; j < 4; j++) mine[4 * q + j] += v[j];
    }
  }
  u32 o[8];
#pragma unroll
  for (int q = 0; q < 8; q++) {
    float v0 = fmaf(gb[q >> 1][(2 * q) & 3], mine[2 * q], bflo(xr[2 * q]));
    float v1 = fmaf(gb[q >> 1][(2 * q + 1) & 3], mine[2 * q + 1], bflo(xr[2 * q + 1]));
    o[q] = pk(v0, v1);
  }
  u32x4* op = (u32x4*)(out + (size_t)tok * D + dbase);
  op[0] = u32x4{o[0], o[1], o[2], o[3]};
  op[1] = u32x4{o[4], o[5], o[6], o[7]};
}

extern "C" void kernel_launch(void* const* d_in, const int* in_sizes, int n_in, void* d_out, int out_size, void* d_ws,
                              size_t ws_size, hipStream_t stream) {
  const u16* const* in = (const u16* const*)d_in;
  char* ws = (char*)d_ws;
  k1<<<880, 256, 0, stream>>>(in[0], in[1], in[2], in[3], in[4], in[5], in[6], in[7], in[12], in[13], in[14], ws);
  k2<<<480, 512, 0, stream>>>(in[8], in[10], in[11], ws);
  k3<<<256, 512, 0, stream>>>(ws);
  k4<<<256, 256, 0, stream>>>(ws, (u16*)d_out);
}
```

```cpp
#include <hip/hip_runtime.h>
#include <stdint.h>

typedef unsigned int u32;
typedef unsigned short u16;
typedef __attribute__((ext_vector_type(2))) float f32x2;
typedef __attribute__((ext_vector_type(4))) float f32x4;
typedef __attribute__((ext_vector_type(16))) float f32x16;
typedef __attribute__((ext_vector_type(2))) u32 u32x2;
typedef __attribute__((ext_vector_type(4))) u32 u32x4;
typedef __attribute__((ext_vector_type(2))) __bf16 bf16x2;
typedef __attribute__((ext_vector_type(8))) __bf16 bf16x8;
typedef __attribute__((ext_vector_type(4))) short s16x4;
#define LDS_AS __attribute__((address_space(3)))
#if defined(__HIP_DEVICE_COMPILE__)
#define NOPK __attribute__((target("no-packed-fp32-ops")))
#else
#define NOPK
#endif
#define DEVINL __device__ __forceinline__ NOPK

constexpr int L = 2048, D = 512, NS = 128, F = 1792;

constexpr size_t WS_XT = 0;
constexpr size_t WS_R = WS_XT + (4u << 20);
constexpr size_t WS_X1 = WS_R + (2u << 20) + 4096;
constexpr size_t WS_W1F = WS_X1 + (2u << 20);
constexpr size_t WS_W2F = WS_W1F + 112 * 32 * 1024;
constexpr size_t WS_HF = WS_W2F + 16 * 112 * 1024;
constexpr size_t WS_MODP = WS_HF + 64 * 112 * 1024;
constexpr size_t WS_CD = WS_MODP + 8 * 3072 * 4;
constexpr size_t WS_GB = WS_CD + 8 * 112 * 64 * 4;
#define K4_PF 10

#define u2f(x) __builtin_bit_cast(float, (u32)(x))
#define i2f(x) __builtin_bit_cast(float, (int)(x))
#define f2u(x) __builtin_bit_cast(u32, (float)(x))
#define f2i(x) __builtin_bit_cast(int, (float)(x))
DEVINL void block_sync() {
  __builtin_amdgcn_fence(__ATOMIC_RELEASE, "workgroup");
  __builtin_amdgcn_s_barrier();
  __builtin_amdgcn_fence(__ATOMIC_ACQUIRE, "workgroup");
}
#define TSDECL
#define TS(kid, slot)
#define TSW(kid, slot)
#define TSEND(kid)
DEVINL float bflo(u32 u) { return u2f(u << 16); }
DEVINL float bfhi(u32 u) { return u2f(u & 0xffff0000u); }
DEVINL u32 pk(float a, float b) {
  f32x2 v = {a, b};
  return __builtin_bit_cast(u32, __builtin_convertvector(v, bf16x2));
}
DEVINL float rbf(float a) { return bflo(pk(a, a)); }
template <int CTRL> DEVINL float dpp_add(float v) {
  return v + i2f(__builtin_amdgcn_update_dpp(0, f2i(v), CTRL, 0xf, 0xf, true));
}
DEVINL float wave_sum(float v) {
  v = dpp_add<0xB1>(v);
  v = dpp_add<0x4E>(v);
  v = dpp_add<0x141>(v);
  v = dpp_add<0x140>(v);
  auto a = __builtin_amdgcn_permlane16_swap(f2u(v), f2u(v), false, false);
  v = u2f(a[0]) + u2f(a[1]);
  auto b = __builtin_amdgcn_permlane32_swap(f2u(v), f2u(v), false, false);
  return u2f(b[0]) + u2f(b[1]);
}
DEVINL f32x4 mfma16(u32x4 a, u32x4 b, f32x4 c) {
  return __builtin_amdgcn_mfma_f32_16x16x32_bf16(__builtin_bit_cast(bf16x8, a), __builtin_bit_cast(bf16x8, b), c, 0, 0, 0);
}
DEVINL f32x16 mfma32(u32x4 a, u32x4 b, f32x16 c) {
  return __builtin_amdgcn_mfma_f32_32x32x16_bf16(__builtin_bit_cast(bf16x8, a), __builtin_bit_cast(bf16x8, b), c, 0, 0, 0);
}
DEVINL float dot2(u32 a, u32 b, float c) {
  return __builtin_amdgcn_fdot2_f32_bf16(__builtin_bit_cast(bf16x2, a), __builtin_bit_cast(bf16x2, b), c, false);
}
DEVINL void raw_barrier() {
  asm volatile("s_waitcnt lgkmcnt(0)" ::: "memory");
  __builtin_amdgcn_s_barrier();
  asm volatile("" ::: "memory");
}

DEVINL void st16(const void* sbase, u32 voff, u32x4 v) {
  asm volatile("global_store_dwordx4 %0, %1, %2 sc1\n\ts_nop 1" ::"v"(voff), "v"(v), "s"(sbase) : "memory");
}
DEVINL void touch(u32& t, const void* sbase, u32 voff) {
  asm volatile("global_load_dword %0, %1, %2" : "+v"(t) : "v"(voff), "s"(sbase) : "memory");
}
DEVINL void touch_end(u32& t) { asm volatile("s_waitcnt vmcnt(0)" : "+v"(t)::"memory"); }
DEVINL u32x2 tr_read(const char* p) {
  return __builtin_bit_cast(u32x2, __builtin_amdgcn_ds_read_tr16_b64_v4i16((LDS_AS s16x4*)p));
}
template <int N> DEVINL void wait_vm() { asm volatile("s_waitcnt vmcnt(%0)" ::"n"(N) : "memory"); }
DEVINL float rdl(float v, int l) { return i2f(__builtin_amdgcn_readlane(f2i(v), l)); }
DEVINL float sum8(float v) { return dpp_add<0x141>(dpp_add<0x4E>(dpp_add<0xB1>(v))); }
constexpr float LOG2E = 1.4426950408889634f, LN2 = 0.6931471805599453f, INV2PI = 0.15915494309189535f;

constexpr int K1_TAB = 0, K1_P = 8 * 1536, K1_KC = K1_P + 24576, K1_KCS = 4112, K1_SIDE = K1_KC + 2 * K1_KCS;
constexpr int K1_LDS = K1_SIDE + 32 * 1056;

__global__ NOPK __launch_bounds__(512) void k1(const u16* __restrict__ x, const u16* __restrict__ Are, const u16* __restrict__ Aim,
                                          const u16* __restrict__ Bre, const u16* __restrict__ Bim, const u16* __restrict__ Cre,
                                          const u16* __restrict__ Cim, const u16* __restrict__ logdt, const u16* __restrict__ w2,
                                          const u16* __restrict__ cvec, const u16* __restrict__ wmod, const u16* __restrict__ w1,
                                          const u16* __restrict__ w3, char* __restrict__ ws) {
  __shared__ __attribute__((aligned(16))) char smem[K1_LDS];
  const int b = blockIdx.x, tid = threadIdx.x, lane = tid & 63;
  const int w = __builtin_amdgcn_readfirstlane(tid >> 6);
  TSDECL; TS(0, 0);
  const int ch = w >> 2, d = 2 * b + ch;
  const int job = (b >> 3) & 1, jb = (b & 7) | ((b >> 4) << 3);
  const int so = b * 256 + w * 32 + (lane & 31);
  const u32 vare = Are[so], vaim = Aim[so], vbre = Bre[so], vbim = Bim[so], vcre = Cre[so], vcim = Cim[so];
  const u32 ldt = logdt[d];
  u32x4 sv[4];
  u32x2 cv;
  u32 t2[8];
  const int rg = tid / 24, cc = tid - rg * 24;
  if (job == 0) {
    const u32x4* xr = (const u32x4*)x + (size_t)(jb * 16 + w * 2) * 64 + lane;
    sv[0] = xr[0]; sv[1] = xr[64];
  } else {
    if (w < 6) {
      const int row0 = (jb >> 4) * 64 + rg * 4;
      const u32x4* wp = (const u32x4*)(wmod + (size_t)row0 * 3072 + (jb & 15) * 192 + cc * 8);
#pragma unroll
      for (int i = 0; i < 4; i++) sv[i] = wp[i * 384];
      cv = *(const u32x2*)(cvec + row0);
    }
    if (jb < 112) {
      const u32* wp = (const u32*)(w2 + (size_t)(jb * 16 + (tid >> 8) * 8) * D) + (tid & 255);
#pragma unroll
      for (int i = 0; i < 8; i++) t2[i] = wp[i * 256];
    }
  }
  u32 tch = 0;
  if (w < 3) {
    const int row = tid / 3, seg = tid - row * 3;
    touch(tch, (b >> 7 ? w3 : w1) + (size_t)(b & 7) * 64 * F + ((b >> 3) & 15) * 112, row * (F * 2) + seg * 108);
  }
  TS(0, 1);
  char* tab = smem + K1_TAB + w * 1536;
  {
    const float dt = rbf(__builtin_amdgcn_exp2f(bflo(ldt) * LOG2E));
    const float a = dt * bflo(vare) * LOG2E, bq = dt * bflo(vaim) * INV2PI;
    const float br = dt * bflo(vbre), bi = dt * bflo(vbim), cr = bflo(vcre), ci = bflo(vcim);
    const float m = lane < 32 ? 1.f : 256.f;
    const float e = __builtin_amdgcn_exp2f(a * m), ph = __builtin_amdgcn_fractf(-bq * m);
    const float ex = e * __builtin_amdgcn_cosf(ph), ey = e * __builtin_amdgcn_sinf(ph);
    char* t = tab + (lane & 31) * 48;
    *(f32x2*)(t + 16 + (lane >> 5) * 8) = f32x2{ex, ey};
    f32x4 o = {a, bq, cr * br - ci * bi, cr * bi + ci * br};
    if (lane >= 32) o = f32x4{ex, ey, ex * ex - ey * ey, (ex + ex) * ey};
    *(f32x4*)(t + (lane >> 5) * 32) = o;
  }
  TS(0, 2);
  if (job == 0) {
    float f[2][8], m[2], rs[2];
#pragma unroll
    for (int i = 0; i < 2; i++) {
#pragma unroll
      for (int j = 0; j < 4; j++) { f[i][2 * j] = bflo(sv[i][j]); f[i][2 * j + 1] = bfhi(sv[i][j]); }
      float s = ((f[i][0] + f[i][1]) + (f[i][2] + f[i][3])) + ((f[i][4] + f[i][5]) + (f[i][6] + f[i][7]));
      m[i] = wave_sum(s) * (1.f / D);
    }
#pragma unroll
    for (int i = 0; i < 2; i++) {
      float s = 0.f;
#pragma unroll
      for (int j = 0; j < 8; j++) { f[i][j] -= m[i]; s = fmaf(f[i][j], f[i][j], s); }
      rs[i] = __builtin_amdgcn_rsqf(fmaf(wave_sum(s), 1.f / D, 1e-5f));
    }
#pragma unroll
    for (int i = 0; i < 2; i++) {
      u32x4 o;
#pragma unroll
      for (int j = 0; j < 4; j++) o[j] = pk(f[i][2 * j] * rs[i], f[i][2 * j + 1] * rs[i]);
      *(u32x4*)(smem + K1_SIDE + (w * 2 + i) * 1056 + lane * 16) = o;
      *(u32x4*)(smem + K1_SIDE + (16 + w * 2 + i) * 1056 + lane * 16) = sv[i];
    }
  } else {
    if (w < 6) {
      float acc[8] = {0.f, 0.f, 0.f, 0.f, 0.f, 0.f, 0.f, 0.f};
#pragma unroll
      for (int i = 0; i < 4; i++) {
        const float cx = (i & 1) ? bfhi(cv[i >> 1]) : bflo(cv[i >> 1]);
        const float sl = rbf(cx * __builtin_amdgcn_rcpf(1.f + __builtin_amdgcn_exp2f(cx * -LOG2E)));
#pragma unroll
        for (int j = 0; j < 4; j++) {
          acc[2 * j] = fmaf(sl, bflo(sv[i][j]), acc[2 * j]);
          acc[2 * j + 1] = fmaf(sl, bfhi(sv[i][j]), acc[2 * j + 1]);
        }
      }
      *(f32x4*)(smem + K1_SIDE + tid * 32) = f32x4{acc[0], acc[1], acc[2], acc[3]};
      *(f32x4*)(smem + K1_SIDE + tid * 32 + 16) = f32x4{acc[4], acc[5], acc[6], acc[7]};
    }
    if (jb < 112) {
      const u32 vo = (((tid >> 4) & 15) * 112 * 64 + (tid >> 8) * 32 + (tid & 15)) * 16;
#pragma unroll
      for (int e = 0; e < 2; e++) {
        u32x4 o;
#pragma unroll
        for (int j = 0; j < 4; j++) o[j] = __builtin_amdgcn_perm(t2[2 * j + 1], t2[2 * j], e ? 0x07060302u : 0x05040100u);
        st16(ws + WS_W2F + jb * 1024 + e * 256, vo, o);
      }
    }
  }
  TS(0, 3);
  const int r = lane & 15, g = lane >> 4;
  const int gx = r < 8 ? 2 * r : (r == 8 ? 512 : r == 9 ? 1280 : r == 10 ? 2048 : r == 11 ? 520 : r == 12 ? 1288 : r == 13 ? 516 : r == 14 ? 524 : 4096);
  {
    asm volatile("" ::: "memory");
    const float mF = (float)(16 * r), mX = (float)gx;
    const char* tp = tab + g * 8 * 48;
    const char* tq = tp + (r < 8 ? 16 : 32);
    f32x4 acc[3];
#pragma unroll
    for (int s = 0; s < 3; s++) acc[s] = f32x4{0.f, 0.f, 0.f, 0.f};
#pragma unroll
    for (int ks = 0; ks < 2; ks++) {
      u32x4 Ff, Gf[3];
#pragma unroll
      for (int i = 0; i < 4; i++) {
        const f32x4 s0 = *(const f32x4*)(tp + (ks * 4 + i) * 48), s1 = *(const f32x4*)(tq + (ks * 4 + i) * 48);
        const float e0 = __builtin_amdgcn_exp2f(s0[0] * mF), p0 = __builtin_amdgcn_fractf(s0[1] * mF);
        const float zx = e0 * __builtin_amdgcn_cosf(p0), zy = e0 * __builtin_amdgcn_sinf(p0);
        Ff[i] = pk(s0[2] * zx - s0[3] * zy, s0[2] * zy + s0[3] * zx);
        const float e1 = __builtin_amdgcn_exp2f(s0[0] * mX), p1 = __builtin_amdgcn_fractf(s0[1] * -mX);
        const float xx = e1 * __builtin_amdgcn_cosf(p1), xy = e1 * __builtin_amdgcn_sinf(p1);
        Gf[0][i] = pk(xx, xy);
        Gf[1][i] = pk(xx * s1[0] - xy * s1[1], xx * s1[1] + xy * s1[0]);
        Gf[2][i] = pk(xx * s1[2] - xy * s1[3], xx * s1[3] + xy * s1[2]);
      }
#pragma unroll
      for (int s = 0; s < 3; s++) acc[s] = mfma16(Ff, Gf[s], acc[s]);
    }
#pragma unroll
    for (int s = 0; s < 3; s++) *(f32x4*)(smem + K1_P + w * 3072 + s * 1024 + lane * 16) = acc[s];
  }
  TS(0, 4);
  block_sync();
  TS(0, 5);
  u16* Kc = (u16*)(smem + K1_KC + ch * K1_KCS);
  if ((w & 3) < 3) {
    const int s = w & 3;
    const char* pp = smem + K1_P + ch * 12288 + s * 1024 + lane * 16;
    const f32x4 v = (*(const f32x4*)(pp) + *(const f32x4*)(pp + 3072)) + (*(const f32x4*)(pp + 6144) + *(const f32x4*)(pp + 9216));
    const int pos = gx + 64 * g + (r < 8 ? (s == 0 ? 0 : s == 1 ? 1 : 256) : s * 256);
    const u32 o0 = pk(v[0], v[1]), o1 = pk(v[2], v[3]);
    if (pos <= 2048) Kc[pos] = (u16)o0;
    if (pos < 2048) {
      Kc[pos + 16] = (u16)(o0 >> 16);
      Kc[pos + 32] = (u16)o1;
      Kc[pos + 48] = (u16)(o1 >> 16);
    }
  }
  if (job == 0) {
    const char* rp = smem + K1_SIDE + (g * 8 + (r >> 2)) * 1056 + w * 128 + (r & 3) * 8;
    const u32 vo = w * 4096 + (r >> 1) * 128 + (g >> 1) * 64 + (r & 1) * 32 + (g & 1) * 16;
#pragma unroll
    for (int q = 0; q < 4; q++) {
      const u32x2 lo = tr_read(rp + q * 32), hi = tr_read(rp + q * 32 + 4 * 1056);
      st16(ws + WS_XT + jb * 32768 + q * 1024, vo, u32x4{lo[0], lo[1], hi[0], hi[1]});
    }
  } else if ((w & 3) == 3) {
    const int col = ch * 96 + lane;
#pragma unroll
    for (int hcol = 0; hcol < 2; hcol++) {
      if (hcol == 0 || lane < 32) {
        float s = 0.f;
#pragma unroll
        for (int i = 0; i < 16; i++) s += *(const float*)(smem + K1_SIDE + i * 768 + (col + hcol * 64) * 4);
        ((float*)(ws + WS_MODP))[(jb >> 4) * 3072 + (jb & 15) * 192 + col + hcol * 64] = s;
      }
    }
  }
  TS(0, 6);
  block_sync();
  {
    u32x4 o;
    const int ki = tid & 255;
#pragma unroll
    for (int j = 0; j < 4; j++) {
      const float l0 = (float)(2047 - 8 * ki - 2 * j);
      const u32 bits = pk(l0, l0 - 1.f);
      o[j] = (u32)Kc[(int)bflo(bits)] | ((u32)Kc[(int)bfhi(bits)] << 16);
    }
    st16(ws + WS_R + b * 8192, tid * 16, o);
  }
  touch_end(tch);
  TSEND(0);
}

constexpr int K2_KC = 0, K2_KCS = 4160, K2_KCH = 4 * 4160, K2_U = 2 * K2_KCH, K2_US = 4640, K2_UCH = 2 * 4640;
constexpr int K2_Y = K2_U + 2 * K2_UCH, K2_RED = K2_Y + 40960, K2_LDS = K2_RED + 8192;

template <int TG> DEVINL void conv_load(const char* pa, const char* pb, int nu, u32x4 (&A)[8], u32x4 (&B)[8]) {
  constexpr int T0 = TG ? 6 : 7;
#pragma unroll
  for (int i = 0; i <= T0; i++) {
    A[i] = *(const u32x4*)(pa + 512 * (7 - i) + 64 * (3 - nu));
    B[T0 - i] = *(const u32x4*)(pb + 512 * (T0 - i) + 64 * (3 - nu));
  }
}
template <int TG> DEVINL void conv_mma(const u32x4 (&A)[8], const u32x4 (&B)[8], f32x4 (&acc)[4]) {
  constexpr int T0 = TG ? 6 : 7, T1 = TG ? 1 : 0, T2 = TG ? 5 : 4, T3 = TG ? 2 : 3;
#pragma unroll
  for (int mu = 0; mu <= T0; mu++) {
    acc[0] = mfma16(A[mu], B[T0 - mu], acc[0]);
    if (mu >= T0 - T2) acc[2] = mfma16(A[mu - (T0 - T2)], B[T0 - mu], acc[2]);
    if (mu >= T0 - T3) acc[3] = mfma16(A[mu - (T0 - T3)], B[T0 - mu], acc[3]);
    if (mu >= T0 - T1) acc[1] = mfma16(A[mu - (T0 - T1)], B[T0 - mu], acc[1]);
  }
}
template <int TG, class FN> DEVINL void conv_body(const char* pa, const char* pb, f32x4 (&acc)[4], FN&& filler) {
  u32x4 A0[8], B0[8], A1[8], B1[8];
  conv_load<TG>(pa, pb, 0, A0, B0);
  conv_load<TG>(pa, pb, 1, A1, B1);
  __builtin_amdgcn_sched_barrier(0);
  conv_mma<TG>(A0, B0, acc);
  __builtin_amdgcn_sched_barrier(0);
  conv_load<TG>(pa, pb, 2, A0, B0);
  __builtin_amdgcn_sched_barrier(0);
  conv_mma<TG>(A1, B1, acc);
  __builtin_amdgcn_sched_barrier(0);
  conv_load<TG>(pa, pb, 3, A1, B1);
  __builtin_amdgcn_sched_barrier(0);
  conv_mma<TG>(A0, B0, acc);
  filler();
  conv_mma<TG>(A1, B1, acc);
}

__global__ NOPK __launch_bounds__(512) void k2(const u16* __restrict__ dskip, const u16* __restrict__ w1, const u16* __restrict__ w3,
                                          char* __restrict__ ws) {
  __shared__ __attribute__((aligned(16))) char smem[K2_LDS];
  const int p = blockIdx.x, tid = threadIdx.x, lane = tid & 63;
  const int w = __builtin_amdgcn_readfirstlane(tid >> 6);
  TSDECL; TS(1, 0);
  const float* modp = (const float*)(ws + WS_MODP);
  const int dq = p & 7, fb = (p >> 3) & 15, mat = p >> 7, d0 = dq * 64 + w * 8;
  const int kk = lane >> 4;
  const float mc = modp[(lane & 7) * 3072 + (kk == 3 ? 5 : kk) * 512 + 2 * p + ((lane >> 3) & 1)];
  const char* Rrow = ws + WS_R + p * 8192 + tid * 16;
  const u32x4 r0 = *(const u32x4*)Rrow;
  u32x2 r1 = *(const u32x2*)(Rrow + 16);
  const int g8 = (tid >> 3) * 4 + (tid & 3), cc = (tid >> 2) & 1;
  const char* xtp = ws + WS_XT + (size_t)((g8 >> 1) * 256 + p) * 128 + cc * 32 + (g8 & 1) * 16;
  const u32x4 xn = *(const u32x4*)xtp, xv = *(const u32x4*)(xtp + 64);
  const float mcb = modp[(lane & 7) * 3072 + 4 * 512 + d0 + (lane >> 3)], msb = modp[(lane & 7) * 3072 + 3 * 512 + d0 + (lane >> 3)];
  const u32 dsk = *(const u32*)(dskip + 2 * p);
  u32 v[8];
  {
    const u32* wp = (const u32*)((mat ? w3 : w1) + (size_t)d0 * F + fb * 112) + (lane < 56 ? lane : 55);
#pragma unroll
    for (int i = 0; i < 8; i++) v[i] = wp[i * (F / 2)];
  }
  TS(1, 1);
  if (tid < 32) *(u32x4*)(smem + K2_KC + (tid >> 2) * K2_KCS + 4096 + (tid & 3) * 16) = u32x4{0, 0, 0, 0};
  else if (tid >= 64 && tid < 64 + 132) {
    int i = tid - 64, cp = i / 33, e = i - cp * 33;
    if (e < 32 || (cp & 1)) *(u32x2*)(smem + K2_U + cp * K2_US + e * 16) = u32x2{0, 0};
    if (e < 32) *(u32x2*)(smem + K2_U + cp * K2_US + e * 16 + 8) = u32x2{0, 0};
  }
  const float mv = rbf(sum8(mc)), mv1 = rbf(1.f + mv);
  const float sh = cc ? rdl(mv, 8) : rdl(mv, 0), sc = cc ? rdl(mv1, 24) : rdl(mv1, 16), ga = cc ? rdl(mv, 40) : rdl(mv, 32);
  if (tid == 48 || tid == 56) ((float*)(ws + WS_GB))[2 * p + ((tid >> 3) & 1)] = mv;
  if ((tid & 255) == 255) r1 = u32x2{0, 0};
  {
    u32 a0 = __builtin_amdgcn_alignbit(r0[1], r0[0], 16), a1 = __builtin_amdgcn_alignbit(r0[2], r0[1], 16);
    u32 a2 = __builtin_amdgcn_alignbit(r0[3], r0[2], 16), a3 = __builtin_amdgcn_alignbit(r1[0], r0[3], 16);
    u32 a4 = __builtin_amdgcn_alignbit(r1[1], r1[0], 16);
    char* kc = smem + K2_KC + (tid >> 8) * K2_KCH + (tid & 255) * 16;
    *(u32x4*)(kc) = r0;
    *(u32x4*)(kc + K2_KCS) = u32x4{a0, a1, a2, a3};
    *(u32x4*)(kc + 2 * K2_KCS) = u32x4{r0[1], r0[2], r0[3], r1[0]};
    *(u32x4*)(kc + 3 * K2_KCS) = u32x4{a1, a2, a3, a4};
  }
  u32x4 hp;
  {
#pragma unroll
    for (int j = 0; j < 4; j++) hp[j] = pk(fmaf(bflo(xn[j]), sc, sh), fmaf(bfhi(xn[j]), sc, sh));
    char* u = smem + K2_U + cc * K2_UCH + 512 + g8 * 16;
    *(u32x4*)(u) = hp;
    *(u32x2*)(u + K2_US + 8) = u32x2{hp[0], hp[1]};
    *(u32x2*)(u + K2_US + 16) = u32x2{hp[2], hp[3]};
  }
  TS(1, 2);
  block_sync();
  TS(1, 3);
  const int ch = w & 1, vh = (w >> 1) & 1, tg = w >> 2;
  const int r = lane & 15, g = lane >> 4;
  const char* pa = smem + K2_KC + ch * K2_KCH + (3 - (r & 3)) * K2_KCS + 2 * (2040 - 8 * (r >> 2) + 8 * g) - 512 * 7 - 64 * (4 * vh + 3);
  const char* pb = smem + K2_U + ch * K2_UCH + ((r & 1) ? 0 : K2_US) + 2 * (256 + 32 * (r >> 1) + 8 * g) - 64 * (4 * vh + 3);
  f32x4 acc[4];
#pragma unroll
  for (int i = 0; i < 4; i++) acc[i] = f32x4{0.f, 0.f, 0.f, 0.f};
  auto prep = [&]() NOPK {
    const float kf = mat ? LN2 : LOG2E;
    const float cs = rbf(1.f + rbf(sum8(mcb))) * kf, ss = rbf(sum8(msb)) * kf;
    u32x4 o0, o1;
    f32x4 sums = {0.f, 0.f, 0.f, 0.f};
#pragma unroll
    for (int j = 0; j < 4; j++) {
      const float c0 = rdl(cs, 16 * j), c1 = rdl(cs, 16 * j + 8), s0 = rdl(ss, 16 * j), s1 = rdl(ss, 16 * j + 8);
      const float a0 = bflo(v[2 * j]), a1 = bflo(v[2 * j + 1]), b0 = bfhi(v[2 * j]), b1 = bfhi(v[2 * j + 1]);
      o0[j] = pk(a0 * c0, a1 * c1);
      o1[j] = pk(b0 * c0, b1 * c1);
      sums[0] = dot2(o0[j], 0x3f803f80u, sums[0]);
      sums[1] = dot2(o1[j], 0x3f803f80u, sums[1]);
      sums[2] = fmaf(a0, s0, fmaf(a1, s1, sums[2]));
      sums[3] = fmaf(b0, s0, fmaf(b1, s1, sums[3]));
    }
    *(f32x4*)(smem + K2_RED + tid * 16) = sums;
    if (lane < 56) {
      const char* dst = ws + WS_W1F + (size_t)(fb * 7 * 32 + (d0 >> 4)) * 1024 + (w & 1) * 512 + mat * 256;
      const u32 vo = (lane >> 3) * 32768 + (lane & 7) * 16;
      st16(dst, vo, o0);
      st16(dst + 128, vo, o1);
    }
  };
  if (tg == 0) conv_body<0>(pa, pb, acc, prep); else conv_body<1>(pa, pb, acc, prep);
  TS(1, 4);
  {
    float* Y = (float*)(smem + K2_Y) + (vh * 2 + ch) * 2560 + 40 * (r >> 1) + 4 * (r & 1) + 8 * g;
    const int t0 = tg ? 6 : 7, t1 = tg ? 1 : 0, t2 = tg ? 5 : 4, t3 = tg ? 2 : 3;
    *(f32x4*)(Y + 320 * t0) = acc[0];
    *(f32x4*)(Y + 320 * t1) = acc[1];
    *(f32x4*)(Y + 320 * t2) = acc[2];
    *(f32x4*)(Y + 320 * t3) = acc[3];
  }
  block_sync();
  TS(1, 5);
  {
    const float* Y = (const float*)(smem + K2_Y) + cc * 2560 + 8 * g8 + 8 * (g8 >> 2);
    const f32x4 ya = *(const f32x4*)Y + *(const f32x4*)(Y + 5120), yb = *(const f32x4*)(Y + 4) + *(const f32x4*)(Y + 5124);
    const float dk = cc ? bfhi(dsk) : bflo(dsk);
    u32x4 o;
#pragma unroll
    for (int j = 0; j < 4; j++) {
      const float y0 = j < 2 ? ya[2 * j] : yb[2 * j - 4], y1 = j < 2 ? ya[2 * j + 1] : yb[2 * j - 3];
      o[j] = pk(fmaf(ga, fmaf(bflo(hp[j]), dk, y0), bflo(xv[j])), fmaf(ga, fmaf(bfhi(hp[j]), dk, y1), bfhi(xv[j])));
    }
    st16(ws + WS_X1 + p * 128, (tid >> 3) * 32768 + (tid & 7) * 16, o);
  }
  if (tid < 224) {
    float s = 0.f;
#pragma unroll
    for (int q = 0; q < 8; q++) s += *(const float*)(smem + K2_RED + q * 1024 + tid * 4);
    const int fl = ((tid >> 2) & 7) * 2 + (tid & 1), kind = (tid >> 1) & 1, ft = fb * 7 + (tid >> 5);
    const int row = (fl & 3) + 4 * (fl >> 3) + 16 * ((fl >> 2) & 1) + 8 * mat;
    ((float*)(ws + WS_CD))[((dq * 112 + ft) * 2 + kind) * 32 + row] = s;
  }
  TSEND(1);
}

DEVINL void glds4k(const void* sbase, u32 voff, u32 ldsaddr) {
  asm volatile("s_mov_b32 m0, %2\n\ts_nop 0\n\t"
               "global_load_lds_dwordx4 %0, %1\n\tglobal_load_lds_dwordx4 %0, %1 offset:1024\n\t"
               "global_load_lds_dwordx4 %0, %1 offset:2048\n\tglobal_load_lds_dwordx4 %0, %1 offset:3072"
               ::"v"(voff), "s"(sbase), "s"(ldsaddr) : "memory");
}
DEVINL void gld4(const void* sbase, u32 voff, u32x4& a, u32x4& b, u32x4& c, u32x4& d) {
  asm volatile("global_load_dwordx4 %0, %4, %5\n\tglobal_load_dwordx4 %1, %4, %5 offset:1024\n\t"
               "global_load_dwordx4 %2, %4, %5 offset:2048\n\tglobal_load_dwordx4 %3, %4, %5 offset:3072"
               : "=&v"(a), "=&v"(b), "=&v"(c), "=&v"(d) : "v"(voff), "s"(sbase) : "memory");
}
template <int N> DEVINL void wait_tie(u32x4& a, u32x4& b, u32x4& c, u32x4& d) {
  asm volatile("s_waitcnt vmcnt(%4)" : "+v"(a), "+v"(b), "+v"(c), "+v"(d) : "n"(N) : "memory");
}
DEVINL void bar() {
  __builtin_amdgcn_s_barrier();
  asm volatile("" ::: "memory");
}
DEVINL u32x4 tr8(const char* p) {
  const u32x2 lo = tr_read(p), hi = tr_read(p + 256);
  return u32x4{lo[0], lo[1], hi[0], hi[1]};
}
struct Ep { float q0, v; };
DEVINL float swi(const f32x16& a, int i, const Ep& q) {
  const float G = a[(i & 3) + 8 * (i >> 2)], U = a[(i & 3) + 8 * (i >> 2) + 4];
  const float e = __builtin_amdgcn_exp2f(G * q.q0);
  return (G * U) * __builtin_amdgcn_rcpf(fmaf(e, q.v, q.v));
}
DEVINL u32 ln2_fold(f32x2 s, int h, Ep& q) {
  const float m = s[0] * (1.f / D);
  const float v = fmaxf(fmaf(-m, m, s[1] * (1.f / D)), 0.f) + 1e-5f;
  const float rs = __builtin_amdgcn_rsqf(v);
  q.q0 = -rs; q.v = v;
  const float val = h ? v * rs : -m;
  const float vhi = rbf(val);
  return pk(vhi, val - vhi);
}

__global__ NOPK __launch_bounds__(512) void k3(char* __restrict__ ws) {
  __shared__ __attribute__((aligned(16))) char smem[131072 + 1024];
  const int b = blockIdx.x, tid = threadIdx.x, lane = tid & 63;
  const int w = __builtin_amdgcn_readfirstlane(tid >> 6);
  TSDECL; TS(2, 0);
  const int xcd = b & 7, idx = b >> 3;
  const int tt = (xcd & 1) * 8 + (idx & 7), fs = (xcd >> 1) * 4 + (idx >> 3);
  f32x2* raw = (f32x2*)(smem + 131072);
  if (w == 0) {
    __builtin_amdgcn_s_setprio(3);
    const char* xb = ws + WS_X1 + tt * 131072;
    const u32 voff = lane * 16;
    const u32 lbase = (u32)(size_t)(LDS_AS char*)smem;
#pragma unroll
    for (int c = 0; c < 3; c++)
#pragma unroll
      for (int T = 0; T < 3; T++) glds4k(xb + T * 32768 + c * 4096, voff, lbase + T * 32768 + c * 4096);
    const char* rb = smem + (lane >> 2) * 64 + (lane & 3) * 8;
    const char* rb2 = rb + 65536;
    const u32x4 ones = {0x3f803f80u, 0x3f803f80u, 0x3f803f80u, 0x3f803f80u};
    f32x4 sg[8], sx[8];
#pragma unroll
    for (int i = 0; i < 8; i++) { sg[i] = f32x4{0.f, 0.f, 0.f, 0.f}; sx[i] = f32x4{0.f, 0.f, 0.f, 0.f}; }
    const bool diag = (lane >> 4) == ((lane & 15) >> 2);
    u32 tch = 0;
#pragma unroll
    for (int c = 0; c < 8; c++) {
      if (c == 0) wait_vm<24>(); else if (c <= 5) wait_vm<12>(); else if (c == 6) wait_vm<20>(); else wait_vm<39>();
      bar();
      TS(2, 1 + c);
      if (c >= 1 && c <= 5) {
#pragma unroll
        for (int T = 0; T < 3; T++) glds4k(xb + T * 32768 + (c + 2) * 4096, voff, lbase + T * 32768 + (c + 2) * 4096);
      }
      if (c == 5) {
#pragma unroll
        for (int q = 0; q < 2; q++) glds4k(xb + 98304 + q * 4096, voff, lbase + 98304 + q * 4096);
      }
      if (c == 6) {
#pragma unroll
        for (int q = 2; q < 8; q++) glds4k(xb + 98304 + q * 4096, voff, lbase + 98304 + q * 4096);
#pragma unroll
        for (int q = 0; q < 7; q++) touch(tch, ws + WS_W2F + idx * 57344 + q * 8192, lane * 128);
      }
#pragma unroll
      for (int kp = 0; kp < 2; kp++)
#pragma unroll
        for (int gi = 0; gi < 6; gi++) {
          const char* pp = (gi < 4 ? rb + (gi >> 1) * 32768 : rb2) + (c * 4 + kp * 2) * 1024 + (gi & 1) * 32;
          const u32x2 lo = tr_read(pp), hi = tr_read(pp + 1024);
          const u32x4 fr = {lo[0], lo[1], hi[0], hi[1]};
          sg[gi] = mfma16(fr, fr, sg[gi]);
          sx[gi] = mfma16(ones, fr, sx[gi]);
        }
    }
#pragma unroll
    for (int gi = 0; gi < 6; gi++) {
      const float gsel = (lane & 2) ? ((lane & 1) ? sg[gi][3] : sg[gi][2]) : ((lane & 1) ? sg[gi][1] : sg[gi][0]);
      if (diag) raw[gi * 16 + (lane & 15)] = f32x2{sx[gi][0], gsel};
    }
    wait_vm<7>();
    raw_barrier();
    TS(2, 9);
#pragma unroll
    for (int kp = 0; kp < 16; kp++)
#pragma unroll
      for (int gi = 6; gi < 8; gi++) {
        const char* pp = rb2 + 32768 + kp * 2048 + (gi & 1) * 32;
        const u32x2 lo = tr_read(pp), hi = tr_read(pp + 1024);
        const u32x4 fr = {lo[0], lo[1], hi[0], hi[1]};
        sg[gi] = mfma16(fr, fr, sg[gi]);
        sx[gi] = mfma16(ones, fr, sx[gi]);
      }
#pragma unroll
    for (int gi = 6; gi < 8; gi++) {
      const float gsel = (lane & 2) ? ((lane & 1) ? sg[gi][3] : sg[gi][2]) : ((lane & 1) ? sg[gi][1] : sg[gi][0]);
      if (diag) raw[gi * 16 + (lane & 15)] = f32x2{sx[gi][0], gsel};
    }
    TS(2, 10);
    touch_end(tch);
    raw_barrier();
  } else {
    const int h = lane >> 5, c = lane & 31;
    const int ft = fs * 7 + w - 1;
    const char* wb = ws + WS_W1F + (size_t)ft * 32768;
    const int fl = (c & 3) + 8 * ((c >> 2) & 1) + 4 * (c >> 4);
    const u32 voff = (h * 32 + ((c >> 3) & 1) * 16 + (fl & 1) * 8 + (fl >> 1)) * 16;
    u32x4 A[32];
#pragma unroll
    for (int i = 0; i < 3; i++) gld4(wb + i * 4096, voff, A[4 * i], A[4 * i + 1], A[4 * i + 2], A[4 * i + 3]);
    const char* rb = smem + h * 512 + ((lane >> 2) & 3) * 64 + ((lane >> 4) & 1) * 32 + (lane & 3) * 8;
    const char* rb2 = rb + 65536;
    u32x4 cda, cdb;
    f32x16 acc[4];
#pragma unroll
    for (int T = 0; T < 4; T++)
#pragma unroll
      for (int i = 0; i < 16; i++) acc[T][i] = 0.f;
    wait_tie<8>(A[0], A[1], A[2], A[3]);
    bar();
    TS(2, 1);
    u32x4 Bc[3], Bn[3];
    Bc[0] = tr8(rb); Bc[1] = tr8(rb + 32768); Bc[2] = tr8(rb2);
    f32x2 rw[3];
#pragma unroll
    for (int ks = 0; ks < 32; ks++) {
      if ((ks & 3) == 3) {
        const int c1 = (ks >> 2) + 1;
        if (c1 <= 5) wait_tie<4>(A[4 * c1], A[4 * c1 + 1], A[4 * c1 + 2], A[4 * c1 + 3]);
        else if (c1 == 6) wait_tie<12>(A[4 * c1], A[4 * c1 + 1], A[4 * c1 + 2], A[4 * c1 + 3]);
        else if (c1 == 7) wait_tie<8>(A[4 * c1], A[4 * c1 + 1], A[4 * c1 + 2], A[4 * c1 + 3]);
        else asm volatile("s_waitcnt vmcnt(0)" : "+v"(cda), "+v"(cdb)::"memory");
        bar();
        TS(2, 1 + c1);
        if (c1 <= 5) gld4(wb + (c1 + 2) * 4096, voff, A[4 * c1 + 8], A[4 * c1 + 9], A[4 * c1 + 10], A[4 * c1 + 11]);
        if (c1 == 5) {
          const char* cp = ws + WS_CD + ft * 256;
#pragma unroll
          for (int q = 0; q < 4; q++) {
            asm volatile("global_load_dword %0, %1, %2" : "=v"(cda[q]) : "v"(lane * 4), "s"(cp + q * 28672) : "memory");
            asm volatile("global_load_dword %0, %1, %2" : "=v"(cdb[q]) : "v"(lane * 4), "s"(cp + (q + 4) * 28672) : "memory");
          }
        }
      }
      if (ks < 31) {
        Bn[0] = tr8(rb + (ks + 1) * 1024); Bn[1] = tr8(rb + 32768 + (ks + 1) * 1024); Bn[2] = tr8(rb2 + (ks + 1) * 1024);
      } else {
        Bn[0] = tr8(rb2 + 32768);
#pragma unroll
        for (int T = 0; T < 3; T++) rw[T] = raw[T * 32 + c];
      }
      __builtin_amdgcn_sched_barrier(0);
#pragma unroll
      for (int T = 0; T < 3; T++) acc[T] = mfma32(A[ks], Bc[T], acc[T]);
      __builtin_amdgcn_sched_barrier(0);
#pragma unroll
      for (int T = 0; T < 3; T++) Bc[T] = Bn[T];
    }
    const float cd = ((u2f(cda[0]) + u2f(cda[1])) + (u2f(cda[2]) + u2f(cda[3]))) + ((u2f(cdb[0]) + u2f(cdb[1])) + (u2f(cdb[2]) + u2f(cdb[3])));
    const float chi = rbf(cd);
    const u32x4 Ax = {pk(chi, chi), pk(cd - chi, cd - chi), 0, 0};
    const char* hb = ws + WS_HF + ((size_t)(tt * 4) * 112 + ft) * 1024;
    Ep q[4];
    float o[8];
#pragma unroll
    for (int ks = 0; ks < 32; ks++) {
      if (ks < 31) Bn[0] = tr8(rb2 + 32768 + (ks + 1) * 1024);
      __builtin_amdgcn_sched_barrier(0);
      acc[3] = mfma32(A[ks], Bc[0], acc[3]);
      if (ks < 3) {
        const u32 be = ln2_fold(rw[ks], h, q[ks]);
        acc[ks] = mfma32(Ax, u32x4{be, be, 0, 0}, acc[ks]);
      } else if (ks >= 4 && ks < 28) {
        const int T = (ks - 4) >> 3, i = (ks - 4) & 7;
        o[i] = swi(acc[T], i, q[T]);
        if (i == 7) st16(hb + T * 114688, lane * 16, u32x4{pk(o[0], o[1]), pk(o[2], o[3]), pk(o[4], o[5]), pk(o[6], o[7])});
      }
      __builtin_amdgcn_sched_barrier(0);
      Bc[0] = Bn[0];
    }
    TS(2, 10);
    bar();
    TS(2, 11);
    {
      const u32 be = ln2_fold(raw[96 + c], h, q[3]);
      acc[3] = mfma32(Ax, u32x4{be, be, 0, 0}, acc[3]);
#pragma unroll
      for (int i = 0; i < 8; i++) o[i] = swi(acc[3], i, q[3]);
      st16(hb + 3 * 114688, lane * 16, u32x4{pk(o[0], o[1]), pk(o[2], o[3]), pk(o[4], o[5]), pk(o[6], o[7])});
    }
  }
  TSEND(2);
}

__global__ NOPK __launch_bounds__(256) void k4(const char* __restrict__ ws, u16* __restrict__ out) {
  __shared__ __attribute__((aligned(16))) char smem[4 * 4 * 4096];
  const int b = blockIdx.x, tid = threadIdx.x, lane = tid & 63;
  const int w = __builtin_amdgcn_readfirstlane(tid >> 6);
  TSDECL; TS(3, 0);
  const int xcd = b & 7, idx = b >> 3;
  const int tt = (xcd & 1) * 16 + (xcd >> 1) * 4 + (idx & 3), dt = idx >> 2;
  const int h = lane >> 5, c = lane & 31;
  const int dl = (c & 3) + 4 * (c >> 3) + 16 * ((c >> 2) & 1);
  const u32x4* ap = (const u32x4*)(ws + WS_W2F) + ((size_t)(dt * 2) * 112 + w * 28) * 64 + (h * 32 + (dl & 1) * 16 + (dl >> 1));
  const u32x4* bp = (const u32x4*)(ws + WS_HF) + ((size_t)(tt * 2) * 112 + w * 28) * 64 + lane;
  constexpr int PF = K4_PF;
  u32x4 A0[28], A1[28], B0[28], B1[28];
#pragma unroll
  for (int i = 0; i < PF; i++) {
    B0[i] = bp[i * 64]; A0[i] = ap[i * 64]; B1[i] = bp[(112 + i) * 64]; A1[i] = ap[(112 + i) * 64];
  }
  const int dbase = dt * 64 + (w & 1) * 32 + h * 16, tok = tt * 64 + (w >> 1) * 32 + c;
  u16 xr[16];
  f32x4 gb[4];
  f32x16 acc[4];
#pragma unroll
  for (int T = 0; T < 4; T++)
#pragma unroll
    for (int i = 0; i < 16; i++) acc[T][i] = 0.f;
#pragma unroll
  for (int i = 0; i < 28; i++) {
    __builtin_amdgcn_sched_barrier(0);
    if (i + PF < 28) {
      const int j = i + PF;
      B0[j] = bp[j * 64]; A0[j] = ap[j * 64]; B1[j] = bp[(112 + j) * 64]; A1[j] = ap[(112 + j) * 64];
    }
    if (i == 28 - PF) {
      const u16* xp = (const u16*)(ws + WS_X1) + ((size_t)(tok >> 5) * 512 + dbase) * 32 + c;
#pragma unroll
      for (int q = 0; q < 16; q++) xr[q] = xp[q * 32];
#pragma unroll
      for (int q = 0; q < 4; q++) gb[q] = *(const f32x4*)(ws + WS_GB + (dbase + q * 4) * 4);
    }
    __builtin_amdgcn_sched_barrier(0);
    acc[0] = mfma32(A0[i], B0[i], acc[0]);
    acc[2] = mfma32(A0[i], B1[i], acc[2]);
    acc[1] = mfma32(A1[i], B0[i], acc[1]);
    acc[3] = mfma32(A1[i], B1[i], acc[3]);
    if (i == 0) TS(3, 1);
    if (i == 13) TS(3, 2);
  }
  TS(3, 3);
#pragma unroll
  for (int T = 0; T < 4; T++) {
    if (T != w) {
#pragma unroll
      for (int q = 0; q < 4; q++)
        *(f32x4*)(smem + ((T * 4 + w) * 4 + q) * 1024 + lane * 16) = f32x4{acc[T][4 * q], acc[T][4 * q + 1], acc[T][4 * q + 2], acc[T][4 * q + 3]};
    }
  }
  f32x16 mine = w == 0 ? acc[0] : w == 1 ? acc[1] : w == 2 ? acc[2] : acc[3];
  TS(3, 4);
  block_sync();
  TS(3, 5);
#pragma unroll
  for (int s = 1; s < 4; s++) {
    const int src = (w + s) & 3;
#pragma unroll
    for (int q = 0; q < 4; q++) {
      f32x4 v = *(const f32x4*)(smem + ((w * 4 + src) * 4 + q) * 1024 + lane * 16);
#pragma unroll
      for (int j = 0; j < 4; j++) mine[4 * q + j] += v[j];
    }
  }
  u32 o[8];
#pragma unroll
  for (int q = 0; q < 8; q++) {
    float v0 = fmaf(gb[q >> 1][(2 * q) & 3], mine[2 * q], bflo(xr[2 * q]));
    float v1 = fmaf(gb[q >> 1][(2 * q + 1) & 3], mine[2 * q + 1], bflo(xr[2 * q + 1]));
    o[q] = pk(v0, v1);
  }
  u32x4* op = (u32x4*)(out + (size_t)tok * D + dbase);
  op[0] = u32x4{o[0], o[1], o[2], o[3]};
  op[1] = u32x4{o[4], o[5], o[6], o[7]};
  TSEND(3);
}

extern "C" void kernel_launch(void* const* d_in, const int* in_sizes, int n_in, void* d_out, int out_size, void* d_ws,
                              size_t ws_size, hipStream_t stream) {
  const u16* const* in = (const u16* const*)d_in;
  char* ws = (char*)d_ws;
  k1<<<256, 512, 0, stream>>>(in[0], in[1], in[2], in[3], in[4], in[5], in[6], in[7], in[12], in[13], in[14], in[10], in[11], ws);
  k2<<<256, 512, 0, stream>>>(in[8], in[10], in[11], ws);
  k3<<<256, 512, 0, stream>>>(ws);
  k4<<<256, 256, 0, stream>>>(ws, (u16*)d_out);
}
```

```cpp
#include <hip/hip_runtime.h>
#include <stdint.h>

typedef unsigned int u32;
typedef unsigned short u16;
typedef __attribute__((ext_vector_type(2))) float f32x2;
typedef __attribute__((ext_vector_type(4))) float f32x4;
typedef __attribute__((ext_vector_type(16))) float f32x16;
typedef __attribute__((ext_vector_type(2))) u32 u32x2;
typedef __attribute__((ext_vector_type(4))) u32 u32x4;
typedef __attribute__((ext_vector_type(2))) __bf16 bf16x2;
typedef __attribute__((ext_vector_type(8))) __bf16 bf16x8;
typedef __attribute__((ext_vector_type(4))) short s16x4;
#define LDS_AS __attribute__((address_space(3)))
#if defined(__HIP_DEVICE_COMPILE__)
#define NOPK __attribute__((target("no-packed-fp32-ops")))
#else
#define NOPK
#endif
#define DEVINL __device__ __forceinline__ NOPK

constexpr int L = 2048, D = 512, NS = 128, F = 1792;

constexpr size_t WS_E = 0;
constexpr size_t WS_FW = 4096;
constexpr size_t WS_FX = WS_FW + 32768;
constexpr size_t WS_FH = WS_FX + 32768;
constexpr size_t WS_MG = WS_FH + 65536;
constexpr size_t WS_GB = WS_MG + 98304;
constexpr size_t WS_XG = 262144;
constexpr size_t WS_X1 = WS_XG + (4u << 20);
constexpr size_t WS_W1F = WS_X1 + (2u << 20);
constexpr size_t WS_W2F = WS_W1F + 112 * 32 * 1024;
constexpr size_t WS_HF = WS_W2F + 16 * 112 * 1024;
constexpr size_t WS_CD = WS_HF + 64 * 112 * 1024;
constexpr int SPIN_CAP = 40000;
#define W1A 2
#define FXL 4

#define u2f(x) __builtin_bit_cast(float, (u32)(x))
#define i2f(x) __builtin_bit_cast(float, (int)(x))
#define f2u(x) __builtin_bit_cast(u32, (float)(x))
#define f2i(x) __builtin_bit_cast(int, (float)(x))
#define TSDECL
#define TS(slot)
#define TC(slot)
#define TSF(lo, hi)
#define TSEND()
DEVINL float bflo(u32 u) { return u2f(u << 16); }
DEVINL float bfhi(u32 u) { return u2f(u & 0xffff0000u); }
DEVINL u32 pk(float a, float b) {
  f32x2 v = {a, b};
  return __builtin_bit_cast(u32, __builtin_convertvector(v, bf16x2));
}
DEVINL float rbf(float a) { return bflo(pk(a, a)); }
template <int CTRL> DEVINL float dpp_add(float v) {
  return v + i2f(__builtin_amdgcn_update_dpp(0, f2i(v), CTRL, 0xf, 0xf, true));
}
DEVINL float row4_sum(float v) {
  auto a = __builtin_amdgcn_permlane16_swap(f2u(v), f2u(v), false, false);
  v = u2f(a[0]) + u2f(a[1]);
  auto b = __builtin_amdgcn_permlane32_swap(f2u(v), f2u(v), false, false);
  return u2f(b[0]) + u2f(b[1]);
}
DEVINL float rs32(float a, float b) {
  auto r = __builtin_amdgcn_permlane32_swap(f2u(a), f2u(b), false, false);
  return u2f(r[0]) + u2f(r[1]);
}
DEVINL float rs16(float a, float b) {
  auto r = __builtin_amdgcn_permlane16_swap(f2u(a), f2u(b), false, false);
  return u2f(r[0]) + u2f(r[1]);
}
DEVINL float wave_sum(float v) { return row4_sum(dpp_add<0x140>(dpp_add<0x141>(dpp_add<0x4E>(dpp_add<0xB1>(v))))); }
DEVINL f32x4 mfma16(u32x4 a, u32x4 b, f32x4 c) {
  return __builtin_amdgcn_mfma_f32_16x16x32_bf16(__builtin_bit_cast(bf16x8, a), __builtin_bit_cast(bf16x8, b), c, 0, 0, 0);
}
DEVINL f32x16 mfma32(u32x4 a, u32x4 b, f32x16 c) {
  return __builtin_amdgcn_mfma_f32_32x32x16_bf16(__builtin_bit_cast(bf16x8, a), __builtin_bit_cast(bf16x8, b), c, 0, 0, 0);
}
DEVINL float dot2(u32 a, u32 b, float c) {
  return __builtin_amdgcn_fdot2_f32_bf16(__builtin_bit_cast(bf16x2, a), __builtin_bit_cast(bf16x2, b), c, false);
}
DEVINL void lds_barrier() {
  asm volatile("s_waitcnt lgkmcnt(0)" ::: "memory");
  __builtin_amdgcn_s_barrier();
  asm volatile("" ::: "memory");
}
DEVINL void bar() {
  asm volatile("" ::: "memory");
  __builtin_amdgcn_s_barrier();
  asm volatile("" ::: "memory");
}
DEVINL void st16(const void* sbase, u32 voff, u32x4 v) {
  asm volatile("global_store_dwordx4 %0, %1, %2 sc1\n\ts_nop 1" ::"v"(voff), "v"(v), "s"(sbase) : "memory");
}
DEVINL void st8(const void* sbase, u32 voff, u32x2 v) {
  asm volatile("global_store_dwordx2 %0, %1, %2 sc1\n\ts_nop 1" ::"v"(voff), "v"(v), "s"(sbase) : "memory");
}
DEVINL void st4(const void* sbase, u32 voff, u32 v) {
  asm volatile("global_store_dword %0, %1, %2 sc1\n\ts_nop 1" ::"v"(voff), "v"(v), "s"(sbase) : "memory");
}
template <int LANES> DEVINL void st8m(const void* sbase, u32 voff, u32x2 v) {
  asm volatile("s_mov_b64 exec, %3\n\tglobal_store_dwordx2 %0, %1, %2 sc1\n\ts_mov_b64 exec, -1\n\ts_nop 0"
               ::"v"(voff), "v"(v), "s"(sbase), "n"((1 << LANES) - 1) : "memory");
}
DEVINL void st4one(const void* sbase, u32 v) {
  asm volatile("s_mov_b64 exec, 1\n\tglobal_store_dword %0, %1, %2 sc1\n\ts_mov_b64 exec, -1\n\ts_nop 0"
               ::"v"(0), "v"(v), "s"(sbase) : "memory");
}
DEVINL void st16x2_56(const void* sbase, u32 voff, u32x4 a, u32x4 b) {
  asm volatile("s_mov_b32 exec_hi, 0xffffff\n\tglobal_store_dwordx4 %0, %1, %3 sc1\n\tglobal_store_dwordx4 %0, %2, %3 offset:128 sc1\n\t"
               "s_mov_b32 exec_hi, -1\n\ts_nop 0" ::"v"(voff), "v"(a), "v"(b), "s"(sbase) : "memory");
}
DEVINL void sld4(u32& d, const void* p) { asm volatile("s_load_dword %0, %1, 0" : "=s"(d) : "s"(p) : "memory"); }
template <int OFF = 0> DEVINL void ld16(u32x4& d, const void* sbase, u32 voff) {
  asm volatile("global_load_dwordx4 %0, %1, %2 offset:%3 sc1" : "=v"(d) : "v"(voff), "s"(sbase), "n"(OFF) : "memory");
}
DEVINL void ld16k(u32x4& d, const char* sbase, int kb, u32 voff) {
  const char* sb = sbase + (kb >> 2) * 4096;
  if ((kb & 3) == 0) ld16<0>(d, sb, voff); else if ((kb & 3) == 1) ld16<1024>(d, sb, voff);
  else if ((kb & 3) == 2) ld16<2048>(d, sb, voff); else ld16<3072>(d, sb, voff);
}
DEVINL void ld8(u32x2& d, const void* sbase, u32 voff) {
  asm volatile("global_load_dwordx2 %0, %1, %2 sc1" : "=v"(d) : "v"(voff), "s"(sbase) : "memory");
}
template <int OFF = 0> DEVINL void ld4(u32& d, const void* sbase, u32 voff) {
  asm volatile("global_load_dword %0, %1, %2 offset:%3 sc1" : "=v"(d) : "v"(voff), "s"(sbase), "n"(OFF) : "memory");
}
template <int OFF = 0> DEVINL void lp16(u32x4& d, const void* sbase, u32 voff) {
  asm volatile("global_load_dwordx4 %0, %1, %2 offset:%3" : "=v"(d) : "v"(voff), "s"(sbase), "n"(OFF) : "memory");
}
template <int OFF = 0> DEVINL void lp4(u32& d, const void* sbase, u32 voff) {
  asm volatile("global_load_dword %0, %1, %2 offset:%3" : "=v"(d) : "v"(voff), "s"(sbase), "n"(OFF) : "memory");
}
template <int OFF = 0> DEVINL void lp2(u32& d, const void* sbase, u32 voff) {
  asm volatile("global_load_ushort %0, %1, %2 offset:%3" : "=v"(d) : "v"(voff), "s"(sbase), "n"(OFF) : "memory");
}
template <class T> DEVINL void tie(T& a) { asm volatile("" : "+v"(a)); }
template <int N> DEVINL void wait_vm() { asm volatile("s_waitcnt vmcnt(%0)" ::"n"(N) : "memory"); }
DEVINL bool all_lanes(bool ok) { return __builtin_amdgcn_ballot_w64(!ok) == 0; }
DEVINL void spin4(u32 f, u32 tag, const void* sbase, u32 voff) {
#pragma unroll 1
  for (int it = 0; it < SPIN_CAP && !all_lanes(f == tag); it++) {
    __builtin_amdgcn_s_sleep(1);
    ld4(f, sbase, voff); wait_vm<0>(); tie(f);
  }
}
DEVINL bool arrive_last(char* smem_word, int n) {
  u32 old = 0;
  if (__builtin_amdgcn_mbcnt_hi(~0u, __builtin_amdgcn_mbcnt_lo(~0u, 0)) == 0)
    old = __hip_atomic_fetch_add((LDS_AS u32*)smem_word, 1u, __ATOMIC_RELAXED, __HIP_MEMORY_SCOPE_WORKGROUP);
  return __builtin_amdgcn_readfirstlane(old) == (u32)(n - 1);
}
DEVINL u32x2 tr_read(const char* p) {
  return __builtin_bit_cast(u32x2, __builtin_amdgcn_ds_read_tr16_b64_v4i16((LDS_AS s16x4*)p));
}
DEVINL float rdl(float v, int l) { return i2f(__builtin_amdgcn_readlane(f2i(v), l)); }
DEVINL float sel2(int c, float a, float b) { return c ? b : a; }
DEVINL u32 swap2(u32 v) { return (u32)__builtin_amdgcn_update_dpp(0, (int)v, 0x4E, 0xf, 0xf, true); }
DEVINL u32 swap1(u32 v) { return (u32)__builtin_amdgcn_update_dpp(0, (int)v, 0xB1, 0xf, 0xf, true); }
constexpr float LOG2E = 1.4426950408889634f, LN2 = 0.6931471805599453f, INV2PI = 0.15915494309189535f;

constexpr int A_TAB = 0, A_P = 8 * 1536, A_KC = A_P + 24576, A_KCS = 4112, A_GV = A_KC + 2 * A_KCS;
constexpr int B_KC = 47360, B_KCS = 4160, B_KCH = 4 * 4160, B_U = B_KC + 2 * B_KCH, B_US = 4640, B_UCH = 2 * 4640;
constexpr int B_Y = B_U + 2 * B_UCH, B_RED = B_Y + 40960;
constexpr int A_XT = B_Y;
constexpr int D_SRC = 16 * 576, D_TILE = 8 * D_SRC;
constexpr int C_RAW = 148480, L_CNT = C_RAW + 1024, LDS_BYTES = L_CNT + 64;
static_assert(A_GV + 2048 <= B_KC && B_RED + 8192 <= C_RAW && 2 * D_TILE <= C_RAW, "lds map");

template <int TG> DEVINL void conv_load(const char* pa, const char* pb, int nu, u32x4 (&A)[8], u32x4 (&B)[8]) {
  constexpr int T0 = TG ? 6 : 7;
#pragma unroll
  for (int i = 0; i <= T0; i++) {
    A[i] = *(const u32x4*)(pa + 512 * (7 - i) + 64 * (3 - nu));
    B[T0 - i] = *(const u32x4*)(pb + 512 * (T0 - i) + 64 * (3 - nu));
  }
}
template <int TG> DEVINL void conv_mma(const u32x4 (&A)[8], const u32x4 (&B)[8], f32x4 (&acc)[4]) {
  constexpr int T0 = TG ? 6 : 7, T1 = TG ? 1 : 0, T2 = TG ? 5 : 4, T3 = TG ? 2 : 3;
#pragma unroll
  for (int mu = 0; mu <= T0; mu++) {
    acc[0] = mfma16(A[mu], B[T0 - mu], acc[0]);
    if (mu >= T0 - T2) acc[2] = mfma16(A[mu - (T0 - T2)], B[T0 - mu], acc[2]);
    if (mu >= T0 - T3) acc[3] = mfma16(A[mu - (T0 - T3)], B[T0 - mu], acc[3]);
    if (mu >= T0 - T1) acc[1] = mfma16(A[mu - (T0 - T1)], B[T0 - mu], acc[1]);
  }
}
template <int TG, class FN, class FM> DEVINL void conv_body(const char* pa, const char* pb, f32x4 (&acc)[4], FN&& filler, FM&& mid) {
  u32x4 A0[8], B0[8], A1[8], B1[8];
  conv_load<TG>(pa, pb, 0, A0, B0);
  conv_load<TG>(pa, pb, 1, A1, B1);
  __builtin_amdgcn_sched_barrier(0);
  filler();
  conv_mma<TG>(A0, B0, acc);
  __builtin_amdgcn_sched_barrier(0);
  conv_load<TG>(pa, pb, 2, A0, B0);
  __builtin_amdgcn_sched_barrier(0);
  conv_mma<TG>(A1, B1, acc);
  __builtin_amdgcn_sched_barrier(0);
  conv_load<TG>(pa, pb, 3, A1, B1);
  __builtin_amdgcn_sched_barrier(0);
  conv_mma<TG>(A0, B0, acc);
  __builtin_amdgcn_sched_barrier(0);
  mid();
  __builtin_amdgcn_sched_barrier(0);
  conv_mma<TG>(A1, B1, acc);
}
DEVINL void glds4k(const void* sbase, u32 voff, u32 ldsaddr) {
  asm volatile("s_mov_b32 m0, %2\n\ts_nop 0\n\t"
               "global_load_lds_dwordx4 %0, %1 sc1\n\tglobal_load_lds_dwordx4 %0, %1 offset:1024 sc1\n\t"
               "global_load_lds_dwordx4 %0, %1 offset:2048 sc1\n\tglobal_load_lds_dwordx4 %0, %1 offset:3072 sc1"
               ::"v"(voff), "s"(sbase), "s"(ldsaddr) : "memory");
}
DEVINL u32x4 tr8(const char* p) {
  const u32x2 lo = tr_read(p), hi = tr_read(p + 256);
  return u32x4{lo[0], lo[1], hi[0], hi[1]};
}
struct Ep { float q0, v; };
DEVINL float swi(const f32x16& a, int i, const Ep& q) {
  const float G = a[(i & 3) + 8 * (i >> 2)], U = a[(i & 3) + 8 * (i >> 2) + 4];
  const float e = __builtin_amdgcn_exp2f(G * q.q0);
  return (G * U) * __builtin_amdgcn_rcpf(fmaf(e, q.v, q.v));
}
DEVINL u32 ln2_fold(f32x2 s, int h, Ep& q) {
  const float m = s[0] * (1.f / D);
  const float v = fmaxf(fmaf(-m, m, s[1] * (1.f / D)), 0.f) + 1e-5f;
  const float rs = __builtin_amdgcn_rsqf(v);
  q.q0 = -rs; q.v = v;
  const float val = h ? v * rs : -m;
  const float vhi = rbf(val);
  return pk(vhi, val - vhi);
}
DEVINL void ln2_acc(const char* rb, int kp0, int kp1, f32x4 (&sg)[2][2], f32x4 (&sx)[2][2]) {
  const u32x4 ones = {0x3f803f80u, 0x3f803f80u, 0x3f803f80u, 0x3f803f80u};
  u32x4 fr[4][2];
  auto rd = [&](int n) NOPK {
#pragma unroll
    for (int gi = 0; gi < 2; gi++) {
      const char* pp = rb + (n & 1) * 32768 + (kp0 + (n >> 1)) * 2048 + gi * 32;
      const u32x2 lo = tr_read(pp), hi = tr_read(pp + 1024);
      fr[n & 3][gi] = u32x4{lo[0], lo[1], hi[0], hi[1]};
    }
  };
  rd(0); rd(1); rd(2);
#pragma unroll
  for (int n = 0; n < 2 * (kp1 - kp0); n++) {
    if (n + 3 < 2 * (kp1 - kp0)) rd(n + 3);
    __builtin_amdgcn_sched_barrier(0);
#pragma unroll
    for (int gi = 0; gi < 2; gi++) {
      sg[n & 1][gi] = mfma16(fr[n & 3][gi], fr[n & 3][gi], sg[n & 1][gi]);
      sx[n & 1][gi] = mfma16(ones, fr[n & 3][gi], sx[n & 1][gi]);
    }
    __builtin_amdgcn_sched_barrier(0);
  }
}
DEVINL void ln2_put(f32x2* raw, int lane, const f32x4 (&sg)[2], const f32x4 (&sx)[2]) {
#pragma unroll
  for (int gi = 0; gi < 2; gi++) {
    const float gsel = (lane & 2) ? ((lane & 1) ? sg[gi][3] : sg[gi][2]) : ((lane & 1) ? sg[gi][1] : sg[gi][0]);
    if ((lane >> 4) == ((lane & 15) >> 2)) raw[gi * 16 + (lane & 15)] = f32x2{sx[gi][0], gsel};
  }
}

typedef __attribute__((address_space(1))) unsigned long long gu64;
typedef __attribute__((address_space(1))) u32 gu32;
DEVINL void put_granule(char* p, u32 val, u32 tag) {
  __hip_atomic_store((gu64*)p, ((unsigned long long)tag << 32) | val, __ATOMIC_RELAXED, __HIP_MEMORY_SCOPE_AGENT);
}
DEVINL void put_flag(char* p, u32 tag) { __hip_atomic_store((gu32*)p, tag, __ATOMIC_RELAXED, __HIP_MEMORY_SCOPE_AGENT); }

__global__ NOPK __launch_bounds__(512, 2) void fused(
    const u16* __restrict__ x, const u16* __restrict__ Are, const u16* __restrict__ Aim, const u16* __restrict__ Bre,
    const u16* __restrict__ Bim, const u16* __restrict__ Cre, const u16* __restrict__ Cim, const u16* __restrict__ logdt,
    const u16* __restrict__ dskip, const u16* __restrict__ w1, const u16* __restrict__ w3, const u16* __restrict__ w2,
    const u16* __restrict__ cvec, const u16* __restrict__ wmod, char* ws, u16* __restrict__ out) {
  __shared__ __attribute__((aligned(16))) char smem[LDS_BYTES];
  const int b = blockIdx.x, tid = threadIdx.x, lane = tid & 63;
  const int w = __builtin_amdgcn_readfirstlane(tid >> 6);
  TSDECL; TS(0);
  const int xg = b & 7, idx = b >> 3;
  const int p = xg * 32 + idx;
  const int r = lane & 15, g = lane >> 4;
  asm volatile("" ::"s"(x), "s"(Are), "s"(Aim), "s"(Bre), "s"(Bim), "s"(Cre), "s"(Cim), "s"(logdt), "s"(dskip), "s"(w1), "s"(w3),
               "s"(w2), "s"(cvec), "s"(wmod), "s"(ws), "s"(out));
  u32x4 xr, wm0, wm1;
  u32 vare, vaim, vbre, vbim, vcre, vcim, c0, c1;
  const int cg = xg * 24 + (idx < 24 ? idx : 23);
  u32 e_prev, ldt2, dsk;
  lp16(xr, x + b * 4096, tid * 16);
  sld4(e_prev, ws + WS_E); sld4(ldt2, logdt + 2 * p); sld4(dsk, dskip + 2 * p);
  {
    const u32 so = (p * 256 + w * 32 + (lane & 31)) * 2;
    lp2(vare, Are, so); lp2(vaim, Aim, so); lp2(vbre, Bre, so); lp2(vbim, Bim, so); lp2(vcre, Cre, so); lp2(vcim, Cim, so);
    const u32 mo = (tid >> 3) * 6144 + (tid & 7) * 16;
    const u16* wmp = wmod + (cg & 3) * 128 * 3072 + (cg >> 2) * 64;
    lp16(wm0, wmp, mo); lp16(wm1, wmp + 64 * 3072, mo);
    lp2(c0, cvec + (cg & 3) * 128, (tid >> 3) * 2); lp2<128>(c1, cvec + (cg & 3) * 128, (tid >> 3) * 2);
  }
  TS(1);
  if (tid < 32) *(u32x4*)(smem + B_KC + (tid >> 2) * B_KCS + 4096 + (tid & 3) * 16) = u32x4{0, 0, 0, 0};
  else if (tid < 36) *(u32x4*)(smem + L_CNT + (tid - 32) * 16) = u32x4{0, 0, 0, 0};
  else if (tid >= 64 && tid < 64 + 132) {
    int i = tid - 64, cp = i / 33, e = i - cp * 33;
    if (e < 32 || (cp & 1)) *(u32x2*)(smem + B_U + cp * B_US + e * 16) = u32x2{0, 0};
    if (e < 32) *(u32x2*)(smem + B_U + cp * B_US + e * 16 + 8) = u32x2{0, 0};
  }
  asm volatile("s_waitcnt lgkmcnt(0)" : "+s"(e_prev), "+s"(ldt2), "+s"(dsk)::"memory");
  const u32 tag = e_prev * 1664525u + 1013904223u;
  wait_vm<10>(); tie(xr);
  {
    *(u32x4*)(smem + A_XT + w * 1056 + lane * 16) = xr;
    float f[8];
#pragma unroll
    for (int j = 0; j < 4; j++) { f[2 * j] = bflo(xr[j]); f[2 * j + 1] = bfhi(xr[j]); }
    const float m = wave_sum(((f[0] + f[1]) + (f[2] + f[3])) + ((f[4] + f[5]) + (f[6] + f[7]))) * (1.f / D);
    float s = 0.f;
#pragma unroll
    for (int j = 0; j < 8; j++) { f[j] -= m; s = fmaf(f[j], f[j], s); }
    const float sd = __builtin_amdgcn_sqrtf(rbf(rbf(wave_sum(s) * (1.f / D)) + 1e-5f));
    if (lane == 0) *(u32*)(smem + A_XT + w * 1056 + 1024) = pk(m, sd);
  }
  TS(2);
  wait_vm<4>(); tie(vare); tie(vaim); tie(vbre); tie(vbim); tie(vcre); tie(vcim);
  char* tab = smem + A_TAB + w * 1536;
  {
    const float dt = rbf(__builtin_amdgcn_exp2f((w >> 2 ? bfhi(ldt2) : bflo(ldt2)) * LOG2E));
    const float a = dt * bflo(vare) * LOG2E, bq = dt * bflo(vaim) * INV2PI;
    const float br = dt * bflo(vbre), bi = dt * bflo(vbim), cr = bflo(vcre), ci = bflo(vcim);
    const float m = lane < 32 ? 1.f : 256.f;
    const float e = __builtin_amdgcn_exp2f(a * m), ph = __builtin_amdgcn_fractf(-bq * m);
    const float ex = e * __builtin_amdgcn_cosf(ph), ey = e * __builtin_amdgcn_sinf(ph);
    char* t = tab + (lane & 31) * 48;
    *(f32x2*)(t + 16 + (lane >> 5) * 8) = f32x2{ex, ey};
    f32x4 o = {a, bq, cr * br - ci * bi, cr * bi + ci * br};
    if (lane >= 32) o = f32x4{ex, ey, ex * ex - ey * ey, (ex + ex) * ey};
    *(f32x4*)(t + (lane >> 5) * 32) = o;
  }
  TS(3);
  wait_vm<0>(); tie(wm0); tie(wm1); tie(c0); tie(c1);
  const int fb = idx & 15, mat = idx >> 4, d0 = xg * 64 + w * 8;
  const bool rt2 = b < 224 && w < 4;
  u32 v[8], t2[8];
  {
    const float cx0 = bflo(c0), cx1 = bflo(c1);
    const float s0 = rbf(cx0 * __builtin_amdgcn_rcpf(1.f + __builtin_amdgcn_exp2f(cx0 * -LOG2E)));
    const float s1 = rbf(cx1 * __builtin_amdgcn_rcpf(1.f + __builtin_amdgcn_exp2f(cx1 * -LOG2E)));
    float acc[8];
#pragma unroll
    for (int j = 0; j < 4; j++) {
      acc[2 * j] = fmaf(s0, bflo(wm0[j]), s1 * bflo(wm1[j]));
      acc[2 * j + 1] = fmaf(s0, bfhi(wm0[j]), s1 * bfhi(wm1[j]));
    }
    const float q0 = dpp_add<0x128>(rs16(rs32(acc[0], acc[4]), rs32(acc[2], acc[6])));
    const float q1 = dpp_add<0x128>(rs16(rs32(acc[1], acc[5]), rs32(acc[3], acc[7])));
    *(float*)(smem + A_GV + w * 256 + (lane & 7) * 32 + (lane >> 3) * 4) = (lane & 8) ? q1 : q0;
  if (rt2) {
#pragma unroll
    for (int i = 0; i < 2; i++) {
      const u16* wp = w2 + (size_t)(b * 8 + i * 4) * D;
      lp4<0>(t2[4 * i], wp, (tid & 255) * 4); lp4<1024>(t2[4 * i + 1], wp, (tid & 255) * 4);
      lp4<2048>(t2[4 * i + 2], wp, (tid & 255) * 4); lp4<3072>(t2[4 * i + 3], wp, (tid & 255) * 4);
    }
  }
    lds_barrier();
    if (w == 0 && idx < 24) {
      float s = 0.f;
#pragma unroll
      for (int i = 0; i < 8; i++) s += *(const float*)(smem + A_GV + i * 256 + lane * 4);
      st8(ws + WS_MG + (cg & 3) * 24576 + (cg >> 2) * 512, lane * 8, u32x2{f2u(s), tag});
    }
    const char* xt = smem + A_XT + (lane & 7) * 1056;
    const u32 st = *(const u32*)(xt + 1024);
#pragma unroll
    for (int e = 0; e < 2; e++) {
      const u32x2 xq = *(const u32x2*)(xt + (e * 64 + w * 8 + (lane >> 3)) * 8);
      st16(ws + WS_XG + (size_t)(e * 64 + w * 8) * 32768 + b * 128, (lane >> 3) * 32768 + (lane & 7) * 16, u32x4{xq[0], xq[1], st, tag});
    }
  }
  {
    const u16* wp = (mat ? w3 : w1) + (size_t)d0 * F + fb * 112;
    const u32 vo = (lane < 56 ? lane : 55) * 4;
#pragma unroll
    for (int i = 0; i < 4; i++) { lp4<0>(v[2 * i], wp + 2 * i * F, vo); lp4<2 * F>(v[2 * i + 1], wp + 2 * i * F, vo); }
  }
  TS(4);
  const u32 gcode = __builtin_amdgcn_perm(0x400b0916u, 0x0a201408u, (r & 7) | 0x0c0c0c00u);
  const int gx = r < 8 ? 2 * r : (int)((gcode >> 2) * 256 + (gcode & 3) * 4);
  {
    asm volatile("" ::: "memory");
    const float mF = (float)(16 * r), mX = (float)gx;
    const char* tp = tab + g * 8 * 48;
    const char* tq = tp + (r < 8 ? 16 : 32);
    f32x4 acc[3];
#pragma unroll
    for (int s = 0; s < 3; s++) acc[s] = f32x4{0.f, 0.f, 0.f, 0.f};
#pragma unroll
    for (int ks = 0; ks < 2; ks++) {
      u32x4 Ff, Gf[3];
#pragma unroll
      for (int i = 0; i < 4; i++) {
        const f32x4 s0 = *(const f32x4*)(tp + (ks * 4 + i) * 48), s1 = *(const f32x4*)(tq + (ks * 4 + i) * 48);
        const float e0 = __builtin_amdgcn_exp2f(s0[0] * mF), p0 = __builtin_amdgcn_fractf(s0[1] * mF);
        const float zx = e0 * __builtin_amdgcn_cosf(p0), zy = e0 * __builtin_amdgcn_sinf(p0);
        Ff[i] = pk(s0[2] * zx - s0[3] * zy, s0[2] * zy + s0[3] * zx);
        const float e1 = __builtin_amdgcn_exp2f(s0[0] * mX), p1 = __builtin_amdgcn_fractf(s0[1] * -mX);
        const float xx = e1 * __builtin_amdgcn_cosf(p1), xy = e1 * __builtin_amdgcn_sinf(p1);
        Gf[0][i] = pk(xx, xy);
        Gf[1][i] = pk(xx * s1[0] - xy * s1[1], xx * s1[1] + xy * s1[0]);
        Gf[2][i] = pk(xx * s1[2] - xy * s1[3], xx * s1[3] + xy * s1[2]);
      }
#pragma unroll
      for (int s = 0; s < 3; s++) acc[s] = mfma16(Ff, Gf[s], acc[s]);
    }
#pragma unroll
    for (int s = 0; s < 3; s++) *(f32x4*)(smem + A_P + w * 3072 + s * 1024 + lane * 16) = acc[s];
  }
  TS(5);
  if (rt2) {
    wait_vm<10>();
#pragma unroll
    for (int i = 0; i < 8; i++) tie(t2[i]);
    const u32 vo = (((tid >> 4) & 15) * 112 * 64 + (tid & 15)) * 16;
#pragma unroll
    for (int e = 0; e < 2; e++) {
      u32x4 o;
#pragma unroll
      for (int j = 0; j < 4; j++) o[j] = __builtin_amdgcn_perm(t2[2 * j + 1], t2[2 * j], e ? 0x07060302u : 0x05040100u);
      st16(ws + WS_W2F + b * 512 + e * 256, vo, o);
    }
  }
  u32x4 xg4[4];
  u32x2 mg, mh;
  const char* xgb = ws + WS_XG + (size_t)(p >> 1) * 32768;
  const u32 xgo = (tid >> 2) * 256 + (tid & 3) * 16;
  u32 mga = (4 - ((lane >> 3) & 1)) * 512 + d0 + (lane & 7), mgb = ((0x5210 >> ((lane & 6) * 2)) & 7) * 512 + 2 * p + (lane & 1);
  tie(mga); tie(mgb);
  const u32 mgo = ((lane & 16) ? mgb : mga) * 8 + (lane >> 5) * 24576;
  auto look = [&]() NOPK {
    ld8(mg, ws + WS_MG, mgo); ld8(mh, ws + WS_MG + 49152, mgo);
    ld16<0>(xg4[0], xgb, xgo); ld16<64>(xg4[1], xgb, xgo); ld16<128>(xg4[2], xgb, xgo); ld16<192>(xg4[3], xgb, xgo);
  };
  lds_barrier();
  TS(6);
  TSF(0, 6);
  const int kch = tid >> 8;
  u16* Kc = (u16*)(smem + A_KC + kch * A_KCS);
  if ((w & 3) < 3) {
    const int s = w & 3;
    const char* pp = smem + A_P + kch * 12288 + s * 1024 + lane * 16;
    const f32x4 q = (*(const f32x4*)(pp) + *(const f32x4*)(pp + 3072)) + (*(const f32x4*)(pp + 6144) + *(const f32x4*)(pp + 9216));
    const int pos = gx + 64 * g + (r < 8 ? (s == 0 ? 0 : s == 1 ? 1 : 256) : s * 256);
    const u32 o0 = pk(q[0], q[1]), o1 = pk(q[2], q[3]);
    if (pos <= 2048) Kc[pos] = (u16)o0;
    if (pos < 2048) {
      Kc[pos + 16] = (u16)(o0 >> 16);
      Kc[pos + 32] = (u16)o1;
      Kc[pos + 48] = (u16)(o1 >> 16);
    }
  }
  look();
  lds_barrier();
  TS(7);
  {
    u32 e[6];
    const int ki = tid & 255;
#pragma unroll
    for (int j = 0; j < 6; j++) {
      const float l0 = (float)(2047 - 8 * ki - 2 * j);
      const u32 bits = pk(fmaxf(l0, 0.f), fmaxf(l0 - 1.f, 0.f));
      e[j] = (u32)Kc[(int)bflo(bits)];
      if (j < 5) e[j] |= (u32)Kc[(int)bfhi(bits)] << 16;
    }
    if (ki == 255) { e[4] = 0; e[5] = 0; }
    const u32 a0 = __builtin_amdgcn_alignbit(e[1], e[0], 16), a1 = __builtin_amdgcn_alignbit(e[2], e[1], 16);
    const u32 a2 = __builtin_amdgcn_alignbit(e[3], e[2], 16), a3 = __builtin_amdgcn_alignbit(e[4], e[3], 16);
    const u32 a4 = __builtin_amdgcn_alignbit(e[5], e[4], 16);
    char* kc = smem + B_KC + kch * B_KCH + ki * 16;
    *(u32x4*)(kc) = u32x4{e[0], e[1], e[2], e[3]};
    *(u32x4*)(kc + B_KCS) = u32x4{a0, a1, a2, a3};
    *(u32x4*)(kc + 2 * B_KCS) = u32x4{e[1], e[2], e[3], e[4]};
    *(u32x4*)(kc + 3 * B_KCS) = u32x4{a1, a2, a3, a4};
  }
  TS(8);
  wait_vm<0>(); tie(xg4[0]); tie(xg4[1]); tie(xg4[2]); tie(xg4[3]); tie(mg); tie(mh);
#pragma unroll
  for (int i = 0; i < 8; i++) tie(v[i]);
#pragma unroll 1
  for (int it = 0; it < SPIN_CAP && !all_lanes(xg4[0][3] == tag && xg4[1][3] == tag && xg4[2][3] == tag && xg4[3][3] == tag && mg[1] == tag && mh[1] == tag); it++) {
    __builtin_amdgcn_s_sleep(1);
    look();
    wait_vm<0>(); tie(xg4[0]); tie(xg4[1]); tie(xg4[2]); tie(xg4[3]); tie(mg); tie(mh);
  }
  TS(9);
  const int g8 = tid >> 1, cc = tid & 1;
  const float mq = u2f(mg[0]) + u2f(mh[0]);
  const float mv = rbf(rs32(mq, mq)), mv1 = rbf(1.f + mv);
  const float ga = sel2(cc, rdl(mv, 20), rdl(mv, 21));
  u32x4 hp, xv;
  {
    const float sh0 = rdl(mv, 16), sh1 = rdl(mv, 17), sc0 = rdl(mv1, 18), sc1 = rdl(mv1, 19);
    const u32 psel = cc ? 0x03020706u : 0x05040100u;
    const bool up = tid & 2;
    u32 ph[4], px[4];
#pragma unroll
    for (int i = 0; i < 4; i++) {
      const u32 xq = (p & 1) ? xg4[i][1] : xg4[i][0];
      const float m = bflo(xg4[i][2]), rs = __builtin_amdgcn_rcpf(bfhi(xg4[i][2]));
      const u32 n = pk((bflo(xq) - m) * rs, (bfhi(xq) - m) * rs);
      const u32 hq = pk(fmaf(bflo(n), sc0, sh0), fmaf(bfhi(n), sc1, sh1));
      ph[i] = __builtin_amdgcn_perm(swap1(hq), hq, psel);
      px[i] = __builtin_amdgcn_perm(swap1(xq), xq, psel);
    }
#pragma unroll
    for (int j = 0; j < 2; j++) {
      const u32 rh = swap2(up ? ph[j] : ph[2 + j]), kh = up ? ph[2 + j] : ph[j];
      hp[2 * j] = up ? rh : kh; hp[2 * j + 1] = up ? kh : rh;
      const u32 rx = swap2(up ? px[j] : px[2 + j]), kx = up ? px[2 + j] : px[j];
      xv[2 * j] = up ? rx : kx; xv[2 * j + 1] = up ? kx : rx;
    }
    char* u = smem + B_U + cc * B_UCH + 512 + g8 * 16;
    *(u32x4*)(u) = hp;
    *(u32x2*)(u + B_US + 8) = u32x2{hp[0], hp[1]};
    *(u32x2*)(u + B_US + 16) = u32x2{hp[2], hp[3]};
  }
  TS(10);
  lds_barrier();
  TS(11);
  const int tt = (xg & 1) * 8 + (idx & 7), fs = (xg >> 1) * 4 + (idx >> 3);
  u32 fw;
  {
    const int ch = w & 1, vh = (w >> 1) & 1, tg = w >> 2;
    const char* pa = smem + B_KC + ch * B_KCH + (3 - (r & 3)) * B_KCS + 2 * (2040 - 8 * (r >> 2) + 8 * g) - 512 * 7 - 64 * (4 * vh + 3);
    const char* pb = smem + B_U + ch * B_UCH + ((r & 1) ? 0 : B_US) + 2 * (256 + 32 * (r >> 1) + 8 * g) - 64 * (4 * vh + 3);
    f32x4 acc[4];
#pragma unroll
    for (int i = 0; i < 4; i++) acc[i] = f32x4{0.f, 0.f, 0.f, 0.f};
    auto prep = [&]() NOPK {
      const float kf = mat ? LN2 : LOG2E;
      const float cs = mv1 * kf, ss = mv * kf;
      u32x4 o0, o1;
      f32x4 sums = {0.f, 0.f, 0.f, 0.f};
#pragma unroll
      for (int j = 0; j < 4; j++) {
        const float k0 = rdl(cs, 2 * j), k1 = rdl(cs, 2 * j + 1), s0 = rdl(ss, 8 + 2 * j), s1 = rdl(ss, 8 + 2 * j + 1);
        const float a0 = bflo(v[2 * j]), a1 = bflo(v[2 * j + 1]), b0 = bfhi(v[2 * j]), b1 = bfhi(v[2 * j + 1]);
        o0[j] = pk(a0 * k0, a1 * k1);
        o1[j] = pk(b0 * k0, b1 * k1);
        sums[0] = dot2(o0[j], 0x3f803f80u, sums[0]);
        sums[1] = dot2(o1[j], 0x3f803f80u, sums[1]);
        sums[2] = fmaf(a0, s0, fmaf(a1, s1, sums[2]));
        sums[3] = fmaf(b0, s0, fmaf(b1, s1, sums[3]));
      }
      st16x2_56(ws + WS_W1F + (size_t)(fb * 7 * 32 + (d0 >> 4)) * 1024 + (w & 1) * 512 + mat * 256, (lane >> 3) * 32768 + (lane & 7) * 16, o0, o1);
      *(f32x4*)(smem + B_RED + tid * 16) = sums;
    };
    auto mid = [&]() NOPK {
      wait_vm<0>();
      if (arrive_last(smem + L_CNT, 8)) st4one(ws + WS_FW + (fb * 16 + mat * 8 + xg) * 128, tag);
    };
    if (tg == 0) conv_body<0>(pa, pb, acc, prep, mid); else conv_body<1>(pa, pb, acc, prep, mid);
    TS(12);
    float* Y = (float*)(smem + B_Y) + (vh * 2 + ch) * 2560 + 40 * (r >> 1) + 4 * (r & 1) + 8 * g;
    const int t0 = tg ? 6 : 7, t1 = tg ? 1 : 0, t2_ = tg ? 5 : 4, t3 = tg ? 2 : 3;
    *(f32x4*)(Y + 320 * t0) = acc[0];
    *(f32x4*)(Y + 320 * t1) = acc[1];
    *(f32x4*)(Y + 320 * t2_) = acc[2];
    *(f32x4*)(Y + 320 * t3) = acc[3];
  }
  lds_barrier();
  TS(13);
  TSF(7, 13);
  {
    const float* Y = (const float*)(smem + B_Y) + cc * 2560 + 8 * g8 + 8 * (g8 >> 2);
    const f32x4 ya = *(const f32x4*)Y + *(const f32x4*)(Y + 5120), yb = *(const f32x4*)(Y + 4) + *(const f32x4*)(Y + 5124);
    const float dk = cc ? bfhi(dsk) : bflo(dsk);
    u32x4 o;
#pragma unroll
    for (int j = 0; j < 4; j++) {
      const float y0 = j < 2 ? ya[2 * j] : yb[2 * j - 4], y1 = j < 2 ? ya[2 * j + 1] : yb[2 * j - 3];
      o[j] = pk(fmaf(ga, fmaf(bflo(hp[j]), dk, y0), bflo(xv[j])), fmaf(ga, fmaf(bfhi(hp[j]), dk, y1), bfhi(xv[j])));
    }
    ld4(fw, ws + WS_FW + fs * 2048, (lane & 15) * 128);
    st16(ws + WS_X1 + p * 128, (g8 >> 2) * 32768 + cc * 64 + (g8 & 3) * 16, o);
  }
  TS(14);
  const int h = lane >> 5, c = lane & 31;
  const int tt2 = xg * 2 + (idx >> 4), sel = (idx >> 3) & 1, dt = idx & 7;
  const int dl = (c & 3) + 4 * (c >> 3) + 16 * ((c >> 2) & 1);
  const int rlo = ((dt ^ sel) & 1) * 7168, rhi = 7168 - rlo;
  const char* w2b = ws + WS_W2F + ((size_t)(dt * 2) * 112 + w * 14) * 1024;
  const u32 w2o = (h * 32 + (dl & 1) * 16 + (dl >> 1)) * 16;
  f32x2* raw = (f32x2*)(smem + C_RAW);
  u32x4 W2[28];
  u32 fh, pf = 0;
  const int rot = (2 * (idx >> 3) + idx) & 7;
  const char* xa = ws + WS_X1 + tt * 131072 + (w & 1) * 32768 + (((w >> 1) + rot) & 7) * 4096;
  const char* xc = ws + WS_X1 + tt * 131072 + (w & 1) * 32768 + (((w >> 1) + rot + 4) & 7) * 4096;
  const u32 lpc = (u32)(size_t)(LDS_AS char*)smem + (w & 1) * 32768 + (w >> 1) * 4096;
  auto warm_w2 = [&]() NOPK {
    const char* pb = ws + WS_W2F + (idx * 8 + w) * 7168;
    const u32 po = (lane < 56 ? lane : 55) * 128;
    asm volatile("global_load_dword %0, %1, %2 sc1\n\tglobal_load_dword %0, %1, %2 offset:64 sc1" : "+v"(pf) : "v"(po), "s"(pb) : "memory");
  };
  auto ldw2 = [&](int i) NOPK { ld16k(W2[i], w2b + (i & 1) * 114688 + (i < 14 ? rlo : rhi), (i >> 1) % 7, w2o); };
  wait_vm<1>(); tie(fw);
  if (w == 0) {
    __builtin_amdgcn_s_setprio(3);
    {
      f32x4 s = {0.f, 0.f, 0.f, 0.f};
#pragma unroll
      for (int q = 0; q < 8; q++) s += *(const f32x4*)(smem + B_RED + q * 1024 + lane * 16);
      st16(ws + WS_CD + ((xg * 16 + fb) * 2 + mat) * 1024, lane * 16, __builtin_bit_cast(u32x4, s));
      st8m<1>(ws + WS_GB + p * 8, 0, u32x2{f2u(rdl(mv, 22)), f2u(rdl(mv, 23))});
    }
    wait_vm<0>();
    TS(15);
#pragma unroll 1
    for (int it = 0; it < SPIN_CAP * 16; it++) {
      if (__builtin_amdgcn_readfirstlane(*(volatile LDS_AS u32*)(smem + L_CNT + 4)) == 7) break;
      __builtin_amdgcn_s_sleep(1);
    }
    st4one(ws + WS_FX + (b & 63) * 128 + (b >> 6) * 4, tag);
    TS(16);
    warm_w2();
#pragma unroll 1
    for (int it = 0; it < SPIN_CAP; it++) {
      u32x4 f;
      ld16(f, ws + WS_FX, lane * 128); wait_vm<0>(); tie(f);
      if (all_lanes(f[0] == tag && f[1] == tag && f[2] == tag && f[3] == tag)) break;
    }
    tie(pf);
    TS(17);
    bar();
    TS(18);
    glds4k(xa, lane * 16, lpc); glds4k(xc, lane * 16, lpc + 16384);
    const char* rb = smem + (lane >> 2) * 64 + (lane & 3) * 8;
    f32x4 sg[2][2], sx[2][2];
#pragma unroll
    for (int i = 0; i < 4; i++) { sg[i >> 1][i & 1] = f32x4{0.f, 0.f, 0.f, 0.f}; sx[i >> 1][i & 1] = f32x4{0.f, 0.f, 0.f, 0.f}; }
    wait_vm<4>(); bar();
    TS(19); TC(20);
    TSF(14, 20);
    glds4k(xa + 65536, lane * 16, lpc + 65536); glds4k(xc + 65536, lane * 16, lpc + 65536 + 16384);
    if (b == 0) st4one(ws + WS_E, tag);
    ln2_acc(rb, 0, 8, sg, sx);
    TS(21);
    wait_vm<8>(); bar();
    TS(22);
    ln2_acc(rb, 8, 16, sg, sx);
    ln2_put(raw, lane, sg[0], sx[0]); ln2_put(raw + 32, lane, sg[1], sx[1]);
    TS(23);
    wait_vm<0>();
    lds_barrier();
    TS(24);
#pragma unroll
    for (int i = 0; i < 4; i++) { sg[i >> 1][i & 1] = f32x4{0.f, 0.f, 0.f, 0.f}; sx[i >> 1][i & 1] = f32x4{0.f, 0.f, 0.f, 0.f}; }
    ln2_acc(rb + 65536, 0, 16, sg, sx);
    ln2_put(raw + 64, lane, sg[0], sx[0]); ln2_put(raw + 96, lane, sg[1], sx[1]);
    asm volatile("" ::: "memory");
    *(volatile LDS_AS u32*)(smem + L_CNT + 16) = 1;
    TS(25);
    __builtin_amdgcn_s_setprio(0);
#pragma unroll
    for (int i = 0; i < 28; i++) ldw2(i);
    TS(26); TS(27); TC(28);
    TSF(21, 28);
    wait_vm<0>();
#pragma unroll
    for (int i = 0; i < 28; i++) tie(W2[i]);
    TS(29);
  } else {
    const int ft = fs * 7 + w - 1;
    const char* wb = ws + WS_W1F + (size_t)ft * 32768;
    const int fl = (c & 3) + 8 * ((c >> 2) & 1) + 4 * (c >> 4);
    const u32 voff = (h * 32 + ((c >> 3) & 1) * 16 + (fl & 1) * 8 + (fl >> 1)) * 16;
    auto x1_done = [&]() NOPK {
      asm volatile("s_waitcnt lgkmcnt(0)" ::: "memory");
      if (lane == 0) __hip_atomic_fetch_add((LDS_AS u32*)(smem + L_CNT + 4), 1u, __ATOMIC_RELAXED, __HIP_MEMORY_SCOPE_WORKGROUP);
    };
    const bool wok = all_lanes(fw == tag);
    if (!wok) {
      wait_vm<0>();
      x1_done();
      spin4(fw, tag, ws + WS_FW + fs * 2048, (lane & 15) * 128);
    }
    TS(15);
    u32x4 A[32];
#pragma unroll
    for (int i = 0; i < 8; i++) {
      if (i == W1A) {
        wait_vm<4 * W1A>();
        if (wok) x1_done();
        TS(16);
      }
      const char* wq = wb + ((i + rot) & 7) * 4096;
      ld16<0>(A[4 * i], wq, voff); ld16<1024>(A[4 * i + 1], wq, voff);
      ld16<2048>(A[4 * i + 2], wq, voff); ld16<3072>(A[4 * i + 3], wq, voff);
    }
    warm_w2();
    const char* rb = smem + h * 512 + ((lane >> 2) & 3) * 64 + ((lane >> 4) & 1) * 32 + (lane & 3) * 8;
    const char* rb2 = rb + 65536;
    u32 cdv[8];
    f32x16 acc[4];
#pragma unroll
    for (int T = 0; T < 4; T++)
#pragma unroll
      for (int i = 0; i < 16; i++) acc[T][i] = 0.f;
    TS(17);
    lds_barrier();
    TS(18);
    glds4k(xa, lane * 16, lpc); glds4k(xc, lane * 16, lpc + 16384);
    wait_vm<4>();
#pragma unroll
    for (int i = 0; i < 32; i++) tie(A[i]);
    tie(pf);
    bar();
    TS(19); TC(20);
    TSF(14, 20);
    u32x4 Bc[2], Bn[2];
    Bc[0] = tr8(rb); Bc[1] = tr8(rb + 32768);
    f32x2 rw[2];
#pragma unroll
    for (int ks = 0; ks < 32; ks++) {
      if (ks == 15) {
        TS(21);
        wait_vm<16>();
        bar();
        TS(22);
      } else if (ks == 31) {
        TS(23);
        wait_vm<0>();
#pragma unroll
        for (int q = 0; q < 8; q++) tie(cdv[q]);
        bar();
        TS(24);
      }
      if (ks < 31) {
        Bn[0] = tr8(rb + (ks + 1) * 1024); Bn[1] = tr8(rb + 32768 + (ks + 1) * 1024);
      } else {
        Bn[0] = tr8(rb2); Bn[1] = tr8(rb2 + 32768);
        rw[0] = raw[c]; rw[1] = raw[32 + c];
      }
      __builtin_amdgcn_sched_barrier(0);
      acc[0] = mfma32(A[ks], Bc[0], acc[0]);
      acc[1] = mfma32(A[ks], Bc[1], acc[1]);
      __builtin_amdgcn_sched_barrier(0);
      if (ks == 3) glds4k(xa + 65536, lane * 16, lpc + 65536);
      if (ks == 7) {
        const u32 co = ((c >> 3) & 1) * 1024 + ((w - 1) * 8 + (fl >> 1)) * 16 + h * 8 + (fl & 1) * 4;
#pragma unroll
        for (int q = 0; q < 8; q++) ld4(cdv[q], ws + WS_CD + (q * 16 + fs) * 2048, co);
      }
      if (ks == 11) glds4k(xc + 65536, lane * 16, lpc + 65536 + 16384);
      Bc[0] = Bn[0]; Bc[1] = Bn[1];
    }
    const float cd = ((u2f(cdv[0]) + u2f(cdv[1])) + (u2f(cdv[2]) + u2f(cdv[3]))) + ((u2f(cdv[4]) + u2f(cdv[5])) + (u2f(cdv[6]) + u2f(cdv[7])));
    const float chi = rbf(cd);
    const u32x4 Ax = {pk(chi, chi), pk(cd - chi, cd - chi), 0, 0};
    const char* hb = ws + WS_HF + ((size_t)(tt * 4) * 112 + ft) * 1024;
    Ep q[4];
    float o[8];
#pragma unroll
    for (int s = 0; s < 32; s++) {
      const int n = s + 1;
      const int ta = s < 28 ? 2 : 3, tb_ = s < 24 ? 3 : ta, ka = s < 24 ? s : 24 + 2 * (s & 3), kb = s < 24 ? s : ka + 1;
      if (s == 21) {
        TS(25);
#pragma unroll 1
        for (int it = 0; it < SPIN_CAP * 16; it++)
          if (__builtin_amdgcn_readfirstlane(*(volatile LDS_AS u32*)(smem + L_CNT + 16)) == 1) break;
        asm volatile("" ::: "memory");
        TS(26);
        rw[0] = raw[64 + c]; rw[1] = raw[96 + c];
      }
      if (n < 24) {
        Bn[0] = tr8(rb2 + n * 1024); Bn[1] = tr8(rb2 + 32768 + n * 1024);
      } else if (n < 32) {
        const char* rp = rb2 + (n < 28 ? 0 : 32768) + (24 + 2 * (n & 3)) * 1024;
        Bn[0] = tr8(rp); Bn[1] = tr8(rp + 1024);
      }
      __builtin_amdgcn_sched_barrier(0);
      acc[ta] = mfma32(A[ka], Bc[0], acc[ta]);
      if (s < 2 || s == 22 || s == 23) {
        const int T = s < 2 ? s : s - 20;
        const u32 be = ln2_fold(rw[T & 1], h, q[T]);
        acc[T] = mfma32(Ax, u32x4{be, be, 0, 0}, acc[T]);
      }
      const int j = s >= 3 && s < 11 ? 2 * (s - 3) : s >= 28 ? 16 + 2 * (s - 28) : -1;
      if (j >= 0) o[j & 7] = swi(acc[j >> 3], j & 7, q[j >> 3]);
      __builtin_amdgcn_sched_barrier(0);
      acc[tb_] = mfma32(A[kb], Bc[1], acc[tb_]);
      if (j >= 0) {
        o[(j + 1) & 7] = swi(acc[j >> 3], (j + 1) & 7, q[j >> 3]);
        if ((j & 7) == 6) st16(hb + (j >> 3) * 114688, lane * 16, u32x4{pk(o[0], o[1]), pk(o[2], o[3]), pk(o[4], o[5]), pk(o[6], o[7])});
      }
      __builtin_amdgcn_sched_barrier(0);
      if (s < 28) ldw2(s);
      if (s == 27) {
        wait_vm<18>();
        if (arrive_last(smem + L_CNT + 8, 7)) st4one(ws + WS_FH + (tt * 16 + fs) * 128, tag);
        TS(27);
      }
      Bc[0] = Bn[0]; Bc[1] = Bn[1];
    }
    TC(28);
    TSF(21, 28);
#pragma unroll
    for (int i = 0; i < 8; i++) o[i] = swi(acc[3], i, q[3]);
    st16(hb + 3 * 114688, lane * 16, u32x4{pk(o[0], o[1]), pk(o[2], o[3]), pk(o[4], o[5]), pk(o[6], o[7])});
    wait_vm<2>();
#pragma unroll
    for (int i = 0; i < 28; i++) tie(W2[i]);
    TS(29);
  }
  {
    const int te = tt2 * 4 + sel, tl = tt2 * 4 + 3 - sel;
    const char* hbe = ws + WS_HF + ((size_t)te * 112 + w * 14) * 1024;
    const char* hbl = ws + WS_HF + ((size_t)tl * 112 + w * 14) * 1024;
    const int u = lane >> 2, tk = w * 4 + (lane & 3);
    u32 xe[4], xl[4];
    u32x4 gbv;
    u32x4 H[14];
    ld4(fh, ws + WS_FH + tt2 * 2048, (lane & 15) * 128);
    wait_vm<0>(); tie(fh);
    if (w > 0 && arrive_last(smem + L_CNT + 12, 7)) st4one(ws + WS_FH + 32768 + (tt * 16 + fs) * 128, tag);
    TS(30);
    spin4(fh, tag, ws + WS_FH + tt2 * 2048, (lane & 15) * 128);
    bar();
    TS(31);
#pragma unroll
    for (int i = 0; i < 14; i++) ld16k(H[i], hbe + (i < 7 ? rlo : rhi), i % 7, lane * 16);
    const u32 xo = (dt * 64 + u * 4) * 64 + (tk >> 1) * 4;
    ld4<0>(xe[0], ws + WS_X1 + te * 32768, xo); ld4<64>(xe[1], ws + WS_X1 + te * 32768, xo);
    ld4<128>(xe[2], ws + WS_X1 + te * 32768, xo); ld4<192>(xe[3], ws + WS_X1 + te * 32768, xo);
    ld4<0>(xl[0], ws + WS_X1 + tl * 32768, xo); ld4<64>(xl[1], ws + WS_X1 + tl * 32768, xo);
    ld4<128>(xl[2], ws + WS_X1 + tl * 32768, xo); ld4<192>(xl[3], ws + WS_X1 + tl * 32768, xo);
    ld16<0>(gbv, ws + WS_GB + dt * 256, u * 16);
    ld4(fh, ws + WS_FH + 32768 + tt2 * 2048, (lane & 15) * 128);
    TS(32);
    f32x16 acc[2];
#pragma unroll
    for (int i = 0; i < 16; i++) { acc[0][i] = 0.f; acc[1][i] = 0.f; }
    char* wr = smem + w * D_SRC + h * 4 * 576 + c * 16;
    const char* rd = smem + u * 576 + tk * 16;
#pragma unroll
    for (int i = 0; i < 14; i++) {
      if (i == 0 || i == 4 || i == 9) {
        __builtin_amdgcn_sched_barrier(0);
        if (i == 0) wait_vm<20>(); else if (i == 4) wait_vm<15>(); else wait_vm<10>();
#pragma unroll
        for (int k = i; k < (i == 0 ? 4 : i == 4 ? 9 : 14); k++) tie(H[k]);
      }
      acc[0] = mfma32(W2[2 * i], H[i], acc[0]);
      acc[1] = mfma32(W2[2 * i + 1], H[i], acc[1]);
    }
    __builtin_amdgcn_sched_barrier(0);
    wait_vm<0>(); tie(fh);
#pragma unroll
    for (int i = 0; i < 4; i++) { tie(xe[i]); tie(xl[i]); }
    tie(gbv);
    TS(33);
    spin4(fh, tag, ws + WS_FH + 32768 + tt2 * 2048, (lane & 15) * 128);
    TS(34);
#pragma unroll
    for (int i = 0; i < 14; i++) ld16k(H[i], hbl + (i < 7 ? rlo : rhi), i % 7, lane * 16);
    __builtin_amdgcn_sched_barrier(0);
    TS(35);
#pragma unroll
    for (int di = 0; di < 2; di++)
#pragma unroll
      for (int q = 0; q < 4; q++)
        *(f32x4*)(wr + (di * 8 + q) * 576) = f32x4{acc[di][4 * q], acc[di][4 * q + 1], acc[di][4 * q + 2], acc[di][4 * q + 3]};
    __builtin_amdgcn_sched_barrier(0);
    TS(36);
    TSF(29, 36);
    auto finish = [&](const char* rp, const u32 (&xq)[4]) NOPK {
      f32x4 s = *(const f32x4*)rp;
#pragma unroll
      for (int q = 1; q < 8; q++) s += *(const f32x4*)(rp + q * D_SRC);
      float y[4];
#pragma unroll
      for (int j = 0; j < 4; j++) y[j] = fmaf(u2f(gbv[j]), s[j], (tk & 1) ? bfhi(xq[j]) : bflo(xq[j]));
      return u32x2{pk(y[0], y[1]), pk(y[2], y[3])};
    };
#pragma unroll
    for (int i = 0; i < 16; i++) { acc[0][i] = 0.f; acc[1][i] = 0.f; }
#pragma unroll
    for (int i = 0; i < 14; i++) {
      if (i == 0 || i == 4 || i == 9) {
        __builtin_amdgcn_sched_barrier(0);
        if (i == 0) wait_vm<10>(); else if (i == 4) wait_vm<5>(); else wait_vm<0>();
#pragma unroll
        for (int k = i; k < (i == 0 ? 4 : i == 4 ? 9 : 14); k++) tie(H[k]);
      }
      acc[0] = mfma32(W2[2 * i], H[i], acc[0]);
      acc[1] = mfma32(W2[2 * i + 1], H[i], acc[1]);
    }
    TS(37);
#pragma unroll
    for (int di = 0; di < 2; di++)
#pragma unroll
      for (int q = 0; q < 4; q++)
        *(f32x4*)(wr + D_TILE + (di * 8 + q) * 576) = f32x4{acc[di][4 * q], acc[di][4 * q + 1], acc[di][4 * q + 2], acc[di][4 * q + 3]};
    lds_barrier();
    TS(38);
    *(u32x2*)(out + (size_t)(tl * 32 + tk) * D + dt * 64 + u * 4) = finish(rd + D_TILE, xl);
    *(u32x2*)(out + (size_t)(te * 32 + tk) * D + dt * 64 + u * 4) = finish(rd, xe);
    TS(39);
    TS(40);
    TSF(37, 40);
    TSEND();
  }
}

extern "C" void kernel_launch(void* const* d_in, const int* in_sizes, int n_in, void* d_out, int out_size, void* d_ws,
                              size_t ws_size, hipStream_t stream) {
  const u16* const* in = (const u16* const*)d_in;
  fused<<<256, 512, 0, stream>>>(in[0], in[1], in[2], in[3], in[4], in[5], in[6], in[7], in[8], in[10], in[11], in[12], in[13], in[14],
                                 (char*)d_ws, (u16*)d_out);
}
```

```cpp
#define DIAG 0
#define DIAGB 37
#define DIAGC 0
#include <hip/hip_runtime.h>
#include <stdint.h>

typedef unsigned int u32;
typedef unsigned short u16;
typedef __attribute__((ext_vector_type(2))) float f32x2;
typedef __attribute__((ext_vector_type(4))) float f32x4;
typedef __attribute__((ext_vector_type(16))) float f32x16;
typedef __attribute__((ext_vector_type(2))) u32 u32x2;
typedef __attribute__((ext_vector_type(4))) u32 u32x4;
typedef __attribute__((ext_vector_type(2))) __bf16 bf16x2;
typedef __attribute__((ext_vector_type(8))) __bf16 bf16x8;
typedef __attribute__((ext_vector_type(4))) short s16x4;
#define LDS_AS __attribute__((address_space(3)))
#if defined(__HIP_DEVICE_COMPILE__)
#define NOPK __attribute__((target("no-packed-fp32-ops")))
#else
#define NOPK
#endif
#define DEVINL __device__ __forceinline__ NOPK

constexpr int L = 2048, D = 512, NS = 128, F = 1792;

constexpr size_t WS_E = 0;
constexpr size_t WS_FW = 4096;
constexpr size_t WS_FX = WS_FW + 32768;
constexpr size_t WS_FH = WS_FX + 32768;
constexpr size_t WS_MG = WS_FH + 65536;
constexpr size_t WS_GB = WS_MG + 98304;
constexpr size_t WS_XG = 262144;
constexpr size_t WS_X1 = WS_XG + (4u << 20);
constexpr size_t WS_W1F = WS_X1 + (2u << 20);
constexpr size_t WS_W2F = WS_W1F + 112 * 32 * 1024;
constexpr size_t WS_HF = WS_W2F + 16 * 112 * 1024;
constexpr size_t WS_CD = WS_HF + 64 * 112 * 1024;
constexpr int SPIN_CAP = 40000;
#define W1A 2
#define FXL 4

#define u2f(x) __builtin_bit_cast(float, (u32)(x))
#define i2f(x) __builtin_bit_cast(float, (int)(x))
#define f2u(x) __builtin_bit_cast(u32, (float)(x))
#define f2i(x) __builtin_bit_cast(int, (float)(x))
#define TSDECL unsigned long long dg0 = 0, dgk = 0, dc0 = 0, dck = 0
#define TS(slot) do { if ((slot) == 0) { asm volatile("s_memrealtime %0\n\ts_memtime %1\n\ts_waitcnt lgkmcnt(0)" : "=s"(dg0), "=s"(dc0) :: "memory"); dgk = dg0; dck = dc0; } \
  else if ((slot) == DIAG) asm volatile("s_memrealtime %0\n\ts_memtime %1\n\ts_waitcnt lgkmcnt(0)" : "=s"(dgk), "=s"(dck) :: "memory"); } while (0)
#define TC(slot)
#define TSF(lo, hi)
#define TSEND() do { if (blockIdx.x == DIAGB && w == 1) { unsigned long long tgt = dg0 + 6000 + (DIAGC ? (dck - dc0) / 8 : 4 * (dgk - dg0)), now; \
  do { asm volatile("s_sleep 1\n\ts_memrealtime %0\n\ts_waitcnt lgkmcnt(0)" : "=s"(now) :: "memory"); } while (now < tgt); } } while (0)
DEVINL float bflo(u32 u) { return u2f(u << 16); }
DEVINL float bfhi(u32 u) { return u2f(u & 0xffff0000u); }
DEVINL u32 pk(float a, float b) {
  f32x2 v = {a, b};
  return __builtin_bit_cast(u32, __builtin_convertvector(v, bf16x2));
}
DEVINL float rbf(float a) { return bflo(pk(a, a)); }
template <int CTRL> DEVINL float dpp_add(float v) {
  return v + i2f(__builtin_amdgcn_update_dpp(0, f2i(v), CTRL, 0xf, 0xf, true));
}
DEVINL float row4_sum(float v) {
  auto a = __builtin_amdgcn_permlane16_swap(f2u(v), f2u(v), false, false);
  v = u2f(a[0]) + u2f(a[1]);
  auto b = __builtin_amdgcn_permlane32_swap(f2u(v), f2u(v), false, false);
  return u2f(b[0]) + u2f(b[1]);
}
DEVINL float rs32(float a, float b) {
  auto r = __builtin_amdgcn_permlane32_swap(f2u(a), f2u(b), false, false);
  return u2f(r[0]) + u2f(r[1]);
}
DEVINL float rs16(float a, float b) {
  auto r = __builtin_amdgcn_permlane16_swap(f2u(a), f2u(b), false, false);
  return u2f(r[0]) + u2f(r[1]);
}
DEVINL float wave_sum(float v) { return row4_sum(dpp_add<0x140>(dpp_add<0x141>(dpp_add<0x4E>(dpp_add<0xB1>(v))))); }
DEVINL f32x4 mfma16(u32x4 a, u32x4 b, f32x4 c) {
  return __builtin_amdgcn_mfma_f32_16x16x32_bf16(__builtin_bit_cast(bf16x8, a), __builtin_bit_cast(bf16x8, b), c, 0, 0, 0);
}
DEVINL f32x16 mfma32(u32x4 a, u32x4 b, f32x16 c) {
  return __builtin_amdgcn_mfma_f32_32x32x16_bf16(__builtin_bit_cast(bf16x8, a), __builtin_bit_cast(bf16x8, b), c, 0, 0, 0);
}
DEVINL float dot2(u32 a, u32 b, float c) {
  return __builtin_amdgcn_fdot2_f32_bf16(__builtin_bit_cast(bf16x2, a), __builtin_bit_cast(bf16x2, b), c, false);
}
DEVINL void lds_barrier() {
  asm volatile("s_waitcnt lgkmcnt(0)" ::: "memory");
  __builtin_amdgcn_s_barrier();
  asm volatile("" ::: "memory");
}
DEVINL void bar() {
  asm volatile("" ::: "memory");
  __builtin_amdgcn_s_barrier();
  asm volatile("" ::: "memory");
}
DEVINL void st16(const void* sbase, u32 voff, u32x4 v) {
  asm volatile("global_store_dwordx4 %0, %1, %2 sc1\n\ts_nop 1" ::"v"(voff), "v"(v), "s"(sbase) : "memory");
}
DEVINL void st8(const void* sbase, u32 voff, u32x2 v) {
  asm volatile("global_store_dwordx2 %0, %1, %2 sc1\n\ts_nop 1" ::"v"(voff), "v"(v), "s"(sbase) : "memory");
}
DEVINL void st4(const void* sbase, u32 voff, u32 v) {
  asm volatile("global_store_dword %0, %1, %2 sc1\n\ts_nop 1" ::"v"(voff), "v"(v), "s"(sbase) : "memory");
}
template <int LANES> DEVINL void st8m(const void* sbase, u32 voff, u32x2 v) {
  asm volatile("s_mov_b64 exec, %3\n\tglobal_store_dwordx2 %0, %1, %2 sc1\n\ts_mov_b64 exec, -1\n\ts_nop 0"
               ::"v"(voff), "v"(v), "s"(sbase), "n"((1 << LANES) - 1) : "memory");
}
DEVINL void st4one(const void* sbase, u32 v) {
  asm volatile("s_mov_b64 exec, 1\n\tglobal_store_dword %0, %1, %2 sc1\n\ts_mov_b64 exec, -1\n\ts_nop 0"
               ::"v"(0), "v"(v), "s"(sbase) : "memory");
}
DEVINL void st16x2_56(const void* sbase, u32 voff, u32x4 a, u32x4 b) {
  asm volatile("s_mov_b32 exec_hi, 0xffffff\n\tglobal_store_dwordx4 %0, %1, %3 sc1\n\tglobal_store_dwordx4 %0, %2, %3 offset:128 sc1\n\t"
               "s_mov_b32 exec_hi, -1\n\ts_nop 0" ::"v"(voff), "v"(a), "v"(b), "s"(sbase) : "memory");
}
DEVINL void sld4(u32& d, const void* p) { asm volatile("s_load_dword %0, %1, 0" : "=s"(d) : "s"(p) : "memory"); }
template <int OFF = 0> DEVINL void ld16(u32x4& d, const void* sbase, u32 voff) {
  asm volatile("global_load_dwordx4 %0, %1, %2 offset:%3 sc1" : "=v"(d) : "v"(voff), "s"(sbase), "n"(OFF) : "memory");
}
DEVINL void ld16k(u32x4& d, const char* sbase, int kb, u32 voff) {
  const char* sb = sbase + (kb >> 2) * 4096;
  if ((kb & 3) == 0) ld16<0>(d, sb, voff); else if ((kb & 3) == 1) ld16<1024>(d, sb, voff);
  else if ((kb & 3) == 2) ld16<2048>(d, sb, voff); else ld16<3072>(d, sb, voff);
}
DEVINL void ld8(u32x2& d, const void* sbase, u32 voff) {
  asm volatile("global_load_dwordx2 %0, %1, %2 sc1" : "=v"(d) : "v"(voff), "s"(sbase) : "memory");
}
template <int OFF = 0> DEVINL void ld4(u32& d, const void* sbase, u32 voff) {
  asm volatile("global_load_dword %0, %1, %2 offset:%3 sc1" : "=v"(d) : "v"(voff), "s"(sbase), "n"(OFF) : "memory");
}
template <int OFF = 0> DEVINL void lp16(u32x4& d, const void* sbase, u32 voff) {
  asm volatile("global_load_dwordx4 %0, %1, %2 offset:%3" : "=v"(d) : "v"(voff), "s"(sbase), "n"(OFF) : "memory");
}
template <int OFF = 0> DEVINL void lp4(u32& d, const void* sbase, u32 voff) {
  asm volatile("global_load_dword %0, %1, %2 offset:%3" : "=v"(d) : "v"(voff), "s"(sbase), "n"(OFF) : "memory");
}
template <int OFF = 0> DEVINL void lp2(u32& d, const void* sbase, u32 voff) {
  asm volatile("global_load_ushort %0, %1, %2 offset:%3" : "=v"(d) : "v"(voff), "s"(sbase), "n"(OFF) : "memory");
}
template <class T> DEVINL void tie(T& a) { asm volatile("" : "+v"(a)); }
template <int N> DEVINL void wait_vm() { asm volatile("s_waitcnt vmcnt(%0)" ::"n"(N) : "memory"); }
DEVINL bool all_lanes(bool ok) { return __builtin_amdgcn_ballot_w64(!ok) == 0; }
DEVINL void spin4(u32 f, u32 tag, const void* sbase, u32 voff) {
#pragma unroll 1
  for (int it = 0; it < SPIN_CAP && !all_lanes(f == tag); it++) {
    __builtin_amdgcn_s_sleep(1);
    ld4(f, sbase, voff); wait_vm<0>(); tie(f);
  }
}
DEVINL bool arrive_last(char* smem_word, int n) {
  u32 old = 0;
  if (__builtin_amdgcn_mbcnt_hi(~0u, __builtin_amdgcn_mbcnt_lo(~0u, 0)) == 0)
    old = __hip_atomic_fetch_add((LDS_AS u32*)smem_word, 1u, __ATOMIC_RELAXED, __HIP_MEMORY_SCOPE_WORKGROUP);
  return __builtin_amdgcn_readfirstlane(old) == (u32)(n - 1);
}
DEVINL u32x2 tr_read(const char* p) {
  return __builtin_bit_cast(u32x2, __builtin_amdgcn_ds_read_tr16_b64_v4i16((LDS_AS s16x4*)p));
}
DEVINL float rdl(float v, int l) { return i2f(__builtin_amdgcn_readlane(f2i(v), l)); }
DEVINL float sel2(int c, float a, float b) { return c ? b : a; }
DEVINL u32 swap2(u32 v) { return (u32)__builtin_amdgcn_update_dpp(0, (int)v, 0x4E, 0xf, 0xf, true); }
DEVINL u32 swap1(u32 v) { return (u32)__builtin_amdgcn_update_dpp(0, (int)v, 0xB1, 0xf, 0xf, true); }
constexpr float LOG2E = 1.4426950408889634f, LN2 = 0.6931471805599453f, INV2PI = 0.15915494309189535f;

constexpr int A_TAB = 0, A_P = 8 * 1536, A_KC = A_P + 24576, A_KCS = 4112, A_GV = A_KC + 2 * A_KCS;
constexpr int B_KC = 47360, B_KCS = 4160, B_KCH = 4 * 4160, B_U = B_KC + 2 * B_KCH, B_US = 4640, B_UCH = 2 * 4640;
constexpr int B_Y = B_U + 2 * B_UCH, B_RED = B_Y + 40960;
constexpr int A_XT = B_Y;
constexpr int D_SRC = 16 * 576, D_TILE = 8 * D_SRC;
constexpr int C_RAW = 148480, L_CNT = C_RAW + 1024, LDS_BYTES = L_CNT + 64;
static_assert(A_GV + 2048 <= B_KC && B_RED + 8192 <= C_RAW && 2 * D_TILE <= C_RAW, "lds map");

template <int TG> DEVINL void conv_load(const char* pa, const char* pb, int nu, u32x4 (&A)[8], u32x4 (&B)[8]) {
  constexpr int T0 = TG ? 6 : 7;
#pragma unroll
  for (int i = 0; i <= T0; i++) {
    A[i] = *(const u32x4*)(pa + 512 * (7 - i) + 64 * (3 - nu));
    B[T0 - i] = *(const u32x4*)(pb + 512 * (T0 - i) + 64 * (3 - nu));
  }
}
template <int TG> DEVINL void conv_mma(const u32x4 (&A)[8], const u32x4 (&B)[8], f32x4 (&acc)[4]) {
  constexpr int T0 = TG ? 6 : 7, T1 = TG ? 1 : 0, T2 = TG ? 5 : 4, T3 = TG ? 2 : 3;
#pragma unroll
  for (int mu = 0; mu <= T0; mu++) {
    acc[0] = mfma16(A[mu], B[T0 - mu], acc[0]);
    if (mu >= T0 - T2) acc[2] = mfma16(A[mu - (T0 - T2)], B[T0 - mu], acc[2]);
    if (mu >= T0 - T3) acc[3] = mfma16(A[mu - (T0 - T3)], B[T0 - mu], acc[3]);
    if (mu >= T0 - T1) acc[1] = mfma16(A[mu - (T0 - T1)], B[T0 - mu], acc[1]);
  }
}
template <int TG, class FN, class FM> DEVINL void conv_body(const char* pa, const char* pb, f32x4 (&acc)[4], FN&& filler, FM&& mid) {
  u32x4 A0[8], B0[8], A1[8], B1[8];
  conv_load<TG>(pa, pb, 0, A0, B0);
  conv_load<TG>(pa, pb, 1, A1, B1);
  __builtin_amdgcn_sched_barrier(0);
  filler();
  conv_mma<TG>(A0, B0, acc);
  __builtin_amdgcn_sched_barrier(0);
  conv_load<TG>(pa, pb, 2, A0, B0);
  __builtin_amdgcn_sched_barrier(0);
  conv_mma<TG>(A1, B1, acc);
  __builtin_amdgcn_sched_barrier(0);
  conv_load<TG>(pa, pb, 3, A1, B1);
  __builtin_amdgcn_sched_barrier(0);
  conv_mma<TG>(A0, B0, acc);
  __builtin_amdgcn_sched_barrier(0);
  mid();
  __builtin_amdgcn_sched_barrier(0);
  conv_mma<TG>(A1, B1, acc);
}
DEVINL void glds4k(const void* sbase, u32 voff, u32 ldsaddr) {
  asm volatile("s_mov_b32 m0, %2\n\ts_nop 0\n\t"
               "global_load_lds_dwordx4 %0, %1 sc1\n\tglobal_load_lds_dwordx4 %0, %1 offset:1024 sc1\n\t"
               "global_load_lds_dwordx4 %0, %1 offset:2048 sc1\n\tglobal_load_lds_dwordx4 %0, %1 offset:3072 sc1"
               ::"v"(voff), "s"(sbase), "s"(ldsaddr) : "memory");
}
DEVINL u32x4 tr8(const char* p) {
  const u32x2 lo = tr_read(p), hi = tr_read(p + 256);
  return u32x4{lo[0], lo[1], hi[0], hi[1]};
}
struct Ep { float q0, v; };
DEVINL float swi(const f32x16& a, int i, const Ep& q) {
  const float G = a[(i & 3) + 8 * (i >> 2)], U = a[(i & 3) + 8 * (i >> 2) + 4];
  const float e = __builtin_amdgcn_exp2f(G * q.q0);
  return (G * U) * __builtin_amdgcn_rcpf(fmaf(e, q.v, q.v));
}
DEVINL u32 ln2_fold(f32x2 s, int h, Ep& q) {
  const float m = s[0] * (1.f / D);
  const float v = fmaxf(fmaf(-m, m, s[1] * (1.f / D)), 0.f) + 1e-5f;
  const float rs = __builtin_amdgcn_rsqf(v);
  q.q0 = -rs; q.v = v;
  const float val = h ? v * rs : -m;
  const float vhi = rbf(val);
  return pk(vhi, val - vhi);
}
DEVINL void ln2_acc(const char* rb, int kp0, int kp1, f32x4 (&sg)[2][2], f32x4 (&sx)[2][2]) {
  const u32x4 ones = {0x3f803f80u, 0x3f803f80u, 0x3f803f80u, 0x3f803f80u};
  u32x4 fr[4][2];
  auto rd = [&](int n) NOPK {
#pragma unroll
    for (int gi = 0; gi < 2; gi++) {
      const char* pp = rb + (n & 1) * 32768 + (kp0 + (n >> 1)) * 2048 + gi * 32;
      const u32x2 lo = tr_read(pp), hi = tr_read(pp + 1024);
      fr[n & 3][gi] = u32x4{lo[0], lo[1], hi[0], hi[1]};
    }
  };
  rd(0); rd(1); rd(2);
#pragma unroll
  for (int n = 0; n < 2 * (kp1 - kp0); n++) {
    if (n + 3 < 2 * (kp1 - kp0)) rd(n + 3);
    __builtin_amdgcn_sched_barrier(0);
#pragma unroll
    for (int gi = 0; gi < 2; gi++) {
      sg[n & 1][gi] = mfma16(fr[n & 3][gi], fr[n & 3][gi], sg[n & 1][gi]);
      sx[n & 1][gi] = mfma16(ones, fr[n & 3][gi], sx[n & 1][gi]);
    }
    __builtin_amdgcn_sched_barrier(0);
  }
}
DEVINL void ln2_put(f32x2* raw, int lane, const f32x4 (&sg)[2], const f32x4 (&sx)[2]) {
#pragma unroll
  for (int gi = 0; gi < 2; gi++) {
    const float gsel = (lane & 2) ? ((lane & 1) ? sg[gi][3] : sg[gi][2]) : ((lane & 1) ? sg[gi][1] : sg[gi][0]);
    if ((lane >> 4) == ((lane & 15) >> 2)) raw[gi * 16 + (lane & 15)] = f32x2{sx[gi][0], gsel};
  }
}

typedef __attribute__((address_space(1))) unsigned long long gu64;
typedef __attribute__((address_space(1))) u32 gu32;
DEVINL void put_granule(char* p, u32 val, u32 tag) {
  __hip_atomic_store((gu64*)p, ((unsigned long long)tag << 32) | val, __ATOMIC_RELAXED, __HIP_MEMORY_SCOPE_AGENT);
}
DEVINL void put_flag(char* p, u32 tag) { __hip_atomic_store((gu32*)p, tag, __ATOMIC_RELAXED, __HIP_MEMORY_SCOPE_AGENT); }

__global__ NOPK __launch_bounds__(512, 2) void fused(
    const u16* __restrict__ x, const u16* __restrict__ Are, const u16* __restrict__ Aim, const u16* __restrict__ Bre,
    const u16* __restrict__ Bim, const u16* __restrict__ Cre, const u16* __restrict__ Cim, const u16* __restrict__ logdt,
    const u16* __restrict__ dskip, const u16* __restrict__ w1, const u16* __restrict__ w3, const u16* __restrict__ w2,
    const u16* __restrict__ cvec, const u16* __restrict__ wmod, char* ws, u16* __restrict__ out) {
  __shared__ __attribute__((aligned(16))) char smem[LDS_BYTES];
  const int b = blockIdx.x, tid = threadIdx.x, lane = tid & 63;
  const int w = __builtin_amdgcn_readfirstlane(tid >> 6);
  TSDECL; TS(0);
  const int xg = b & 7, idx = b >> 3;
  const int p = xg * 32 + idx;
  const int r = lane & 15, g = lane >> 4;
  asm volatile("" ::"s"(x), "s"(Are), "s"(Aim), "s"(Bre), "s"(Bim), "s"(Cre), "s"(Cim), "s"(logdt), "s"(dskip), "s"(w1), "s"(w3),
               "s"(w2), "s"(cvec), "s"(wmod), "s"(ws), "s"(out));
  u32x4 xr, wm0, wm1;
  u32 vare, vaim, vbre, vbim, vcre, vcim, c0, c1;
  const int cg = xg * 24 + (idx < 24 ? idx : 23);
  u32 e_prev, ldt2, dsk;
  lp16(xr, x + b * 4096, tid * 16);
  sld4(e_prev, ws + WS_E); sld4(ldt2, logdt + 2 * p); sld4(dsk, dskip + 2 * p);
  {
    const u32 so = (p * 256 + w * 32 + (lane & 31)) * 2;
    lp2(vare, Are, so); lp2(vaim, Aim, so); lp2(vbre, Bre, so); lp2(vbim, Bim, so); lp2(vcre, Cre, so); lp2(vcim, Cim, so);
    const u32 mo = (tid >> 3) * 6144 + (tid & 7) * 16;
    const u16* wmp = wmod + (cg & 3) * 128 * 3072 + (cg >> 2) * 64;
    lp16(wm0, wmp, mo); lp16(wm1, wmp + 64 * 3072, mo);
    lp2(c0, cvec + (cg & 3) * 128, (tid >> 3) * 2); lp2<128>(c1, cvec + (cg & 3) * 128, (tid >> 3) * 2);
  }
  TS(1);
  if (tid < 32) *(u32x4*)(smem + B_KC + (tid >> 2) * B_KCS + 4096 + (tid & 3) * 16) = u32x4{0, 0, 0, 0};
  else if (tid < 36) *(u32x4*)(smem + L_CNT + (tid - 32) * 16) = u32x4{0, 0, 0, 0};
  else if (tid >= 64 && tid < 64 + 132) {
    int i = tid - 64, cp = i / 33, e = i - cp * 33;
    if (e < 32 || (cp & 1)) *(u32x2*)(smem + B_U + cp * B_US + e * 16) = u32x2{0, 0};
    if (e < 32) *(u32x2*)(smem + B_U + cp * B_US + e * 16 + 8) = u32x2{0, 0};
  }
  asm volatile("s_waitcnt lgkmcnt(0)" : "+s"(e_prev), "+s"(ldt2), "+s"(dsk)::"memory");
  const u32 tag = e_prev * 1664525u + 1013904223u;
  wait_vm<10>(); tie(xr);
  {
    *(u32x4*)(smem + A_XT + w * 1056 + lane * 16) = xr;
    float f[8];
#pragma unroll
    for (int j = 0; j < 4; j++) { f[2 * j] = bflo(xr[j]); f[2 * j + 1] = bfhi(xr[j]); }
    const float m = wave_sum(((f[0] + f[1]) + (f[2] + f[3])) + ((f[4] + f[5]) + (f[6] + f[7]))) * (1.f / D);
    float s = 0.f;
#pragma unroll
    for (int j = 0; j < 8; j++) { f[j] -= m; s = fmaf(f[j], f[j], s); }
    const float sd = __builtin_amdgcn_sqrtf(rbf(rbf(wave_sum(s) * (1.f / D)) + 1e-5f));
    if (lane == 0) *(u32*)(smem + A_XT + w * 1056 + 1024) = pk(m, sd);
  }
  TS(2);
  wait_vm<4>(); tie(vare); tie(vaim); tie(vbre); tie(vbim); tie(vcre); tie(vcim);
  char* tab = smem + A_TAB + w * 1536;
  {
    const float dt = rbf(__builtin_amdgcn_exp2f((w >> 2 ? bfhi(ldt2) : bflo(ldt2)) * LOG2E));
    const float a = dt * bflo(vare) * LOG2E, bq = dt * bflo(vaim) * INV2PI;
    const float br = dt * bflo(vbre), bi = dt * bflo(vbim), cr = bflo(vcre), ci = bflo(vcim);
    const float m = lane < 32 ? 1.f : 256.f;
    const float e = __builtin_amdgcn_exp2f(a * m), ph = __builtin_amdgcn_fractf(-bq * m);
    const float ex = e * __builtin_amdgcn_cosf(ph), ey = e * __builtin_amdgcn_sinf(ph);
    char* t = tab + (lane & 31) * 48;
    *(f32x2*)(t + 16 + (lane >> 5) * 8) = f32x2{ex, ey};
    f32x4 o = {a, bq, cr * br - ci * bi, cr * bi + ci * br};
    if (lane >= 32) o = f32x4{ex, ey, ex * ex - ey * ey, (ex + ex) * ey};
    *(f32x4*)(t + (lane >> 5) * 32) = o;
  }
  TS(3);
  wait_vm<0>(); tie(wm0); tie(wm1); tie(c0); tie(c1);
  const int fb = idx & 15, mat = idx >> 4, d0 = xg * 64 + w * 8;
  const bool rt2 = b < 224 && w < 4;
  u32 v[8], t2[8];
  {
    const float cx0 = bflo(c0), cx1 = bflo(c1);
    const float s0 = rbf(cx0 * __builtin_amdgcn_rcpf(1.f + __builtin_amdgcn_exp2f(cx0 * -LOG2E)));
    const float s1 = rbf(cx1 * __builtin_amdgcn_rcpf(1.f + __builtin_amdgcn_exp2f(cx1 * -LOG2E)));
    float acc[8];
#pragma unroll
    for (int j = 0; j < 4; j++) {
      acc[2 * j] = fmaf(s0, bflo(wm0[j]), s1 * bflo(wm1[j]));
      acc[2 * j + 1] = fmaf(s0, bfhi(wm0[j]), s1 * bfhi(wm1[j]));
    }
    const float q0 = dpp_add<0x128>(rs16(rs32(acc[0], acc[4]), rs32(acc[2], acc[6])));
    const float q1 = dpp_add<0x128>(rs16(rs32(acc[1], acc[5]), rs32(acc[3], acc[7])));
    *(float*)(smem + A_GV + w * 256 + (lane & 7) * 32 + (lane >> 3) * 4) = (lane & 8) ? q1 : q0;
  if (rt2) {
#pragma unroll
    for (int i = 0; i < 2; i++) {
      const u16* wp = w2 + (size_t)(b * 8 + i * 4) * D;
      lp4<0>(t2[4 * i], wp, (tid & 255) * 4); lp4<1024>(t2[4 * i + 1], wp, (tid & 255) * 4);
      lp4<2048>(t2[4 * i + 2], wp, (tid & 255) * 4); lp4<3072>(t2[4 * i + 3], wp, (tid & 255) * 4);
    }
  }
    lds_barrier();
    if (w == 0 && idx < 24) {
      float s = 0.f;
#pragma unroll
      for (int i = 0; i < 8; i++) s += *(const float*)(smem + A_GV + i * 256 + lane * 4);
      st8(ws + WS_MG + (cg & 3) * 24576 + (cg >> 2) * 512, lane * 8, u32x2{f2u(s), tag});
    }
    const char* xt = smem + A_XT + (lane & 7) * 1056;
    const u32 st = *(const u32*)(xt + 1024);
#pragma unroll
    for (int e = 0; e < 2; e++) {
      const u32x2 xq = *(const u32x2*)(xt + (e * 64 + w * 8 + (lane >> 3)) * 8);
      st16(ws + WS_XG + (size_t)(e * 64 + w * 8) * 32768 + b * 128, (lane >> 3) * 32768 + (lane & 7) * 16, u32x4{xq[0], xq[1], st, tag});
    }
  }
  {
    const u16* wp = (mat ? w3 : w1) + (size_t)d0 * F + fb * 112;
    const u32 vo = (lane < 56 ? lane : 55) * 4;
#pragma unroll
    for (int i = 0; i < 4; i++) { lp4<0>(v[2 * i], wp + 2 * i * F, vo); lp4<2 * F>(v[2 * i + 1], wp + 2 * i * F, vo); }
  }
  TS(4);
  const u32 gcode = __builtin_amdgcn_perm(0x400b0916u, 0x0a201408u, (r & 7) | 0x0c0c0c00u);
  const int gx = r < 8 ? 2 * r : (int)((gcode >> 2) * 256 + (gcode & 3) * 4);
  {
    asm volatile("" ::: "memory");
    const float mF = (float)(16 * r), mX = (float)gx;
    const char* tp = tab + g * 8 * 48;
    const char* tq = tp + (r < 8 ? 16 : 32);
    f32x4 acc[3];
#pragma unroll
    for (int s = 0; s < 3; s++) acc[s] = f32x4{0.f, 0.f, 0.f, 0.f};
#pragma unroll
    for (int ks = 0; ks < 2; ks++) {
      u32x4 Ff, Gf[3];
#pragma unroll
      for (int i = 0; i < 4; i++) {
        const f32x4 s0 = *(const f32x4*)(tp + (ks * 4 + i) * 48), s1 = *(const f32x4*)(tq + (ks * 4 + i) * 48);
        const float e0 = __builtin_amdgcn_exp2f(s0[0] * mF), p0 = __builtin_amdgcn_fractf(s0[1] * mF);
        const float zx = e0 * __builtin_amdgcn_cosf(p0), zy = e0 * __builtin_amdgcn_sinf(p0);
        Ff[i] = pk(s0[2] * zx - s0[3] * zy, s0[2] * zy + s0[3] * zx);
        const float e1 = __builtin_amdgcn_exp2f(s0[0] * mX), p1 = __builtin_amdgcn_fractf(s0[1] * -mX);
        const float xx = e1 * __builtin_amdgcn_cosf(p1), xy = e1 * __builtin_amdgcn_sinf(p1);
        Gf[0][i] = pk(xx, xy);
        Gf[1][i] = pk(xx * s1[0] - xy * s1[1], xx * s1[1] + xy * s1[0]);
        Gf[2][i] = pk(xx * s1[2] - xy * s1[3], xx * s1[3] + xy * s1[2]);
      }
#pragma unroll
      for (int s = 0; s < 3; s++) acc[s] = mfma16(Ff, Gf[s], acc[s]);
    }
#pragma unroll
    for (int s = 0; s < 3; s++) *(f32x4*)(smem + A_P + w * 3072 + s * 1024 + lane * 16) = acc[s];
  }
  TS(5);
  if (rt2) {
    wait_vm<10>();
#pragma unroll
    for (int i = 0; i < 8; i++) tie(t2[i]);
    const u32 vo = (((tid >> 4) & 15) * 112 * 64 + (tid & 15)) * 16;
#pragma unroll
    for (int e = 0; e < 2; e++) {
      u32x4 o;
#pragma unroll
      for (int j = 0; j < 4; j++) o[j] = __builtin_amdgcn_perm(t2[2 * j + 1], t2[2 * j], e ? 0x07060302u : 0x05040100u);
      st16(ws + WS_W2F + b * 512 + e * 256, vo, o);
    }
  }
  u32x4 xg4[4];
  u32x2 mg, mh;
  const char* xgb = ws + WS_XG + (size_t)(p >> 1) * 32768;
  const u32 xgo = (tid >> 2) * 256 + (tid & 3) * 16;
  u32 mga = (4 - ((lane >> 3) & 1)) * 512 + d0 + (lane & 7), mgb = ((0x5210 >> ((lane & 6) * 2)) & 7) * 512 + 2 * p + (lane & 1);
  tie(mga); tie(mgb);
  const u32 mgo = ((lane & 16) ? mgb : mga) * 8 + (lane >> 5) * 24576;
  auto look = [&]() NOPK {
    ld8(mg, ws + WS_MG, mgo); ld8(mh, ws + WS_MG + 49152, mgo);
    ld16<0>(xg4[0], xgb, xgo); ld16<64>(xg4[1], xgb, xgo); ld16<128>(xg4[2], xgb, xgo); ld16<192>(xg4[3], xgb, xgo);
  };
  lds_barrier();
  TS(6);
  TSF(0, 6);
  const int kch = tid >> 8;
  u16* Kc = (u16*)(smem + A_KC + kch * A_KCS);
  if ((w & 3) < 3) {
    const int s = w & 3;
    const char* pp = smem + A_P + kch * 12288 + s * 1024 + lane * 16;
    const f32x4 q = (*(const f32x4*)(pp) + *(const f32x4*)(pp + 3072)) + (*(const f32x4*)(pp + 6144) + *(const f32x4*)(pp + 9216));
    const int pos = gx + 64 * g + (r < 8 ? (s == 0 ? 0 : s == 1 ? 1 : 256) : s * 256);
    const u32 o0 = pk(q[0], q[1]), o1 = pk(q[2], q[3]);
    if (pos <= 2048) Kc[pos] = (u16)o0;
    if (pos < 2048) {
      Kc[pos + 16] = (u16)(o0 >> 16);
      Kc[pos + 32] = (u16)o1;
      Kc[pos + 48] = (u16)(o1 >> 16);
    }
  }
  look();
  lds_barrier();
  TS(7);
  {
    u32 e[6];
    const int ki = tid & 255;
#pragma unroll
    for (int j = 0; j < 6; j++) {
      const float l0 = (float)(2047 - 8 * ki - 2 * j);
      const u32 bits = pk(fmaxf(l0, 0.f), fmaxf(l0 - 1.f, 0.f));
      e[j] = (u32)Kc[(int)bflo(bits)];
      if (j < 5) e[j] |= (u32)Kc[(int)bfhi(bits)] << 16;
    }
    if (ki == 255) { e[4] = 0; e[5] = 0; }
    const u32 a0 = __builtin_amdgcn_alignbit(e[1], e[0], 16), a1 = __builtin_amdgcn_alignbit(e[2], e[1], 16);
    const u32 a2 = __builtin_amdgcn_alignbit(e[3], e[2], 16), a3 = __builtin_amdgcn_alignbit(e[4], e[3], 16);
    const u32 a4 = __builtin_amdgcn_alignbit(e[5], e[4], 16);
    char* kc = smem + B_KC + kch * B_KCH + ki * 16;
    *(u32x4*)(kc) = u32x4{e[0], e[1], e[2], e[3]};
    *(u32x4*)(kc + B_KCS) = u32x4{a0, a1, a2, a3};
    *(u32x4*)(kc + 2 * B_KCS) = u32x4{e[1], e[2], e[3], e[4]};
    *(u32x4*)(kc + 3 * B_KCS) = u32x4{a1, a2, a3, a4};
  }
  TS(8);
  wait_vm<0>(); tie(xg4[0]); tie(xg4[1]); tie(xg4[2]); tie(xg4[3]); tie(mg); tie(mh);
#pragma unroll
  for (int i = 0; i < 8; i++) tie(v[i]);
#pragma unroll 1
  for (int it = 0; it < SPIN_CAP && !all_lanes(xg4[0][3] == tag && xg4[1][3] == tag && xg4[2][3] == tag && xg4[3][3] == tag && mg[1] == tag && mh[1] == tag); it++) {
    __builtin_amdgcn_s_sleep(1);
    look();
    wait_vm<0>(); tie(xg4[0]); tie(xg4[1]); tie(xg4[2]); tie(xg4[3]); tie(mg); tie(mh);
  }
  TS(9);
  const int g8 = tid >> 1, cc = tid & 1;
  const float mq = u2f(mg[0]) + u2f(mh[0]);
  const float mv = rbf(rs32(mq, mq)), mv1 = rbf(1.f + mv);
  const float ga = sel2(cc, rdl(mv, 20), rdl(mv, 21));
  u32x4 hp, xv;
  {
    const float sh0 = rdl(mv, 16), sh1 = rdl(mv, 17), sc0 = rdl(mv1, 18), sc1 = rdl(mv1, 19);
    const u32 psel = cc ? 0x03020706u : 0x05040100u;
    const bool up = tid & 2;
    u32 ph[4], px[4];
#pragma unroll
    for (int i = 0; i < 4; i++) {
      const u32 xq = (p & 1) ? xg4[i][1] : xg4[i][0];
      const float m = bflo(xg4[i][2]), rs = __builtin_amdgcn_rcpf(bfhi(xg4[i][2]));
      const u32 n = pk((bflo(xq) - m) * rs, (bfhi(xq) - m) * rs);
      const u32 hq = pk(fmaf(bflo(n), sc0, sh0), fmaf(bfhi(n), sc1, sh1));
      ph[i] = __builtin_amdgcn_perm(swap1(hq), hq, psel);
      px[i] = __builtin_amdgcn_perm(swap1(xq), xq, psel);
    }
#pragma unroll
    for (int j = 0; j < 2; j++) {
      const u32 rh = swap2(up ? ph[j] : ph[2 + j]), kh = up ? ph[2 + j] : ph[j];
      hp[2 * j] = up ? rh : kh; hp[2 * j + 1] = up ? kh : rh;
      const u32 rx = swap2(up ? px[j] : px[2 + j]), kx = up ? px[2 + j] : px[j];
      xv[2 * j] = up ? rx : kx; xv[2 * j + 1] = up ? kx : rx;
    }
    char* u = smem + B_U + cc * B_UCH + 512 + g8 * 16;
    *(u32x4*)(u) = hp;
    *(u32x2*)(u + B_US + 8) = u32x2{hp[0], hp[1]};
    *(u32x2*)(u + B_US + 16) = u32x2{hp[2], hp[3]};
  }
  TS(10);
  lds_barrier();
  TS(11);
  const int tt = (xg & 1) * 8 + (idx & 7), fs = (xg >> 1) * 4 + (idx >> 3);
  u32 fw;
  {
    const int ch = w & 1, vh = (w >> 1) & 1, tg = w >> 2;
    const char* pa = smem + B_KC + ch * B_KCH + (3 - (r & 3)) * B_KCS + 2 * (2040 - 8 * (r >> 2) + 8 * g) - 512 * 7 - 64 * (4 * vh + 3);
    const char* pb = smem + B_U + ch * B_UCH + ((r & 1) ? 0 : B_US) + 2 * (256 + 32 * (r >> 1) + 8 * g) - 64 * (4 * vh + 3);
    f32x4 acc[4];
#pragma unroll
    for (int i = 0; i < 4; i++) acc[i] = f32x4{0.f, 0.f, 0.f, 0.f};
    auto prep = [&]() NOPK {
      const float kf = mat ? LN2 : LOG2E;
      const float cs = mv1 * kf, ss = mv * kf;
      u32x4 o0, o1;
      f32x4 sums = {0.f, 0.f, 0.f, 0.f};
#pragma unroll
      for (int j = 0; j < 4; j++) {
        const float k0 = rdl(cs, 2 * j), k1 = rdl(cs, 2 * j + 1), s0 = rdl(ss, 8 + 2 * j), s1 = rdl(ss, 8 + 2 * j + 1);
        const float a0 = bflo(v[2 * j]), a1 = bflo(v[2 * j + 1]), b0 = bfhi(v[2 * j]), b1 = bfhi(v[2 * j + 1]);
        o0[j] = pk(a0 * k0, a1 * k1);
        o1[j] = pk(b0 * k0, b1 * k1);
        sums[0] = dot2(o0[j], 0x3f803f80u, sums[0]);
        sums[1] = dot2(o1[j], 0x3f803f80u, sums[1]);
        sums[2] = fmaf(a0, s0, fmaf(a1, s1, sums[2]));
        sums[3] = fmaf(b0, s0, fmaf(b1, s1, sums[3]));
      }
      st16x2_56(ws + WS_W1F + (size_t)(fb * 7 * 32 + (d0 >> 4)) * 1024 + (w & 1) * 512 + mat * 256, (lane >> 3) * 32768 + (lane & 7) * 16, o0, o1);
      *(f32x4*)(smem + B_RED + tid * 16) = sums;
    };
    auto mid = [&]() NOPK {
      wait_vm<0>();
      if (arrive_last(smem + L_CNT, 8)) st4one(ws + WS_FW + (fb * 16 + mat * 8 + xg) * 128, tag);
    };
    if (tg == 0) conv_body<0>(pa, pb, acc, prep, mid); else conv_body<1>(pa, pb, acc, prep, mid);
    TS(12);
    float* Y = (float*)(smem + B_Y) + (vh * 2 + ch) * 2560 + 40 * (r >> 1) + 4 * (r & 1) + 8 * g;
    const int t0 = tg ? 6 : 7, t1 = tg ? 1 : 0, t2_ = tg ? 5 : 4, t3 = tg ? 2 : 3;
    *(f32x4*)(Y + 320 * t0) = acc[0];
    *(f32x4*)(Y + 320 * t1) = acc[1];
    *(f32x4*)(Y + 320 * t2_) = acc[2];
    *(f32x4*)(Y + 320 * t3) = acc[3];
  }
  lds_barrier();
  TS(13);
  TSF(7, 13);
  {
    const float* Y = (const float*)(smem + B_Y) + cc * 2560 + 8 * g8 + 8 * (g8 >> 2);
    const f32x4 ya = *(const f32x4*)Y + *(const f32x4*)(Y + 5120), yb = *(const f32x4*)(Y + 4) + *(const f32x4*)(Y + 5124);
    const float dk = cc ? bfhi(dsk) : bflo(dsk);
    u32x4 o;
#pragma unroll
    for (int j = 0; j < 4; j++) {
      const float y0 = j < 2 ? ya[2 * j] : yb[2 * j - 4], y1 = j < 2 ? ya[2 * j + 1] : yb[2 * j - 3];
      o[j] = pk(fmaf(ga, fmaf(bflo(hp[j]), dk, y0), bflo(xv[j])), fmaf(ga, fmaf(bfhi(hp[j]), dk, y1), bfhi(xv[j])));
    }
    ld4(fw, ws + WS_FW + fs * 2048, (lane & 15) * 128);
    st16(ws + WS_X1 + p * 128, (g8 >> 2) * 32768 + cc * 64 + (g8 & 3) * 16, o);
  }
  TS(14);
  const int h = lane >> 5, c = lane & 31;
  const int tt2 = xg * 2 + (idx >> 4), sel = (idx >> 3) & 1, dt = idx & 7;
  const int dl = (c & 3) + 4 * (c >> 3) + 16 * ((c >> 2) & 1);
  const int rlo = ((dt ^ sel) & 1) * 7168, rhi = 7168 - rlo;
  const char* w2b = ws + WS_W2F + ((size_t)(dt * 2) * 112 + w * 14) * 1024;
  const u32 w2o = (h * 32 + (dl & 1) * 16 + (dl >> 1)) * 16;
  f32x2* raw = (f32x2*)(smem + C_RAW);
  u32x4 W2[28];
  u32 fh, pf = 0;
  const int rot = (2 * (idx >> 3) + idx) & 7;
  const char* xa = ws + WS_X1 + tt * 131072 + (w & 1) * 32768 + (((w >> 1) + rot) & 7) * 4096;
  const char* xc = ws + WS_X1 + tt * 131072 + (w & 1) * 32768 + (((w >> 1) + rot + 4) & 7) * 4096;
  const u32 lpc = (u32)(size_t)(LDS_AS char*)smem + (w & 1) * 32768 + (w >> 1) * 4096;
  auto warm_w2 = [&]() NOPK {
    const char* pb = ws + WS_W2F + (idx * 8 + w) * 7168;
    const u32 po = (lane < 56 ? lane : 55) * 128;
    asm volatile("global_load_dword %0, %1, %2 sc1\n\tglobal_load_dword %0, %1, %2 offset:64 sc1" : "+v"(pf) : "v"(po), "s"(pb) : "memory");
  };
  auto ldw2 = [&](int i) NOPK { ld16k(W2[i], w2b + (i & 1) * 114688 + (i < 14 ? rlo : rhi), (i >> 1) % 7, w2o); };
  wait_vm<1>(); tie(fw);
  if (w == 0) {
    __builtin_amdgcn_s_setprio(3);
    {
      f32x4 s = {0.f, 0.f, 0.f, 0.f};
#pragma unroll
      for (int q = 0; q < 8; q++) s += *(const f32x4*)(smem + B_RED + q * 1024 + lane * 16);
      st16(ws + WS_CD + ((xg * 16 + fb) * 2 + mat) * 1024, lane * 16, __builtin_bit_cast(u32x4, s));
      st8m<1>(ws + WS_GB + p * 8, 0, u32x2{f2u(rdl(mv, 22)), f2u(rdl(mv, 23))});
    }
    wait_vm<0>();
    TS(15);
#pragma unroll 1
    for (int it = 0; it < SPIN_CAP * 16; it++) {
      if (__builtin_amdgcn_readfirstlane(*(volatile LDS_AS u32*)(smem + L_CNT + 4)) == 7) break;
      __builtin_amdgcn_s_sleep(1);
    }
    st4one(ws + WS_FX + (b & 63) * 128 + (b >> 6) * 4, tag);
    TS(16);
    warm_w2();
#pragma unroll 1
    for (int it = 0; it < SPIN_CAP; it++) {
      u32x4 f;
      ld16(f, ws + WS_FX, lane * 128); wait_vm<0>(); tie(f);
      if (all_lanes(f[0] == tag && f[1] == tag && f[2] == tag && f[3] == tag)) break;
    }
    tie(pf);
    TS(17);
    bar();
    TS(18);
    glds4k(xa, lane * 16, lpc); glds4k(xc, lane * 16, lpc + 16384);
    const char* rb = smem + (lane >> 2) * 64 + (lane & 3) * 8;
    f32x4 sg[2][2], sx[2][2];
#pragma unroll
    for (int i = 0; i < 4; i++) { sg[i >> 1][i & 1] = f32x4{0.f, 0.f, 0.f, 0.f}; sx[i >> 1][i & 1] = f32x4{0.f, 0.f, 0.f, 0.f}; }
    wait_vm<4>(); bar();
    TS(19); TC(20);
    TSF(14, 20);
    glds4k(xa + 65536, lane * 16, lpc + 65536); glds4k(xc + 65536, lane * 16, lpc + 65536 + 16384);
    if (b == 0) st4one(ws + WS_E, tag);
    ln2_acc(rb, 0, 8, sg, sx);
    TS(21);
    wait_vm<8>(); bar();
    TS(22);
    ln2_acc(rb, 8, 16, sg, sx);
    ln2_put(raw, lane, sg[0], sx[0]); ln2_put(raw + 32, lane, sg[1], sx[1]);
    TS(23);
    wait_vm<0>();
    lds_barrier();
    TS(24);
#pragma unroll
    for (int i = 0; i < 4; i++) { sg[i >> 1][i & 1] = f32x4{0.f, 0.f, 0.f, 0.f}; sx[i >> 1][i & 1] = f32x4{0.f, 0.f, 0.f, 0.f}; }
    ln2_acc(rb + 65536, 0, 16, sg, sx);
    ln2_put(raw + 64, lane, sg[0], sx[0]); ln2_put(raw + 96, lane, sg[1], sx[1]);
    asm volatile("" ::: "memory");
    *(volatile LDS_AS u32*)(smem + L_CNT + 16) = 1;
    TS(25);
    __builtin_amdgcn_s_setprio(0);
#pragma unroll
    for (int i = 0; i < 28; i++) ldw2(i);
    TS(26); TS(27); TC(28);
    TSF(21, 28);
    wait_vm<0>();
#pragma unroll
    for (int i = 0; i < 28; i++) tie(W2[i]);
    TS(29);
  } else {
    const int ft = fs * 7 + w - 1;
    const char* wb = ws + WS_W1F + (size_t)ft * 32768;
    const int fl = (c & 3) + 8 * ((c >> 2) & 1) + 4 * (c >> 4);
    const u32 voff = (h * 32 + ((c >> 3) & 1) * 16 + (fl & 1) * 8 + (fl >> 1)) * 16;
    auto x1_done = [&]() NOPK {
      asm volatile("s_waitcnt lgkmcnt(0)" ::: "memory");
      if (lane == 0) __hip_atomic_fetch_add((LDS_AS u32*)(smem + L_CNT + 4), 1u, __ATOMIC_RELAXED, __HIP_MEMORY_SCOPE_WORKGROUP);
    };
    const bool wok = all_lanes(fw == tag);
    if (!wok) {
      wait_vm<0>();
      x1_done();
      spin4(fw, tag, ws + WS_FW + fs * 2048, (lane & 15) * 128);
    }
    TS(15);
    u32x4 A[32];
#pragma unroll
    for (int i = 0; i < 8; i++) {
      if (i == W1A) {
        wait_vm<4 * W1A>();
        if (wok) x1_done();
        TS(16);
      }
      const char* wq = wb + ((i + rot) & 7) * 4096;
      ld16<0>(A[4 * i], wq, voff); ld16<1024>(A[4 * i + 1], wq, voff);
      ld16<2048>(A[4 * i + 2], wq, voff); ld16<3072>(A[4 * i + 3], wq, voff);
    }
    warm_w2();
    const char* rb = smem + h * 512 + ((lane >> 2) & 3) * 64 + ((lane >> 4) & 1) * 32 + (lane & 3) * 8;
    const char* rb2 = rb + 65536;
    u32 cdv[8];
    f32x16 acc[4];
#pragma unroll
    for (int T = 0; T < 4; T++)
#pragma unroll
      for (int i = 0; i < 16; i++) acc[T][i] = 0.f;
    TS(17);
    lds_barrier();
    TS(18);
    glds4k(xa, lane * 16, lpc); glds4k(xc, lane * 16, lpc + 16384);
    wait_vm<4>();
#pragma unroll
    for (int i = 0; i < 32; i++) tie(A[i]);
    tie(pf);
    bar();
    TS(19); TC(20);
    TSF(14, 20);
    u32x4 Bc[2], Bn[2];
    Bc[0] = tr8(rb); Bc[1] = tr8(rb + 32768);
    f32x2 rw[2];
#pragma unroll
    for (int ks = 0; ks < 32; ks++) {
      if (ks == 15) {
        TS(21);
        wait_vm<16>();
        bar();
        TS(22);
      } else if (ks == 31) {
        TS(23);
        wait_vm<0>();
#pragma unroll
        for (int q = 0; q < 8; q++) tie(cdv[q]);
        bar();
        TS(24);
      }
      if (ks < 31) {
        Bn[0] = tr8(rb + (ks + 1) * 1024); Bn[1] = tr8(rb + 32768 + (ks + 1) * 1024);
      } else {
        Bn[0] = tr8(rb2); Bn[1] = tr8(rb2 + 32768);
        rw[0] = raw[c]; rw[1] = raw[32 + c];
      }
      __builtin_amdgcn_sched_barrier(0);
      acc[0] = mfma32(A[ks], Bc[0], acc[0]);
      acc[1] = mfma32(A[ks], Bc[1], acc[1]);
      __builtin_amdgcn_sched_barrier(0);
      if (ks == 3) glds4k(xa + 65536, lane * 16, lpc + 65536);
      if (ks == 7) {
        const u32 co = ((c >> 3) & 1) * 1024 + ((w - 1) * 8 + (fl >> 1)) * 16 + h * 8 + (fl & 1) * 4;
#pragma unroll
        for (int q = 0; q < 8; q++) ld4(cdv[q], ws + WS_CD + (q * 16 + fs) * 2048, co);
      }
      if (ks == 11) glds4k(xc + 65536, lane * 16, lpc + 65536 + 16384);
      Bc[0] = Bn[0]; Bc[1] = Bn[1];
    }
    const float cd = ((u2f(cdv[0]) + u2f(cdv[1])) + (u2f(cdv[2]) + u2f(cdv[3]))) + ((u2f(cdv[4]) + u2f(cdv[5])) + (u2f(cdv[6]) + u2f(cdv[7])));
    const float chi = rbf(cd);
    const u32x4 Ax = {pk(chi, chi), pk(cd - chi, cd - chi), 0, 0};
    const char* hb = ws + WS_HF + ((size_t)(tt * 4) * 112 + ft) * 1024;
    Ep q[4];
    float o[8];
#pragma unroll
    for (int s = 0; s < 32; s++) {
      const int n = s + 1;
      const int ta = s < 28 ? 2 : 3, tb_ = s < 24 ? 3 : ta, ka = s < 24 ? s : 24 + 2 * (s & 3), kb = s < 24 ? s : ka + 1;
      if (s == 21) {
        TS(25);
#pragma unroll 1
        for (int it = 0; it < SPIN_CAP * 16; it++)
          if (__builtin_amdgcn_readfirstlane(*(volatile LDS_AS u32*)(smem + L_CNT + 16)) == 1) break;
        asm volatile("" ::: "memory");
        TS(26);
        rw[0] = raw[64 + c]; rw[1] = raw[96 + c];
      }
      if (n < 24) {
        Bn[0] = tr8(rb2 + n * 1024); Bn[1] = tr8(rb2 + 32768 + n * 1024);
      } else if (n < 32) {
        const char* rp = rb2 + (n < 28 ? 0 : 32768) + (24 + 2 * (n & 3)) * 1024;
        Bn[0] = tr8(rp); Bn[1] = tr8(rp + 1024);
      }
      __builtin_amdgcn_sched_barrier(0);
      acc[ta] = mfma32(A[ka], Bc[0], acc[ta]);
      if (s < 2 || s == 22 || s == 23) {
        const int T = s < 2 ? s : s - 20;
        const u32 be = ln2_fold(rw[T & 1], h, q[T]);
        acc[T] = mfma32(Ax, u32x4{be, be, 0, 0}, acc[T]);
      }
      const int j = s >= 3 && s < 11 ? 2 * (s - 3) : s >= 28 ? 16 + 2 * (s - 28) : -1;
      if (j >= 0) o[j & 7] = swi(acc[j >> 3], j & 7, q[j >> 3]);
      __builtin_amdgcn_sched_barrier(0);
      acc[tb_] = mfma32(A[kb], Bc[1], acc[tb_]);
      if (j >= 0) {
        o[(j + 1) & 7] = swi(acc[j >> 3], (j + 1) & 7, q[j >> 3]);
        if ((j & 7) == 6) st16(hb + (j >> 3) * 114688, lane * 16, u32x4{pk(o[0], o[1]), pk(o[2], o[3]), pk(o[4], o[5]), pk(o[6], o[7])});
      }
      __builtin_amdgcn_sched_barrier(0);
      if (s < 28) ldw2(s);
      if (s == 27) {
        wait_vm<18>();
        if (arrive_last(smem + L_CNT + 8, 7)) st4one(ws + WS_FH + (tt * 16 + fs) * 128, tag);
        TS(27);
      }
      Bc[0] = Bn[0]; Bc[1] = Bn[1];
    }
    TC(28);
    TSF(21, 28);
#pragma unroll
    for (int i = 0; i < 8; i++) o[i] = swi(acc[3], i, q[3]);
    st16(hb + 3 * 114688, lane * 16, u32x4{pk(o[0], o[1]), pk(o[2], o[3]), pk(o[4], o[5]), pk(o[6], o[7])});
    wait_vm<2>();
#pragma unroll
    for (int i = 0; i < 28; i++) tie(W2[i]);
    TS(29);
  }
  {
    const int te = tt2 * 4 + sel, tl = tt2 * 4 + 3 - sel;
    const char* hbe = ws + WS_HF + ((size_t)te * 112 + w * 14) * 1024;
    const char* hbl = ws + WS_HF + ((size_t)tl * 112 + w * 14) * 1024;
    const int u = lane >> 2, tk = w * 4 + (lane & 3);
    u32 xe[4], xl[4];
    u32x4 gbv;
    u32x4 H[14];
    ld4(fh, ws + WS_FH + tt2 * 2048, (lane & 15) * 128);
    wait_vm<0>(); tie(fh);
    if (w > 0 && arrive_last(smem + L_CNT + 12, 7)) st4one(ws + WS_FH + 32768 + (tt * 16 + fs) * 128, tag);
    TS(30);
    spin4(fh, tag, ws + WS_FH + tt2 * 2048, (lane & 15) * 128);
    bar();
    TS(31);
#pragma unroll
    for (int i = 0; i < 14; i++) ld16k(H[i], hbe + (i < 7 ? rlo : rhi), i % 7, lane * 16);
    const u32 xo = (dt * 64 + u * 4) * 64 + (tk >> 1) * 4;
    ld4<0>(xe[0], ws + WS_X1 + te * 32768, xo); ld4<64>(xe[1], ws + WS_X1 + te * 32768, xo);
    ld4<128>(xe[2], ws + WS_X1 + te * 32768, xo); ld4<192>(xe[3], ws + WS_X1 + te * 32768, xo);
    ld4<0>(xl[0], ws + WS_X1 + tl * 32768, xo); ld4<64>(xl[1], ws + WS_X1 + tl * 32768, xo);
    ld4<128>(xl[2], ws + WS_X1 + tl * 32768, xo); ld4<192>(xl[3], ws + WS_X1 + tl * 32768, xo);
    ld16<0>(gbv, ws + WS_GB + dt * 256, u * 16);
    ld4(fh, ws + WS_FH + 32768 + tt2 * 2048, (lane & 15) * 128);
    TS(32);
    f32x16 acc[2];
#pragma unroll
    for (int i = 0; i < 16; i++) { acc[0][i] = 0.f; acc[1][i] = 0.f; }
    char* wr = smem + w * D_SRC + h * 4 * 576 + c * 16;
    const char* rd = smem + u * 576 + tk * 16;
#pragma unroll
    for (int i = 0; i < 14; i++) {
      if (i == 0 || i == 4 || i == 9) {
        __builtin_amdgcn_sched_barrier(0);
        if (i == 0) wait_vm<20>(); else if (i == 4) wait_vm<15>(); else wait_vm<10>();
#pragma unroll
        for (int k = i; k < (i == 0 ? 4 : i == 4 ? 9 : 14); k++) tie(H[k]);
      }
      acc[0] = mfma32(W2[2 * i], H[i], acc[0]);
      acc[1] = mfma32(W2[2 * i + 1], H[i], acc[1]);
    }
    __builtin_amdgcn_sched_barrier(0);
    wait_vm<0>(); tie(fh);
#pragma unroll
    for (int i = 0; i < 4; i++) { tie(xe[i]); tie(xl[i]); }
    tie(gbv);
    TS(33);
    spin4(fh, tag, ws + WS_FH + 32768 + tt2 * 2048, (lane & 15) * 128);
    TS(34);
#pragma unroll
    for (int i = 0; i < 14; i++) ld16k(H[i], hbl + (i < 7 ? rlo : rhi), i % 7, lane * 16);
    __builtin_amdgcn_sched_barrier(0);
    TS(35);
#pragma unroll
    for (int di = 0; di < 2; di++)
#pragma unroll
      for (int q = 0; q < 4; q++)
        *(f32x4*)(wr + (di * 8 + q) * 576) = f32x4{acc[di][4 * q], acc[di][4 * q + 1], acc[di][4 * q + 2], acc[di][4 * q + 3]};
    __builtin_amdgcn_sched_barrier(0);
    TS(36);
    TSF(29, 36);
    auto finish = [&](const char* rp, const u32 (&xq)[4]) NOPK {
      f32x4 s = *(const f32x4*)rp;
#pragma unroll
      for (int q = 1; q < 8; q++) s += *(const f32x4*)(rp + q * D_SRC);
      float y[4];
#pragma unroll
      for (int j = 0; j < 4; j++) y[j] = fmaf(u2f(gbv[j]), s[j], (tk & 1) ? bfhi(xq[j]) : bflo(xq[j]));
      return u32x2{pk(y[0], y[1]), pk(y[2], y[3])};
    };
#pragma unroll
    for (int i = 0; i < 16; i++) { acc[0][i] = 0.f; acc[1][i] = 0.f; }
#pragma unroll
    for (int i = 0; i < 14; i++) {
      if (i == 0 || i == 4 || i == 9) {
        __builtin_amdgcn_sched_barrier(0);
        if (i == 0) wait_vm<10>(); else if (i == 4) wait_vm<5>(); else wait_vm<0>();
#pragma unroll
        for (int k = i; k < (i == 0 ? 4 : i == 4 ? 9 : 14); k++) tie(H[k]);
      }
      acc[0] = mfma32(W2[2 * i], H[i], acc[0]);
      acc[1] = mfma32(W2[2 * i + 1], H[i], acc[1]);
    }
    TS(37);
#pragma unroll
    for (int di = 0; di < 2; di++)
#pragma unroll
      for (int q = 0; q < 4; q++)
        *(f32x4*)(wr + D_TILE + (di * 8 + q) * 576) = f32x4{acc[di][4 * q], acc[di][4 * q + 1], acc[di][4 * q + 2], acc[di][4 * q + 3]};
    lds_barrier();
    TS(38);
    *(u32x2*)(out + (size_t)(tl * 32 + tk) * D + dt * 64 + u * 4) = finish(rd + D_TILE, xl);
    *(u32x2*)(out + (size_t)(te * 32 + tk) * D + dt * 64 + u * 4) = finish(rd, xe);
    TS(39);
    TS(40);
    TSF(37, 40);
    TSEND();
  }
}

extern "C" void kernel_launch(void* const* d_in, const int* in_sizes, int n_in, void* d_out, int out_size, void* d_ws,
                              size_t ws_size, hipStream_t stream) {
  const u16* const* in = (const u16* const*)d_in;
  fused<<<256, 512, 0, stream>>>(in[0], in[1], in[2], in[3], in[4], in[5], in[6], in[7], in[8], in[10], in[11], in[12], in[13], in[14],
                                 (char*)d_ws, (u16*)d_out);
}
```

```cpp
#define MODE 2
#include <hip/hip_runtime.h>
#include <stdint.h>

typedef unsigned int u32;
typedef unsigned short u16;
typedef __attribute__((ext_vector_type(2))) float f32x2;
typedef __attribute__((ext_vector_type(4))) float f32x4;
typedef __attribute__((ext_vector_type(16))) float f32x16;
typedef __attribute__((ext_vector_type(2))) u32 u32x2;
typedef __attribute__((ext_vector_type(4))) u32 u32x4;
typedef __attribute__((ext_vector_type(2))) __bf16 bf16x2;
typedef __attribute__((ext_vector_type(8))) __bf16 bf16x8;
typedef __attribute__((ext_vector_type(4))) short s16x4;
#define LDS_AS __attribute__((address_space(3)))
#if defined(__HIP_DEVICE_COMPILE__)
#define NOPK __attribute__((target("no-packed-fp32-ops")))
#else
#define NOPK
#endif
#define DEVINL __device__ __forceinline__ NOPK

constexpr int L = 2048, D = 512, NS = 128, F = 1792;

constexpr size_t WS_E = 0;
constexpr size_t WS_FW = 4096;
constexpr size_t WS_FX = WS_FW + 32768;
constexpr size_t WS_FH = WS_FX + 32768;
constexpr size_t WS_MG = WS_FH + 65536;
constexpr size_t WS_GB = WS_MG + 98304;
constexpr size_t WS_XG = 262144;
constexpr size_t WS_X1 = WS_XG + (4u << 20);
constexpr size_t WS_W1F = WS_X1 + (2u << 20);
constexpr size_t WS_W2F = WS_W1F + 112 * 32 * 1024;
constexpr size_t WS_HF = WS_W2F + 16 * 112 * 1024;
constexpr size_t WS_CD = WS_HF + 64 * 112 * 1024;
constexpr int SPIN_CAP = 40000;
#define W1A 2
#define FXL 4

#define u2f(x) __builtin_bit_cast(float, (u32)(x))
#define i2f(x) __builtin_bit_cast(float, (int)(x))
#define f2u(x) __builtin_bit_cast(u32, (float)(x))
#define f2i(x) __builtin_bit_cast(int, (float)(x))
#define TSDECL unsigned long long dg0 = 0
#define TS(slot) do { if ((slot) == 0) { asm volatile("s_memrealtime %0\n\ts_waitcnt lgkmcnt(0)" : "=s"(dg0) :: "memory"); } } while (0)
#define TC(slot)
#define TSF(lo, hi)
__device__ __forceinline__ unsigned long long rt() { unsigned long long t; asm volatile("s_memrealtime %0\n\ts_waitcnt lgkmcnt(0)" : "=s"(t) :: "memory"); return t; }
#define TSEND() do { if (blockIdx.x == 37 && w == 1) { unsigned long long k0 = *(volatile unsigned long long*)(ws + (128u << 20)); \
  unsigned long long tgt = k0 + 6000 + ((MODE & 1) ? 4 * (dg0 - k0) : 0); \
  if (tgt > dg0 + 30000) tgt = dg0; \
  while (rt() < tgt) __builtin_amdgcn_s_sleep(1); } } while (0)
__global__ void k0(char* ws) { if (blockIdx.x == 0 && threadIdx.x == 0) *(volatile unsigned long long*)(ws + (128u << 20)) = rt(); }
__global__ void k2(char* ws) { unsigned long long t = rt(); unsigned long long a = *(volatile unsigned long long*)(ws + (128u << 20)), e = *(volatile unsigned long long*)(ws + (128u << 20) + 8);
  unsigned long long tgt = a + 6000 + ((MODE & 1) ? 4 * (t - e) : 0); if (tgt > t + 30000) tgt = t; while (rt() < tgt) __builtin_amdgcn_s_sleep(1); }
DEVINL float bflo(u32 u) { return u2f(u << 16); }
DEVINL float bfhi(u32 u) { return u2f(u & 0xffff0000u); }
DEVINL u32 pk(float a, float b) {
  f32x2 v = {a, b};
  return __builtin_bit_cast(u32, __builtin_convertvector(v, bf16x2));
}
DEVINL float rbf(float a) { return bflo(pk(a, a)); }
template <int CTRL> DEVINL float dpp_add(float v) {
  return v + i2f(__builtin_amdgcn_update_dpp(0, f2i(v), CTRL, 0xf, 0xf, true));
}
DEVINL float row4_sum(float v) {
  auto a = __builtin_amdgcn_permlane16_swap(f2u(v), f2u(v), false, false);
  v = u2f(a[0]) + u2f(a[1]);
  auto b = __builtin_amdgcn_permlane32_swap(f2u(v), f2u(v), false, false);
  return u2f(b[0]) + u2f(b[1]);
}
DEVINL float rs32(float a, float b) {
  auto r = __builtin_amdgcn_permlane32_swap(f2u(a), f2u(b), false, false);
  return u2f(r[0]) + u2f(r[1]);
}
DEVINL float rs16(float a, float b) {
  auto r = __builtin_amdgcn_permlane16_swap(f2u(a), f2u(b), false, false);
  return u2f(r[0]) + u2f(r[1]);
}
DEVINL float wave_sum(float v) { return row4_sum(dpp_add<0x140>(dpp_add<0x141>(dpp_add<0x4E>(dpp_add<0xB1>(v))))); }
DEVINL f32x4 mfma16(u32x4 a, u32x4 b, f32x4 c) {
  return __builtin_amdgcn_mfma_f32_16x16x32_bf16(__builtin_bit_cast(bf16x8, a), __builtin_bit_cast(bf16x8, b), c, 0, 0, 0);
}
DEVINL f32x16 mfma32(u32x4 a, u32x4 b, f32x16 c) {
  return __builtin_amdgcn_mfma_f32_32x32x16_bf16(__builtin_bit_cast(bf16x8, a), __builtin_bit_cast(bf16x8, b), c, 0, 0, 0);
}
DEVINL float dot2(u32 a, u32 b, float c) {
  return __builtin_amdgcn_fdot2_f32_bf16(__builtin_bit_cast(bf16x2, a), __builtin_bit_cast(bf16x2, b), c, false);
}
DEVINL void lds_barrier() {
  asm volatile("s_waitcnt lgkmcnt(0)" ::: "memory");
  __builtin_amdgcn_s_barrier();
  asm volatile("" ::: "memory");
}
DEVINL void bar() {
  asm volatile("" ::: "memory");
  __builtin_amdgcn_s_barrier();
  asm volatile("" ::: "memory");
}
DEVINL void st16(const void* sbase, u32 voff, u32x4 v) {
  asm volatile("global_store_dwordx4 %0, %1, %2 sc1\n\ts_nop 1" ::"v"(voff), "v"(v), "s"(sbase) : "memory");
}
DEVINL void st8(const void* sbase, u32 voff, u32x2 v) {
  asm volatile("global_store_dwordx2 %0, %1, %2 sc1\n\ts_nop 1" ::"v"(voff), "v"(v), "s"(sbase) : "memory");
}
DEVINL void st4(const void* sbase, u32 voff, u32 v) {
  asm volatile("global_store_dword %0, %1, %2 sc1\n\ts_nop 1" ::"v"(voff), "v"(v), "s"(sbase) : "memory");
}
template <int LANES> DEVINL void st8m(const void* sbase, u32 voff, u32x2 v) {
  asm volatile("s_mov_b64 exec, %3\n\tglobal_store_dwordx2 %0, %1, %2 sc1\n\ts_mov_b64 exec, -1\n\ts_nop 0"
               ::"v"(voff), "v"(v), "s"(sbase), "n"((1 << LANES) - 1) : "memory");
}
DEVINL void st4one(const void* sbase, u32 v) {
  asm volatile("s_mov_b64 exec, 1\n\tglobal_store_dword %0, %1, %2 sc1\n\ts_mov_b64 exec, -1\n\ts_nop 0"
               ::"v"(0), "v"(v), "s"(sbase) : "memory");
}
DEVINL void st16x2_56(const void* sbase, u32 voff, u32x4 a, u32x4 b) {
  asm volatile("s_mov_b32 exec_hi, 0xffffff\n\tglobal_store_dwordx4 %0, %1, %3 sc1\n\tglobal_store_dwordx4 %0, %2, %3 offset:128 sc1\n\t"
               "s_mov_b32 exec_hi, -1\n\ts_nop 0" ::"v"(voff), "v"(a), "v"(b), "s"(sbase) : "memory");
}
DEVINL void sld4(u32& d, const void* p) { asm volatile("s_load_dword %0, %1, 0" : "=s"(d) : "s"(p) : "memory"); }
template <int OFF = 0> DEVINL void ld16(u32x4& d, const void* sbase, u32 voff) {
  asm volatile("global_load_dwordx4 %0, %1, %2 offset:%3 sc1" : "=v"(d) : "v"(voff), "s"(sbase), "n"(OFF) : "memory");
}
DEVINL void ld16k(u32x4& d, const char* sbase, int kb, u32 voff) {
  const char* sb = sbase + (kb >> 2) * 4096;
  if ((kb & 3) == 0) ld16<0>(d, sb, voff); else if ((kb & 3) == 1) ld16<1024>(d, sb, voff);
  else if ((kb & 3) == 2) ld16<2048>(d, sb, voff); else ld16<3072>(d, sb, voff);
}
DEVINL void ld8(u32x2& d, const void* sbase, u32 voff) {
  asm volatile("global_load_dwordx2 %0, %1, %2 sc1" : "=v"(d) : "v"(voff), "s"(sbase) : "memory");
}
template <int OFF = 0> DEVINL void ld4(u32& d, const void* sbase, u32 voff) {
  asm volatile("global_load_dword %0, %1, %2 offset:%3 sc1" : "=v"(d) : "v"(voff), "s"(sbase), "n"(OFF) : "memory");
}
template <int OFF = 0> DEVINL void lp16(u32x4& d, const void* sbase, u32 voff) {
  asm volatile("global_load_dwordx4 %0, %1, %2 offset:%3" : "=v"(d) : "v"(voff), "s"(sbase), "n"(OFF) : "memory");
}
template <int OFF = 0> DEVINL void lp4(u32& d, const void* sbase, u32 voff) {
  asm volatile("global_load_dword %0, %1, %2 offset:%3" : "=v"(d) : "v"(voff), "s"(sbase), "n"(OFF) : "memory");
}
template <int OFF = 0> DEVINL void lp2(u32& d, const void* sbase, u32 voff) {
  asm volatile("global_load_ushort %0, %1, %2 offset:%3" : "=v"(d) : "v"(voff), "s"(sbase), "n"(OFF) : "memory");
}
template <class T> DEVINL void tie(T& a) { asm volatile("" : "+v"(a)); }
template <int N> DEVINL void wait_vm() { asm volatile("s_waitcnt vmcnt(%0)" ::"n"(N) : "memory"); }
DEVINL bool all_lanes(bool ok) { return __builtin_amdgcn_ballot_w64(!ok) == 0; }
DEVINL void spin4(u32 f, u32 tag, const void* sbase, u32 voff) {
#pragma unroll 1
  for (int it = 0; it < SPIN_CAP && !all_lanes(f == tag); it++) {
    __builtin_amdgcn_s_sleep(1);
    ld4(f, sbase, voff); wait_vm<0>(); tie(f);
  }
}
DEVINL bool arrive_last(char* smem_word, int n) {
  u32 old = 0;
  if (__builtin_amdgcn_mbcnt_hi(~0u, __builtin_amdgcn_mbcnt_lo(~0u, 0)) == 0)
    old = __hip_atomic_fetch_add((LDS_AS u32*)smem_word, 1u, __ATOMIC_RELAXED, __HIP_MEMORY_SCOPE_WORKGROUP);
  return __builtin_amdgcn_readfirstlane(old) == (u32)(n - 1);
}
DEVINL u32x2 tr_read(const char* p) {
  return __builtin_bit_cast(u32x2, __builtin_amdgcn_ds_read_tr16_b64_v4i16((LDS_AS s16x4*)p));
}
DEVINL float rdl(float v, int l) { return i2f(__builtin_amdgcn_readlane(f2i(v), l)); }
DEVINL float sel2(int c, float a, float b) { return c ? b : a; }
DEVINL u32 swap2(u32 v) { return (u32)__builtin_amdgcn_update_dpp(0, (int)v, 0x4E, 0xf, 0xf, true); }
DEVINL u32 swap1(u32 v) { return (u32)__builtin_amdgcn_update_dpp(0, (int)v, 0xB1, 0xf, 0xf, true); }
constexpr float LOG2E = 1.4426950408889634f, LN2 = 0.6931471805599453f, INV2PI = 0.15915494309189535f;

constexpr int A_TAB = 0, A_P = 8 * 1536, A_KC = A_P + 24576, A_KCS = 4112, A_GV = A_KC + 2 * A_KCS;
constexpr int B_KC = 47360, B_KCS = 4160, B_KCH = 4 * 4160, B_U = B_KC + 2 * B_KCH, B_US = 4640, B_UCH = 2 * 4640;
constexpr int B_Y = B_U + 2 * B_UCH, B_RED = B_Y + 40960;
constexpr int A_XT = B_Y;
constexpr int D_SRC = 16 * 576, D_TILE = 8 * D_SRC;
constexpr int C_RAW = 148480, L_CNT = C_RAW + 1024, LDS_BYTES = L_CNT + 64;
static_assert(A_GV + 2048 <= B_KC && B_RED + 8192 <= C_RAW && 2 * D_TILE <= C_RAW, "lds map");

template <int TG> DEVINL void conv_load(const char* pa, const char* pb, int nu, u32x4 (&A)[8], u32x4 (&B)[8]) {
  constexpr int T0 = TG ? 6 : 7;
#pragma unroll
  for (int i = 0; i <= T0; i++) {
    A[i] = *(const u32x4*)(pa + 512 * (7 - i) + 64 * (3 - nu));
    B[T0 - i] = *(const u32x4*)(pb + 512 * (T0 - i) + 64 * (3 - nu));
  }
}
template <int TG> DEVINL void conv_mma(const u32x4 (&A)[8], const u32x4 (&B)[8], f32x4 (&acc)[4]) {
  constexpr int T0 = TG ? 6 : 7, T1 = TG ? 1 : 0, T2 = TG ? 5 : 4, T3 = TG ? 2 : 3;
#pragma unroll
  for (int mu = 0; mu <= T0; mu++) {
    acc[0] = mfma16(A[mu], B[T0 - mu], acc[0]);
    if (mu >= T0 - T2) acc[2] = mfma16(A[mu - (T0 - T2)], B[T0 - mu], acc[2]);
    if (mu >= T0 - T3) acc[3] = mfma16(A[mu - (T0 - T3)], B[T0 - mu], acc[3]);
    if (mu >= T0 - T1) acc[1] = mfma16(A[mu - (T0 - T1)], B[T0 - mu], acc[1]);
  }
}
template <int TG, class FN, class FM> DEVINL void conv_body(const char* pa, const char* pb, f32x4 (&acc)[4], FN&& filler, FM&& mid) {
  u32x4 A0[8], B0[8], A1[8], B1[8];
  conv_load<TG>(pa, pb, 0, A0, B0);
  conv_load<TG>(pa, pb, 1, A1, B1);
  __builtin_amdgcn_sched_barrier(0);
  filler();
  conv_mma<TG>(A0, B0, acc);
  __builtin_amdgcn_sched_barrier(0);
  conv_load<TG>(pa, pb, 2, A0, B0);
  __builtin_amdgcn_sched_barrier(0);
  conv_mma<TG>(A1, B1, acc);
  __builtin_amdgcn_sched_barrier(0);
  conv_load<TG>(pa, pb, 3, A1, B1);
  __builtin_amdgcn_sched_barrier(0);
  conv_mma<TG>(A0, B0, acc);
  __builtin_amdgcn_sched_barrier(0);
  mid();
  __builtin_amdgcn_sched_barrier(0);
  conv_mma<TG>(A1, B1, acc);
}
DEVINL void glds4k(const void* sbase, u32 voff, u32 ldsaddr) {
  asm volatile("s_mov_b32 m0, %2\n\ts_nop 0\n\t"
               "global_load_lds_dwordx4 %0, %1 sc1\n\tglobal_load_lds_dwordx4 %0, %1 offset:1024 sc1\n\t"
               "global_load_lds_dwordx4 %0, %1 offset:2048 sc1\n\tglobal_load_lds_dwordx4 %0, %1 offset:3072 sc1"
               ::"v"(voff), "s"(sbase), "s"(ldsaddr) : "memory");
}
DEVINL u32x4 tr8(const char* p) {
  const u32x2 lo = tr_read(p), hi = tr_read(p + 256);
  return u32x4{lo[0], lo[1], hi[0], hi[1]};
}
struct Ep { float q0, v; };
DEVINL float swi(const f32x16& a, int i, const Ep& q) {
  const float G = a[(i & 3) + 8 * (i >> 2)], U = a[(i & 3) + 8 * (i >> 2) + 4];
  const float e = __builtin_amdgcn_exp2f(G * q.q0);
  return (G * U) * __builtin_amdgcn_rcpf(fmaf(e, q.v, q.v));
}
DEVINL u32 ln2_fold(f32x2 s, int h, Ep& q) {
  const float m = s[0] * (1.f / D);
  const float v = fmaxf(fmaf(-m, m, s[1] * (1.f / D)), 0.f) + 1e-5f;
  const float rs = __builtin_amdgcn_rsqf(v);
  q.q0 = -rs; q.v = v;
  const float val = h ? v * rs : -m;
  const float vhi = rbf(val);
  return pk(vhi, val - vhi);
}
DEVINL void ln2_acc(const char* rb, int kp0, int kp1, f32x4 (&sg)[2][2], f32x4 (&sx)[2][2]) {
  const u32x4 ones = {0x3f803f80u, 0x3f803f80u, 0x3f803f80u, 0x3f803f80u};
  u32x4 fr[4][2];
  auto rd = [&](int n) NOPK {
#pragma unroll
    for (int gi = 0; gi < 2; gi++) {
      const char* pp = rb + (n & 1) * 32768 + (kp0 + (n >> 1)) * 2048 + gi * 32;
      const u32x2 lo = tr_read(pp), hi = tr_read(pp + 1024);
      fr[n & 3][gi] = u32x4{lo[0], lo[1], hi[0], hi[1]};
    }
  };
  rd(0); rd(1); rd(2);
#pragma unroll
  for (int n = 0; n < 2 * (kp1 - kp0); n++) {
    if (n + 3 < 2 * (kp1 - kp0)) rd(n + 3);
    __builtin_amdgcn_sched_barrier(0);
#pragma unroll
    for (int gi = 0; gi < 2; gi++) {
      sg[n & 1][gi] = mfma16(fr[n & 3][gi], fr[n & 3][gi], sg[n & 1][gi]);
      sx[n & 1][gi] = mfma16(ones, fr[n & 3][gi], sx[n & 1][gi]);
    }
    __builtin_amdgcn_sched_barrier(0);
  }
}
DEVINL void ln2_put(f32x2* raw, int lane, const f32x4 (&sg)[2], const f32x4 (&sx)[2]) {
#pragma unroll
  for (int gi = 0; gi < 2; gi++) {
    const float gsel = (lane & 2) ? ((lane & 1) ? sg[gi][3] : sg[gi][2]) : ((lane & 1) ? sg[gi][1] : sg[gi][0]);
    if ((lane >> 4) == ((lane & 15) >> 2)) raw[gi * 16 + (lane & 15)] = f32x2{sx[gi][0], gsel};
  }
}

typedef __attribute__((address_space(1))) unsigned long long gu64;
typedef __attribute__((address_space(1))) u32 gu32;
DEVINL void put_granule(char* p, u32 val, u32 tag) {
  __hip_atomic_store((gu64*)p, ((unsigned long long)tag << 32) | val, __ATOMIC_RELAXED, __HIP_MEMORY_SCOPE_AGENT);
}
DEVINL void put_flag(char* p, u32 tag) { __hip_atomic_store((gu32*)p, tag, __ATOMIC_RELAXED, __HIP_MEMORY_SCOPE_AGENT); }

__global__ NOPK __launch_bounds__(512, 2) void fused(
    const u16* __restrict__ x, const u16* __restrict__ Are, const u16* __restrict__ Aim, const u16* __restrict__ Bre,
    const u16* __restrict__ Bim, const u16* __restrict__ Cre, const u16* __restrict__ Cim, const u16* __restrict__ logdt,
    const u16* __restrict__ dskip, const u16* __restrict__ w1, const u16* __restrict__ w3, const u16* __restrict__ w2,
    const u16* __restrict__ cvec, const u16* __restrict__ wmod, char* ws, u16* __restrict__ out) {
  __shared__ __attribute__((aligned(16))) char smem[LDS_BYTES];
  const int b = blockIdx.x, tid = threadIdx.x, lane = tid & 63;
  const int w = __builtin_amdgcn_readfirstlane(tid >> 6);
  TSDECL; TS(0);
  const int xg = b & 7, idx = b >> 3;
  const int p = xg * 32 + idx;
  const int r = lane & 15, g = lane >> 4;
  asm volatile("" ::"s"(x), "s"(Are), "s"(Aim), "s"(Bre), "s"(Bim), "s"(Cre), "s"(Cim), "s"(logdt), "s"(dskip), "s"(w1), "s"(w3),
               "s"(w2), "s"(cvec), "s"(wmod), "s"(ws), "s"(out));
  u32x4 xr, wm0, wm1;
  u32 vare, vaim, vbre, vbim, vcre, vcim, c0, c1;
  const int cg = xg * 24 + (idx < 24 ? idx : 23);
  u32 e_prev, ldt2, dsk;
  lp16(xr, x + b * 4096, tid * 16);
  sld4(e_prev, ws + WS_E); sld4(ldt2, logdt + 2 * p); sld4(dsk, dskip + 2 * p);
  {
    const u32 so = (p * 256 + w * 32 + (lane & 31)) * 2;
    lp2(vare, Are, so); lp2(vaim, Aim, so); lp2(vbre, Bre, so); lp2(vbim, Bim, so); lp2(vcre, Cre, so); lp2(vcim, Cim, so);
    const u32 mo = (tid >> 3) * 6144 + (tid & 7) * 16;
    const u16* wmp = wmod + (cg & 3) * 128 * 3072 + (cg >> 2) * 64;
    lp16(wm0, wmp, mo); lp16(wm1, wmp + 64 * 3072, mo);
    lp2(c0, cvec + (cg & 3) * 128, (tid >> 3) * 2); lp2<128>(c1, cvec + (cg & 3) * 128, (tid >> 3) * 2);
  }
  TS(1);
  if (tid < 32) *(u32x4*)(smem + B_KC + (tid >> 2) * B_KCS + 4096 + (tid & 3) * 16) = u32x4{0, 0, 0, 0};
  else if (tid < 36) *(u32x4*)(smem + L_CNT + (tid - 32) * 16) = u32x4{0, 0, 0, 0};
  else if (tid >= 64 && tid < 64 + 132) {
    int i = tid - 64, cp = i / 33, e = i - cp * 33;
    if (e < 32 || (cp & 1)) *(u32x2*)(smem + B_U + cp * B_US + e * 16) = u32x2{0, 0};
    if (e < 32) *(u32x2*)(smem + B_U + cp * B_US + e * 16 + 8) = u32x2{0, 0};
  }
  asm volatile("s_waitcnt lgkmcnt(0)" : "+s"(e_prev), "+s"(ldt2), "+s"(dsk)::"memory");
  const u32 tag = e_prev * 1664525u + 1013904223u;
  wait_vm<10>(); tie(xr);
  {
    *(u32x4*)(smem + A_XT + w * 1056 + lane * 16) = xr;
    float f[8];
#pragma unroll
    for (int j = 0; j < 4; j++) { f[2 * j] = bflo(xr[j]); f[2 * j + 1] = bfhi(xr[j]); }
    const float m = wave_sum(((f[0] + f[1]) + (f[2] + f[3])) + ((f[4] + f[5]) + (f[6] + f[7]))) * (1.f / D);
    float s = 0.f;
#pragma unroll
    for (int j = 0; j < 8; j++) { f[j] -= m; s = fmaf(f[j], f[j], s); }
    const float sd = __builtin_amdgcn_sqrtf(rbf(rbf(wave_sum(s) * (1.f / D)) + 1e-5f));
    if (lane == 0) *(u32*)(smem + A_XT + w * 1056 + 1024) = pk(m, sd);
  }
  TS(2);
  wait_vm<4>(); tie(vare); tie(vaim); tie(vbre); tie(vbim); tie(vcre); tie(vcim);
  char* tab = smem + A_TAB + w * 1536;
  {
    const float dt = rbf(__builtin_amdgcn_exp2f((w >> 2 ? bfhi(ldt2) : bflo(ldt2)) * LOG2E));
    const float a = dt * bflo(vare) * LOG2E, bq = dt * bflo(vaim) * INV2PI;
    const float br = dt * bflo(vbre), bi = dt * bflo(vbim), cr = bflo(vcre), ci = bflo(vcim);
    const float m = lane < 32 ? 1.f : 256.f;
    const float e = __builtin_amdgcn_exp2f(a * m), ph = __builtin_amdgcn_fractf(-bq * m);
    const float ex = e * __builtin_amdgcn_cosf(ph), ey = e * __builtin_amdgcn_sinf(ph);
    char* t = tab + (lane & 31) * 48;
    *(f32x2*)(t + 16 + (lane >> 5) * 8) = f32x2{ex, ey};
    f32x4 o = {a, bq, cr * br - ci * bi, cr * bi + ci * br};
    if (lane >= 32) o = f32x4{ex, ey, ex * ex - ey * ey, (ex + ex) * ey};
    *(f32x4*)(t + (lane >> 5) * 32) = o;
  }
  TS(3);
  wait_vm<0>(); tie(wm0); tie(wm1); tie(c0); tie(c1);
  const int fb = idx & 15, mat = idx >> 4, d0 = xg * 64 + w * 8;
  const bool rt2 = b < 224 && w < 4;
  u32 v[8], t2[8];
  {
    const float cx0 = bflo(c0), cx1 = bflo(c1);
    const float s0 = rbf(cx0 * __builtin_amdgcn_rcpf(1.f + __builtin_amdgcn_exp2f(cx0 * -LOG2E)));
    const float s1 = rbf(cx1 * __builtin_amdgcn_rcpf(1.f + __builtin_amdgcn_exp2f(cx1 * -LOG2E)));
    float acc[8];
#pragma unroll
    for (int j = 0; j < 4; j++) {
      acc[2 * j] = fmaf(s0, bflo(wm0[j]), s1 * bflo(wm1[j]));
      acc[2 * j + 1] = fmaf(s0, bfhi(wm0[j]), s1 * bfhi(wm1[j]));
    }
    const float q0 = dpp_add<0x128>(rs16(rs32(acc[0], acc[4]), rs32(acc[2], acc[6])));
    const float q1 = dpp_add<0x128>(rs16(rs32(acc[1], acc[5]), rs32(acc[3], acc[7])));
    *(float*)(smem + A_GV + w * 256 + (lane & 7) * 32 + (lane >> 3) * 4) = (lane & 8) ? q1 : q0;
  if (rt2) {
#pragma unroll
    for (int i = 0; i < 2; i++) {
      const u16* wp = w2 + (size_t)(b * 8 + i * 4) * D;
      lp4<0>(t2[4 * i], wp, (tid & 255) * 4); lp4<1024>(t2[4 * i + 1], wp, (tid & 255) * 4);
      lp4<2048>(t2[4 * i + 2], wp, (tid & 255) * 4); lp4<3072>(t2[4 * i + 3], wp, (tid & 255) * 4);
    }
  }
    lds_barrier();
    if (w == 0 && idx < 24) {
      float s = 0.f;
#pragma unroll
      for (int i = 0; i < 8; i++) s += *(const float*)(smem + A_GV + i * 256 + lane * 4);
      st8(ws + WS_MG + (cg & 3) * 24576 + (cg >> 2) * 512, lane * 8, u32x2{f2u(s), tag});
    }
    const char* xt = smem + A_XT + (lane & 7) * 1056;
    const u32 st = *(const u32*)(xt + 1024);
#pragma unroll
    for (int e = 0; e < 2; e++) {
      const u32x2 xq = *(const u32x2*)(xt + (e * 64 + w * 8 + (lane >> 3)) * 8);
      st16(ws + WS_XG + (size_t)(e * 64 + w * 8) * 32768 + b * 128, (lane >> 3) * 32768 + (lane & 7) * 16, u32x4{xq[0], xq[1], st, tag});
    }
  }
  {
    const u16* wp = (mat ? w3 : w1) + (size_t)d0 * F + fb * 112;
    const u32 vo = (lane < 56 ? lane : 55) * 4;
#pragma unroll
    for (int i = 0; i < 4; i++) { lp4<0>(v[2 * i], wp + 2 * i * F, vo); lp4<2 * F>(v[2 * i + 1], wp + 2 * i * F, vo); }
  }
  TS(4);
  const u32 gcode = __builtin_amdgcn_perm(0x400b0916u, 0x0a201408u, (r & 7) | 0x0c0c0c00u);
  const int gx = r < 8 ? 2 * r : (int)((gcode >> 2) * 256 + (gcode & 3) * 4);
  {
    asm volatile("" ::: "memory");
    const float mF = (float)(16 * r), mX = (float)gx;
    const char* tp = tab + g * 8 * 48;
    const char* tq = tp + (r < 8 ? 16 : 32);
    f32x4 acc[3];
#pragma unroll
    for (int s = 0; s < 3; s++) acc[s] = f32x4{0.f, 0.f, 0.f, 0.f};
#pragma unroll
    for (int ks = 0; ks < 2; ks++) {
      u32x4 Ff, Gf[3];
#pragma unroll
      for (int i = 0; i < 4; i++) {
        const f32x4 s0 = *(const f32x4*)(tp + (ks * 4 + i) * 48), s1 = *(const f32x4*)(tq + (ks * 4 + i) * 48);
        const float e0 = __builtin_amdgcn_exp2f(s0[0] * mF), p0 = __builtin_amdgcn_fractf(s0[1] * mF);
        const float zx = e0 * __builtin_amdgcn_cosf(p0), zy = e0 * __builtin_amdgcn_sinf(p0);
        Ff[i] = pk(s0[2] * zx - s0[3] * zy, s0[2] * zy + s0[3] * zx);
        const float e1 = __builtin_amdgcn_exp2f(s0[0] * mX), p1 = __builtin_amdgcn_fractf(s0[1] * -mX);
        const float xx = e1 * __builtin_amdgcn_cosf(p1), xy = e1 * __builtin_amdgcn_sinf(p1);
        Gf[0][i] = pk(xx, xy);
        Gf[1][i] = pk(xx * s1[0] - xy * s1[1], xx * s1[1] + xy * s1[0]);
        Gf[2][i] = pk(xx * s1[2] - xy * s1[3], xx * s1[3] + xy * s1[2]);
      }
#pragma unroll
      for (int s = 0; s < 3; s++) acc[s] = mfma16(Ff, Gf[s], acc[s]);
    }
#pragma unroll
    for (int s = 0; s < 3; s++) *(f32x4*)(smem + A_P + w * 3072 + s * 1024 + lane * 16) = acc[s];
  }
  TS(5);
  if (rt2) {
    wait_vm<10>();
#pragma unroll
    for (int i = 0; i < 8; i++) tie(t2[i]);
    const u32 vo = (((tid >> 4) & 15) * 112 * 64 + (tid & 15)) * 16;
#pragma unroll
    for (int e = 0; e < 2; e++) {
      u32x4 o;
#pragma unroll
      for (int j = 0; j < 4; j++) o[j] = __builtin_amdgcn_perm(t2[2 * j + 1], t2[2 * j], e ? 0x07060302u : 0x05040100u);
      st16(ws + WS_W2F + b * 512 + e * 256, vo, o);
    }
  }
  u32x4 xg4[4];
  u32x2 mg, mh;
  const char* xgb = ws + WS_XG + (size_t)(p >> 1) * 32768;
  const u32 xgo = (tid >> 2) * 256 + (tid & 3) * 16;
  u32 mga = (4 - ((lane >> 3) & 1)) * 512 + d0 + (lane & 7), mgb = ((0x5210 >> ((lane & 6) * 2)) & 7) * 512 + 2 * p + (lane & 1);
  tie(mga); tie(mgb);
  const u32 mgo = ((lane & 16) ? mgb : mga) * 8 + (lane >> 5) * 24576;
  auto look = [&]() NOPK {
    ld8(mg, ws + WS_MG, mgo); ld8(mh, ws + WS_MG + 49152, mgo);
    ld16<0>(xg4[0], xgb, xgo); ld16<64>(xg4[1], xgb, xgo); ld16<128>(xg4[2], xgb, xgo); ld16<192>(xg4[3], xgb, xgo);
  };
  lds_barrier();
  TS(6);
  TSF(0, 6);
  const int kch = tid >> 8;
  u16* Kc = (u16*)(smem + A_KC + kch * A_KCS);
  if ((w & 3) < 3) {
    const int s = w & 3;
    const char* pp = smem + A_P + kch * 12288 + s * 1024 + lane * 16;
    const f32x4 q = (*(const f32x4*)(pp) + *(const f32x4*)(pp + 3072)) + (*(const f32x4*)(pp + 6144) + *(const f32x4*)(pp + 9216));
    const int pos = gx + 64 * g + (r < 8 ? (s == 0 ? 0 : s == 1 ? 1 : 256) : s * 256);
    const u32 o0 = pk(q[0], q[1]), o1 = pk(q[2], q[3]);
    if (pos <= 2048) Kc[pos] = (u16)o0;
    if (pos < 2048) {
      Kc[pos + 16] = (u16)(o0 >> 16);
      Kc[pos + 32] = (u16)o1;
      Kc[pos + 48] = (u16)(o1 >> 16);
    }
  }
  look();
  lds_barrier();
  TS(7);
  {
    u32 e[6];
    const int ki = tid & 255;
#pragma unroll
    for (int j = 0; j < 6; j++) {
      const float l0 = (float)(2047 - 8 * ki - 2 * j);
      const u32 bits = pk(fmaxf(l0, 0.f), fmaxf(l0 - 1.f, 0.f));
      e[j] = (u32)Kc[(int)bflo(bits)];
      if (j < 5) e[j] |= (u32)Kc[(int)bfhi(bits)] << 16;
    }
    if (ki == 255) { e[4] = 0; e[5] = 0; }
    const u32 a0 = __builtin_amdgcn_alignbit(e[1], e[0], 16), a1 = __builtin_amdgcn_alignbit(e[2], e[1], 16);
    const u32 a2 = __builtin_amdgcn_alignbit(e[3], e[2], 16), a3 = __builtin_amdgcn_alignbit(e[4], e[3], 16);
    const u32 a4 = __builtin_amdgcn_alignbit(e[5], e[4], 16);
    char* kc = smem + B_KC + kch * B_KCH + ki * 16;
    *(u32x4*)(kc) = u32x4{e[0], e[1], e[2], e[3]};
    *(u32x4*)(kc + B_KCS) = u32x4{a0, a1, a2, a3};
    *(u32x4*)(kc + 2 * B_KCS) = u32x4{e[1], e[2], e[3], e[4]};
    *(u32x4*)(kc + 3 * B_KCS) = u32x4{a1, a2, a3, a4};
  }
  TS(8);
  wait_vm<0>(); tie(xg4[0]); tie(xg4[1]); tie(xg4[2]); tie(xg4[3]); tie(mg); tie(mh);
#pragma unroll
  for (int i = 0; i < 8; i++) tie(v[i]);
#pragma unroll 1
  for (int it = 0; it < SPIN_CAP && !all_lanes(xg4[0][3] == tag && xg4[1][3] == tag && xg4[2][3] == tag && xg4[3][3] == tag && mg[1] == tag && mh[1] == tag); it++) {
    __builtin_amdgcn_s_sleep(1);
    look();
    wait_vm<0>(); tie(xg4[0]); tie(xg4[1]); tie(xg4[2]); tie(xg4[3]); tie(mg); tie(mh);
  }
  TS(9);
  const int g8 = tid >> 1, cc = tid & 1;
  const float mq = u2f(mg[0]) + u2f(mh[0]);
  const float mv = rbf(rs32(mq, mq)), mv1 = rbf(1.f + mv);
  const float ga = sel2(cc, rdl(mv, 20), rdl(mv, 21));
  u32x4 hp, xv;
  {
    const float sh0 = rdl(mv, 16), sh1 = rdl(mv, 17), sc0 = rdl(mv1, 18), sc1 = rdl(mv1, 19);
    const u32 psel = cc ? 0x03020706u : 0x05040100u;
    const bool up = tid & 2;
    u32 ph[4], px[4];
#pragma unroll
    for (int i = 0; i < 4; i++) {
      const u32 xq = (p & 1) ? xg4[i][1] : xg4[i][0];
      const float m = bflo(xg4[i][2]), rs = __builtin_amdgcn_rcpf(bfhi(xg4[i][2]));
      const u32 n = pk((bflo(xq) - m) * rs, (bfhi(xq) - m) * rs);
      const u32 hq = pk(fmaf(bflo(n), sc0, sh0), fmaf(bfhi(n), sc1, sh1));
      ph[i] = __builtin_amdgcn_perm(swap1(hq), hq, psel);
      px[i] = __builtin_amdgcn_perm(swap1(xq), xq, psel);
    }
#pragma unroll
    for (int j = 0; j < 2; j++) {
      const u32 rh = swap2(up ? ph[j] : ph[2 + j]), kh = up ? ph[2 + j] : ph[j];
      hp[2 * j] = up ? rh : kh; hp[2 * j + 1] = up ? kh : rh;
      const u32 rx = swap2(up ? px[j] : px[2 + j]), kx = up ? px[2 + j] : px[j];
      xv[2 * j] = up ? rx : kx; xv[2 * j + 1] = up ? kx : rx;
    }
    char* u = smem + B_U + cc * B_UCH + 512 + g8 * 16;
    *(u32x4*)(u) = hp;
    *(u32x2*)(u + B_US + 8) = u32x2{hp[0], hp[1]};
    *(u32x2*)(u + B_US + 16) = u32x2{hp[2], hp[3]};
  }
  TS(10);
  lds_barrier();
  TS(11);
  const int tt = (xg & 1) * 8 + (idx & 7), fs = (xg >> 1) * 4 + (idx >> 3);
  u32 fw;
  {
    const int ch = w & 1, vh = (w >> 1) & 1, tg = w >> 2;
    const char* pa = smem + B_KC + ch * B_KCH + (3 - (r & 3)) * B_KCS + 2 * (2040 - 8 * (r >> 2) + 8 * g) - 512 * 7 - 64 * (4 * vh + 3);
    const char* pb = smem + B_U + ch * B_UCH + ((r & 1) ? 0 : B_US) + 2 * (256 + 32 * (r >> 1) + 8 * g) - 64 * (4 * vh + 3);
    f32x4 acc[4];
#pragma unroll
    for (int i = 0; i < 4; i++) acc[i] = f32x4{0.f, 0.f, 0.f, 0.f};
    auto prep = [&]() NOPK {
      const float kf = mat ? LN2 : LOG2E;
      const float cs = mv1 * kf, ss = mv * kf;
      u32x4 o0, o1;
      f32x4 sums = {0.f, 0.f, 0.f, 0.f};
#pragma unroll
      for (int j = 0; j < 4; j++) {
        const float k0 = rdl(cs, 2 * j), k1 = rdl(cs, 2 * j + 1), s0 = rdl(ss, 8 + 2 * j), s1 = rdl(ss, 8 + 2 * j + 1);
        const float a0 = bflo(v[2 * j]), a1 = bflo(v[2 * j + 1]), b0 = bfhi(v[2 * j]), b1 = bfhi(v[2 * j + 1]);
        o0[j] = pk(a0 * k0, a1 * k1);
        o1[j] = pk(b0 * k0, b1 * k1);
        sums[0] = dot2(o0[j], 0x3f803f80u, sums[0]);
        sums[1] = dot2(o1[j], 0x3f803f80u, sums[1]);
        sums[2] = fmaf(a0, s0, fmaf(a1, s1, sums[2]));
        sums[3] = fmaf(b0, s0, fmaf(b1, s1, sums[3]));
      }
      st16x2_56(ws + WS_W1F + (size_t)(fb * 7 * 32 + (d0 >> 4)) * 1024 + (w & 1) * 512 + mat * 256, (lane >> 3) * 32768 + (lane & 7) * 16, o0, o1);
      *(f32x4*)(smem + B_RED + tid * 16) = sums;
    };
    auto mid = [&]() NOPK {
      wait_vm<0>();
      if (arrive_last(smem + L_CNT, 8)) st4one(ws + WS_FW + (fb * 16 + mat * 8 + xg) * 128, tag);
    };
    if (tg == 0) conv_body<0>(pa, pb, acc, prep, mid); else conv_body<1>(pa, pb, acc, prep, mid);
    TS(12);
    float* Y = (float*)(smem + B_Y) + (vh * 2 + ch) * 2560 + 40 * (r >> 1) + 4 * (r & 1) + 8 * g;
    const int t0 = tg ? 6 : 7, t1 = tg ? 1 : 0, t2_ = tg ? 5 : 4, t3 = tg ? 2 : 3;
    *(f32x4*)(Y + 320 * t0) = acc[0];
    *(f32x4*)(Y + 320 * t1) = acc[1];
    *(f32x4*)(Y + 320 * t2_) = acc[2];
    *(f32x4*)(Y + 320 * t3) = acc[3];
  }
  lds_barrier();
  TS(13);
  TSF(7, 13);
  {
    const float* Y = (const float*)(smem + B_Y) + cc * 2560 + 8 * g8 + 8 * (g8 >> 2);
    const f32x4 ya = *(const f32x4*)Y + *(const f32x4*)(Y + 5120), yb = *(const f32x4*)(Y + 4) + *(const f32x4*)(Y + 5124);
    const float dk = cc ? bfhi(dsk) : bflo(dsk);
    u32x4 o;
#pragma unroll
    for (int j = 0; j < 4; j++) {
      const float y0 = j < 2 ? ya[2 * j] : yb[2 * j - 4], y1 = j < 2 ? ya[2 * j + 1] : yb[2 * j - 3];
      o[j] = pk(fmaf(ga, fmaf(bflo(hp[j]), dk, y0), bflo(xv[j])), fmaf(ga, fmaf(bfhi(hp[j]), dk, y1), bfhi(xv[j])));
    }
    ld4(fw, ws + WS_FW + fs * 2048, (lane & 15) * 128);
    st16(ws + WS_X1 + p * 128, (g8 >> 2) * 32768 + cc * 64 + (g8 & 3) * 16, o);
  }
  TS(14);
  const int h = lane >> 5, c = lane & 31;
  const int tt2 = xg * 2 + (idx >> 4), sel = (idx >> 3) & 1, dt = idx & 7;
  const int dl = (c & 3) + 4 * (c >> 3) + 16 * ((c >> 2) & 1);
  const int rlo = ((dt ^ sel) & 1) * 7168, rhi = 7168 - rlo;
  const char* w2b = ws + WS_W2F + ((size_t)(dt * 2) * 112 + w * 14) * 1024;
  const u32 w2o = (h * 32 + (dl & 1) * 16 + (dl >> 1)) * 16;
  f32x2* raw = (f32x2*)(smem + C_RAW);
  u32x4 W2[28];
  u32 fh, pf = 0;
  const int rot = (2 * (idx >> 3) + idx) & 7;
  const char* xa = ws + WS_X1 + tt * 131072 + (w & 1) * 32768 + (((w >> 1) + rot) & 7) * 4096;
  const char* xc = ws + WS_X1 + tt * 131072 + (w & 1) * 32768 + (((w >> 1) + rot + 4) & 7) * 4096;
  const u32 lpc = (u32)(size_t)(LDS_AS char*)smem + (w & 1) * 32768 + (w >> 1) * 4096;
  auto warm_w2 = [&]() NOPK {
    const char* pb = ws + WS_W2F + (idx * 8 + w) * 7168;
    const u32 po = (lane < 56 ? lane : 55) * 128;
    asm volatile("global_load_dword %0, %1, %2 sc1\n\tglobal_load_dword %0, %1, %2 offset:64 sc1" : "+v"(pf) : "v"(po), "s"(pb) : "memory");
  };
  auto ldw2 = [&](int i) NOPK { ld16k(W2[i], w2b + (i & 1) * 114688 + (i < 14 ? rlo : rhi), (i >> 1) % 7, w2o); };
  wait_vm<1>(); tie(fw);
  if (w == 0) {
    __builtin_amdgcn_s_setprio(3);
    {
      f32x4 s = {0.f, 0.f, 0.f, 0.f};
#pragma unroll
      for (int q = 0; q < 8; q++) s += *(const f32x4*)(smem + B_RED + q * 1024 + lane * 16);
      st16(ws + WS_CD + ((xg * 16 + fb) * 2 + mat) * 1024, lane * 16, __builtin_bit_cast(u32x4, s));
      st8m<1>(ws + WS_GB + p * 8, 0, u32x2{f2u(rdl(mv, 22)), f2u(rdl(mv, 23))});
    }
    wait_vm<0>();
    TS(15);
#pragma unroll 1
    for (int it = 0; it < SPIN_CAP * 16; it++) {
      if (__builtin_amdgcn_readfirstlane(*(volatile LDS_AS u32*)(smem + L_CNT + 4)) == 7) break;
      __builtin_amdgcn_s_sleep(1);
    }
    st4one(ws + WS_FX + (b & 63) * 128 + (b >> 6) * 4, tag);
    TS(16);
    warm_w2();
#pragma unroll 1
    for (int it = 0; it < SPIN_CAP; it++) {
      u32x4 f;
      ld16(f, ws + WS_FX, lane * 128); wait_vm<0>(); tie(f);
      if (all_lanes(f[0] == tag && f[1] == tag && f[2] == tag && f[3] == tag)) break;
    }
    tie(pf);
    TS(17);
    bar();
    TS(18);
    glds4k(xa, lane * 16, lpc); glds4k(xc, lane * 16, lpc + 16384);
    const char* rb = smem + (lane >> 2) * 64 + (lane & 3) * 8;
    f32x4 sg[2][2], sx[2][2];
#pragma unroll
    for (int i = 0; i < 4; i++) { sg[i >> 1][i & 1] = f32x4{0.f, 0.f, 0.f, 0.f}; sx[i >> 1][i & 1] = f32x4{0.f, 0.f, 0.f, 0.f}; }
    wait_vm<4>(); bar();
    TS(19); TC(20);
    TSF(14, 20);
    glds4k(xa + 65536, lane * 16, lpc + 65536); glds4k(xc + 65536, lane * 16, lpc + 65536 + 16384);
    if (b == 0) st4one(ws + WS_E, tag);
    ln2_acc(rb, 0, 8, sg, sx);
    TS(21);
    wait_vm<8>(); bar();
    TS(22);
    ln2_acc(rb, 8, 16, sg, sx);
    ln2_put(raw, lane, sg[0], sx[0]); ln2_put(raw + 32, lane, sg[1], sx[1]);
    TS(23);
    wait_vm<0>();
    lds_barrier();
    TS(24);
#pragma unroll
    for (int i = 0; i < 4; i++) { sg[i >> 1][i & 1] = f32x4{0.f, 0.f, 0.f, 0.f}; sx[i >> 1][i & 1] = f32x4{0.f, 0.f, 0.f, 0.f}; }
    ln2_acc(rb + 65536, 0, 16, sg, sx);
    ln2_put(raw + 64, lane, sg[0], sx[0]); ln2_put(raw + 96, lane, sg[1], sx[1]);
    asm volatile("" ::: "memory");
    *(volatile LDS_AS u32*)(smem + L_CNT + 16) = 1;
    TS(25);
    __builtin_amdgcn_s_setprio(0);
#pragma unroll
    for (int i = 0; i < 28; i++) ldw2(i);
    TS(26); TS(27); TC(28);
    TSF(21, 28);
    wait_vm<0>();
#pragma unroll
    for (int i = 0; i < 28; i++) tie(W2[i]);
    TS(29);
  } else {
    const int ft = fs * 7 + w - 1;
    const char* wb = ws + WS_W1F + (size_t)ft * 32768;
    const int fl = (c & 3) + 8 * ((c >> 2) & 1) + 4 * (c >> 4);
    const u32 voff = (h * 32 + ((c >> 3) & 1) * 16 + (fl & 1) * 8 + (fl >> 1)) * 16;
    auto x1_done = [&]() NOPK {
      asm volatile("s_waitcnt lgkmcnt(0)" ::: "memory");
      if (lane == 0) __hip_atomic_fetch_add((LDS_AS u32*)(smem + L_CNT + 4), 1u, __ATOMIC_RELAXED, __HIP_MEMORY_SCOPE_WORKGROUP);
    };
    const bool wok = all_lanes(fw == tag);
    if (!wok) {
      wait_vm<0>();
      x1_done();
      spin4(fw, tag, ws + WS_FW + fs * 2048, (lane & 15) * 128);
    }
    TS(15);
    u32x4 A[32];
#pragma unroll
    for (int i = 0; i < 8; i++) {
      if (i == W1A) {
        wait_vm<4 * W1A>();
        if (wok) x1_done();
        TS(16);
      }
      const char* wq = wb + ((i + rot) & 7) * 4096;
      ld16<0>(A[4 * i], wq, voff); ld16<1024>(A[4 * i + 1], wq, voff);
      ld16<2048>(A[4 * i + 2], wq, voff); ld16<3072>(A[4 * i + 3], wq, voff);
    }
    warm_w2();
    const char* rb = smem + h * 512 + ((lane >> 2) & 3) * 64 + ((lane >> 4) & 1) * 32 + (lane & 3) * 8;
    const char* rb2 = rb + 65536;
    u32 cdv[8];
    f32x16 acc[4];
#pragma unroll
    for (int T = 0; T < 4; T++)
#pragma unroll
      for (int i = 0; i < 16; i++) acc[T][i] = 0.f;
    TS(17);
    lds_barrier();
    TS(18);
    glds4k(xa, lane * 16, lpc); glds4k(xc, lane * 16, lpc + 16384);
    wait_vm<4>();
#pragma unroll
    for (int i = 0; i < 32; i++) tie(A[i]);
    tie(pf);
    bar();
    TS(19); TC(20);
    TSF(14, 20);
    u32x4 Bc[2], Bn[2];
    Bc[0] = tr8(rb); Bc[1] = tr8(rb + 32768);
    f32x2 rw[2];
#pragma unroll
    for (int ks = 0; ks < 32; ks++) {
      if (ks == 15) {
        TS(21);
        wait_vm<16>();
        bar();
        TS(22);
      } else if (ks == 31) {
        TS(23);
        wait_vm<0>();
#pragma unroll
        for (int q = 0; q < 8; q++) tie(cdv[q]);
        bar();
        TS(24);
      }
      if (ks < 31) {
        Bn[0] = tr8(rb + (ks + 1) * 1024); Bn[1] = tr8(rb + 32768 + (ks + 1) * 1024);
      } else {
        Bn[0] = tr8(rb2); Bn[1] = tr8(rb2 + 32768);
        rw[0] = raw[c]; rw[1] = raw[32 + c];
      }
      __builtin_amdgcn_sched_barrier(0);
      acc[0] = mfma32(A[ks], Bc[0], acc[0]);
      acc[1] = mfma32(A[ks], Bc[1], acc[1]);
      __builtin_amdgcn_sched_barrier(0);
      if (ks == 3) glds4k(xa + 65536, lane * 16, lpc + 65536);
      if (ks == 7) {
        const u32 co = ((c >> 3) & 1) * 1024 + ((w - 1) * 8 + (fl >> 1)) * 16 + h * 8 + (fl & 1) * 4;
#pragma unroll
        for (int q = 0; q < 8; q++) ld4(cdv[q], ws + WS_CD + (q * 16 + fs) * 2048, co);
      }
      if (ks == 11) glds4k(xc + 65536, lane * 16, lpc + 65536 + 16384);
      Bc[0] = Bn[0]; Bc[1] = Bn[1];
    }
    const float cd = ((u2f(cdv[0]) + u2f(cdv[1])) + (u2f(cdv[2]) + u2f(cdv[3]))) + ((u2f(cdv[4]) + u2f(cdv[5])) + (u2f(cdv[6]) + u2f(cdv[7])));
    const float chi = rbf(cd);
    const u32x4 Ax = {pk(chi, chi), pk(cd - chi, cd - chi), 0, 0};
    const char* hb = ws + WS_HF + ((size_t)(tt * 4) * 112 + ft) * 1024;
    Ep q[4];
    float o[8];
#pragma unroll
    for (int s = 0; s < 32; s++) {
      const int n = s + 1;
      const int ta = s < 28 ? 2 : 3, tb_ = s < 24 ? 3 : ta, ka = s < 24 ? s : 24 + 2 * (s & 3), kb = s < 24 ? s : ka + 1;
      if (s == 21) {
        TS(25);
#pragma unroll 1
        for (int it = 0; it < SPIN_CAP * 16; it++)
          if (__builtin_amdgcn_readfirstlane(*(volatile LDS_AS u32*)(smem + L_CNT + 16)) == 1) break;
        asm volatile("" ::: "memory");
        TS(26);
        rw[0] = raw[64 + c]; rw[1] = raw[96 + c];
      }
      if (n < 24) {
        Bn[0] = tr8(rb2 + n * 1024); Bn[1] = tr8(rb2 + 32768 + n * 1024);
      } else if (n < 32) {
        const char* rp = rb2 + (n < 28 ? 0 : 32768) + (24 + 2 * (n & 3)) * 1024;
        Bn[0] = tr8(rp); Bn[1] = tr8(rp + 1024);
      }
      __builtin_amdgcn_sched_barrier(0);
      acc[ta] = mfma32(A[ka], Bc[0], acc[ta]);
      if (s < 2 || s == 22 || s == 23) {
        const int T = s < 2 ? s : s - 20;
        const u32 be = ln2_fold(rw[T & 1], h, q[T]);
        acc[T] = mfma32(Ax, u32x4{be, be, 0, 0}, acc[T]);
      }
      const int j = s >= 3 && s < 11 ? 2 * (s - 3) : s >= 28 ? 16 + 2 * (s - 28) : -1;
      if (j >= 0) o[j & 7] = swi(acc[j >> 3], j & 7, q[j >> 3]);
      __builtin_amdgcn_sched_barrier(0);
      acc[tb_] = mfma32(A[kb], Bc[1], acc[tb_]);
      if (j >= 0) {
        o[(j + 1) & 7] = swi(acc[j >> 3], (j + 1) & 7, q[j >> 3]);
        if ((j & 7) == 6) st16(hb + (j >> 3) * 114688, lane * 16, u32x4{pk(o[0], o[1]), pk(o[2], o[3]), pk(o[4], o[5]), pk(o[6], o[7])});
      }
      __builtin_amdgcn_sched_barrier(0);
      if (s < 28) ldw2(s);
      if (s == 27) {
        wait_vm<18>();
        if (arrive_last(smem + L_CNT + 8, 7)) st4one(ws + WS_FH + (tt * 16 + fs) * 128, tag);
        TS(27);
      }
      Bc[0] = Bn[0]; Bc[1] = Bn[1];
    }
    TC(28);
    TSF(21, 28);
#pragma unroll
    for (int i = 0; i < 8; i++) o[i] = swi(acc[3], i, q[3]);
    st16(hb + 3 * 114688, lane * 16, u32x4{pk(o[0], o[1]), pk(o[2], o[3]), pk(o[4], o[5]), pk(o[6], o[7])});
    wait_vm<2>();
#pragma unroll
    for (int i = 0; i < 28; i++) tie(W2[i]);
    TS(29);
  }
  {
    const int te = tt2 * 4 + sel, tl = tt2 * 4 + 3 - sel;
    const char* hbe = ws + WS_HF + ((size_t)te * 112 + w * 14) * 1024;
    const char* hbl = ws + WS_HF + ((size_t)tl * 112 + w * 14) * 1024;
    const int u = lane >> 2, tk = w * 4 + (lane & 3);
    u32 xe[4], xl[4];
    u32x4 gbv;
    u32x4 H[14];
    ld4(fh, ws + WS_FH + tt2 * 2048, (lane & 15) * 128);
    wait_vm<0>(); tie(fh);
    if (w > 0 && arrive_last(smem + L_CNT + 12, 7)) st4one(ws + WS_FH + 32768 + (tt * 16 + fs) * 128, tag);
    TS(30);
    spin4(fh, tag, ws + WS_FH + tt2 * 2048, (lane & 15) * 128);
    bar();
    TS(31);
#pragma unroll
    for (int i = 0; i < 14; i++) ld16k(H[i], hbe + (i < 7 ? rlo : rhi), i % 7, lane * 16);
    const u32 xo = (dt * 64 + u * 4) * 64 + (tk >> 1) * 4;
    ld4<0>(xe[0], ws + WS_X1 + te * 32768, xo); ld4<64>(xe[1], ws + WS_X1 + te * 32768, xo);
    ld4<128>(xe[2], ws + WS_X1 + te * 32768, xo); ld4<192>(xe[3], ws + WS_X1 + te * 32768, xo);
    ld4<0>(xl[0], ws + WS_X1 + tl * 32768, xo); ld4<64>(xl[1], ws + WS_X1 + tl * 32768, xo);
    ld4<128>(xl[2], ws + WS_X1 + tl * 32768, xo); ld4<192>(xl[3], ws + WS_X1 + tl * 32768, xo);
    ld16<0>(gbv, ws + WS_GB + dt * 256, u * 16);
    ld4(fh, ws + WS_FH + 32768 + tt2 * 2048, (lane & 15) * 128);
    TS(32);
    f32x16 acc[2];
#pragma unroll
    for (int i = 0; i < 16; i++) { acc[0][i] = 0.f; acc[1][i] = 0.f; }
    char* wr = smem + w * D_SRC + h * 4 * 576 + c * 16;
    const char* rd = smem + u * 576 + tk * 16;
#pragma unroll
    for (int i = 0; i < 14; i++) {
      if (i == 0 || i == 4 || i == 9) {
        __builtin_amdgcn_sched_barrier(0);
        if (i == 0) wait_vm<20>(); else if (i == 4) wait_vm<15>(); else wait_vm<10>();
#pragma unroll
        for (int k = i; k < (i == 0 ? 4 : i == 4 ? 9 : 14); k++) tie(H[k]);
      }
      acc[0] = mfma32(W2[2 * i], H[i], acc[0]);
      acc[1] = mfma32(W2[2 * i + 1], H[i], acc[1]);
    }
    __builtin_amdgcn_sched_barrier(0);
    wait_vm<0>(); tie(fh);
#pragma unroll
    for (int i = 0; i < 4; i++) { tie(xe[i]); tie(xl[i]); }
    tie(gbv);
    TS(33);
    spin4(fh, tag, ws + WS_FH + 32768 + tt2 * 2048, (lane & 15) * 128);
    TS(34);
#pragma unroll
    for (int i = 0; i < 14; i++) ld16k(H[i], hbl + (i < 7 ? rlo : rhi), i % 7, lane * 16);
    __builtin_amdgcn_sched_barrier(0);
    TS(35);
#pragma unroll
    for (int di = 0; di < 2; di++)
#pragma unroll
      for (int q = 0; q < 4; q++)
        *(f32x4*)(wr + (di * 8 + q) * 576) = f32x4{acc[di][4 * q], acc[di][4 * q + 1], acc[di][4 * q + 2], acc[di][4 * q + 3]};
    __builtin_amdgcn_sched_barrier(0);
    TS(36);
    TSF(29, 36);
    auto finish = [&](const char* rp, const u32 (&xq)[4]) NOPK {
      f32x4 s = *(const f32x4*)rp;
#pragma unroll
      for (int q = 1; q < 8; q++) s += *(const f32x4*)(rp + q * D_SRC);
      float y[4];
#pragma unroll
      for (int j = 0; j < 4; j++) y[j] = fmaf(u2f(gbv[j]), s[j], (tk & 1) ? bfhi(xq[j]) : bflo(xq[j]));
      return u32x2{pk(y[0], y[1]), pk(y[2], y[3])};
    };
#pragma unroll
    for (int i = 0; i < 16; i++) { acc[0][i] = 0.f; acc[1][i] = 0.f; }
#pragma unroll
    for (int i = 0; i < 14; i++) {
      if (i == 0 || i == 4 || i == 9) {
        __builtin_amdgcn_sched_barrier(0);
        if (i == 0) wait_vm<10>(); else if (i == 4) wait_vm<5>(); else wait_vm<0>();
#pragma unroll
        for (int k = i; k < (i == 0 ? 4 : i == 4 ? 9 : 14); k++) tie(H[k]);
      }
      acc[0] = mfma32(W2[2 * i], H[i], acc[0]);
      acc[1] = mfma32(W2[2 * i + 1], H[i], acc[1]);
    }
    TS(37);
#pragma unroll
    for (int di = 0; di < 2; di++)
#pragma unroll
      for (int q = 0; q < 4; q++)
        *(f32x4*)(wr + D_TILE + (di * 8 + q) * 576) = f32x4{acc[di][4 * q], acc[di][4 * q + 1], acc[di][4 * q + 2], acc[di][4 * q + 3]};
    lds_barrier();
    TS(38);
    *(u32x2*)(out + (size_t)(tl * 32 + tk) * D + dt * 64 + u * 4) = finish(rd + D_TILE, xl);
    *(u32x2*)(out + (size_t)(te * 32 + tk) * D + dt * 64 + u * 4) = finish(rd, xe);
    TS(39);
    TS(40);
    TSF(37, 40);
    TSEND();
  }
}

extern "C" void kernel_launch(void* const* d_in, const int* in_sizes, int n_in, void* d_out, int out_size, void* d_ws,
                              size_t ws_size, hipStream_t stream) {
  const u16* const* in = (const u16* const*)d_in;
  k0<<<256, 512, 0, stream>>>((char*)d_ws);
  fused<<<256, 512, 0, stream>>>(in[0], in[1], in[2], in[3], in[4], in[5], in[6], in[7], in[8], in[10], in[11], in[12], in[13], in[14],
                                 (char*)d_ws, (u16*)d_out);
}
```

```cpp
#include <hip/hip_runtime.h>
#include <stdint.h>

typedef unsigned int u32;
typedef unsigned short u16;
typedef __attribute__((ext_vector_type(2))) float f32x2;
typedef __attribute__((ext_vector_type(4))) float f32x4;
typedef __attribute__((ext_vector_type(16))) float f32x16;
typedef __attribute__((ext_vector_type(2))) u32 u32x2;
typedef __attribute__((ext_vector_type(4))) u32 u32x4;
typedef __attribute__((ext_vector_type(2))) __bf16 bf16x2;
typedef __attribute__((ext_vector_type(8))) __bf16 bf16x8;
typedef __attribute__((ext_vector_type(4))) short s16x4;
#define LDS_AS __attribute__((address_space(3)))
#if defined(__HIP_DEVICE_COMPILE__)
#define NOPK __attribute__((target("no-packed-fp32-ops")))
#else
#define NOPK
#endif
#define DEVINL __device__ __forceinline__ NOPK

constexpr int L = 2048, D = 512, NS = 128, F = 1792;

constexpr size_t WS_E = 0;
constexpr size_t WS_FW = 4096;
constexpr size_t WS_FX = WS_FW + 32768;
constexpr size_t WS_FH = WS_FX + 32768;
constexpr size_t WS_MG = WS_FH + 65536;
constexpr size_t WS_GB = WS_MG + 98304;
constexpr size_t WS_XG = 262144;
constexpr size_t WS_X1 = WS_XG + (4u << 20);
constexpr size_t WS_W1F = WS_X1 + (2u << 20);
constexpr size_t WS_W2F = WS_W1F + 112 * 32 * 1024;
constexpr size_t WS_HF = WS_W2F + 16 * 112 * 1024;
constexpr size_t WS_CD = WS_HF + 64 * 112 * 1024;
constexpr int SPIN_CAP = 40000;
#define SLAG 3
#define LST 2
#define SPRE 4

#define u2f(x) __builtin_bit_cast(float, (u32)(x))
#define i2f(x) __builtin_bit_cast(float, (int)(x))
#define f2u(x) __builtin_bit_cast(u32, (float)(x))
#define f2i(x) __builtin_bit_cast(int, (float)(x))
#define TSDECL
#define TS(slot)
#define TC(slot)
#define TSF(lo, hi)
#define TSEND()
DEVINL float bflo(u32 u) { return u2f(u << 16); }
DEVINL float bfhi(u32 u) { return u2f(u & 0xffff0000u); }
DEVINL u32 pk(float a, float b) {
  f32x2 v = {a, b};
  return __builtin_bit_cast(u32, __builtin_convertvector(v, bf16x2));
}
DEVINL float rbf(float a) { return bflo(pk(a, a)); }
template <int CTRL> DEVINL float dpp_add(float v) {
  return v + i2f(__builtin_amdgcn_update_dpp(0, f2i(v), CTRL, 0xf, 0xf, true));
}
DEVINL float row4_sum(float v) {
  auto a = __builtin_amdgcn_permlane16_swap(f2u(v), f2u(v), false, false);
  v = u2f(a[0]) + u2f(a[1]);
  auto b = __builtin_amdgcn_permlane32_swap(f2u(v), f2u(v), false, false);
  return u2f(b[0]) + u2f(b[1]);
}
DEVINL float rs32(float a, float b) {
  auto r = __builtin_amdgcn_permlane32_swap(f2u(a), f2u(b), false, false);
  return u2f(r[0]) + u2f(r[1]);
}
DEVINL float rs16(float a, float b) {
  auto r = __builtin_amdgcn_permlane16_swap(f2u(a), f2u(b), false, false);
  return u2f(r[0]) + u2f(r[1]);
}
DEVINL float wave_sum(float v) { return row4_sum(dpp_add<0x140>(dpp_add<0x141>(dpp_add<0x4E>(dpp_add<0xB1>(v))))); }
DEVINL f32x4 mfma16(u32x4 a, u32x4 b, f32x4 c) {
  return __builtin_amdgcn_mfma_f32_16x16x32_bf16(__builtin_bit_cast(bf16x8, a), __builtin_bit_cast(bf16x8, b), c, 0, 0, 0);
}
DEVINL f32x16 mfma32(u32x4 a, u32x4 b, f32x16 c) {
  return __builtin_amdgcn_mfma_f32_32x32x16_bf16(__builtin_bit_cast(bf16x8, a), __builtin_bit_cast(bf16x8, b), c, 0, 0, 0);
}
DEVINL float dot2(u32 a, u32 b, float c) {
  return __builtin_amdgcn_fdot2_f32_bf16(__builtin_bit_cast(bf16x2, a), __builtin_bit_cast(bf16x2, b), c, false);
}
DEVINL void lds_barrier() {
  asm volatile("s_waitcnt lgkmcnt(0)" ::: "memory");
  __builtin_amdgcn_s_barrier();
  asm volatile("" ::: "memory");
}
DEVINL void bar() {
  asm volatile("" ::: "memory");
  __builtin_amdgcn_s_barrier();
  asm volatile("" ::: "memory");
}
DEVINL void st16(const void* sbase, u32 voff, u32x4 v) {
  asm volatile("global_store_dwordx4 %0, %1, %2 sc1\n\ts_nop 1" ::"v"(voff), "v"(v), "s"(sbase) : "memory");
}
DEVINL void st8(const void* sbase, u32 voff, u32x2 v) {
  asm volatile("global_store_dwordx2 %0, %1, %2 sc1\n\ts_nop 1" ::"v"(voff), "v"(v), "s"(sbase) : "memory");
}
DEVINL void st4(const void* sbase, u32 voff, u32 v) {
  asm volatile("global_store_dword %0, %1, %2 sc1\n\ts_nop 1" ::"v"(voff), "v"(v), "s"(sbase) : "memory");
}
template <int LANES> DEVINL void st8m(const void* sbase, u32 voff, u32x2 v) {
  asm volatile("s_mov_b64 exec, %3\n\tglobal_store_dwordx2 %0, %1, %2 sc1\n\ts_mov_b64 exec, -1\n\ts_nop 0"
               ::"v"(voff), "v"(v), "s"(sbase), "n"((1 << LANES) - 1) : "memory");
}
DEVINL void st4one(const void* sbase, u32 v) {
  asm volatile("s_mov_b64 exec, 1\n\tglobal_store_dword %0, %1, %2 sc1\n\ts_mov_b64 exec, -1\n\ts_nop 0"
               ::"v"(0), "v"(v), "s"(sbase) : "memory");
}
DEVINL void st16x2_56(const void* sbase, u32 voff, u32x4 a, u32x4 b) {
  asm volatile("s_mov_b32 exec_hi, 0xffffff\n\tglobal_store_dwordx4 %0, %1, %3 sc1\n\tglobal_store_dwordx4 %0, %2, %3 offset:256 sc1\n\t"
               "s_mov_b32 exec_hi, -1\n\ts_nop 0" ::"v"(voff), "v"(a), "v"(b), "s"(sbase) : "memory");
}
DEVINL void sld4(u32& d, const void* p) { asm volatile("s_load_dword %0, %1, 0" : "=s"(d) : "s"(p) : "memory"); }
template <int OFF = 0> DEVINL void ld16(u32x4& d, const void* sbase, u32 voff) {
  asm volatile("global_load_dwordx4 %0, %1, %2 offset:%3 sc1" : "=v"(d) : "v"(voff), "s"(sbase), "n"(OFF) : "memory");
}
DEVINL void ld16k(u32x4& d, const char* sbase, int kb, u32 voff) {
  const char* sb = sbase + (kb >> 2) * 4096;
  if ((kb & 3) == 0) ld16<0>(d, sb, voff); else if ((kb & 3) == 1) ld16<1024>(d, sb, voff);
  else if ((kb & 3) == 2) ld16<2048>(d, sb, voff); else ld16<3072>(d, sb, voff);
}
DEVINL void ld8(u32x2& d, const void* sbase, u32 voff) {
  asm volatile("global_load_dwordx2 %0, %1, %2 sc1" : "=v"(d) : "v"(voff), "s"(sbase) : "memory");
}
template <int OFF = 0> DEVINL void ld4(u32& d, const void* sbase, u32 voff) {
  asm volatile("global_load_dword %0, %1, %2 offset:%3 sc1" : "=v"(d) : "v"(voff), "s"(sbase), "n"(OFF) : "memory");
}
template <int OFF = 0> DEVINL void lp16(u32x4& d, const void* sbase, u32 voff) {
  asm volatile("global_load_dwordx4 %0, %1, %2 offset:%3" : "=v"(d) : "v"(voff), "s"(sbase), "n"(OFF) : "memory");
}
template <int OFF = 0> DEVINL void lp4(u32& d, const void* sbase, u32 voff) {
  asm volatile("global_load_dword %0, %1, %2 offset:%3" : "=v"(d) : "v"(voff), "s"(sbase), "n"(OFF) : "memory");
}
template <int OFF = 0> DEVINL void lp2(u32& d, const void* sbase, u32 voff) {
  asm volatile("global_load_ushort %0, %1, %2 offset:%3" : "=v"(d) : "v"(voff), "s"(sbase), "n"(OFF) : "memory");
}
template <class T> DEVINL void tie(T& a) { asm volatile("" : "+v"(a)); }
template <int N> DEVINL void wait_vm() { asm volatile("s_waitcnt vmcnt(%0)" ::"n"(N) : "memory"); }
DEVINL bool all_lanes(bool ok) { return __builtin_amdgcn_ballot_w64(!ok) == 0; }
DEVINL void spin4(u32 f, u32 tag, const void* sbase, u32 voff) {
#pragma unroll 1
  for (int it = 0; it < SPIN_CAP && !all_lanes(f == tag); it++) {
    __builtin_amdgcn_s_sleep(1);
    ld4(f, sbase, voff); wait_vm<0>(); tie(f);
  }
}
DEVINL bool arrive_last(char* smem_word, int n) {
  u32 old;
  asm volatile("s_mov_b64 exec, 1\n\tds_add_rtn_u32 %0, %1, %2\n\ts_waitcnt lgkmcnt(0)\n\ts_mov_b64 exec, -1"
               : "=&v"(old) : "v"((u32)(size_t)(LDS_AS char*)smem_word), "v"(1) : "memory");
  return __builtin_amdgcn_readfirstlane(old) == (u32)(n - 1);
}
DEVINL void scale8(u32x4& a, const u32x4& s, const u32x4& t, int on) {
  u32 a0 = a[0], a1 = a[1], a2 = a[2], a3 = a[3], x0, x1, x2, x3;
  asm("s_cmp_eq_u32 %16, 0\n\ts_cbranch_scc1 .Lsk%=\n\tv_dot2_f32_bf16 %4, %0, %8, 0\n\tv_dot2_f32_bf16 %0, %0, %9, 0\n\tv_dot2_f32_bf16 %5, %1, %10, 0\n\tv_dot2_f32_bf16 %1, %1, %11, 0\n\t"
      "v_dot2_f32_bf16 %6, %2, %12, 0\n\tv_dot2_f32_bf16 %2, %2, %13, 0\n\tv_dot2_f32_bf16 %7, %3, %14, 0\n\tv_dot2_f32_bf16 %3, %3, %15, 0\n\t"
      "v_cvt_pk_bf16_f32 %0, %4, %0\n\tv_cvt_pk_bf16_f32 %1, %5, %1\n\tv_cvt_pk_bf16_f32 %2, %6, %2\n\ts_nop 0\n\tv_cvt_pk_bf16_f32 %3, %7, %3\n.Lsk%=:"
      : "+v"(a0), "+v"(a1), "+v"(a2), "+v"(a3), "=&v"(x0), "=&v"(x1), "=&v"(x2), "=&v"(x3)
      : "v"(s[0]), "v"(s[1]), "v"(s[2]), "v"(s[3]), "v"(t[0]), "v"(t[1]), "v"(t[2]), "v"(t[3]), "s"(on) : "scc");
  a = u32x4{a0, a1, a2, a3};
}
DEVINL u32x2 tr_read(const char* p) {
  return __builtin_bit_cast(u32x2, __builtin_amdgcn_ds_read_tr16_b64_v4i16((LDS_AS s16x4*)p));
}
DEVINL float rdl(float v, int l) { return i2f(__builtin_amdgcn_readlane(f2i(v), l)); }
DEVINL float sel2(int c, float a, float b) { return c ? b : a; }
DEVINL u32 swap2(u32 v) { return (u32)__builtin_amdgcn_update_dpp(0, (int)v, 0x4E, 0xf, 0xf, true); }
DEVINL u32 swap1(u32 v) { return (u32)__builtin_amdgcn_update_dpp(0, (int)v, 0xB1, 0xf, 0xf, true); }
constexpr float LOG2E = 1.4426950408889634f, LN2 = 0.6931471805599453f, INV2PI = 0.15915494309189535f;

constexpr int A_TAB = 0, A_P = 8 * 1536, A_KC = A_P + 24576, A_KCS = 4112, A_GV = A_KC + 2 * A_KCS;
constexpr int B_KC = 47360, B_KCS = 4160, B_KCH = 4 * 4160, B_U = B_KC + 2 * B_KCH, B_US = 5152, B_UCH = 2 * B_US;
constexpr int B_Y = B_U + 2 * B_UCH, B_RED = 4096, B_SC = 0;
constexpr int A_XT = B_Y;
constexpr int D_SRC = 16 * 576, D_TILE = 8 * D_SRC;
constexpr int C_RAW = 148480, L_CNT = C_RAW + 1024, D_RES = L_CNT + 64, D_GB = D_RES + 8192, LDS_BYTES = D_GB + 256;
static_assert(A_GV + 2048 <= B_KC && B_Y + 40960 <= C_RAW && 2 * D_TILE <= C_RAW && B_RED + 8192 <= A_KC && LDS_BYTES <= 163840, "lds map");

DEVINL void glds4k(const void* sbase, u32 voff, u32 ldsaddr) {
  asm volatile("s_mov_b32 m0, %2\n\ts_nop 0\n\t"
               "global_load_lds_dwordx4 %0, %1 sc1\n\tglobal_load_lds_dwordx4 %0, %1 offset:1024 sc1\n\t"
               "global_load_lds_dwordx4 %0, %1 offset:2048 sc1\n\tglobal_load_lds_dwordx4 %0, %1 offset:3072 sc1"
               ::"v"(voff), "s"(sbase), "s"(ldsaddr) : "memory");
}
DEVINL u32x4 tr8(const char* p) {
  const u32x2 lo = tr_read(p), hi = tr_read(p + 256);
  return u32x4{lo[0], lo[1], hi[0], hi[1]};
}
#define ksl(o) ((o) < 28 ? (o) + (o) / 7 : 8 * ((o) - 28) + 7)
struct Ep { float q0, v; };
DEVINL float swi(const f32x16& a, int i, const Ep& q) {
  const float G = a[(i & 3) + 8 * (i >> 2)], U = a[(i & 3) + 8 * (i >> 2) + 4];
  const float e = __builtin_amdgcn_exp2f(G * q.q0);
  return (G * U) * __builtin_amdgcn_rcpf(fmaf(e, q.v, q.v));
}
DEVINL u32 ln2_fold(f32x2 s, int h, Ep& q) {
  const float m = s[0] * (1.f / D);
  const float v = fmaxf(fmaf(-m, m, s[1] * (1.f / D)), 0.f) + 1e-5f;
  const float rs = __builtin_amdgcn_rsqf(v);
  q.q0 = rs * -LOG2E; q.v = v;
  const float val = h ? v * rs : -m;
  const float vhi = rbf(val);
  return pk(vhi, val - vhi);
}
DEVINL void ln2_acc(const char* rb, int kp0, int kp1, f32x4 (&sg)[2][2], f32x4 (&sx)[2][2]) {
  const u32x4 ones = {0x3f803f80u, 0x3f803f80u, 0x3f803f80u, 0x3f803f80u};
  u32x4 fr[4][2];
  auto rd = [&](int n) NOPK {
#pragma unroll
    for (int gi = 0; gi < 2; gi++) {
      const char* pp = rb + (n & 1) * 32768 + (kp0 + (n >> 1)) * 2048 + gi * 32;
      const u32x2 lo = tr_read(pp), hi = tr_read(pp + 1024);
      fr[n & 3][gi] = u32x4{lo[0], lo[1], hi[0], hi[1]};
    }
  };
  rd(0); rd(1); rd(2);
#pragma unroll
  for (int n = 0; n < 2 * (kp1 - kp0); n++) {
    if (n + 3 < 2 * (kp1 - kp0)) rd(n + 3);
    __builtin_amdgcn_sched_barrier(0);
#pragma unroll
    for (int gi = 0; gi < 2; gi++) {
      sg[n & 1][gi] = mfma16(fr[n & 3][gi], fr[n & 3][gi], sg[n & 1][gi]);
      sx[n & 1][gi] = mfma16(ones, fr[n & 3][gi], sx[n & 1][gi]);
    }
    __builtin_amdgcn_sched_barrier(0);
  }
}
DEVINL void ln2_put(f32x2* raw, f32x2* dummy, int lane, const f32x4 (&sg)[2], const f32x4 (&sx)[2]) {
#pragma unroll
  for (int gi = 0; gi < 2; gi++) {
    const float gsel = (lane & 2) ? ((lane & 1) ? sg[gi][3] : sg[gi][2]) : ((lane & 1) ? sg[gi][1] : sg[gi][0]);
    f32x2* dst = (lane >> 4) == ((lane & 15) >> 2) ? raw + gi * 16 + (lane & 15) : dummy;
    *dst = f32x2{sx[gi][0], gsel};
  }
}

typedef __attribute__((address_space(1))) unsigned long long gu64;
typedef __attribute__((address_space(1))) u32 gu32;
DEVINL void put_granule(char* p, u32 val, u32 tag) {
  __hip_atomic_store((gu64*)p, ((unsigned long long)tag << 32) | val, __ATOMIC_RELAXED, __HIP_MEMORY_SCOPE_AGENT);
}
DEVINL void put_flag(char* p, u32 tag) { __hip_atomic_store((gu32*)p, tag, __ATOMIC_RELAXED, __HIP_MEMORY_SCOPE_AGENT); }

__global__ NOPK __launch_bounds__(512, 2) void fused(
    const u16* __restrict__ x, const u16* __restrict__ Are, const u16* __restrict__ Aim, const u16* __restrict__ Bre,
    const u16* __restrict__ Bim, const u16* __restrict__ Cre, const u16* __restrict__ Cim, const u16* __restrict__ logdt,
    const u16* __restrict__ dskip, const u16* __restrict__ w1, const u16* __restrict__ w3, const u16* __restrict__ w2,
    const u16* __restrict__ cvec, const u16* __restrict__ wmod, char* ws, u16* __restrict__ out) {
  __shared__ __attribute__((aligned(16))) char smem[LDS_BYTES];
  const int b = blockIdx.x, tid = threadIdx.x, lane = tid & 63;
  const int w = __builtin_amdgcn_readfirstlane(tid >> 6);
  TSDECL; TS(0);
  const int xg = b & 7, idx = b >> 3;
  const int p = xg * 32 + idx;
  const int r = lane & 15, g = lane >> 4;
  asm volatile("" ::"s"(x), "s"(Are), "s"(Aim), "s"(Bre), "s"(Bim), "s"(Cre), "s"(Cim), "s"(logdt), "s"(dskip), "s"(w1), "s"(w3),
               "s"(w2), "s"(cvec), "s"(wmod), "s"(ws), "s"(out));
  u32x4 xr, wm0, wm1;
  u32 vare, vaim, vbre, vbim, vcre, vcim, c0, c1;
  const int cg = xg * 24 + (idx < 24 ? idx : 23);
  u32 e_prev, ldt2, dsk;
  lp16(xr, x + b * 4096, tid * 16);
  sld4(e_prev, ws + WS_E); sld4(ldt2, logdt + 2 * p); sld4(dsk, dskip + 2 * p);
  {
    const u32 so = (p * 256 + w * 32 + (lane & 31)) * 2;
    lp2(vare, Are, so); lp2(vaim, Aim, so); lp2(vbre, Bre, so); lp2(vbim, Bim, so); lp2(vcre, Cre, so); lp2(vcim, Cim, so);
    const u32 mo = (tid >> 3) * 6144 + (tid & 7) * 16;
    const u16* wmp = wmod + (cg & 3) * 128 * 3072 + (cg >> 2) * 64;
    lp16(wm0, wmp, mo); lp16(wm1, wmp + 64 * 3072, mo);
    lp2(c0, cvec + (cg & 3) * 128, (tid >> 3) * 2); lp2<128>(c1, cvec + (cg & 3) * 128, (tid >> 3) * 2);
  }
  TS(1);
  if (tid < 32) *(u32x4*)(smem + B_KC + (tid >> 2) * B_KCS + 4096 + (tid & 3) * 16) = u32x4{0, 0, 0, 0};
  else if (tid < 36) *(u32x4*)(smem + L_CNT + (tid - 32) * 16) = u32x4{0, 0, 0, 0};
  else if (tid < 38) *(u32x2*)(smem + B_U + (tid & 1) * B_UCH + B_US + 1024) = u32x2{0, 0};
  else if (tid >= 256) *(u32x4*)(smem + B_U + (w - 4) * B_US + lane * 16) = u32x4{0, 0, 0, 0};
  asm volatile("s_waitcnt lgkmcnt(0)" : "+s"(e_prev), "+s"(ldt2), "+s"(dsk)::"memory");
  const u32 tag = e_prev * 1664525u + 1013904223u;
  wait_vm<10>(); tie(xr);
  {
    *(u32x4*)(smem + A_XT + w * 1056 + lane * 16) = xr;
    float f[8];
#pragma unroll
    for (int j = 0; j < 4; j++) { f[2 * j] = bflo(xr[j]); f[2 * j + 1] = bfhi(xr[j]); }
    const float m = wave_sum(((f[0] + f[1]) + (f[2] + f[3])) + ((f[4] + f[5]) + (f[6] + f[7]))) * (1.f / D);
    float s = 0.f;
#pragma unroll
    for (int j = 0; j < 8; j++) { f[j] -= m; s = fmaf(f[j], f[j], s); }
    const float sd = __builtin_amdgcn_sqrtf(rbf(rbf(wave_sum(s) * (1.f / D)) + 1e-5f));
    if (lane == 0) *(u32*)(smem + A_XT + w * 1056 + 1024) = pk(m, sd);
  }
  TS(2);
  wait_vm<4>(); tie(vare); tie(vaim); tie(vbre); tie(vbim); tie(vcre); tie(vcim);
  char* tab = smem + A_TAB + w * 1536;
  {
    const float dt = rbf(__builtin_amdgcn_exp2f((w >> 2 ? bfhi(ldt2) : bflo(ldt2)) * LOG2E));
    const float a = dt * bflo(vare) * LOG2E, bq = dt * bflo(vaim) * INV2PI;
    const float br = dt * bflo(vbre), bi = dt * bflo(vbim), cr = bflo(vcre), ci = bflo(vcim);
    const float m = lane < 32 ? 1.f : 256.f;
    const float e = __builtin_amdgcn_exp2f(a * m), ph = __builtin_amdgcn_fractf(-bq * m);
    const float ex = e * __builtin_amdgcn_cosf(ph), ey = e * __builtin_amdgcn_sinf(ph);
    char* t = tab + (lane & 31) * 48;
    *(f32x2*)(t + 16 + (lane >> 5) * 8) = f32x2{ex, ey};
    f32x4 o = {a, bq, cr * br - ci * bi, cr * bi + ci * br};
    if (lane >= 32) o = f32x4{ex, ey, ex * ex - ey * ey, (ex + ex) * ey};
    *(f32x4*)(t + (lane >> 5) * 32) = o;
  }
  TS(3);
  lds_barrier();
  {
    const char* xt = smem + A_XT + (lane & 7) * 1056;
    const u32 st = *(const u32*)(xt + 1024);
#pragma unroll
    for (int e = 0; e < 2; e++) {
      const u32x2 xq = *(const u32x2*)(xt + (e * 64 + w * 8 + (lane >> 3)) * 8);
      st16(ws + WS_XG + (size_t)(e * 64 + w * 8) * 32768 + b * 128, (lane >> 3) * 32768 + (lane & 7) * 16, u32x4{xq[0], xq[1], st, tag});
    }
  }
  TS(41);
  const int fb = (xg >> 1) * 4 + (idx & 3), mat = (idx >> 2) & 1, dq = (xg & 1) * 4 + (idx >> 3), d0 = dq * 64 + w * 8;
  u32 v[8], t2[8];
  {
    const u16* wp = (mat ? w3 : w1) + (size_t)d0 * F + fb * 112;
    const u32 vo = (lane < 56 ? lane : 55) * 4;
#pragma unroll
    for (int i = 0; i < 4; i++) { lp4<0>(v[2 * i], wp + 2 * i * F, vo); lp4<2 * F>(v[2 * i + 1], wp + 2 * i * F, vo); }
  }
  auto gemv = [&]() NOPK
  {
    wait_vm<10>(); tie(wm0); tie(wm1); tie(c0); tie(c1);
    const float cx0 = bflo(c0), cx1 = bflo(c1);
    const float s0 = rbf(cx0 * __builtin_amdgcn_rcpf(1.f + __builtin_amdgcn_exp2f(cx0 * -LOG2E)));
    const float s1 = rbf(cx1 * __builtin_amdgcn_rcpf(1.f + __builtin_amdgcn_exp2f(cx1 * -LOG2E)));
    float acc[8];
#pragma unroll
    for (int j = 0; j < 4; j++) {
      acc[2 * j] = fmaf(s0, bflo(wm0[j]), s1 * bflo(wm1[j]));
      acc[2 * j + 1] = fmaf(s0, bfhi(wm0[j]), s1 * bfhi(wm1[j]));
    }
    const float q0 = dpp_add<0x128>(rs16(rs32(acc[0], acc[4]), rs32(acc[2], acc[6])));
    const float q1 = dpp_add<0x128>(rs16(rs32(acc[1], acc[5]), rs32(acc[3], acc[7])));
    *(float*)(smem + A_GV + w * 256 + (lane & 7) * 32 + (lane >> 3) * 4) = (lane & 8) ? q1 : q0;
    if (arrive_last(smem + L_CNT, 8) && idx < 24) {
      float s = 0.f;
#pragma unroll
      for (int i = 0; i < 8; i++) s += *(const float*)(smem + A_GV + i * 256 + lane * 4);
      st8(ws + WS_MG + (cg & 3) * 24576 + (cg >> 2) * 512, lane * 8, u32x2{f2u(s), tag});
    }
  };
  TS(4);
  const u32 gcode = __builtin_amdgcn_perm(0x400b0916u, 0x0a201408u, (r & 7) | 0x0c0c0c00u);
  const int gx = r < 8 ? 2 * r : (int)((gcode >> 2) * 256 + (gcode & 3) * 4);
  {
    asm volatile("" ::: "memory");
    const float mF = (float)(16 * r), mX = (float)gx;
    const char* tp = tab + g * 8 * 48;
    const char* tq = tp + (r < 8 ? 16 : 32);
    f32x4 acc[3];
#pragma unroll
    for (int s = 0; s < 3; s++) acc[s] = f32x4{0.f, 0.f, 0.f, 0.f};
#pragma unroll
    for (int ks = 0; ks < 2; ks++) {
      u32x4 Ff, Gf[3];
#pragma unroll
      for (int i = 0; i < 4; i++) {
        const f32x4 s0 = *(const f32x4*)(tp + (ks * 4 + i) * 48), s1 = *(const f32x4*)(tq + (ks * 4 + i) * 48);
        const float e0 = __builtin_amdgcn_exp2f(s0[0] * mF), p0 = __builtin_amdgcn_fractf(s0[1] * mF);
        const float zx = e0 * __builtin_amdgcn_cosf(p0), zy = e0 * __builtin_amdgcn_sinf(p0);
        Ff[i] = pk(s0[2] * zx - s0[3] * zy, s0[2] * zy + s0[3] * zx);
        const float e1 = __builtin_amdgcn_exp2f(s0[0] * mX), p1 = __builtin_amdgcn_fractf(s0[1] * -mX);
        const float xx = e1 * __builtin_amdgcn_cosf(p1), xy = e1 * __builtin_amdgcn_sinf(p1);
        Gf[0][i] = pk(xx, xy);
        Gf[1][i] = pk(xx * s1[0] - xy * s1[1], xx * s1[1] + xy * s1[0]);
        Gf[2][i] = pk(xx * s1[2] - xy * s1[3], xx * s1[3] + xy * s1[2]);
      }
#pragma unroll
      for (int s = 0; s < 3; s++) acc[s] = mfma16(Ff, Gf[s], acc[s]);
      if (ks == 0) { TS(45); gemv(); TS(44); }
    }
#pragma unroll
    for (int s = 0; s < 3; s++) *(f32x4*)(smem + A_P + w * 3072 + s * 1024 + lane * 16) = acc[s];
  }
  TS(5);
  u32x4 xg4[4];
  u32x2 mg, mh, mc[4];
  const char* xgb = ws + WS_XG + (size_t)(p >> 1) * 32768;
  const u32 xgo = (tid >> 2) * 256 + (tid & 3) * 16;
  u32 mga = (4 - ((lane >> 3) & 1)) * 512 + d0 + (lane & 7), mgb = ((0x5210 >> ((lane & 6) * 2)) & 7) * 512 + 2 * p + (lane & 1);
  tie(mga); tie(mgb);
  const u32 mgo = ((lane & 16) ? mgb : mga) * 8 + (lane >> 5) * 24576;
  auto lookx = [&]() NOPK {
    ld16<0>(xg4[0], xgb, xgo); ld16<64>(xg4[1], xgb, xgo); ld16<128>(xg4[2], xgb, xgo); ld16<192>(xg4[3], xgb, xgo);
  };
  auto lookm = [&]() NOPK {
    ld8(mg, ws + WS_MG, mgo); ld8(mh, ws + WS_MG + 49152, mgo);
  };
  auto lookc = [&]() NOPK {
#pragma unroll
    for (int q = 0; q < 4; q++) ld8(mc[q], ws + WS_MG + q * 24576 + 16384, tid * 8);
  };
  lookx();
  lds_barrier();
  TS(6);
  TSF(0, 6); TSF(41, 41); TSF(44, 45);
  const int kch = tid >> 8;
  u16* Kc = (u16*)(smem + A_KC + kch * A_KCS);
  if ((w & 3) < 3) {
    const int s = w & 3;
    const char* pp = smem + A_P + kch * 12288 + s * 1024 + lane * 16;
    const f32x4 q = (*(const f32x4*)(pp) + *(const f32x4*)(pp + 3072)) + (*(const f32x4*)(pp + 6144) + *(const f32x4*)(pp + 9216));
    const int pos = gx + 64 * g + (r < 8 ? (s == 0 ? 0 : s == 1 ? 1 : 256) : s * 256);
    const u32 o0 = pk(q[0], q[1]), o1 = pk(q[2], q[3]);
    if (pos <= 2048) Kc[pos] = (u16)o0;
    if (pos < 2048) {
      Kc[pos + 16] = (u16)(o0 >> 16);
      Kc[pos + 32] = (u16)o1;
      Kc[pos + 48] = (u16)(o1 >> 16);
    }
  }
  wait_vm<4>();
#pragma unroll
  for (int i = 0; i < 8; i++) tie(v[i]);
  {
    u32x4 o0, o1;
#pragma unroll
    for (int j = 0; j < 4; j++) {
      o0[j] = __builtin_amdgcn_perm(v[2 * j + 1], v[2 * j], 0x05040100u);
      o1[j] = __builtin_amdgcn_perm(v[2 * j + 1], v[2 * j], 0x07060302u);
    }
    st16x2_56(ws + WS_W1F + (size_t)(fb * 7 * 32 + (d0 >> 4)) * 1024 + (w & 1) * 512 + mat * 128, (lane >> 3) * 32768 + (lane & 7) * 16, o0, o1);
  }
  lds_barrier();
  TS(7);
  lookm(); lookc();
  {
    u32 e[6];
    const int ki = tid & 255;
#pragma unroll
    for (int j = 0; j < 6; j++) {
      const float l0 = (float)(2047 - 8 * ki - 2 * j);
      const u32 bits = pk(fmaxf(l0, 0.f), fmaxf(l0 - 1.f, 0.f));
      e[j] = (u32)Kc[(int)bflo(bits)];
      if (j < 5) e[j] |= (u32)Kc[(int)bfhi(bits)] << 16;
    }
    if (ki == 255) { e[4] = 0; e[5] = 0; }
    const u32 a0 = __builtin_amdgcn_alignbit(e[1], e[0], 16), a1 = __builtin_amdgcn_alignbit(e[2], e[1], 16);
    const u32 a2 = __builtin_amdgcn_alignbit(e[3], e[2], 16), a3 = __builtin_amdgcn_alignbit(e[4], e[3], 16);
    const u32 a4 = __builtin_amdgcn_alignbit(e[5], e[4], 16);
    char* kc = smem + B_KC + kch * B_KCH + ki * 16;
    *(u32x4*)(kc) = u32x4{e[0], e[1], e[2], e[3]};
    *(u32x4*)(kc + B_KCS) = u32x4{a0, a1, a2, a3};
    *(u32x4*)(kc + 2 * B_KCS) = u32x4{e[1], e[2], e[3], e[4]};
    *(u32x4*)(kc + 3 * B_KCS) = u32x4{a1, a2, a3, a4};
  }
  TS(8);
  wait_vm<0>(); tie(xg4[0]); tie(xg4[1]); tie(xg4[2]); tie(xg4[3]); tie(mg); tie(mh); tie(mc[0]); tie(mc[1]); tie(mc[2]); tie(mc[3]);
  if (arrive_last(smem + L_CNT + 4, 8)) st4one(ws + WS_FW + (fb * 16 + mat * 8 + dq) * 128, tag);
  TS(47);
#pragma unroll 1
  for (int it = 0; it < SPIN_CAP && !all_lanes(xg4[0][3] == tag && xg4[1][3] == tag && xg4[2][3] == tag && xg4[3][3] == tag); it++) {
    lookx();
    wait_vm<0>(); tie(xg4[0]); tie(xg4[1]); tie(xg4[2]); tie(xg4[3]);
  }
#pragma unroll 1
  for (int it = 0; it < SPIN_CAP && !all_lanes(mg[1] == tag && mh[1] == tag); it++) {
    lookm();
    wait_vm<0>(); tie(mg); tie(mh);
  }
  TS(9);
  const int tt = (xg & 1) * 8 + (idx & 7), fs = (xg >> 1) * 4 + (idx >> 3);
  u32 fw;
  const int b2 = b < 224 ? b : 223;
  const u32 fwo = fs * 2048 + (lane & 15) * 128;
  const int g8 = tid >> 1, cc = tid & 1;
  const float mq = u2f(mg[0]) + u2f(mh[0]);
  const float mv = rbf(rs32(mq, mq)), mv1 = rbf(1.f + mv);
  const float ga = sel2(cc, rdl(mv, 20), rdl(mv, 21));
  u32x4 hp, xv;
  {
    const float sh0 = rdl(mv, 16), sh1 = rdl(mv, 17), sc0 = rdl(mv1, 18), sc1 = rdl(mv1, 19);
    const u32 psel = cc ? 0x03020706u : 0x05040100u;
    const bool up = tid & 2;
    u32 ph[4], px[4];
#pragma unroll
    for (int i = 0; i < 4; i++) {
      const u32 xq = (p & 1) ? xg4[i][1] : xg4[i][0];
      const float m = bflo(xg4[i][2]), rs = __builtin_amdgcn_rcpf(bfhi(xg4[i][2]));
      const u32 n = pk((bflo(xq) - m) * rs, (bfhi(xq) - m) * rs);
      const u32 hq = pk(fmaf(bflo(n), sc0, sh0), fmaf(bfhi(n), sc1, sh1));
      ph[i] = __builtin_amdgcn_perm(swap1(hq), hq, psel);
      px[i] = __builtin_amdgcn_perm(swap1(xq), xq, psel);
    }
#pragma unroll
    for (int j = 0; j < 2; j++) {
      const u32 rh = swap2(up ? ph[j] : ph[2 + j]), kh = up ? ph[2 + j] : ph[j];
      hp[2 * j] = up ? rh : kh; hp[2 * j + 1] = up ? kh : rh;
      const u32 rx = swap2(up ? px[j] : px[2 + j]), kx = up ? px[2 + j] : px[j];
      xv[2 * j] = up ? rx : kx; xv[2 * j + 1] = up ? kx : rx;
    }
    char* u = smem + B_U + cc * B_UCH + 1024 + g8 * 16;
    *(u32x4*)(u) = hp;
    *(u32x2*)(u + B_US + 8) = u32x2{hp[0], hp[1]};
    *(u32x2*)(u + B_US + 16) = u32x2{hp[2], hp[3]};
  }
#pragma unroll 1
  for (int it = 0; it < SPIN_CAP && !all_lanes(mc[0][1] == tag && mc[1][1] == tag && mc[2][1] == tag && mc[3][1] == tag); it++) {
    lookc();
    wait_vm<0>(); tie(mc[0]); tie(mc[1]); tie(mc[2]); tie(mc[3]);
  }
  {
    const float sc = 1.f + rbf((u2f(mc[0][0]) + u2f(mc[2][0])) + (u2f(mc[1][0]) + u2f(mc[3][0])));
    const u32 e = pk(sc, sc) & ((tid & 1) ? 0xffff0000u : 0xffffu);
    *(u32*)(smem + B_SC + tid * 4) = e;
    *(u32*)(smem + B_SC + 2048 + tid * 4) = e;
  }
  TS(10);
  lds_barrier();
  TS(11);
  const int h = lane >> 5, c = lane & 31;
  const int tt2 = xg * 2 + (idx >> 4), sel = (idx >> 3) & 1, dt = idx & 7;
  const int dl = (c & 3) + 4 * (c >> 3) + 16 * ((c >> 2) & 1);
  const int rlo = ((dt ^ sel) & 1) * 7168, rhi = 7168 - rlo;
  const char* w2b = ws + WS_W2F + ((size_t)(dt * 2) * 112 + w * 14) * 1024;
  const u32 w2o = lane * 16;
  const int rot = (2 * (idx >> 3) + idx) & 7;
  const int ft = fs * 7 + w - 1;
  u32x4 A[32];
  const u32 ao = lane * 16;
  auto lda = [&](int i) NOPK {
    const char* wq = ws + WS_W1F + (size_t)ft * 32768 + (((i >> 2) + rot) & 7) * 4096;
    asm volatile("s_cmp_eq_u32 %4, 0\n\ts_cbranch_scc1 .Lna%=\n\tglobal_load_dwordx4 %0, %1, %2 offset:%3 sc1\n.Lna%=:"
                 : "=v"(A[i]) : "v"(ao), "s"(wq), "n"((i & 3) * 1024), "s"(w) : "memory", "scc");
  };
  const char* tb = smem + B_SC + rot * 256 + h * 32;
  u32x4 tq[3][2];
  auto ldt = [&](int i) NOPK { tq[i % 3][0] = *(const u32x4*)(tb + i * 64); tq[i % 3][1] = *(const u32x4*)(tb + i * 64 + 16); };
  auto scl = [&](int i) NOPK { tie(A[i]); scale8(A[i], tq[i % 3][0], tq[i % 3][1], w); tie(A[i]); };
  auto scl_run = [&](int lo, int hi) NOPK {
#pragma unroll
    for (int i = lo; i < hi + 2; i++) {
      if (i < hi) ldt(i);
      __builtin_amdgcn_sched_barrier(0);
      if (i >= lo + 2) scl(i - 2);
      __builtin_amdgcn_sched_barrier(0);
    }
  };
  {
    const int ch = w & 1, vh = (w >> 1) & 1, tg = w >> 2;
    const char* pa = smem + B_KC + ch * B_KCH + (3 - (r & 3)) * B_KCS + 2 * (2040 - 8 * (r >> 2) + 8 * g) - 512 * 7 - 64 * (4 * vh + 3);
    const char* pb = smem + B_U + ch * B_UCH + ((r & 1) ? 0 : B_US) + 2 * (512 + 32 * (r >> 1) + 8 * g) - 64 * (4 * vh + 3) - 512 * tg;
    f32x4 acc[4];
#pragma unroll
    for (int i = 0; i < 4; i++) acc[i] = f32x4{0.f, 0.f, 0.f, 0.f};
    u32x4 fr[2][8];
    auto fld = [&](int st, int n) NOPK {
      const int e = n == 0 ? 0 : n < 5 ? 9 - 2 * n : 2 * n - 8;
      const bool isa = ((e ^ st) & 1) == 0;
      fr[st & 1][e] = *(const u32x4*)((isa ? pa + 512 * (7 - e) : pb + 512 * e) + 64 * (3 - (st >> 1)));
    };
#pragma unroll
    for (int n = 0; n < 8; n++) fld(0, n);
    __builtin_amdgcn_sched_barrier(0);
    {
      f32x4 sums = {0.f, 0.f, 0.f, 0.f};
#pragma unroll
      for (int j = 0; j < 4; j++) {
        const float k0 = rdl(mv1, 2 * j), k1 = rdl(mv1, 2 * j + 1), s0 = rdl(mv, 8 + 2 * j), s1 = rdl(mv, 8 + 2 * j + 1);
        const float a0 = bflo(v[2 * j]), a1 = bflo(v[2 * j + 1]), b0 = bfhi(v[2 * j]), b1 = bfhi(v[2 * j + 1]);
        sums[0] = dot2(pk(a0 * k0, a1 * k1), 0x3f803f80u, sums[0]);
        sums[1] = dot2(pk(b0 * k0, b1 * k1), 0x3f803f80u, sums[1]);
        sums[2] = fmaf(a0, s0, fmaf(a1, s1, sums[2]));
        sums[3] = fmaf(b0, s0, fmaf(b1, s1, sums[3]));
      }
      *(f32x4*)(smem + B_RED + tid * 16) = sums;
    }
    ld4(fw, ws + WS_FW, fwo);
#pragma unroll
    for (int i = 0; i < 2; i++) {
      const u16* wq = w2 + (size_t)(b2 * 8 + i * 4) * D;
      const u32 to = ((tid & ~31) + dl) * 2;
      lp2<0>(t2[4 * i], wq, to); lp2<1024>(t2[4 * i + 1], wq, to); lp2<2048>(t2[4 * i + 2], wq, to); lp2<3072>(t2[4 * i + 3], wq, to);
    }
    __builtin_amdgcn_sched_barrier(0);
#pragma unroll
    for (int st = 0; st < 8; st++) {
      if (st == LST) {
        wait_vm<8>(); tie(fw);
        spin4(fw, tag, ws + WS_FW, fwo);
        TS(46);
      }
#pragma unroll
      for (int n = 0; n < 10; n++) {
        const int ev = n < 4 ? 0 : n < 7 ? 2 : n < 9 ? 4 : 6, ai = n < 4 ? n : n < 7 ? n - 4 : n < 9 ? n - 7 : 0, od = 7 - 2 * ai - ev;
        if (st < 7 && n < 8) fld(st + 1, n);
        __builtin_amdgcn_sched_barrier(0);
        acc[ai] = (st & 1) ? mfma16(fr[1][od], fr[1][ev], acc[ai]) : mfma16(fr[0][ev], fr[0][od], acc[ai]);
        const int sl = st - LST, ss = sl - SLAG;
        if (sl >= 0 && n < 8 && (n & 1) == 0) lda(4 * sl + n / 2);
        if (ss >= 0 && n < 8 && (n & 1) == 0) ldt(4 * ss + n / 2);
        if (ss >= 0 && n > 2 && (n & 1) == 1) { if (n < 9) wait_vm<4 * SLAG + 1>(); else wait_vm<4 * SLAG>(); scl(4 * ss + (n - 3) / 2); }
        __builtin_amdgcn_sched_barrier(0);
      }
    }
    TS(12);
    float* Y = (float*)(smem + B_Y) + (vh * 2 + ch) * 2560 + 40 * (r >> 1) + 4 * (r & 1) + 8 * g - 320 * tg;
#pragma unroll
    for (int i = 0; i < 4; i++) *(f32x4*)(Y + 320 * (7 - 2 * i)) = acc[i];
  }
  lds_barrier();
  TS(13);
  TSF(7, 13); TSF(46, 47);
  {
    const float* Y = (const float*)(smem + B_Y) + cc * 2560 + 8 * g8 + 8 * (g8 >> 2);
    const f32x4 ya = *(const f32x4*)Y + *(const f32x4*)(Y + 5120), yb = *(const f32x4*)(Y + 4) + *(const f32x4*)(Y + 5124);
    const float dk = cc ? bfhi(dsk) : bflo(dsk);
    u32x4 o;
#pragma unroll
    for (int j = 0; j < 4; j++) {
      const float y0 = j < 2 ? ya[2 * j] : yb[2 * j - 4], y1 = j < 2 ? ya[2 * j + 1] : yb[2 * j - 3];
      o[j] = pk(fmaf(ga, fmaf(bflo(hp[j]), dk, y0), bflo(xv[j])), fmaf(ga, fmaf(bfhi(hp[j]), dk, y1), bfhi(xv[j])));
    }
    st16(ws + WS_X1 + p * 128, (g8 >> 2) * 32768 + cc * 64 + (g8 & 3) * 16, o);
  }
  TS(14);
  constexpr int NA = 4 * (8 - LST), S0 = NA - 4 * SLAG, S1 = S0 + SPRE;
  wait_vm<NA - S1 + 1>();
  if (w == 0) wait_vm<1>();
  {
#pragma unroll
    for (int i = 0; i < 8; i++) tie(t2[i]);
    u32x4 o0;
#pragma unroll
    for (int j = 0; j < 4; j++) o0[j] = __builtin_amdgcn_perm(t2[2 * j + 1], t2[2 * j], 0x05040100u);
    st16(ws + WS_W2F + b2 * 512, ((tid >> 5) * 112 * 64 + c) * 16, o0);
  }
  f32x2* raw = (f32x2*)(smem + C_RAW);
  u32x4 W2x[4];
  u32 fh;
  const char* xa = ws + WS_X1 + tt * 131072 + (w & 1) * 32768 + (((w >> 1) + rot) & 7) * 4096;
  const char* xc = ws + WS_X1 + tt * 131072 + (w & 1) * 32768 + (((w >> 1) + rot + 4) & 7) * 4096;
  const u32 lpc = (u32)(size_t)(LDS_AS char*)smem + (w & 1) * 32768 + (w >> 1) * 4096;
  auto ldw2 = [&](int j) NOPK {
    const char* q = w2b + (j / 14) * 114688 + (j % 14 < 7 ? rlo : rhi);
    if (j < 24) ld16k(A[j + j / 7], q, j % 7, w2o); else ld16k(W2x[j - 24], q, j % 7, w2o);
  };
  if (w == 0) {
    f32x4 s = {0.f, 0.f, 0.f, 0.f};
#pragma unroll
    for (int q = 0; q < 8; q++) s += *(const f32x4*)(smem + B_RED + q * 1024 + lane * 16);
    st16(ws + WS_CD + ((dq * 16 + fb) * 2 + mat) * 1024, lane * 16, __builtin_bit_cast(u32x4, s));
    st8m<1>(ws + WS_GB + p * 8, 0, u32x2{f2u(rdl(mv, 22)), f2u(rdl(mv, 23))});
  }
  scl_run(S0, S1);
  wait_vm<0>();
  TS(15);
  bar();
  if (w == 0) st4one(ws + WS_FX + (b & 63) * 128 + (b >> 6) * 4, tag);
  TS(16);
  const int te = tt2 * 4 + sel, tl = tt2 * 4 + 3 - sel;
  scl_run(S1, S1 + 2);
#pragma unroll
  for (int i = NA; i < 32; i++) lda(i);
  scl_run(S1 + 2, NA);
  wait_vm<0>();
  scl_run(NA, 32);
  if (w == 0) {
    __builtin_amdgcn_s_setprio(3);
#pragma unroll 1
    for (int it = 0; it < SPIN_CAP; it++) {
      u32x4 f;
      ld16(f, ws + WS_FX, lane * 128); wait_vm<0>(); tie(f);
      if (all_lanes(f[0] == tag && f[1] == tag && f[2] == tag && f[3] == tag)) break;
    }
    TS(17);
    bar();
    TS(18);
    glds4k(xa, lane * 16, lpc); glds4k(xc, lane * 16, lpc + 16384);
    const char* rb = smem + (lane >> 2) * 64 + (lane & 3) * 8;
    f32x4 sg[2][2], sx[2][2];
#pragma unroll
    for (int i = 0; i < 4; i++) { sg[i >> 1][i & 1] = f32x4{0.f, 0.f, 0.f, 0.f}; sx[i >> 1][i & 1] = f32x4{0.f, 0.f, 0.f, 0.f}; }
    wait_vm<4>(); bar();
    TS(19); TC(20);
    TSF(14, 20);
    glds4k(xa + 65536, lane * 16, lpc + 65536); glds4k(xc + 65536, lane * 16, lpc + 65536 + 16384);
    glds4k(ws + WS_X1 + te * 32768 + dt * 4096, lane * 16, (u32)(size_t)(LDS_AS char*)smem + D_RES);
    glds4k(ws + WS_X1 + tl * 32768 + dt * 4096, lane * 16, (u32)(size_t)(LDS_AS char*)smem + D_RES + 4096);
    asm volatile("s_mov_b32 m0, %2\n\ts_nop 0\n\tglobal_load_lds_dword %0, %1 sc1"
                 ::"v"(lane * 4), "s"(ws + WS_GB + dt * 256), "s"((u32)(size_t)(LDS_AS char*)smem + D_GB) : "memory");
    if (b == 0) st4one(ws + WS_E, tag);
    ln2_acc(rb, 0, 8, sg, sx);
    TS(21);
    wait_vm<17>(); bar();
    TS(22);
#pragma unroll
    for (int i = 0; i < 28; i++) ldw2(i);
    ln2_acc(rb, 8, 16, sg, sx);
    ln2_put(raw, raw + 132, lane, sg[0], sx[0]); ln2_put(raw + 32, raw + 132, lane, sg[1], sx[1]);
    TS(23);
    wait_vm<28>();
    lds_barrier();
    TS(24);
#pragma unroll
    for (int i = 0; i < 4; i++) { sg[i >> 1][i & 1] = f32x4{0.f, 0.f, 0.f, 0.f}; sx[i >> 1][i & 1] = f32x4{0.f, 0.f, 0.f, 0.f}; }
    ln2_acc(rb + 65536, 0, 16, sg, sx);
    ln2_put(raw + 64, raw + 132, lane, sg[0], sx[0]); ln2_put(raw + 96, raw + 132, lane, sg[1], sx[1]);
    asm volatile("" ::: "memory");
    *(volatile LDS_AS u32*)(smem + L_CNT + 16) = 1;
    TS(25);
    __builtin_amdgcn_s_setprio(0);
    TS(26); TS(27); TC(28);
    TSF(21, 28);
    wait_vm<0>();
#pragma unroll
    for (int i = 0; i < 28; i++) { if (i < 24) tie(A[i + i / 7]); else tie(W2x[i - 24]); }
    TS(29);
  }
  int w_ = w;
  asm volatile("" : "+s"(w_));
  if (w_ != 0) {
    const char* rb = smem + h * 512 + ((lane >> 2) & 3) * 64 + ((lane >> 4) & 1) * 32 + (lane & 3) * 8;
    const char* rb2 = rb + 65536;
    u32 cdv[8];
    f32x16 acc[4];
#pragma unroll
    for (int T = 0; T < 4; T++)
#pragma unroll
      for (int i = 0; i < 16; i++) acc[T][i] = 0.f;
    TS(17);
    lds_barrier();
    TS(18);
    glds4k(xa, lane * 16, lpc); glds4k(xc, lane * 16, lpc + 16384);
    wait_vm<4>();
    bar();
    TS(19); TC(20);
    TSF(14, 20);
    u32x4 Bc[2], Bn[2];
    Bc[0] = tr8(rb); Bc[1] = tr8(rb + 32768);
    f32x2 rw[2];
#pragma unroll
    for (int ks = 0; ks < 32; ks++) {
      if (ks == 15) {
        TS(21);
        wait_vm<16>();
        bar();
        TS(22);
      } else if (ks == 31) {
        TS(23);
        wait_vm<0>();
#pragma unroll
        for (int q = 0; q < 8; q++) tie(cdv[q]);
        bar();
        TS(24);
      }
      if (ks < 31) {
        Bn[0] = tr8(rb + (ks + 1) * 1024); Bn[1] = tr8(rb + 32768 + (ks + 1) * 1024);
      } else {
        Bn[0] = tr8(rb2); Bn[1] = tr8(rb2 + 32768);
        rw[0] = raw[c]; rw[1] = raw[32 + c];
      }
      __builtin_amdgcn_sched_barrier(0);
      acc[0] = mfma32(A[ks], Bc[0], acc[0]);
      acc[1] = mfma32(A[ks], Bc[1], acc[1]);
      __builtin_amdgcn_sched_barrier(0);
      if (ks == 3) glds4k(xa + 65536, lane * 16, lpc + 65536);
      if (ks == 7) {
        const u32 co = ((c >> 3) & 1) * 1024 + ((w - 1) * 8 + (c & 7)) * 16 + h * 8 + (c >> 4) * 4;
#pragma unroll
        for (int q = 0; q < 8; q++) ld4(cdv[q], ws + WS_CD + (q * 16 + fs) * 2048, co);
      }
      if (ks == 11) glds4k(xc + 65536, lane * 16, lpc + 65536 + 16384);
      Bc[0] = Bn[0]; Bc[1] = Bn[1];
    }
    const float cd = ((u2f(cdv[0]) + u2f(cdv[1])) + (u2f(cdv[2]) + u2f(cdv[3]))) + ((u2f(cdv[4]) + u2f(cdv[5])) + (u2f(cdv[6]) + u2f(cdv[7])));
    const float chi = rbf(cd);
    const u32x4 Ax = {pk(chi, chi), pk(cd - chi, cd - chi), 0, 0};
    const char* hb = ws + WS_HF + ((size_t)(tt * 4) * 112 + ft) * 1024;
    Ep q[4];
    float o[8];
#pragma unroll
    for (int s = 0; s < 32; s++) {
      const int n = s + 1;
      const int ta = s < 28 ? 2 : 3, tb_ = s < 24 ? 3 : ta, ka = ksl(s < 24 ? s : 24 + 2 * (s & 3)), kb = ksl(s < 24 ? s : 25 + 2 * (s & 3));
      if (s == 21) {
        TS(25);
#pragma unroll 1
        for (int it = 0; it < SPIN_CAP * 16; it++)
          if (__builtin_amdgcn_readfirstlane(*(volatile LDS_AS u32*)(smem + L_CNT + 16)) == 1) break;
        asm volatile("" ::: "memory");
        TS(26);
        rw[0] = raw[64 + c]; rw[1] = raw[96 + c];
      }
      if (n < 24) {
        Bn[0] = tr8(rb2 + ksl(n) * 1024); Bn[1] = tr8(rb2 + 32768 + ksl(n) * 1024);
      } else if (n < 32) {
        const char* rp = rb2 + (n < 28 ? 0 : 32768);
        Bn[0] = tr8(rp + ksl(24 + 2 * (n & 3)) * 1024); Bn[1] = tr8(rp + ksl(25 + 2 * (n & 3)) * 1024);
      }
      __builtin_amdgcn_sched_barrier(0);
      acc[ta] = mfma32(A[ka], Bc[0], acc[ta]);
      if (s < 2 || s == 22 || s == 23) {
        const int T = s < 2 ? s : s - 20;
        const u32 be = ln2_fold(rw[T & 1], h, q[T]);
        acc[T] = mfma32(Ax, u32x4{be, be, 0, 0}, acc[T]);
      }
      const int j = s >= 3 && s < 11 ? 2 * (s - 3) : s >= 28 ? 16 + 2 * (s - 28) : -1;
      if (j >= 0) o[j & 7] = swi(acc[j >> 3], j & 7, q[j >> 3]);
      __builtin_amdgcn_sched_barrier(0);
      acc[tb_] = mfma32(A[kb], Bc[1], acc[tb_]);
      if (j >= 0) {
        o[(j + 1) & 7] = swi(acc[j >> 3], (j + 1) & 7, q[j >> 3]);
        if ((j & 7) == 6) st16(hb + (j >> 3) * 114688, lane * 16, u32x4{pk(o[0], o[4]), pk(o[1], o[5]), pk(o[2], o[6]), pk(o[3], o[7])});
      }
      __builtin_amdgcn_sched_barrier(0);
      if (s < 28) ldw2(s);
      if (s == 27) {
        wait_vm<18>();
        if (arrive_last(smem + L_CNT + 8, 7)) st4one(ws + WS_FH + (tt * 16 + fs) * 128, tag);
        TS(27);
      }
      Bc[0] = Bn[0]; Bc[1] = Bn[1];
    }
    TC(28);
    TSF(21, 28);
#pragma unroll
    for (int i = 0; i < 8; i++) o[i] = swi(acc[3], i, q[3]);
    st16(hb + 3 * 114688, lane * 16, u32x4{pk(o[0], o[4]), pk(o[1], o[5]), pk(o[2], o[6]), pk(o[3], o[7])});
    wait_vm<2>();
#pragma unroll
    for (int i = 0; i < 28; i++) { if (i < 24) tie(A[i + i / 7]); else tie(W2x[i - 24]); }
    TS(29);
  }
  {
    const char* hbe = ws + WS_HF + ((size_t)te * 112 + w * 14) * 1024;
    const char* hbl = ws + WS_HF + ((size_t)tl * 112 + w * 14) * 1024;
    const int u = lane >> 2, tk = w * 4 + (lane & 3);
    u32 xe[4], xl[4];
    u32x4 gbv;
    u32x4 H[14];
    ld4(fh, ws + WS_FH + tt2 * 2048, (lane & 15) * 128);
    wait_vm<0>(); tie(fh);
    if (w > 0 && arrive_last(smem + L_CNT + 12, 7)) st4one(ws + WS_FH + 32768 + (tt * 16 + fs) * 128, tag);
    TS(30);
    spin4(fh, tag, ws + WS_FH + tt2 * 2048, (lane & 15) * 128);
    TS(31);
#pragma unroll
    for (int i = 0; i < 14; i++) ld16k(H[i], hbe + (i < 7 ? rlo : rhi), i % 7, lane * 16);
    ld4(fh, ws + WS_FH + 32768 + tt2 * 2048, (lane & 15) * 128);
    TS(32);
    f32x16 acc[2];
#pragma unroll
    for (int i = 0; i < 16; i++) { acc[0][i] = 0.f; acc[1][i] = 0.f; }
    char* wr = smem + w * D_SRC + h * 4 * 576 + c * 16;
    const char* rd = smem + u * 576 + tk * 16;
#pragma unroll
    for (int i = 0; i < 14; i++) {
      if (i == 0 || i == 4 || i == 9) {
        __builtin_amdgcn_sched_barrier(0);
        if (i == 0) wait_vm<11>(); else if (i == 4) wait_vm<6>(); else wait_vm<1>();
#pragma unroll
        for (int k = i; k < (i == 0 ? 4 : i == 4 ? 9 : 14); k++) tie(H[k]);
      }
      acc[0] = mfma32(A[i + i / 7], H[i], acc[0]);
      acc[1] = mfma32(i < 10 ? A[16 + i + i / 7] : W2x[i - 10], H[i], acc[1]);
    }
    __builtin_amdgcn_sched_barrier(0);
    wait_vm<0>(); tie(fh);
    TS(33);
    spin4(fh, tag, ws + WS_FH + 32768 + tt2 * 2048, (lane & 15) * 128);
    TS(34);
#pragma unroll
    for (int i = 0; i < 14; i++) ld16k(H[i], hbl + (i < 7 ? rlo : rhi), i % 7, lane * 16);
    __builtin_amdgcn_sched_barrier(0);
    TS(35);
    bar();
#pragma unroll
    for (int j = 0; j < 4; j++) {
      xe[j] = *(const u16*)(smem + D_RES + (u * 4 + j) * 64 + tk * 2);
      xl[j] = *(const u16*)(smem + D_RES + 4096 + (u * 4 + j) * 64 + tk * 2);
    }
    gbv = *(const u32x4*)(smem + D_GB + u * 16);
#pragma unroll
    for (int di = 0; di < 2; di++)
#pragma unroll
      for (int q = 0; q < 4; q++)
        *(f32x4*)(wr + (di * 8 + q) * 576) = f32x4{acc[di][4 * q], acc[di][4 * q + 1], acc[di][4 * q + 2], acc[di][4 * q + 3]};
    __builtin_amdgcn_sched_barrier(0);
    TS(36);
    TSF(29, 36);
    auto finish = [&](const char* rp, const u32 (&xq)[4]) NOPK {
      f32x4 s = *(const f32x4*)rp;
#pragma unroll
      for (int q = 1; q < 8; q++) s += *(const f32x4*)(rp + q * D_SRC);
      float y[4];
#pragma unroll
      for (int j = 0; j < 4; j++) y[j] = fmaf(u2f(gbv[j]), s[j], bflo(xq[j]));
      return u32x2{pk(y[0], y[1]), pk(y[2], y[3])};
    };
#pragma unroll
    for (int i = 0; i < 16; i++) { acc[0][i] = 0.f; acc[1][i] = 0.f; }
#pragma unroll
    for (int i = 0; i < 14; i++) {
      if (i == 0 || i == 4 || i == 9) {
        __builtin_amdgcn_sched_barrier(0);
        if (i == 0) wait_vm<10>(); else if (i == 4) wait_vm<5>(); else wait_vm<0>();
#pragma unroll
        for (int k = i; k < (i == 0 ? 4 : i == 4 ? 9 : 14); k++) tie(H[k]);
      }
      acc[0] = mfma32(A[i + i / 7], H[i], acc[0]);
      acc[1] = mfma32(i < 10 ? A[16 + i + i / 7] : W2x[i - 10], H[i], acc[1]);
    }
    TS(37);
#pragma unroll
    for (int di = 0; di < 2; di++)
#pragma unroll
      for (int q = 0; q < 4; q++)
        *(f32x4*)(wr + D_TILE + (di * 8 + q) * 576) = f32x4{acc[di][4 * q], acc[di][4 * q + 1], acc[di][4 * q + 2], acc[di][4 * q + 3]};
    lds_barrier();
    TS(38);
    *(u32x2*)(out + (size_t)(tl * 32 + tk) * D + dt * 64 + u * 4) = finish(rd + D_TILE, xl);
    *(u32x2*)(out + (size_t)(te * 32 + tk) * D + dt * 64 + u * 4) = finish(rd, xe);
    TS(39);
    TS(40);
    TSF(37, 40);
    TSEND();
  }
}

extern "C" void kernel_launch(void* const* d_in, const int* in_sizes, int n_in, void* d_out, int out_size, void* d_ws,
                              size_t ws_size, hipStream_t stream) {
  const u16* const* in = (const u16* const*)d_in;
  fused<<<256, 512, 0, stream>>>(in[0], in[1], in[2], in[3], in[4], in[5], in[6], in[7], in[8], in[10], in[11], in[12], in[13], in[14],
                                 (char*)d_ws, (u16*)d_out);
}
```

```cpp
#include <hip/hip_runtime.h>
#include <stdint.h>

typedef unsigned int u32;
typedef unsigned short u16;
typedef __attribute__((ext_vector_type(2))) float f32x2;
typedef __attribute__((ext_vector_type(4))) float f32x4;
typedef __attribute__((ext_vector_type(16))) float f32x16;
typedef __attribute__((ext_vector_type(2))) u32 u32x2;
typedef __attribute__((ext_vector_type(4))) u32 u32x4;
typedef __attribute__((ext_vector_type(2))) __bf16 bf16x2;
typedef __attribute__((ext_vector_type(8))) __bf16 bf16x8;
typedef __attribute__((ext_vector_type(4))) short s16x4;
#define LDS_AS __attribute__((address_space(3)))
#if defined(__HIP_DEVICE_COMPILE__)
#define NOPK __attribute__((target("no-packed-fp32-ops")))
#else
#define NOPK
#endif
#define DEVINL __device__ __forceinline__ NOPK

constexpr int L = 2048, D = 512, NS = 128, F = 1792;

constexpr size_t WS_E = 0;
constexpr size_t WS_FW = 4096;
constexpr size_t WS_FX = WS_FW + 32768;
constexpr size_t WS_FH = WS_FX + 32768;
constexpr size_t WS_MG = WS_FH + 65536;
constexpr size_t WS_GB = WS_MG + 98304;
constexpr size_t WS_XG = 262144;
constexpr size_t WS_X1 = WS_XG + (4u << 20);
constexpr size_t WS_W1F = WS_X1 + (2u << 20);
constexpr size_t WS_W2F = WS_W1F + 112 * 32 * 1024;
constexpr size_t WS_HF = WS_W2F + 16 * 112 * 1024;
constexpr size_t WS_CD = WS_HF + 64 * 112 * 1024;
constexpr int SPIN_CAP = 40000;
#define SLAG 3
#define LST 2
#define SPRE 4

#define u2f(x) __builtin_bit_cast(float, (u32)(x))
#define i2f(x) __builtin_bit_cast(float, (int)(x))
#define f2u(x) __builtin_bit_cast(u32, (float)(x))
#define f2i(x) __builtin_bit_cast(int, (float)(x))
#define TSDECL
#define TS(slot)
#define TC(slot)
#define TSF(lo, hi)
#define TSEND()
DEVINL float bflo(u32 u) { return u2f(u << 16); }
DEVINL float bfhi(u32 u) { return u2f(u & 0xffff0000u); }
DEVINL u32 pk(float a, float b) {
  f32x2 v = {a, b};
  return __builtin_bit_cast(u32, __builtin_convertvector(v, bf16x2));
}
DEVINL float rbf(float a) { return bflo(pk(a, a)); }
template <int CTRL> DEVINL float dpp_add(float v) {
  return v + i2f(__builtin_amdgcn_update_dpp(0, f2i(v), CTRL, 0xf, 0xf, true));
}
DEVINL float row4_sum(float v) {
  auto a = __builtin_amdgcn_permlane16_swap(f2u(v), f2u(v), false, false);
  v = u2f(a[0]) + u2f(a[1]);
  auto b = __builtin_amdgcn_permlane32_swap(f2u(v), f2u(v), false, false);
  return u2f(b[0]) + u2f(b[1]);
}
DEVINL float rs32(float a, float b) {
  auto r = __builtin_amdgcn_permlane32_swap(f2u(a), f2u(b), false, false);
  return u2f(r[0]) + u2f(r[1]);
}
DEVINL float rs16(float a, float b) {
  auto r = __builtin_amdgcn_permlane16_swap(f2u(a), f2u(b), false, false);
  return u2f(r[0]) + u2f(r[1]);
}
DEVINL float wave_sum(float v) { return row4_sum(dpp_add<0x140>(dpp_add<0x141>(dpp_add<0x4E>(dpp_add<0xB1>(v))))); }
DEVINL f32x4 mfma16(u32x4 a, u32x4 b, f32x4 c) {
  return __builtin_amdgcn_mfma_f32_16x16x32_bf16(__builtin_bit_cast(bf16x8, a), __builtin_bit_cast(bf16x8, b), c, 0, 0, 0);
}
DEVINL f32x16 mfma32(u32x4 a, u32x4 b, f32x16 c) {
  return __builtin_amdgcn_mfma_f32_32x32x16_bf16(__builtin_bit_cast(bf16x8, a), __builtin_bit_cast(bf16x8, b), c, 0, 0, 0);
}
DEVINL float dot2(u32 a, u32 b, float c) {
  return __builtin_amdgcn_fdot2_f32_bf16(__builtin_bit_cast(bf16x2, a), __builtin_bit_cast(bf16x2, b), c, false);
}
DEVINL void lds_barrier() {
  asm volatile("s_waitcnt lgkmcnt(0)" ::: "memory");
  __builtin_amdgcn_s_barrier();
  asm volatile("" ::: "memory");
}
DEVINL void bar() {
  asm volatile("" ::: "memory");
  __builtin_amdgcn_s_barrier();
  asm volatile("" ::: "memory");
}
DEVINL void st16(const void* sbase, u32 voff, u32x4 v) {
  asm volatile("global_store_dwordx4 %0, %1, %2 sc1\n\ts_nop 1" ::"v"(voff), "v"(v), "s"(sbase) : "memory");
}
DEVINL void st8(const void* sbase, u32 voff, u32x2 v) {
  asm volatile("global_store_dwordx2 %0, %1, %2 sc1\n\ts_nop 1" ::"v"(voff), "v"(v), "s"(sbase) : "memory");
}
DEVINL void st4(const void* sbase, u32 voff, u32 v) {
  asm volatile("global_store_dword %0, %1, %2 sc1\n\ts_nop 1" ::"v"(voff), "v"(v), "s"(sbase) : "memory");
}
template <int LANES> DEVINL void st8m(const void* sbase, u32 voff, u32x2 v) {
  asm volatile("s_mov_b64 exec, %3\n\tglobal_store_dwordx2 %0, %1, %2 sc1\n\ts_mov_b64 exec, -1\n\ts_nop 0"
               ::"v"(voff), "v"(v), "s"(sbase), "n"((1 << LANES) - 1) : "memory");
}
DEVINL void st4one(const void* sbase, u32 v) {
  asm volatile("s_mov_b64 exec, 1\n\tglobal_store_dword %0, %1, %2 sc1\n\ts_mov_b64 exec, -1\n\ts_nop 0"
               ::"v"(0), "v"(v), "s"(sbase) : "memory");
}
DEVINL void st16x2_56(const void* sbase, u32 voff, u32x4 a, u32x4 b) {
  asm volatile("s_mov_b32 exec_hi, 0xffffff\n\tglobal_store_dwordx4 %0, %1, %3 sc1\n\tglobal_store_dwordx4 %0, %2, %3 offset:256 sc1\n\t"
               "s_mov_b32 exec_hi, -1\n\ts_nop 0" ::"v"(voff), "v"(a), "v"(b), "s"(sbase) : "memory");
}
DEVINL void sld4(u32& d, const void* p) { asm volatile("s_load_dword %0, %1, 0" : "=s"(d) : "s"(p) : "memory"); }
template <int OFF = 0> DEVINL void ld16(u32x4& d, const void* sbase, u32 voff) {
  asm volatile("global_load_dwordx4 %0, %1, %2 offset:%3 sc1" : "=v"(d) : "v"(voff), "s"(sbase), "n"(OFF) : "memory");
}
DEVINL void ld16k(u32x4& d, const char* sbase, int kb, u32 voff) {
  const char* sb = sbase + (kb >> 2) * 4096;
  if ((kb & 3) == 0) ld16<0>(d, sb, voff); else if ((kb & 3) == 1) ld16<1024>(d, sb, voff);
  else if ((kb & 3) == 2) ld16<2048>(d, sb, voff); else ld16<3072>(d, sb, voff);
}
DEVINL void ld8(u32x2& d, const void* sbase, u32 voff) {
  asm volatile("global_load_dwordx2 %0, %1, %2 sc1" : "=v"(d) : "v"(voff), "s"(sbase) : "memory");
}
template <int OFF = 0> DEVINL void ld4(u32& d, const void* sbase, u32 voff) {
  asm volatile("global_load_dword %0, %1, %2 offset:%3 sc1" : "=v"(d) : "v"(voff), "s"(sbase), "n"(OFF) : "memory");
}
template <int OFF = 0> DEVINL void lp16(u32x4& d, const void* sbase, u32 voff) {
  asm volatile("global_load_dwordx4 %0, %1, %2 offset:%3" : "=v"(d) : "v"(voff), "s"(sbase), "n"(OFF) : "memory");
}
template <int OFF = 0> DEVINL void lp4(u32& d, const void* sbase, u32 voff) {
  asm volatile("global_load_dword %0, %1, %2 offset:%3" : "=v"(d) : "v"(voff), "s"(sbase), "n"(OFF) : "memory");
}
template <int OFF = 0> DEVINL void lp2(u32& d, const void* sbase, u32 voff) {
  asm volatile("global_load_ushort %0, %1, %2 offset:%3" : "=v"(d) : "v"(voff), "s"(sbase), "n"(OFF) : "memory");
}
template <class T> DEVINL void tie(T& a) { asm volatile("" : "+v"(a)); }
template <int N> DEVINL void wait_vm() { asm volatile("s_waitcnt vmcnt(%0)" ::"n"(N) : "memory"); }
DEVINL bool all_lanes(bool ok) { return __builtin_amdgcn_ballot_w64(!ok) == 0; }
DEVINL void spin4(u32 f, u32 tag, const void* sbase, u32 voff) {
#pragma unroll 1
  for (int it = 0; it < SPIN_CAP && !all_lanes(f == tag); it++) {
    __builtin_amdgcn_s_sleep(1);
    ld4(f, sbase, voff); wait_vm<0>(); tie(f);
  }
}
DEVINL bool arrive_last(char* smem_word, int n) {
  u32 old;
  asm volatile("s_mov_b64 exec, 1\n\tds_add_rtn_u32 %0, %1, %2\n\ts_waitcnt lgkmcnt(0)\n\ts_mov_b64 exec, -1"
               : "=&v"(old) : "v"((u32)(size_t)(LDS_AS char*)smem_word), "v"(1) : "memory");
  return __builtin_amdgcn_readfirstlane(old) == (u32)(n - 1);
}
DEVINL void scale8(u32x4& a, const u32x4& s, const u32x4& t, int on) {
  u32 a0 = a[0], a1 = a[1], a2 = a[2], a3 = a[3], x0, x1, x2, x3;
  asm("s_cmp_eq_u32 %16, 0\n\ts_cbranch_scc1 .Lsk%=\n\tv_dot2_f32_bf16 %4, %0, %8, 0\n\tv_dot2_f32_bf16 %0, %0, %9, 0\n\tv_dot2_f32_bf16 %5, %1, %10, 0\n\tv_dot2_f32_bf16 %1, %1, %11, 0\n\t"
      "v_dot2_f32_bf16 %6, %2, %12, 0\n\tv_dot2_f32_bf16 %2, %2, %13, 0\n\tv_dot2_f32_bf16 %7, %3, %14, 0\n\tv_dot2_f32_bf16 %3, %3, %15, 0\n\t"
      "v_cvt_pk_bf16_f32 %0, %4, %0\n\tv_cvt_pk_bf16_f32 %1, %5, %1\n\tv_cvt_pk_bf16_f32 %2, %6, %2\n\ts_nop 0\n\tv_cvt_pk_bf16_f32 %3, %7, %3\n.Lsk%=:"
      : "+v"(a0), "+v"(a1), "+v"(a2), "+v"(a3), "=&v"(x0), "=&v"(x1), "=&v"(x2), "=&v"(x3)
      : "v"(s[0]), "v"(s[1]), "v"(s[2]), "v"(s[3]), "v"(t[0]), "v"(t[1]), "v"(t[2]), "v"(t[3]), "s"(on) : "scc");
  a = u32x4{a0, a1, a2, a3};
}
DEVINL u32x2 tr_read(const char* p) {
  return __builtin_bit_cast(u32x2, __builtin_amdgcn_ds_read_tr16_b64_v4i16((LDS_AS s16x4*)p));
}
DEVINL float rdl(float v, int l) { return i2f(__builtin_amdgcn_readlane(f2i(v), l)); }
DEVINL float sel2(int c, float a, float b) { return c ? b : a; }
DEVINL u32 swap2(u32 v) { return (u32)__builtin_amdgcn_update_dpp(0, (int)v, 0x4E, 0xf, 0xf, true); }
DEVINL u32 swap1(u32 v) { return (u32)__builtin_amdgcn_update_dpp(0, (int)v, 0xB1, 0xf, 0xf, true); }
constexpr float LOG2E = 1.4426950408889634f, LN2 = 0.6931471805599453f, INV2PI = 0.15915494309189535f;

constexpr int A_TAB = 0, A_P = 8 * 1536, A_KC = A_P + 24576, A_KCS = 4112, A_GV = A_KC + 2 * A_KCS;
constexpr int B_KC = 47360, B_KCS = 4160, B_KCH = 4 * 4160, B_U = B_KC + 2 * B_KCH, B_US = 5152, B_UCH = 2 * B_US;
constexpr int B_Y = B_U + 2 * B_UCH, B_RED = 4096, B_SC = 0;
constexpr int A_XT = B_Y;
constexpr int C_RAW = 148480, L_CNT = C_RAW + 1024, D_RES = L_CNT + 64, D_GB = D_RES + 8192, LDS_BYTES = D_GB + 256;
static_assert(A_GV + 2048 <= B_KC && B_Y + 40960 <= C_RAW && 131072 <= C_RAW && B_RED + 8192 <= A_KC && LDS_BYTES <= 163840, "lds map");

DEVINL void glds4k(const void* sbase, u32 voff, u32 ldsaddr) {
  asm volatile("s_mov_b32 m0, %2\n\ts_nop 0\n\t"
               "global_load_lds_dwordx4 %0, %1 sc1\n\tglobal_load_lds_dwordx4 %0, %1 offset:1024 sc1\n\t"
               "global_load_lds_dwordx4 %0, %1 offset:2048 sc1\n\tglobal_load_lds_dwordx4 %0, %1 offset:3072 sc1"
               ::"v"(voff), "s"(sbase), "s"(ldsaddr) : "memory");
}
DEVINL u32x4 tr8(const char* p) {
  const u32x2 lo = tr_read(p), hi = tr_read(p + 256);
  return u32x4{lo[0], lo[1], hi[0], hi[1]};
}
template <int OFF> DEVINL void glds1k(const void* sbase, u32 voff, u32 ldsaddr) {
  asm volatile("s_mov_b32 m0, %2\n\ts_nop 0\n\tglobal_load_lds_dwordx4 %0, %1 offset:%3 sc1" ::"v"(voff), "s"(sbase), "s"(ldsaddr), "n"(OFF) : "memory");
}
DEVINL const char* far(const char* p) {
  u32 a = (u32)(size_t)(LDS_AS const char*)p;
  asm("" : "+v"(a));
  return (const char*)(LDS_AS const char*)(size_t)a;
}
struct Ep { float q0, v; };
template <int N> struct IC { static constexpr int v = N; };
#define accg(a, i) a[((i) & 3) + 8 * ((i) >> 2)]
#define accu(a, i) a[((i) & 3) + 8 * ((i) >> 2) + 4]
DEVINL float swi(const f32x16& a, int i, const Ep& q) {
  const float e = __builtin_amdgcn_exp2f(accg(a, i) * q.q0);
  return (accg(a, i) * accu(a, i)) * __builtin_amdgcn_rcpf(fmaf(e, q.v, q.v));
}
DEVINL u32 ln2_fold(f32x2 s, int h, Ep& q) {
  const float m = s[0] * (1.f / D);
  const float v = fmaxf(fmaf(-m, m, s[1] * (1.f / D)), 0.f) + 1e-5f;
  const float rs = __builtin_amdgcn_rsqf(v);
  q.q0 = rs * -LOG2E; q.v = v;
  const float val = h ? v * rs : -m;
  const float vhi = rbf(val);
  return pk(vhi, val - vhi);
}
DEVINL void ln2_tile(const char* rb, f32x2* raw, f32x2* dummy, int lane) {
  const u32x4 ones = {0x3f803f80u, 0x3f803f80u, 0x3f803f80u, 0x3f803f80u};
  f32x4 sg[2], sx[2];
#pragma unroll
  for (int i = 0; i < 2; i++) { sg[i] = f32x4{0.f, 0.f, 0.f, 0.f}; sx[i] = f32x4{0.f, 0.f, 0.f, 0.f}; }
  u32x4 fr[4][2];
  auto rd = [&](int n) NOPK {
#pragma unroll
    for (int gi = 0; gi < 2; gi++) {
      const char* pp = rb + n * 2048 + gi * 32;
      const u32x2 lo = tr_read(pp), hi = tr_read(pp + 1024);
      fr[n & 3][gi] = u32x4{lo[0], lo[1], hi[0], hi[1]};
    }
  };
  rd(0); rd(1); rd(2);
#pragma unroll
  for (int n = 0; n < 16; n++) {
    if (n + 3 < 16) rd(n + 3);
    __builtin_amdgcn_sched_barrier(0);
#pragma unroll
    for (int gi = 0; gi < 2; gi++) {
      sg[gi] = mfma16(fr[n & 3][gi], fr[n & 3][gi], sg[gi]);
      sx[gi] = mfma16(ones, fr[n & 3][gi], sx[gi]);
    }
    __builtin_amdgcn_sched_barrier(0);
  }
#pragma unroll
  for (int gi = 0; gi < 2; gi++) {
    const float gsel = (lane & 2) ? ((lane & 1) ? sg[gi][3] : sg[gi][2]) : ((lane & 1) ? sg[gi][1] : sg[gi][0]);
    f32x2* dst = (lane >> 4) == ((lane & 15) >> 2) ? raw + gi * 16 + (lane & 15) : dummy;
    *dst = f32x2{sx[gi][0], gsel};
  }
}

__global__ NOPK __launch_bounds__(512, 2) void fused(
    const u16* __restrict__ x, const u16* __restrict__ Are, const u16* __restrict__ Aim, const u16* __restrict__ Bre,
    const u16* __restrict__ Bim, const u16* __restrict__ Cre, const u16* __restrict__ Cim, const u16* __restrict__ logdt,
    const u16* __restrict__ dskip, const u16* __restrict__ w1, const u16* __restrict__ w3, const u16* __restrict__ w2,
    const u16* __restrict__ cvec, const u16* __restrict__ wmod, char* ws, u16* __restrict__ out) {
  __shared__ __attribute__((aligned(16))) char smem[LDS_BYTES];
  const int b = blockIdx.x, tid = threadIdx.x, lane = tid & 63;
  const int w = __builtin_amdgcn_readfirstlane(tid >> 6);
  TSDECL; TS(0);
  const int xg = b & 7, idx = b >> 3;
  const int p = xg * 32 + idx;
  const int r = lane & 15, g = lane >> 4;
  asm volatile("" ::"s"(x), "s"(Are), "s"(Aim), "s"(Bre), "s"(Bim), "s"(Cre), "s"(Cim), "s"(logdt), "s"(dskip), "s"(w1), "s"(w3),
               "s"(w2), "s"(cvec), "s"(wmod), "s"(ws), "s"(out));
  u32x4 xr, wm0, wm1;
  u32 vare, vaim, vbre, vbim, vcre, vcim, c0, c1;
  const int cg = xg * 24 + (idx < 24 ? idx : 23);
  u32 e_prev, ldt2, dsk;
  lp16(xr, x + b * 4096, tid * 16);
  sld4(e_prev, ws + WS_E); sld4(ldt2, logdt + 2 * p); sld4(dsk, dskip + 2 * p);
  {
    const u32 so = (p * 256 + w * 32 + (lane & 31)) * 2;
    lp2(vare, Are, so); lp2(vaim, Aim, so); lp2(vbre, Bre, so); lp2(vbim, Bim, so); lp2(vcre, Cre, so); lp2(vcim, Cim, so);
    const u32 mo = (tid >> 3) * 6144 + (tid & 7) * 16;
    const u16* wmp = wmod + (cg & 3) * 128 * 3072 + (cg >> 2) * 64;
    lp16(wm0, wmp, mo); lp16(wm1, wmp + 64 * 3072, mo);
    lp2(c0, cvec + (cg & 3) * 128, (tid >> 3) * 2); lp2<128>(c1, cvec + (cg & 3) * 128, (tid >> 3) * 2);
  }
  TS(1);
  if (tid < 32) *(u32x4*)(smem + B_KC + (tid >> 2) * B_KCS + 4096 + (tid & 3) * 16) = u32x4{0, 0, 0, 0};
  else if (tid < 36) *(u32x4*)(smem + L_CNT + (tid - 32) * 16) = u32x4{0, 0, 0, 0};
  else if (tid < 38) *(u32x2*)(smem + B_U + (tid & 1) * B_UCH + B_US + 1024) = u32x2{0, 0};
  else if (tid >= 256) *(u32x4*)(smem + B_U + (w - 4) * B_US + lane * 16) = u32x4{0, 0, 0, 0};
  asm volatile("s_waitcnt lgkmcnt(0)" : "+s"(e_prev), "+s"(ldt2), "+s"(dsk)::"memory");
  const u32 tag = e_prev * 1664525u + 1013904223u;
  wait_vm<10>(); tie(xr);
  {
    *(u32x4*)(smem + A_XT + w * 1056 + lane * 16) = xr;
    float f[8];
#pragma unroll
    for (int j = 0; j < 4; j++) { f[2 * j] = bflo(xr[j]); f[2 * j + 1] = bfhi(xr[j]); }
    const float m = wave_sum(((f[0] + f[1]) + (f[2] + f[3])) + ((f[4] + f[5]) + (f[6] + f[7]))) * (1.f / D);
    float s = 0.f;
#pragma unroll
    for (int j = 0; j < 8; j++) { f[j] -= m; s = fmaf(f[j], f[j], s); }
    const float sd = __builtin_amdgcn_sqrtf(rbf(rbf(wave_sum(s) * (1.f / D)) + 1e-5f));
    if (lane == 0) *(u32*)(smem + A_XT + w * 1056 + 1024) = pk(m, sd);
  }
  TS(2);
  wait_vm<4>(); tie(vare); tie(vaim); tie(vbre); tie(vbim); tie(vcre); tie(vcim);
  char* tab = smem + A_TAB + w * 1536;
  {
    const float dt = rbf(__builtin_amdgcn_exp2f((w >> 2 ? bfhi(ldt2) : bflo(ldt2)) * LOG2E));
    const float a = dt * bflo(vare) * LOG2E, bq = dt * bflo(vaim) * INV2PI;
    const float br = dt * bflo(vbre), bi = dt * bflo(vbim), cr = bflo(vcre), ci = bflo(vcim);
    const float m = lane < 32 ? 1.f : 256.f;
    const float e = __builtin_amdgcn_exp2f(a * m), ph = __builtin_amdgcn_fractf(-bq * m);
    const float ex = e * __builtin_amdgcn_cosf(ph), ey = e * __builtin_amdgcn_sinf(ph);
    char* t = tab + (lane & 31) * 48;
    *(f32x2*)(t + 16 + (lane >> 5) * 8) = f32x2{ex, ey};
    f32x4 o = {a, bq, cr * br - ci * bi, cr * bi + ci * br};
    if (lane >= 32) o = f32x4{ex, ey, ex * ex - ey * ey, (ex + ex) * ey};
    *(f32x4*)(t + (lane >> 5) * 32) = o;
  }
  TS(3);
  lds_barrier();
  {
    const char* xt = smem + A_XT + (lane & 7) * 1056;
    const u32 st = *(const u32*)(xt + 1024);
#pragma unroll
    for (int e = 0; e < 2; e++) {
      const u32x2 xq = *(const u32x2*)(xt + (e * 64 + w * 8 + (lane >> 3)) * 8);
      st16(ws + WS_XG + (size_t)(e * 64 + w * 8) * 32768 + b * 128, (lane >> 3) * 32768 + (lane & 7) * 16, u32x4{xq[0], xq[1], st, tag});
    }
  }
  TS(41);
  const int fb = (xg >> 1) * 4 + (idx & 3), mat = (idx >> 2) & 1, dq = (xg & 1) * 4 + (idx >> 3), d0 = dq * 64 + w * 8;
  u32 v[8], t2[8];
  {
    const u16* wp = (mat ? w3 : w1) + (size_t)d0 * F + fb * 112;
    const u32 vo = (lane < 56 ? lane : 55) * 4;
#pragma unroll
    for (int i = 0; i < 4; i++) { lp4<0>(v[2 * i], wp + 2 * i * F, vo); lp4<2 * F>(v[2 * i + 1], wp + 2 * i * F, vo); }
  }
  auto gemv = [&]() NOPK
  {
    wait_vm<10>(); tie(wm0); tie(wm1); tie(c0); tie(c1);
    const float cx0 = bflo(c0), cx1 = bflo(c1);
    const float s0 = rbf(cx0 * __builtin_amdgcn_rcpf(1.f + __builtin_amdgcn_exp2f(cx0 * -LOG2E)));
    const float s1 = rbf(cx1 * __builtin_amdgcn_rcpf(1.f + __builtin_amdgcn_exp2f(cx1 * -LOG2E)));
    float acc[8];
#pragma unroll
    for (int j = 0; j < 4; j++) {
      acc[2 * j] = fmaf(s0, bflo(wm0[j]), s1 * bflo(wm1[j]));
      acc[2 * j + 1] = fmaf(s0, bfhi(wm0[j]), s1 * bfhi(wm1[j]));
    }
    const float q0 = dpp_add<0x128>(rs16(rs32(acc[0], acc[4]), rs32(acc[2], acc[6])));
    const float q1 = dpp_add<0x128>(rs16(rs32(acc[1], acc[5]), rs32(acc[3], acc[7])));
    *(float*)(smem + A_GV + w * 256 + (lane & 7) * 32 + (lane >> 3) * 4) = (lane & 8) ? q1 : q0;
    if (arrive_last(smem + L_CNT, 8) && idx < 24) {
      float s = 0.f;
#pragma unroll
      for (int i = 0; i < 8; i++) s += *(const float*)(smem + A_GV + i * 256 + lane * 4);
      st8(ws + WS_MG + (cg & 3) * 24576 + (cg >> 2) * 512, lane * 8, u32x2{f2u(s), tag});
    }
  };
  TS(4);
  const u32 gcode = __builtin_amdgcn_perm(0x400b0916u, 0x0a201408u, (r & 7) | 0x0c0c0c00u);
  const int gx = r < 8 ? 2 * r : (int)((gcode >> 2) * 256 + (gcode & 3) * 4);
  {
    asm volatile("" ::: "memory");
    const float mF = (float)(16 * r), mX = (float)gx;
    const char* tp = tab + g * 8 * 48;
    const char* tq = tp + (r < 8 ? 16 : 32);
    f32x4 acc[3];
#pragma unroll
    for (int s = 0; s < 3; s++) acc[s] = f32x4{0.f, 0.f, 0.f, 0.f};
#pragma unroll
    for (int ks = 0; ks < 2; ks++) {
      u32x4 Ff, Gf[3];
#pragma unroll
      for (int i = 0; i < 4; i++) {
        const f32x4 s0 = *(const f32x4*)(tp + (ks * 4 + i) * 48), s1 = *(const f32x4*)(tq + (ks * 4 + i) * 48);
        const float e0 = __builtin_amdgcn_exp2f(s0[0] * mF), p0 = __builtin_amdgcn_fractf(s0[1] * mF);
        const float zx = e0 * __builtin_amdgcn_cosf(p0), zy = e0 * __builtin_amdgcn_sinf(p0);
        Ff[i] = pk(s0[2] * zx - s0[3] * zy, s0[2] * zy + s0[3] * zx);
        const float e1 = __builtin_amdgcn_exp2f(s0[0] * mX), p1 = __builtin_amdgcn_fractf(s0[1] * -mX);
        const float xx = e1 * __builtin_amdgcn_cosf(p1), xy = e1 * __builtin_amdgcn_sinf(p1);
        Gf[0][i] = pk(xx, xy);
        Gf[1][i] = pk(xx * s1[0] - xy * s1[1], xx * s1[1] + xy * s1[0]);
        Gf[2][i] = pk(xx * s1[2] - xy * s1[3], xx * s1[3] + xy * s1[2]);
      }
#pragma unroll
      for (int s = 0; s < 3; s++) acc[s] = mfma16(Ff, Gf[s], acc[s]);
      if (ks == 0) { TS(45); gemv(); TS(44); }
    }
#pragma unroll
    for (int s = 0; s < 3; s++) *(f32x4*)(smem + A_P + w * 3072 + s * 1024 + lane * 16) = acc[s];
  }
  TS(5);
  u32x4 xg4[4];
  u32x2 mg, mh, mc[4];
  const char* xgb = ws + WS_XG + (size_t)(p >> 1) * 32768;
  const u32 xgo = (tid >> 2) * 256 + (tid & 3) * 16;
  u32 mga = (4 - ((lane >> 3) & 1)) * 512 + d0 + (lane & 7), mgb = ((0x5210 >> ((lane & 6) * 2)) & 7) * 512 + 2 * p + (lane & 1);
  tie(mga); tie(mgb);
  const u32 mgo = ((lane & 16) ? mgb : mga) * 8 + (lane >> 5) * 24576;
  auto lookx = [&]() NOPK {
    ld16<0>(xg4[0], xgb, xgo); ld16<64>(xg4[1], xgb, xgo); ld16<128>(xg4[2], xgb, xgo); ld16<192>(xg4[3], xgb, xgo);
  };
  auto lookm = [&]() NOPK {
    ld8(mg, ws + WS_MG, mgo); ld8(mh, ws + WS_MG + 49152, mgo);
  };
  auto lookc = [&]() NOPK {
#pragma unroll
    for (int q = 0; q < 4; q++) ld8(mc[q], ws + WS_MG + q * 24576 + 16384, tid * 8);
  };
  lookx();
  lds_barrier();
  TS(6);
  TSF(0, 6); TSF(41, 41); TSF(44, 45);
  const int kch = tid >> 8;
  u16* Kc = (u16*)(smem + A_KC + kch * A_KCS);
  if ((w & 3) < 3) {
    const int s = w & 3;
    const char* pp = smem + A_P + kch * 12288 + s * 1024 + lane * 16;
    const f32x4 q = (*(const f32x4*)(pp) + *(const f32x4*)(pp + 3072)) + (*(const f32x4*)(pp + 6144) + *(const f32x4*)(pp + 9216));
    const int pos = gx + 64 * g + (r < 8 ? (s == 0 ? 0 : s == 1 ? 1 : 256) : s * 256);
    const u32 o0 = pk(q[0], q[1]), o1 = pk(q[2], q[3]);
    if (pos <= 2048) Kc[pos] = (u16)o0;
    if (pos < 2048) {
      Kc[pos + 16] = (u16)(o0 >> 16);
      Kc[pos + 32] = (u16)o1;
      Kc[pos + 48] = (u16)(o1 >> 16);
    }
  }
  wait_vm<4>();
#pragma unroll
  for (int i = 0; i < 8; i++) tie(v[i]);
  {
    u32x4 o0, o1;
#pragma unroll
    for (int j = 0; j < 4; j++) {
      o0[j] = __builtin_amdgcn_perm(v[2 * j + 1], v[2 * j], 0x05040100u);
      o1[j] = __builtin_amdgcn_perm(v[2 * j + 1], v[2 * j], 0x07060302u);
    }
    st16x2_56(ws + WS_W1F + (size_t)(fb * 7 * 32 + (d0 >> 4)) * 1024 + (w & 1) * 512 + mat * 128, (lane >> 3) * 32768 + (lane & 7) * 16, o0, o1);
  }
  lds_barrier();
  TS(7);
  lookm(); lookc();
  {
    u32 e[6];
    const int ki = tid & 255;
#pragma unroll
    for (int j = 0; j < 6; j++) {
      const float l0 = (float)(2047 - 8 * ki - 2 * j);
      const u32 bits = pk(fmaxf(l0, 0.f), fmaxf(l0 - 1.f, 0.f));
      e[j] = (u32)Kc[(int)bflo(bits)];
      if (j < 5) e[j] |= (u32)Kc[(int)bfhi(bits)] << 16;
    }
    if (ki == 255) { e[4] = 0; e[5] = 0; }
    const u32 a0 = __builtin_amdgcn_alignbit(e[1], e[0], 16), a1 = __builtin_amdgcn_alignbit(e[2], e[1], 16);
    const u32 a2 = __builtin_amdgcn_alignbit(e[3], e[2], 16), a3 = __builtin_amdgcn_alignbit(e[4], e[3], 16);
    const u32 a4 = __builtin_amdgcn_alignbit(e[5], e[4], 16);
    char* kc = smem + B_KC + kch * B_KCH + ki * 16;
    *(u32x4*)(kc) = u32x4{e[0], e[1], e[2], e[3]};
    *(u32x4*)(kc + B_KCS) = u32x4{a0, a1, a2, a3};
    *(u32x4*)(kc + 2 * B_KCS) = u32x4{e[1], e[2], e[3], e[4]};
    *(u32x4*)(kc + 3 * B_KCS) = u32x4{a1, a2, a3, a4};
  }
  TS(8);
  wait_vm<0>(); tie(xg4[0]); tie(xg4[1]); tie(xg4[2]); tie(xg4[3]); tie(mg); tie(mh); tie(mc[0]); tie(mc[1]); tie(mc[2]); tie(mc[3]);
  if (arrive_last(smem + L_CNT + 4, 8)) st4one(ws + WS_FW + (fb * 16 + mat * 8 + dq) * 128, tag);
  TS(47);
#pragma unroll 1
  for (int it = 0; it < SPIN_CAP && !all_lanes(xg4[0][3] == tag && xg4[1][3] == tag && xg4[2][3] == tag && xg4[3][3] == tag); it++) {
    lookx();
    wait_vm<0>(); tie(xg4[0]); tie(xg4[1]); tie(xg4[2]); tie(xg4[3]);
  }
#pragma unroll 1
  for (int it = 0; it < SPIN_CAP && !all_lanes(mg[1] == tag && mh[1] == tag); it++) {
    lookm();
    wait_vm<0>(); tie(mg); tie(mh);
  }
  TS(9);
  const int tt = (xg & 1) * 8 + (idx & 7), fs = (xg >> 1) * 4 + (idx >> 3);
  u32 fw;
  const int b2 = b < 224 ? b : 223;
  const u32 fwo = fs * 2048 + (lane & 15) * 128;
  const int g8 = tid >> 1, cc = tid & 1;
  const float mq = u2f(mg[0]) + u2f(mh[0]);
  const float mv = rbf(rs32(mq, mq)), mv1 = rbf(1.f + mv);
  const float ga = sel2(cc, rdl(mv, 20), rdl(mv, 21));
  u32x4 hp, xv;
  {
    const float sh0 = rdl(mv, 16), sh1 = rdl(mv, 17), sc0 = rdl(mv1, 18), sc1 = rdl(mv1, 19);
    const u32 psel = cc ? 0x03020706u : 0x05040100u;
    const bool up = tid & 2;
    u32 ph[4], px[4];
#pragma unroll
    for (int i = 0; i < 4; i++) {
      const u32 xq = (p & 1) ? xg4[i][1] : xg4[i][0];
      const float m = bflo(xg4[i][2]), rs = __builtin_amdgcn_rcpf(bfhi(xg4[i][2]));
      const u32 n = pk((bflo(xq) - m) * rs, (bfhi(xq) - m) * rs);
      const u32 hq = pk(fmaf(bflo(n), sc0, sh0), fmaf(bfhi(n), sc1, sh1));
      ph[i] = __builtin_amdgcn_perm(swap1(hq), hq, psel);
      px[i] = __builtin_amdgcn_perm(swap1(xq), xq, psel);
    }
#pragma unroll
    for (int j = 0; j < 2; j++) {
      const u32 rh = swap2(up ? ph[j] : ph[2 + j]), kh = up ? ph[2 + j] : ph[j];
      hp[2 * j] = up ? rh : kh; hp[2 * j + 1] = up ? kh : rh;
      const u32 rx = swap2(up ? px[j] : px[2 + j]), kx = up ? px[2 + j] : px[j];
      xv[2 * j] = up ? rx : kx; xv[2 * j + 1] = up ? kx : rx;
    }
    char* u = smem + B_U + cc * B_UCH + 1024 + g8 * 16;
    *(u32x4*)(u) = hp;
    *(u32x2*)(u + B_US + 8) = u32x2{hp[0], hp[1]};
    *(u32x2*)(u + B_US + 16) = u32x2{hp[2], hp[3]};
  }
#pragma unroll 1
  for (int it = 0; it < SPIN_CAP && !all_lanes(mc[0][1] == tag && mc[1][1] == tag && mc[2][1] == tag && mc[3][1] == tag); it++) {
    lookc();
    wait_vm<0>(); tie(mc[0]); tie(mc[1]); tie(mc[2]); tie(mc[3]);
  }
  {
    const float sc = 1.f + rbf((u2f(mc[0][0]) + u2f(mc[2][0])) + (u2f(mc[1][0]) + u2f(mc[3][0])));
    const u32 e = pk(sc, sc) & ((tid & 1) ? 0xffff0000u : 0xffffu);
    *(u32*)(smem + B_SC + tid * 4) = e;
    *(u32*)(smem + B_SC + 2048 + tid * 4) = e;
  }
  TS(10);
  lds_barrier();
  TS(11);
  const int h = lane >> 5, c = lane & 31;
  const int tt2 = (~xg & 1) * 8 + (xg & 6) + (idx >> 4), sel = (idx >> 3) & 1, dt = idx & 7;
  const int rot = (2 * (idx >> 3) + idx) & 7;
  const int ft = fs * 7 + w - 1;
  u32x4 A[32];
  const u32 ao = lane * 16;
  auto lda = [&](int i) NOPK {
    const char* wq = ws + WS_W1F + (size_t)ft * 32768 + (((i >> 2) + rot) & 7) * 4096;
    asm volatile("s_cmp_eq_u32 %4, 0\n\ts_cbranch_scc1 .Lna%=\n\tglobal_load_dwordx4 %0, %1, %2 offset:%3 sc1\n.Lna%=:"
                 : "=v"(A[i]) : "v"(ao), "s"(wq), "n"((i & 3) * 1024), "s"(w) : "memory", "scc");
  };
  const char* tb = smem + B_SC + rot * 256 + h * 32;
  u32x4 tq[3][2];
  auto ldt = [&](int i) NOPK { tq[i % 3][0] = *(const u32x4*)(tb + i * 64); tq[i % 3][1] = *(const u32x4*)(tb + i * 64 + 16); };
  auto scl = [&](int i) NOPK { tie(A[i]); scale8(A[i], tq[i % 3][0], tq[i % 3][1], w); tie(A[i]); };
  auto scl_run = [&](int lo, int hi) NOPK {
#pragma unroll
    for (int i = lo; i < hi + 2; i++) {
      if (i < hi) ldt(i);
      __builtin_amdgcn_sched_barrier(0);
      if (i >= lo + 2) scl(i - 2);
      __builtin_amdgcn_sched_barrier(0);
    }
  };
  {
    const int ch = w & 1, vh = (w >> 1) & 1, tg = w >> 2;
    const char* pa = smem + B_KC + ch * B_KCH + (3 - (r & 3)) * B_KCS + 2 * (2040 - 8 * (r >> 2) + 8 * g) - 512 * 7 - 64 * (4 * vh + 3);
    const char* pb = smem + B_U + ch * B_UCH + ((r & 1) ? 0 : B_US) + 2 * (512 + 32 * (r >> 1) + 8 * g) - 64 * (4 * vh + 3) - 512 * tg;
    f32x4 acc[4];
#pragma unroll
    for (int i = 0; i < 4; i++) acc[i] = f32x4{0.f, 0.f, 0.f, 0.f};
    u32x4 fr[2][8];
    auto fld = [&](int st, int n) NOPK {
      const int e = n == 0 ? 0 : n < 5 ? 9 - 2 * n : 2 * n - 8;
      const bool isa = ((e ^ st) & 1) == 0;
      fr[st & 1][e] = *(const u32x4*)((isa ? pa + 512 * (7 - e) : pb + 512 * e) + 64 * (3 - (st >> 1)));
    };
#pragma unroll
    for (int n = 0; n < 8; n++) fld(0, n);
    __builtin_amdgcn_sched_barrier(0);
    {
      f32x4 sums = {0.f, 0.f, 0.f, 0.f};
#pragma unroll
      for (int j = 0; j < 4; j++) {
        const float k0 = rdl(mv1, 2 * j), k1 = rdl(mv1, 2 * j + 1), s0 = rdl(mv, 8 + 2 * j), s1 = rdl(mv, 8 + 2 * j + 1);
        const float a0 = bflo(v[2 * j]), a1 = bflo(v[2 * j + 1]), b0 = bfhi(v[2 * j]), b1 = bfhi(v[2 * j + 1]);
        sums[0] = dot2(pk(a0 * k0, a1 * k1), 0x3f803f80u, sums[0]);
        sums[1] = dot2(pk(b0 * k0, b1 * k1), 0x3f803f80u, sums[1]);
        sums[2] = fmaf(a0, s0, fmaf(a1, s1, sums[2]));
        sums[3] = fmaf(b0, s0, fmaf(b1, s1, sums[3]));
      }
      *(f32x4*)(smem + B_RED + tid * 16) = sums;
    }
    ld4(fw, ws + WS_FW, fwo);
#pragma unroll
    for (int i = 0; i < 2; i++) {
      const u16* wq = w2 + (size_t)(b2 * 8 + i * 4) * D;
      const u32 to = tid * 2;
      lp2<0>(t2[4 * i], wq, to); lp2<1024>(t2[4 * i + 1], wq, to); lp2<2048>(t2[4 * i + 2], wq, to); lp2<3072>(t2[4 * i + 3], wq, to);
    }
    __builtin_amdgcn_sched_barrier(0);
#pragma unroll
    for (int st = 0; st < 8; st++) {
      if (st == LST) {
        wait_vm<8>(); tie(fw);
        spin4(fw, tag, ws + WS_FW, fwo);
        TS(46);
      }
#pragma unroll
      for (int n = 0; n < 10; n++) {
        const int ev = n < 4 ? 0 : n < 7 ? 2 : n < 9 ? 4 : 6, ai = n < 4 ? n : n < 7 ? n - 4 : n < 9 ? n - 7 : 0, od = 7 - 2 * ai - ev;
        if (st < 7 && n < 8) fld(st + 1, n);
        __builtin_amdgcn_sched_barrier(0);
        acc[ai] = (st & 1) ? mfma16(fr[1][od], fr[1][ev], acc[ai]) : mfma16(fr[0][ev], fr[0][od], acc[ai]);
        const int sl = st - LST, ss = sl - SLAG;
        if (sl >= 0 && n < 8 && (n & 1) == 0) lda(4 * sl + n / 2);
        if (ss >= 0 && n < 8 && (n & 1) == 0) ldt(4 * ss + n / 2);
        if (ss >= 0 && n > 2 && (n & 1) == 1) { if (n < 9) wait_vm<4 * SLAG + 1>(); else wait_vm<4 * SLAG>(); scl(4 * ss + (n - 3) / 2); }
        __builtin_amdgcn_sched_barrier(0);
      }
    }
    TS(12);
    float* Y = (float*)(smem + B_Y) + (vh * 2 + ch) * 2560 + 40 * (r >> 1) + 4 * (r & 1) + 8 * g - 320 * tg;
#pragma unroll
    for (int i = 0; i < 4; i++) *(f32x4*)(Y + 320 * (7 - 2 * i)) = acc[i];
  }
  lds_barrier();
  TS(13);
  TSF(7, 13); TSF(46, 47);
  {
    const float* Y = (const float*)(smem + B_Y) + cc * 2560 + 8 * g8 + 8 * (g8 >> 2);
    const f32x4 ya = *(const f32x4*)Y + *(const f32x4*)(Y + 5120), yb = *(const f32x4*)(Y + 4) + *(const f32x4*)(Y + 5124);
    const float dk = cc ? bfhi(dsk) : bflo(dsk);
    u32x4 o;
#pragma unroll
    for (int j = 0; j < 4; j++) {
      const float y0 = j < 2 ? ya[2 * j] : yb[2 * j - 4], y1 = j < 2 ? ya[2 * j + 1] : yb[2 * j - 3];
      o[j] = pk(fmaf(ga, fmaf(bflo(hp[j]), dk, y0), bflo(xv[j])), fmaf(ga, fmaf(bfhi(hp[j]), dk, y1), bfhi(xv[j])));
    }
    st16(ws + WS_X1 + p * 128, (g8 >> 2) * 32768 + cc * 64 + (g8 & 3) * 16, o);
  }
  TS(14);
  constexpr int NA = 4 * (8 - LST), S0 = NA - 4 * SLAG, S1 = S0 + SPRE;
  wait_vm<NA - S1 + 1>();
  if (w == 0) wait_vm<1>();
  {
#pragma unroll
    for (int i = 0; i < 8; i++) tie(t2[i]);
    u32x4 o0;
#pragma unroll
    for (int j = 0; j < 4; j++) o0[j] = __builtin_amdgcn_perm(t2[2 * j + 1], t2[2 * j], 0x05040100u);
    st16(ws + WS_W2F + w * 229376 + (b2 >> 2) * 4096 + (b2 & 3) * 256, g * 1024 + r * 16, o0);
  }
  f32x2* raw = (f32x2*)(smem + C_RAW);
  u32x4 W2x[12];
  u32 fh;
  const u32 lds0 = (u32)(size_t)(LDS_AS char*)smem;
  const char* xa = ws + WS_X1 + tt * 131072 + ((w + rot) & 7) * 4096;
  const u32 lpc = lds0 + w * 4096;
  const int rt2 = dt + 2 * (idx >> 3), rot2 = rt2 < 7 ? rt2 : rt2 - 7;
#define kof(j) ((j) + rot2 < 7 ? (j) + rot2 : (j) + rot2 - 7)
  const char* w2b = ws + WS_W2F + dt * 229376 + w * 28672;
  const char* fhb = ws + WS_FH + tt2 * 2048 + w * 256;
  const u32 fho = ((lane >> 3) & 1) * 128 + g * 32 + (lane & 7) * 4;
  const char* fpb = ws + WS_FH + (tt * 16 + fs) * 128 + w * 4;
  auto ldw2 = [&](int j) NOPK {
    const char* q = w2b + kof(j >> 2) * 4096;
    if (j < 12) ld16k(W2x[j], q, j & 3, ao); else ld16k(A[j - 12], q, j & 3, ao);
  };
  if (w == 0) {
    f32x4 s = {0.f, 0.f, 0.f, 0.f};
#pragma unroll
    for (int q = 0; q < 8; q++) s += *(const f32x4*)(smem + B_RED + q * 1024 + lane * 16);
    st16(ws + WS_CD + ((dq * 16 + fb) * 2 + mat) * 1024, lane * 16, __builtin_bit_cast(u32x4, s));
    st8m<1>(ws + WS_GB + p * 8, 0, u32x2{f2u(rdl(mv, 22)), f2u(rdl(mv, 23))});
  }
  scl_run(S0, S1);
  wait_vm<0>();
  TS(15);
  bar();
  if (w == 0) st4one(ws + WS_FX + (b & 63) * 128 + (b >> 6) * 4, tag);
  TS(16);
  scl_run(S1, S1 + 2);
#pragma unroll
  for (int i = NA; i < 32; i++) lda(i);
  scl_run(S1 + 2, NA);
  wait_vm<0>();
  scl_run(NA, 32);
  if (w == 0) {
    __builtin_amdgcn_s_setprio(3);
#pragma unroll 1
    for (int it = 0; it < SPIN_CAP; it++) {
      u32x4 f;
      ld16(f, ws + WS_FX, lane * 128); wait_vm<0>(); tie(f);
      if (all_lanes(f[0] == tag && f[1] == tag && f[2] == tag && f[3] == tag)) break;
    }
    TS(17);
    bar();
    TS(18);
    glds4k(xa, ao, lpc); glds4k(xa + 32768, ao, lpc + 32768);
    const char* rb = smem + (lane >> 2) * 64 + (lane & 3) * 8;
    wait_vm<4>(); bar();
    TS(19); TC(20);
    TSF(14, 20);
    glds4k(xa + 65536, ao, lpc + 65536); glds4k(xa + 98304, ao, lpc + 98304);
#pragma unroll
    for (int q = 0; q < 4; q++) {
      const char* xr_ = ws + WS_X1 + (tt2 * 4 + q) * 32768 + dt * 4096 + sel * 32;
      const u32 xo = r * 256 + h * 64 + (g & 1) * 16, la = lds0 + D_RES + q * 2048;
      glds1k<0>(xr_, xo, la); glds1k<0>(xr_ + 128, xo, la + 1024);
    }
    asm volatile("s_mov_b32 m0, %2\n\ts_nop 0\n\tglobal_load_lds_dword %0, %1 sc1"
                 ::"v"(lane * 4), "s"(ws + WS_GB + dt * 256), "s"(lds0 + D_GB) : "memory");
    if (b == 0) st4one(ws + WS_E, tag);
    ln2_tile(rb, raw, raw + 132, lane);
    TS(21);
    wait_vm<17>(); lds_barrier();
    TS(22);
    st4one(fpb, tag); st4one(fpb + 32, tag);
#pragma unroll
    for (int i = 0; i < 14; i++) ldw2(i);
    ln2_tile(rb + 32768, raw + 32, raw + 132, lane);
    TS(23);
    wait_vm<16>(); lds_barrier();
    TS(24);
#pragma unroll
    for (int i = 14; i < 28; i++) ldw2(i);
    rb = far(rb + 65536);
    ln2_tile(rb, raw + 64, raw + 132, lane);
    asm volatile("" ::: "memory");
    *(volatile LDS_AS u32*)(smem + L_CNT + 16) = 1;
    TS(25);
    ln2_tile(rb + 32768, raw + 96, raw + 132, lane);
    asm volatile("" ::: "memory");
    *(volatile LDS_AS u32*)(smem + L_CNT + 16) = 2;
    __builtin_amdgcn_s_setprio(0);
    TS(26); TS(27); TC(28);
    TSF(21, 28);
    ld4(fh, fhb, fho);
    wait_vm<0>(); tie(fh);
#pragma unroll
    for (int i = 0; i < 28; i++) { if (i < 12) tie(W2x[i]); else tie(A[i - 12]); }
    TS(29);
  }
  int w_ = w;
  asm volatile("" : "+s"(w_));
  if (w_ != 0) {
    const char* rb = smem + h * 512 + ((lane >> 2) & 3) * 64 + ((lane >> 4) & 1) * 32 + (lane & 3) * 8;
    const char* rb2 = far(rb + 65536);
    u32 cdv[8];
    f32x16 acc[2], zero;
#pragma unroll
    for (int i = 0; i < 16; i++) zero[i] = 0.f;
    TS(17);
    lds_barrier();
    TS(18);
    glds4k(xa, ao, lpc); glds4k(xa + 32768, ao, lpc + 32768);
    wait_vm<4>();
    bar();
    TS(19); TC(20);
    TSF(14, 20);
    __builtin_amdgcn_s_setprio(3);
    u32x4 Bq[3], Ax, op;
    Bq[0] = tr8(rb); Bq[1] = tr8(rb + 1024);
    f32x2 rw;
    Ep q[2];
    float ev, gu, oa;
    const char* hb = ws + WS_HF + (size_t)(tt * 8 * 56 + (ft >> 1)) * 1024 + (ft & 1) * 512;
    const u32 ho = (c >> 4) * 57344 + h * 256 + r * 16;
    auto stat_wait = [&](u32 need) NOPK {
#pragma unroll 1
      for (int it = 0; it < SPIN_CAP * 16; it++)
        if (__builtin_amdgcn_readfirstlane(*(volatile LDS_AS u32*)(smem + L_CNT + 16)) >= need) break;
      asm volatile("" ::: "memory");
    };
    auto tile = [&](auto tc) NOPK {
    constexpr int T = decltype(tc)::v, E = T - 1;
#pragma unroll
    for (int ks = 0; ks < 32; ks++) {
      const int n = T * 32 + ks, m = n + 2;
      if (n == 30) {
        TS(21);
        wait_vm<8>();
#pragma unroll
        for (int i = 0; i < 8; i++) tie(cdv[i]);
        lds_barrier();
        TS(22);
      } else if (n == 62) {
        TS(23);
        wait_vm<11>();
        lds_barrier();
        TS(24);
      }
      if (n == 30 || n == 62) __builtin_amdgcn_s_setprio(3);
      if (n == 14 || n == 46 || n == 100) __builtin_amdgcn_s_setprio(2);
      if (n == 22 || n == 54 || n == 118) __builtin_amdgcn_s_setprio(1);
      if (n == 27 || n == 59) __builtin_amdgcn_s_setprio(0);
      if (n == 96) { TS(25); tie(acc[0]); stat_wait(1); }
      if (n == 118) { tie(acc[1]); stat_wait(2); }
      if (n == 120) wait_vm<10>();
      if (m < 128) Bq[m % 3] = tr8((m < 64 ? rb : rb2) + (m & 63) * 1024);
      if (T > 0 && ks == 0) rw = raw[E * 32 + c];
      if (n == 118) rw = raw[96 + c];
      if (n >= 120) A[n - 112] = *(const u32x4*)(smem + ((n >> 2) & 1) * 32768 + w * 4096 + (n & 3) * 1024 + lane * 16);
      __builtin_amdgcn_sched_barrier(0);
      acc[T & 1] = mfma32(A[ks], Bq[n % 3], ks == 0 ? zero : acc[T & 1]);
      if (n == 31) {
        const float cd = ((u2f(cdv[0]) + u2f(cdv[1])) + (u2f(cdv[2]) + u2f(cdv[3]))) + ((u2f(cdv[4]) + u2f(cdv[5])) + (u2f(cdv[6]) + u2f(cdv[7])));
        const float chi = rbf(cd);
        Ax = u32x4{pk(chi, chi), pk(cd - chi, cd - chi), 0, 0};
      }
      if ((T > 0 && ks == 2) || n == 120) {
        const int X = (n == 120 ? 3 : E) & 1;
        const u32 be = ln2_fold(rw, h, q[X]);
        acc[X] = mfma32(Ax, u32x4{be, be, 0, 0}, acc[X]);
      }
      if (T > 0 && ks >= 4 && ks < 20) {
        const int k = (ks - 4) >> 1, i = (k >> 1) + 4 * (k & 1);
        if ((ks & 1) == 0) {
          ev = fmaf(__builtin_amdgcn_exp2f(accg(acc[E & 1], i) * q[E & 1].q0), q[E & 1].v, q[E & 1].v);
          gu = accg(acc[E & 1], i) * accu(acc[E & 1], i);
        } else {
          const float o = gu * __builtin_amdgcn_rcpf(ev);
          if (k & 1) op[k >> 1] = pk(oa, o); else oa = o;
        }
      }
      __builtin_amdgcn_sched_barrier(0);
      if (n == 1) {
        const u32 co = ((c >> 3) & 1) * 1024 + ((w - 1) * 8 + (c & 7)) * 16 + h * 8 + (c >> 4) * 4;
#pragma unroll
        for (int i = 0; i < 8; i++) ld4(cdv[i], ws + WS_CD + (i * 16 + fs) * 2048, co);
      }
      if (T == 0 && ks >= 4 && ks < 28 && ks % 3 == 1) {
        const int i = (ks - 4) / 3, o = 65536 + (i >> 2) * 32768;
        if ((i & 3) == 0) glds1k<0>(xa + o, ao, lpc + o); else if ((i & 3) == 1) glds1k<1024>(xa + o, ao, lpc + o);
        else if ((i & 3) == 2) glds1k<2048>(xa + o, ao, lpc + o); else glds1k<3072>(xa + o, ao, lpc + o);
      }
      if (T > 1 && ks == 9) { wait_vm<6>(); st4one(fpb + (T - 2) * 32, tag); }
      if (T > 0 && ks == 20) st16(hb + E * 114688, ho, op);
      if (T > 0 && ks % 3 == 1 && ks < 30) {
        const int i = ks / 3;
        if (T == 3) { if (i < 8) ldw2(12 + i); }
        else if (i < 6) ldw2(6 * E + i);
        else if (i == 6) glds1k<0>(w2b + kof(5 + E) * 4096, ao, lpc + E * 32768);
        else if (i == 7) glds1k<1024>(w2b + kof(5 + E) * 4096, ao, lpc + E * 32768);
        else if (i == 8) glds1k<2048>(w2b + kof(5 + E) * 4096, ao, lpc + E * 32768);
        else glds1k<3072>(w2b + kof(5 + E) * 4096, ao, lpc + E * 32768);
      }
      if (n == 122) ld4(fh, fhb, fho);
    }
    };
    tile(IC<0>{}); tile(IC<1>{}); tile(IC<2>{}); tile(IC<3>{});
    TS(26); TS(27); TC(28);
    TSF(21, 28);
#pragma unroll
    for (int i = 0; i < 4; i++) op[i] = pk(swi(acc[1], i, q[1]), swi(acc[1], i + 4, q[1]));
    st16(hb + 3 * 114688, ho, op);
    __builtin_amdgcn_s_setprio(0);
    wait_vm<1>(); tie(fh);
#pragma unroll
    for (int i = 0; i < 20; i++) { if (i < 12) tie(W2x[i]); else tie(A[i - 12]); }
    TS(29);
  }
  {
    const char* hq = ws + WS_HF + (size_t)((tt2 * 8 + sel) * 56 + w * 7) * 1024;
    u32x4 H[3][7];
    auto need = [&](unsigned long long mask) NOPK {
#pragma unroll 1
      for (int it = 0; it < SPIN_CAP && (__builtin_amdgcn_ballot_w64(fh != tag) & mask) != 0; it++) {
        __builtin_amdgcn_s_sleep(2);
        ld4(fh, fhb, fho); wait_vm<0>(); tie(fh);
      }
    };
    auto ldh = [&](int q) NOPK {
#pragma unroll
      for (int j = 0; j < 7; j++) ld16(H[q % 3][j], hq + q * 114688 + kof(j) * 1024, ao);
    };
    char* wr = smem + w * 4096 + r * 256;
    auto quarter = [&](int q, int yng) NOPK {
      f32x4 acc[4];
#pragma unroll
      for (int j = 0; j < 7; j++) {
        __builtin_amdgcn_sched_barrier(0);
        if (yng == 0) { if (j == 0) wait_vm<5>(); else if (j == 2) wait_vm<3>(); else if (j == 4) wait_vm<1>(); else if (j == 6) wait_vm<0>(); }
        if (yng == 8 && j == 0) wait_vm<8>();
        tie(H[q % 3][j]);
#pragma unroll
        for (int ds = 0; ds < 4; ds++) {
          const int f = 4 * j + ds;
          acc[ds] = mfma16(f < 12 ? W2x[f] : A[f - 12], H[q % 3][j], j == 0 ? f32x4{0.f, 0.f, 0.f, 0.f} : acc[ds]);
        }
      }
      __builtin_amdgcn_sched_barrier(0);
#pragma unroll
      for (int ds = 0; ds < 4; ds++) *(f32x4*)(wr + q * 32768 + (((ds * 4 + g) ^ r) * 16)) = acc[ds];
    };
    const int tk = (tid >> 4) & 15, qh = w >> 2;
    u32 xq[2][4];
    f32x4 gbv;
    auto finish = [&](int qp) NOPK {
      const char* rp = smem + (2 * qp + qh) * 32768 + tk * 256 + ((r ^ tk) * 16);
      f32x4 s = *(const f32x4*)rp;
#pragma unroll
      for (int i = 1; i < 8; i++) s += *(const f32x4*)(rp + i * 4096);
      float y[4];
#pragma unroll
      for (int j = 0; j < 4; j++) y[j] = fmaf(gbv[j], s[j], bflo(xq[qp][j]));
      *(u32x2*)(out + (size_t)((tt2 * 4 + 2 * qp + qh) * 32 + sel * 16 + tk) * D + dt * 64 + r * 4) = u32x2{pk(y[0], y[1]), pk(y[2], y[3])};
    };
    st4one(fpb + 64, tag);
    need(0xffffull);
    ldh(0);
    TS(30);
    {
      const char* xp = smem + D_RES + qh * 2048 + (tk >> 3) * 256 + r * 16 + (tk & 7) * 2;
#pragma unroll
      for (int i = 0; i < 8; i++) xq[i >> 2][i & 3] = *(const u16*)(xp + (i >> 2) * 4096 + (i & 3) * 512);
      gbv = *(const f32x4*)(smem + D_GB + r * 16);
    }
    wait_vm<8>();
    st4one(fpb + 96, tag);
    TS(31);
    need(0xffffull << 16);
    ld4(fh, fhb, fho);
    ldh(1);
    wait_vm<7>(); tie(fh);
    TS(32);
    need(0xffffull << 32);
    ld4(fh, fhb, fho);
    ldh(2);
    quarter(0, -1);
    TS(33);
    wait_vm<7>(); tie(fh);
    need(0xffffull << 48);
    TS(34);
    ldh(3);
    quarter(1, -1);
    TS(35);
    lds_barrier();
    finish(0);
    asm volatile("" ::: "memory");
    TS(36);
    TSF(29, 36);
    quarter(2, 8);
    TS(37);
    quarter(3, 0);
    TS(38);
    lds_barrier();
    finish(1);
    TS(39);
    TS(40);
    TSF(37, 40);
    TSEND();
  }
}

extern "C" void kernel_launch(void* const* d_in, const int* in_sizes, int n_in, void* d_out, int out_size, void* d_ws,
                              size_t ws_size, hipStream_t stream) {
  const u16* const* in = (const u16* const*)d_in;
  fused<<<256, 512, 0, stream>>>(in[0], in[1], in[2], in[3], in[4], in[5], in[6], in[7], in[8], in[10], in[11], in[12], in[13], in[14],
                                 (char*)d_ws, (u16*)d_out);
}
```

```cpp
#include <hip/hip_runtime.h>
#include <stdint.h>

typedef unsigned int u32;
typedef unsigned short u16;
typedef __attribute__((ext_vector_type(2))) float f32x2;
typedef __attribute__((ext_vector_type(4))) float f32x4;
typedef __attribute__((ext_vector_type(16))) float f32x16;
typedef __attribute__((ext_vector_type(2))) u32 u32x2;
typedef __attribute__((ext_vector_type(4))) u32 u32x4;
typedef __attribute__((ext_vector_type(2))) __bf16 bf16x2;
typedef __attribute__((ext_vector_type(8))) __bf16 bf16x8;
typedef __attribute__((ext_vector_type(4))) short s16x4;
#define LDS_AS __attribute__((address_space(3)))
#if defined(__HIP_DEVICE_COMPILE__)
#define NOPK __attribute__((target("no-packed-fp32-ops")))
#else
#define NOPK
#endif
#define DEVINL __device__ __forceinline__ NOPK

constexpr int L = 2048, D = 512, NS = 128, F = 1792;

constexpr size_t WS_E = 0;
constexpr size_t WS_FW = 4096;
constexpr size_t WS_FX = WS_FW + 32768;
constexpr size_t WS_FH = WS_FX + 32768;
constexpr size_t WS_MG = WS_FH + 65536;
constexpr size_t WS_GB = WS_MG + 98304;
constexpr size_t WS_XG = 262144;
constexpr size_t WS_X1 = WS_XG + (4u << 20);
constexpr size_t WS_W1F = WS_X1 + (2u << 20);
constexpr size_t WS_W2F = WS_W1F + 112 * 32 * 1024;
constexpr size_t WS_HF = WS_W2F + 16 * 112 * 1024;
constexpr size_t WS_CD = WS_HF + 64 * 112 * 1024;
constexpr int SPIN_CAP = 40000;
#define SLAG 3
#define LST 2
#define PFM 3
#define PFB 32768
#define SPRE 4

#define u2f(x) __builtin_bit_cast(float, (u32)(x))
#define i2f(x) __builtin_bit_cast(float, (int)(x))
#define f2u(x) __builtin_bit_cast(u32, (float)(x))
#define f2i(x) __builtin_bit_cast(int, (float)(x))
#define TSDECL
#define TS(slot)
#define TC(slot)
#define TSF(lo, hi)
#define TSEND()
DEVINL float bflo(u32 u) { return u2f(u << 16); }
DEVINL float bfhi(u32 u) { return u2f(u & 0xffff0000u); }
DEVINL u32 pk(float a, float b) {
  f32x2 v = {a, b};
  return __builtin_bit_cast(u32, __builtin_convertvector(v, bf16x2));
}
DEVINL float rbf(float a) { return bflo(pk(a, a)); }
template <int CTRL> DEVINL float dpp_add(float v) {
  return v + i2f(__builtin_amdgcn_update_dpp(0, f2i(v), CTRL, 0xf, 0xf, true));
}
DEVINL float row4_sum(float v) {
  auto a = __builtin_amdgcn_permlane16_swap(f2u(v), f2u(v), false, false);
  v = u2f(a[0]) + u2f(a[1]);
  auto b = __builtin_amdgcn_permlane32_swap(f2u(v), f2u(v), false, false);
  return u2f(b[0]) + u2f(b[1]);
}
DEVINL float rs32(float a, float b) {
  auto r = __builtin_amdgcn_permlane32_swap(f2u(a), f2u(b), false, false);
  return u2f(r[0]) + u2f(r[1]);
}
DEVINL float rs16(float a, float b) {
  auto r = __builtin_amdgcn_permlane16_swap(f2u(a), f2u(b), false, false);
  return u2f(r[0]) + u2f(r[1]);
}
DEVINL float wave_sum(float v) { return row4_sum(dpp_add<0x140>(dpp_add<0x141>(dpp_add<0x4E>(dpp_add<0xB1>(v))))); }
DEVINL f32x4 mfma16(u32x4 a, u32x4 b, f32x4 c) {
  return __builtin_amdgcn_mfma_f32_16x16x32_bf16(__builtin_bit_cast(bf16x8, a), __builtin_bit_cast(bf16x8, b), c, 0, 0, 0);
}
DEVINL f32x16 mfma32(u32x4 a, u32x4 b, f32x16 c) {
  return __builtin_amdgcn_mfma_f32_32x32x16_bf16(__builtin_bit_cast(bf16x8, a), __builtin_bit_cast(bf16x8, b), c, 0, 0, 0);
}
DEVINL float dot2(u32 a, u32 b, float c) {
  return __builtin_amdgcn_fdot2_f32_bf16(__builtin_bit_cast(bf16x2, a), __builtin_bit_cast(bf16x2, b), c, false);
}
DEVINL void lds_barrier() {
  asm volatile("s_waitcnt lgkmcnt(0)" ::: "memory");
  __builtin_amdgcn_s_barrier();
  asm volatile("" ::: "memory");
}
DEVINL void bar() {
  asm volatile("" ::: "memory");
  __builtin_amdgcn_s_barrier();
  asm volatile("" ::: "memory");
}
DEVINL void st16(const void* sbase, u32 voff, u32x4 v) {
  asm volatile("global_store_dwordx4 %0, %1, %2 sc1\n\ts_nop 1" ::"v"(voff), "v"(v), "s"(sbase) : "memory");
}
DEVINL void st8(const void* sbase, u32 voff, u32x2 v) {
  asm volatile("global_store_dwordx2 %0, %1, %2 sc1\n\ts_nop 1" ::"v"(voff), "v"(v), "s"(sbase) : "memory");
}
DEVINL void st4(const void* sbase, u32 voff, u32 v) {
  asm volatile("global_store_dword %0, %1, %2 sc1\n\ts_nop 1" ::"v"(voff), "v"(v), "s"(sbase) : "memory");
}
template <int LANES> DEVINL void st8m(const void* sbase, u32 voff, u32x2 v) {
  asm volatile("s_mov_b64 exec, %3\n\tglobal_store_dwordx2 %0, %1, %2 sc1\n\ts_mov_b64 exec, -1\n\ts_nop 0"
               ::"v"(voff), "v"(v), "s"(sbase), "n"((1 << LANES) - 1) : "memory");
}
DEVINL void st4one(const void* sbase, u32 v) {
  asm volatile("s_mov_b64 exec, 1\n\tglobal_store_dword %0, %1, %2 sc1\n\ts_mov_b64 exec, -1\n\ts_nop 0"
               ::"v"(0), "v"(v), "s"(sbase) : "memory");
}
DEVINL void st16x2_56(const void* sbase, u32 voff, u32x4 a, u32x4 b) {
  asm volatile("s_mov_b32 exec_hi, 0xffffff\n\tglobal_store_dwordx4 %0, %1, %3 sc1\n\tglobal_store_dwordx4 %0, %2, %3 offset:256 sc1\n\t"
               "s_mov_b32 exec_hi, -1\n\ts_nop 0" ::"v"(voff), "v"(a), "v"(b), "s"(sbase) : "memory");
}
DEVINL void sld4(u32& d, const void* p) { asm volatile("s_load_dword %0, %1, 0" : "=s"(d) : "s"(p) : "memory"); }
template <int OFF = 0> DEVINL void ld16(u32x4& d, const void* sbase, u32 voff) {
  asm volatile("global_load_dwordx4 %0, %1, %2 offset:%3 sc1" : "=v"(d) : "v"(voff), "s"(sbase), "n"(OFF) : "memory");
}
DEVINL void ld16k(u32x4& d, const char* sbase, int kb, u32 voff) {
  const char* sb = sbase + (kb >> 2) * 4096;
  if ((kb & 3) == 0) ld16<0>(d, sb, voff); else if ((kb & 3) == 1) ld16<1024>(d, sb, voff);
  else if ((kb & 3) == 2) ld16<2048>(d, sb, voff); else ld16<3072>(d, sb, voff);
}
DEVINL void ld8(u32x2& d, const void* sbase, u32 voff) {
  asm volatile("global_load_dwordx2 %0, %1, %2 sc1" : "=v"(d) : "v"(voff), "s"(sbase) : "memory");
}
template <int OFF = 0> DEVINL void ld4(u32& d, const void* sbase, u32 voff) {
  asm volatile("global_load_dword %0, %1, %2 offset:%3 sc1" : "=v"(d) : "v"(voff), "s"(sbase), "n"(OFF) : "memory");
}
template <int OFF = 0> DEVINL void lp16(u32x4& d, const void* sbase, u32 voff) {
  asm volatile("global_load_dwordx4 %0, %1, %2 offset:%3" : "=v"(d) : "v"(voff), "s"(sbase), "n"(OFF) : "memory");
}
template <int OFF = 0> DEVINL void lp4(u32& d, const void* sbase, u32 voff) {
  asm volatile("global_load_dword %0, %1, %2 offset:%3" : "=v"(d) : "v"(voff), "s"(sbase), "n"(OFF) : "memory");
}
template <int OFF = 0> DEVINL void lp2(u32& d, const void* sbase, u32 voff) {
  asm volatile("global_load_ushort %0, %1, %2 offset:%3" : "=v"(d) : "v"(voff), "s"(sbase), "n"(OFF) : "memory");
}
template <class T> DEVINL void tie(T& a) { asm volatile("" : "+v"(a)); }
template <int N> DEVINL void wait_vm() { asm volatile("s_waitcnt vmcnt(%0)" ::"n"(N) : "memory"); }
DEVINL bool all_lanes(bool ok) { return __builtin_amdgcn_ballot_w64(!ok) == 0; }
DEVINL void spin4(u32 f, u32 tag, const void* sbase, u32 voff) {
#pragma unroll 1
  for (int it = 0; it < SPIN_CAP && !all_lanes(f == tag); it++) {
    __builtin_amdgcn_s_sleep(1);
    ld4(f, sbase, voff); wait_vm<0>(); tie(f);
  }
}
DEVINL bool arrive_last(char* smem_word, int n) {
  u32 old;
  asm volatile("s_mov_b64 exec, 1\n\tds_add_rtn_u32 %0, %1, %2\n\ts_waitcnt lgkmcnt(0)\n\ts_mov_b64 exec, -1"
               : "=&v"(old) : "v"((u32)(size_t)(LDS_AS char*)smem_word), "v"(1) : "memory");
  return __builtin_amdgcn_readfirstlane(old) == (u32)(n - 1);
}
DEVINL void scale8(u32x4& a, const u32x4& s, const u32x4& t, int on) {
  u32 a0 = a[0], a1 = a[1], a2 = a[2], a3 = a[3], x0, x1, x2, x3;
  asm("s_cmp_eq_u32 %16, 0\n\ts_cbranch_scc1 .Lsk%=\n\tv_dot2_f32_bf16 %4, %0, %8, 0\n\tv_dot2_f32_bf16 %0, %0, %9, 0\n\tv_dot2_f32_bf16 %5, %1, %10, 0\n\tv_dot2_f32_bf16 %1, %1, %11, 0\n\t"
      "v_dot2_f32_bf16 %6, %2, %12, 0\n\tv_dot2_f32_bf16 %2, %2, %13, 0\n\tv_dot2_f32_bf16 %7, %3, %14, 0\n\tv_dot2_f32_bf16 %3, %3, %15, 0\n\t"
      "v_cvt_pk_bf16_f32 %0, %4, %0\n\tv_cvt_pk_bf16_f32 %1, %5, %1\n\tv_cvt_pk_bf16_f32 %2, %6, %2\n\ts_nop 0\n\tv_cvt_pk_bf16_f32 %3, %7, %3\n.Lsk%=:"
      : "+v"(a0), "+v"(a1), "+v"(a2), "+v"(a3), "=&v"(x0), "=&v"(x1), "=&v"(x2), "=&v"(x3)
      : "v"(s[0]), "v"(s[1]), "v"(s[2]), "v"(s[3]), "v"(t[0]), "v"(t[1]), "v"(t[2]), "v"(t[3]), "s"(on) : "scc");
  a = u32x4{a0, a1, a2, a3};
}
DEVINL u32x2 tr_read(const char* p) {
  return __builtin_bit_cast(u32x2, __builtin_amdgcn_ds_read_tr16_b64_v4i16((LDS_AS s16x4*)p));
}
DEVINL float rdl(float v, int l) { return i2f(__builtin_amdgcn_readlane(f2i(v), l)); }
DEVINL float sel2(int c, float a, float b) { return c ? b : a; }
DEVINL u32 swap2(u32 v) { return (u32)__builtin_amdgcn_update_dpp(0, (int)v, 0x4E, 0xf, 0xf, true); }
DEVINL u32 swap1(u32 v) { return (u32)__builtin_amdgcn_update_dpp(0, (int)v, 0xB1, 0xf, 0xf, true); }
constexpr float LOG2E = 1.4426950408889634f, LN2 = 0.6931471805599453f, INV2PI = 0.15915494309189535f;

constexpr int A_TAB = 0, A_P = 8 * 1536, A_KC = A_P + 24576, A_KCS = 4112, A_GV = A_KC + 2 * A_KCS;
constexpr int B_KC = 47360, B_KCS = 4160, B_KCH = 4 * 4160, B_U = B_KC + 2 * B_KCH, B_US = 5152, B_UCH = 2 * B_US;
constexpr int B_Y = B_U + 2 * B_UCH, B_RED = 4096, B_SC = 0;
constexpr int A_XT = B_Y;
constexpr int C_RAW = 148480, L_CNT = C_RAW + 1024, D_RES = L_CNT + 64, D_GB = D_RES + 8192, LDS_BYTES = D_GB + 256;
static_assert(A_GV + 2048 <= B_KC && B_Y + 40960 <= C_RAW && 131072 <= C_RAW && B_RED + 8192 <= A_KC && LDS_BYTES <= 163840, "lds map");

DEVINL void glds4k(const void* sbase, u32 voff, u32 ldsaddr) {
  asm volatile("s_mov_b32 m0, %2\n\ts_nop 0\n\t"
               "global_load_lds_dwordx4 %0, %1 sc1\n\tglobal_load_lds_dwordx4 %0, %1 offset:1024 sc1\n\t"
               "global_load_lds_dwordx4 %0, %1 offset:2048 sc1\n\tglobal_load_lds_dwordx4 %0, %1 offset:3072 sc1"
               ::"v"(voff), "s"(sbase), "s"(ldsaddr) : "memory");
}
DEVINL u32x4 tr8(const char* p) {
  const u32x2 lo = tr_read(p), hi = tr_read(p + 256);
  return u32x4{lo[0], lo[1], hi[0], hi[1]};
}
template <int OFF> DEVINL void glds1k(const void* sbase, u32 voff, u32 ldsaddr) {
  asm volatile("s_mov_b32 m0, %2\n\ts_nop 0\n\tglobal_load_lds_dwordx4 %0, %1 offset:%3 sc1" ::"v"(voff), "s"(sbase), "s"(ldsaddr), "n"(OFF) : "memory");
}
DEVINL const char* far(const char* p) {
  u32 a = (u32)(size_t)(LDS_AS const char*)p;
  asm("" : "+v"(a));
  return (const char*)(LDS_AS const char*)(size_t)a;
}
struct Ep { float q0, v; };
template <int N> struct IC { static constexpr int v = N; };
#define accg(a, i) a[((i) & 3) + 8 * ((i) >> 2)]
#define accu(a, i) a[((i) & 3) + 8 * ((i) >> 2) + 4]
DEVINL float swi(const f32x16& a, int i, const Ep& q) {
  const float e = __builtin_amdgcn_exp2f(accg(a, i) * q.q0);
  return (accg(a, i) * accu(a, i)) * __builtin_amdgcn_rcpf(fmaf(e, q.v, q.v));
}
DEVINL u32 ln2_fold(f32x2 s, int h, Ep& q) {
  const float m = s[0] * (1.f / D);
  const float v = fmaxf(fmaf(-m, m, s[1] * (1.f / D)), 0.f) + 1e-5f;
  const float rs = __builtin_amdgcn_rsqf(v);
  q.q0 = rs * -LOG2E; q.v = v;
  const float val = h ? v * rs : -m;
  const float vhi = rbf(val);
  return pk(vhi, val - vhi);
}
DEVINL void ln2_tile(const char* rb, f32x2* raw, f32x2* dummy, int lane) {
  const u32x4 ones = {0x3f803f80u, 0x3f803f80u, 0x3f803f80u, 0x3f803f80u};
  f32x4 sg[2], sx[2];
#pragma unroll
  for (int i = 0; i < 2; i++) { sg[i] = f32x4{0.f, 0.f, 0.f, 0.f}; sx[i] = f32x4{0.f, 0.f, 0.f, 0.f}; }
  u32x4 fr[4][2];
  auto rd = [&](int n) NOPK {
#pragma unroll
    for (int gi = 0; gi < 2; gi++) {
      const char* pp = rb + n * 2048 + gi * 32;
      const u32x2 lo = tr_read(pp), hi = tr_read(pp + 1024);
      fr[n & 3][gi] = u32x4{lo[0], lo[1], hi[0], hi[1]};
    }
  };
  rd(0); rd(1); rd(2);
#pragma unroll
  for (int n = 0; n < 16; n++) {
    if (n + 3 < 16) rd(n + 3);
    __builtin_amdgcn_sched_barrier(0);
#pragma unroll
    for (int gi = 0; gi < 2; gi++) {
      sg[gi] = mfma16(fr[n & 3][gi], fr[n & 3][gi], sg[gi]);
      sx[gi] = mfma16(ones, fr[n & 3][gi], sx[gi]);
    }
    __builtin_amdgcn_sched_barrier(0);
  }
#pragma unroll
  for (int gi = 0; gi < 2; gi++) {
    const float gsel = (lane & 2) ? ((lane & 1) ? sg[gi][3] : sg[gi][2]) : ((lane & 1) ? sg[gi][1] : sg[gi][0]);
    f32x2* dst = (lane >> 4) == ((lane & 15) >> 2) ? raw + gi * 16 + (lane & 15) : dummy;
    *dst = f32x2{sx[gi][0], gsel};
  }
}

__global__ NOPK __launch_bounds__(512, 2) void fused(
    const u16* __restrict__ x, const u16* __restrict__ Are, const u16* __restrict__ Aim, const u16* __restrict__ Bre,
    const u16* __restrict__ Bim, const u16* __restrict__ Cre, const u16* __restrict__ Cim, const u16* __restrict__ logdt,
    const u16* __restrict__ dskip, const u16* __restrict__ w1, const u16* __restrict__ w3, const u16* __restrict__ w2,
    const u16* __restrict__ cvec, const u16* __restrict__ wmod, char* ws, u16* __restrict__ out) {
  __shared__ __attribute__((aligned(16))) char smem[LDS_BYTES];
  const int b = blockIdx.x, tid = threadIdx.x, lane = tid & 63;
  const int w = __builtin_amdgcn_readfirstlane(tid >> 6);
  TSDECL; TS(0);
  const int xg = b & 7, idx = b >> 3;
  const int p = xg * 32 + idx;
  const int r = lane & 15, g = lane >> 4;
  asm volatile("" ::"s"(x), "s"(Are), "s"(Aim), "s"(Bre), "s"(Bim), "s"(Cre), "s"(Cim), "s"(logdt), "s"(dskip), "s"(w1), "s"(w3),
               "s"(w2), "s"(cvec), "s"(wmod), "s"(ws), "s"(out));
  u32x4 xr, wm0, wm1;
  u32 vare, vaim, vbre, vbim, vcre, vcim, c0, c1;
  const int cg = xg * 24 + (idx < 24 ? idx : 23);
  u32 e_prev, ldt2, dsk;
  lp16(xr, x + b * 4096, tid * 16);
  sld4(e_prev, ws + WS_E); sld4(ldt2, logdt + 2 * p); sld4(dsk, dskip + 2 * p);
  {
    const u32 so = (p * 256 + w * 32 + (lane & 31)) * 2;
    lp2(vare, Are, so); lp2(vaim, Aim, so); lp2(vbre, Bre, so); lp2(vbim, Bim, so); lp2(vcre, Cre, so); lp2(vcim, Cim, so);
    const u32 mo = (tid >> 3) * 6144 + (tid & 7) * 16;
    const u16* wmp = wmod + (cg & 3) * 128 * 3072 + (cg >> 2) * 64;
    lp16(wm0, wmp, mo); lp16(wm1, wmp + 64 * 3072, mo);
    lp2(c0, cvec + (cg & 3) * 128, (tid >> 3) * 2); lp2<128>(c1, cvec + (cg & 3) * 128, (tid >> 3) * 2);
  }
  u32 pf;
  {
    unsigned long long pc;
    u32 po = ((idx & PFM) * 512 + tid) * (PFB / (PFM + 1) / 512);
    asm volatile("s_getpc_b64 %1\n.Lpf0:\n\tv_min_u32 %2, .Lpf1-.Lpf0, %2\n\tglobal_load_dword %0, %2, %1"
                 : "=v"(pf), "=&s"(pc), "+v"(po)::"memory");
  }
  TS(1);
  if (tid < 32) *(u32x4*)(smem + B_KC + (tid >> 2) * B_KCS + 4096 + (tid & 3) * 16) = u32x4{0, 0, 0, 0};
  else if (tid < 36) *(u32x4*)(smem + L_CNT + (tid - 32) * 16) = u32x4{0, 0, 0, 0};
  else if (tid < 38) *(u32x2*)(smem + B_U + (tid & 1) * B_UCH + B_US + 1024) = u32x2{0, 0};
  else if (tid >= 256) *(u32x4*)(smem + B_U + (w - 4) * B_US + lane * 16) = u32x4{0, 0, 0, 0};
  asm volatile("s_waitcnt lgkmcnt(0)" : "+s"(e_prev), "+s"(ldt2), "+s"(dsk)::"memory");
  const u32 tag = e_prev * 1664525u + 1013904223u;
  wait_vm<11>(); tie(xr);
  {
    *(u32x4*)(smem + A_XT + w * 1056 + lane * 16) = xr;
    float f[8];
#pragma unroll
    for (int j = 0; j < 4; j++) { f[2 * j] = bflo(xr[j]); f[2 * j + 1] = bfhi(xr[j]); }
    const float m = wave_sum(((f[0] + f[1]) + (f[2] + f[3])) + ((f[4] + f[5]) + (f[6] + f[7]))) * (1.f / D);
    float s = 0.f;
#pragma unroll
    for (int j = 0; j < 8; j++) { f[j] -= m; s = fmaf(f[j], f[j], s); }
    const float sd = __builtin_amdgcn_sqrtf(rbf(rbf(wave_sum(s) * (1.f / D)) + 1e-5f));
    if (lane == 0) *(u32*)(smem + A_XT + w * 1056 + 1024) = pk(m, sd);
  }
  TS(2);
  wait_vm<5>(); tie(vare); tie(vaim); tie(vbre); tie(vbim); tie(vcre); tie(vcim);
  char* tab = smem + A_TAB + w * 1536;
  {
    const float dt = rbf(__builtin_amdgcn_exp2f((w >> 2 ? bfhi(ldt2) : bflo(ldt2)) * LOG2E));
    const float a = dt * bflo(vare) * LOG2E, bq = dt * bflo(vaim) * INV2PI;
    const float br = dt * bflo(vbre), bi = dt * bflo(vbim), cr = bflo(vcre), ci = bflo(vcim);
    const float m = lane < 32 ? 1.f : 256.f;
    const float e = __builtin_amdgcn_exp2f(a * m), ph = __builtin_amdgcn_fractf(-bq * m);
    const float ex = e * __builtin_amdgcn_cosf(ph), ey = e * __builtin_amdgcn_sinf(ph);
    char* t = tab + (lane & 31) * 48;
    *(f32x2*)(t + 16 + (lane >> 5) * 8) = f32x2{ex, ey};
    f32x4 o = {a, bq, cr * br - ci * bi, cr * bi + ci * br};
    if (lane >= 32) o = f32x4{ex, ey, ex * ex - ey * ey, (ex + ex) * ey};
    *(f32x4*)(t + (lane >> 5) * 32) = o;
  }
  TS(3);
  lds_barrier();
  {
    const char* xt = smem + A_XT + (lane & 7) * 1056;
    const u32 st = *(const u32*)(xt + 1024);
#pragma unroll
    for (int e = 0; e < 2; e++) {
      const u32x2 xq = *(const u32x2*)(xt + (e * 64 + w * 8 + (lane >> 3)) * 8);
      st16(ws + WS_XG + (size_t)(e * 64 + w * 8) * 32768 + b * 128, (lane >> 3) * 32768 + (lane & 7) * 16, u32x4{xq[0], xq[1], st, tag});
    }
  }
  TS(41);
  const int fb = (xg >> 1) * 4 + (idx & 3), mat = (idx >> 2) & 1, dq = (xg & 1) * 4 + (idx >> 3), d0 = dq * 64 + w * 8;
  u32 v[8], t2[8];
  {
    const u16* wp = (mat ? w3 : w1) + (size_t)d0 * F + fb * 112;
    const u32 vo = (lane < 56 ? lane : 55) * 4;
#pragma unroll
    for (int i = 0; i < 4; i++) { lp4<0>(v[2 * i], wp + 2 * i * F, vo); lp4<2 * F>(v[2 * i + 1], wp + 2 * i * F, vo); }
  }
  auto gemv = [&]() NOPK
  {
    wait_vm<10>(); tie(pf); tie(wm0); tie(wm1); tie(c0); tie(c1);
    const float cx0 = bflo(c0), cx1 = bflo(c1);
    const float s0 = rbf(cx0 * __builtin_amdgcn_rcpf(1.f + __builtin_amdgcn_exp2f(cx0 * -LOG2E)));
    const float s1 = rbf(cx1 * __builtin_amdgcn_rcpf(1.f + __builtin_amdgcn_exp2f(cx1 * -LOG2E)));
    float acc[8];
#pragma unroll
    for (int j = 0; j < 4; j++) {
      acc[2 * j] = fmaf(s0, bflo(wm0[j]), s1 * bflo(wm1[j]));
      acc[2 * j + 1] = fmaf(s0, bfhi(wm0[j]), s1 * bfhi(wm1[j]));
    }
    const float q0 = dpp_add<0x128>(rs16(rs32(acc[0], acc[4]), rs32(acc[2], acc[6])));
    const float q1 = dpp_add<0x128>(rs16(rs32(acc[1], acc[5]), rs32(acc[3], acc[7])));
    *(float*)(smem + A_GV + w * 256 + (lane & 7) * 32 + (lane >> 3) * 4) = (lane & 8) ? q1 : q0;
    if (arrive_last(smem + L_CNT, 8) && idx < 24) {
      float s = 0.f;
#pragma unroll
      for (int i = 0; i < 8; i++) s += *(const float*)(smem + A_GV + i * 256 + lane * 4);
      st8(ws + WS_MG + (cg & 3) * 24576 + (cg >> 2) * 512, lane * 8, u32x2{f2u(s), tag});
    }
  };
  TS(4);
  const u32 gcode = __builtin_amdgcn_perm(0x400b0916u, 0x0a201408u, (r & 7) | 0x0c0c0c00u);
  const int gx = r < 8 ? 2 * r : (int)((gcode >> 2) * 256 + (gcode & 3) * 4);
  {
    asm volatile("" ::: "memory");
    const float mF = (float)(16 * r), mX = (float)gx;
    const char* tp = tab + g * 8 * 48;
    const char* tq = tp + (r < 8 ? 16 : 32);
    f32x4 acc[3];
#pragma unroll
    for (int s = 0; s < 3; s++) acc[s] = f32x4{0.f, 0.f, 0.f, 0.f};
#pragma unroll
    for (int ks = 0; ks < 2; ks++) {
      u32x4 Ff, Gf[3];
#pragma unroll
      for (int i = 0; i < 4; i++) {
        const f32x4 s0 = *(const f32x4*)(tp + (ks * 4 + i) * 48), s1 = *(const f32x4*)(tq + (ks * 4 + i) * 48);
        const float e0 = __builtin_amdgcn_exp2f(s0[0] * mF), p0 = __builtin_amdgcn_fractf(s0[1] * mF);
        const float zx = e0 * __builtin_amdgcn_cosf(p0), zy = e0 * __builtin_amdgcn_sinf(p0);
        Ff[i] = pk(s0[2] * zx - s0[3] * zy, s0[2] * zy + s0[3] * zx);
        const float e1 = __builtin_amdgcn_exp2f(s0[0] * mX), p1 = __builtin_amdgcn_fractf(s0[1] * -mX);
        const float xx = e1 * __builtin_amdgcn_cosf(p1), xy = e1 * __builtin_amdgcn_sinf(p1);
        Gf[0][i] = pk(xx, xy);
        Gf[1][i] = pk(xx * s1[0] - xy * s1[1], xx * s1[1] + xy * s1[0]);
        Gf[2][i] = pk(xx * s1[2] - xy * s1[3], xx * s1[3] + xy * s1[2]);
      }
#pragma unroll
      for (int s = 0; s < 3; s++) acc[s] = mfma16(Ff, Gf[s], acc[s]);
      if (ks == 0) { TS(45); gemv(); TS(44); }
    }
#pragma unroll
    for (int s = 0; s < 3; s++) *(f32x4*)(smem + A_P + w * 3072 + s * 1024 + lane * 16) = acc[s];
  }
  TS(5);
  u32x4 xg4[4];
  u32x2 mg, mh, mc[4];
  const char* xgb = ws + WS_XG + (size_t)(p >> 1) * 32768;
  const u32 xgo = (tid >> 2) * 256 + (tid & 3) * 16;
  u32 mga = (4 - ((lane >> 3) & 1)) * 512 + d0 + (lane & 7), mgb = ((0x5210 >> ((lane & 6) * 2)) & 7) * 512 + 2 * p + (lane & 1);
  tie(mga); tie(mgb);
  const u32 mgo = ((lane & 16) ? mgb : mga) * 8 + (lane >> 5) * 24576;
  auto lookx = [&]() NOPK {
    ld16<0>(xg4[0], xgb, xgo); ld16<64>(xg4[1], xgb, xgo); ld16<128>(xg4[2], xgb, xgo); ld16<192>(xg4[3], xgb, xgo);
  };
  auto lookm = [&]() NOPK {
    ld8(mg, ws + WS_MG, mgo); ld8(mh, ws + WS_MG + 49152, mgo);
  };
  auto lookc = [&]() NOPK {
#pragma unroll
    for (int q = 0; q < 4; q++) ld8(mc[q], ws + WS_MG + q * 24576 + 16384, tid * 8);
  };
  lookx();
  lds_barrier();
  TS(6);
  TSF(0, 6); TSF(41, 41); TSF(44, 45);
  const int kch = tid >> 8;
  u16* Kc = (u16*)(smem + A_KC + kch * A_KCS);
  if ((w & 3) < 3) {
    const int s = w & 3;
    const char* pp = smem + A_P + kch * 12288 + s * 1024 + lane * 16;
    const f32x4 q = (*(const f32x4*)(pp) + *(const f32x4*)(pp + 3072)) + (*(const f32x4*)(pp + 6144) + *(const f32x4*)(pp + 9216));
    const int pos = gx + 64 * g + (r < 8 ? (s == 0 ? 0 : s == 1 ? 1 : 256) : s * 256);
    const u32 o0 = pk(q[0], q[1]), o1 = pk(q[2], q[3]);
    if (pos <= 2048) Kc[pos] = (u16)o0;
    if (pos < 2048) {
      Kc[pos + 16] = (u16)(o0 >> 16);
      Kc[pos + 32] = (u16)o1;
      Kc[pos + 48] = (u16)(o1 >> 16);
    }
  }
  wait_vm<4>();
#pragma unroll
  for (int i = 0; i < 8; i++) tie(v[i]);
  {
    u32x4 o0, o1;
#pragma unroll
    for (int j = 0; j < 4; j++) {
      o0[j] = __builtin_amdgcn_perm(v[2 * j + 1], v[2 * j], 0x05040100u);
      o1[j] = __builtin_amdgcn_perm(v[2 * j + 1], v[2 * j], 0x07060302u);
    }
    st16x2_56(ws + WS_W1F + (size_t)(fb * 7 * 32 + (d0 >> 4)) * 1024 + (w & 1) * 512 + mat * 128, (lane >> 3) * 32768 + (lane & 7) * 16, o0, o1);
  }
  lds_barrier();
  TS(7);
  lookm(); lookc();
  {
    u32 e[6];
    const int ki = tid & 255;
#pragma unroll
    for (int j = 0; j < 6; j++) {
      const float l0 = (float)(2047 - 8 * ki - 2 * j);
      const u32 bits = pk(fmaxf(l0, 0.f), fmaxf(l0 - 1.f, 0.f));
      e[j] = (u32)Kc[(int)bflo(bits)];
      if (j < 5) e[j] |= (u32)Kc[(int)bfhi(bits)] << 16;
    }
    if (ki == 255) { e[4] = 0; e[5] = 0; }
    const u32 a0 = __builtin_amdgcn_alignbit(e[1], e[0], 16), a1 = __builtin_amdgcn_alignbit(e[2], e[1], 16);
    const u32 a2 = __builtin_amdgcn_alignbit(e[3], e[2], 16), a3 = __builtin_amdgcn_alignbit(e[4], e[3], 16);
    const u32 a4 = __builtin_amdgcn_alignbit(e[5], e[4], 16);
    char* kc = smem + B_KC + kch * B_KCH + ki * 16;
    *(u32x4*)(kc) = u32x4{e[0], e[1], e[2], e[3]};
    *(u32x4*)(kc + B_KCS) = u32x4{a0, a1, a2, a3};
    *(u32x4*)(kc + 2 * B_KCS) = u32x4{e[1], e[2], e[3], e[4]};
    *(u32x4*)(kc + 3 * B_KCS) = u32x4{a1, a2, a3, a4};
  }
  TS(8);
  wait_vm<0>(); tie(xg4[0]); tie(xg4[1]); tie(xg4[2]); tie(xg4[3]); tie(mg); tie(mh); tie(mc[0]); tie(mc[1]); tie(mc[2]); tie(mc[3]);
  if (arrive_last(smem + L_CNT + 4, 8)) st4one(ws + WS_FW + (fb * 16 + mat * 8 + dq) * 128, tag);
  TS(47);
#pragma unroll 1
  for (int it = 0; it < SPIN_CAP && !all_lanes(xg4[0][3] == tag && xg4[1][3] == tag && xg4[2][3] == tag && xg4[3][3] == tag); it++) {
    lookx();
    wait_vm<0>(); tie(xg4[0]); tie(xg4[1]); tie(xg4[2]); tie(xg4[3]);
  }
#pragma unroll 1
  for (int it = 0; it < SPIN_CAP && !all_lanes(mg[1] == tag && mh[1] == tag); it++) {
    lookm();
    wait_vm<0>(); tie(mg); tie(mh);
  }
  TS(9);
  const int tt = (xg & 1) * 8 + (idx & 7), fs = (xg >> 1) * 4 + (idx >> 3);
  u32 fw;
  const int b2 = b < 224 ? b : 223;
  const u32 fwo = fs * 2048 + (lane & 15) * 128;
  const int g8 = tid >> 1, cc = tid & 1;
  const float mq = u2f(mg[0]) + u2f(mh[0]);
  const float mv = rbf(rs32(mq, mq)), mv1 = rbf(1.f + mv);
  const float ga = sel2(cc, rdl(mv, 20), rdl(mv, 21));
  u32x4 hp, xv;
  {
    const float sh0 = rdl(mv, 16), sh1 = rdl(mv, 17), sc0 = rdl(mv1, 18), sc1 = rdl(mv1, 19);
    const u32 psel = cc ? 0x03020706u : 0x05040100u;
    const bool up = tid & 2;
    u32 ph[4], px[4];
#pragma unroll
    for (int i = 0; i < 4; i++) {
      const u32 xq = (p & 1) ? xg4[i][1] : xg4[i][0];
      const float m = bflo(xg4[i][2]), rs = __builtin_amdgcn_rcpf(bfhi(xg4[i][2]));
      const u32 n = pk((bflo(xq) - m) * rs, (bfhi(xq) - m) * rs);
      const u32 hq = pk(fmaf(bflo(n), sc0, sh0), fmaf(bfhi(n), sc1, sh1));
      ph[i] = __builtin_amdgcn_perm(swap1(hq), hq, psel);
      px[i] = __builtin_amdgcn_perm(swap1(xq), xq, psel);
    }
#pragma unroll
    for (int j = 0; j < 2; j++) {
      const u32 rh = swap2(up ? ph[j] : ph[2 + j]), kh = up ? ph[2 + j] : ph[j];
      hp[2 * j] = up ? rh : kh; hp[2 * j + 1] = up ? kh : rh;
      const u32 rx = swap2(up ? px[j] : px[2 + j]), kx = up ? px[2 + j] : px[j];
      xv[2 * j] = up ? rx : kx; xv[2 * j + 1] = up ? kx : rx;
    }
    char* u = smem + B_U + cc * B_UCH + 1024 + g8 * 16;
    *(u32x4*)(u) = hp;
    *(u32x2*)(u + B_US + 8) = u32x2{hp[0], hp[1]};
    *(u32x2*)(u + B_US + 16) = u32x2{hp[2], hp[3]};
  }
#pragma unroll 1
  for (int it = 0; it < SPIN_CAP && !all_lanes(mc[0][1] == tag && mc[1][1] == tag && mc[2][1] == tag && mc[3][1] == tag); it++) {
    lookc();
    wait_vm<0>(); tie(mc[0]); tie(mc[1]); tie(mc[2]); tie(mc[3]);
  }
  {
    const float sc = 1.f + rbf((u2f(mc[0][0]) + u2f(mc[2][0])) + (u2f(mc[1][0]) + u2f(mc[3][0])));
    const u32 e = pk(sc, sc) & ((tid & 1) ? 0xffff0000u : 0xffffu);
    *(u32*)(smem + B_SC + tid * 4) = e;
    *(u32*)(smem + B_SC + 2048 + tid * 4) = e;
  }
  TS(10);
  lds_barrier();
  TS(11);
  const int h = lane >> 5, c = lane & 31;
  const int tt2 = (~xg & 1) * 8 + (xg & 6) + (idx >> 4), sel = (idx >> 3) & 1, dt = idx & 7;
  const int rot = (2 * (idx >> 3) + idx) & 7;
  const int ft = fs * 7 + w - 1;
  u32x4 A[32];
  const u32 ao = lane * 16;
  auto lda = [&](int i) NOPK {
    const char* wq = ws + WS_W1F + (size_t)ft * 32768 + (((i >> 2) + rot) & 7) * 4096;
    asm volatile("s_cmp_eq_u32 %4, 0\n\ts_cbranch_scc1 .Lna%=\n\tglobal_load_dwordx4 %0, %1, %2 offset:%3 sc1\n.Lna%=:"
                 : "=v"(A[i]) : "v"(ao), "s"(wq), "n"((i & 3) * 1024), "s"(w) : "memory", "scc");
  };
  const char* tb = smem + B_SC + rot * 256 + h * 32;
  u32x4 tq[3][2];
  auto ldt = [&](int i) NOPK { tq[i % 3][0] = *(const u32x4*)(tb + i * 64); tq[i % 3][1] = *(const u32x4*)(tb + i * 64 + 16); };
  auto scl = [&](int i) NOPK { tie(A[i]); scale8(A[i], tq[i % 3][0], tq[i % 3][1], w); tie(A[i]); };
  auto scl_run = [&](int lo, int hi) NOPK {
#pragma unroll
    for (int i = lo; i < hi + 2; i++) {
      if (i < hi) ldt(i);
      __builtin_amdgcn_sched_barrier(0);
      if (i >= lo + 2) scl(i - 2);
      __builtin_amdgcn_sched_barrier(0);
    }
  };
  {
    const int ch = w & 1, vh = (w >> 1) & 1, tg = w >> 2;
    const char* pa = smem + B_KC + ch * B_KCH + (3 - (r & 3)) * B_KCS + 2 * (2040 - 8 * (r >> 2) + 8 * g) - 512 * 7 - 64 * (4 * vh + 3);
    const char* pb = smem + B_U + ch * B_UCH + ((r & 1) ? 0 : B_US) + 2 * (512 + 32 * (r >> 1) + 8 * g) - 64 * (4 * vh + 3) - 512 * tg;
    f32x4 acc[4];
#pragma unroll
    for (int i = 0; i < 4; i++) acc[i] = f32x4{0.f, 0.f, 0.f, 0.f};
    u32x4 fr[2][8];
    auto fld = [&](int st, int n) NOPK {
      const int e = n == 0 ? 0 : n < 5 ? 9 - 2 * n : 2 * n - 8;
      const bool isa = ((e ^ st) & 1) == 0;
      fr[st & 1][e] = *(const u32x4*)((isa ? pa + 512 * (7 - e) : pb + 512 * e) + 64 * (3 - (st >> 1)));
    };
#pragma unroll
    for (int n = 0; n < 8; n++) fld(0, n);
    __builtin_amdgcn_sched_barrier(0);
    {
      f32x4 sums = {0.f, 0.f, 0.f, 0.f};
#pragma unroll
      for (int j = 0; j < 4; j++) {
        const float k0 = rdl(mv1, 2 * j), k1 = rdl(mv1, 2 * j + 1), s0 = rdl(mv, 8 + 2 * j), s1 = rdl(mv, 8 + 2 * j + 1);
        const float a0 = bflo(v[2 * j]), a1 = bflo(v[2 * j + 1]), b0 = bfhi(v[2 * j]), b1 = bfhi(v[2 * j + 1]);
        sums[0] = dot2(pk(a0 * k0, a1 * k1), 0x3f803f80u, sums[0]);
        sums[1] = dot2(pk(b0 * k0, b1 * k1), 0x3f803f80u, sums[1]);
        sums[2] = fmaf(a0, s0, fmaf(a1, s1, sums[2]));
        sums[3] = fmaf(b0, s0, fmaf(b1, s1, sums[3]));
      }
      *(f32x4*)(smem + B_RED + tid * 16) = sums;
    }
    ld4(fw, ws + WS_FW, fwo);
#pragma unroll
    for (int i = 0; i < 2; i++) {
      const u16* wq = w2 + (size_t)(b2 * 8 + i * 4) * D;
      const u32 to = tid * 2;
      lp2<0>(t2[4 * i], wq, to); lp2<1024>(t2[4 * i + 1], wq, to); lp2<2048>(t2[4 * i + 2], wq, to); lp2<3072>(t2[4 * i + 3], wq, to);
    }
    __builtin_amdgcn_sched_barrier(0);
#pragma unroll
    for (int st = 0; st < 8; st++) {
      if (st == LST) {
        wait_vm<8>(); tie(fw);
        spin4(fw, tag, ws + WS_FW, fwo);
        TS(46);
      }
#pragma unroll
      for (int n = 0; n < 10; n++) {
        const int ev = n < 4 ? 0 : n < 7 ? 2 : n < 9 ? 4 : 6, ai = n < 4 ? n : n < 7 ? n - 4 : n < 9 ? n - 7 : 0, od = 7 - 2 * ai - ev;
        if (st < 7 && n < 8) fld(st + 1, n);
        __builtin_amdgcn_sched_barrier(0);
        acc[ai] = (st & 1) ? mfma16(fr[1][od], fr[1][ev], acc[ai]) : mfma16(fr[0][ev], fr[0][od], acc[ai]);
        const int sl = st - LST, ss = sl - SLAG;
        if (sl >= 0 && n < 8 && (n & 1) == 0) lda(4 * sl + n / 2);
        if (ss >= 0 && n < 8 && (n & 1) == 0) ldt(4 * ss + n / 2);
        if (ss >= 0 && n > 2 && (n & 1) == 1) { if (n < 9) wait_vm<4 * SLAG + 1>(); else wait_vm<4 * SLAG>(); scl(4 * ss + (n - 3) / 2); }
        __builtin_amdgcn_sched_barrier(0);
      }
    }
    TS(12);
    float* Y = (float*)(smem + B_Y) + (vh * 2 + ch) * 2560 + 40 * (r >> 1) + 4 * (r & 1) + 8 * g - 320 * tg;
#pragma unroll
    for (int i = 0; i < 4; i++) *(f32x4*)(Y + 320 * (7 - 2 * i)) = acc[i];
  }
  lds_barrier();
  TS(13);
  TSF(7, 13); TSF(46, 47);
  {
    const float* Y = (const float*)(smem + B_Y) + cc * 2560 + 8 * g8 + 8 * (g8 >> 2);
    const f32x4 ya = *(const f32x4*)Y + *(const f32x4*)(Y + 5120), yb = *(const f32x4*)(Y + 4) + *(const f32x4*)(Y + 5124);
    const float dk = cc ? bfhi(dsk) : bflo(dsk);
    u32x4 o;
#pragma unroll
    for (int j = 0; j < 4; j++) {
      const float y0 = j < 2 ? ya[2 * j] : yb[2 * j - 4], y1 = j < 2 ? ya[2 * j + 1] : yb[2 * j - 3];
      o[j] = pk(fmaf(ga, fmaf(bflo(hp[j]), dk, y0), bflo(xv[j])), fmaf(ga, fmaf(bfhi(hp[j]), dk, y1), bfhi(xv[j])));
    }
    st16(ws + WS_X1 + p * 128, (g8 >> 2) * 32768 + cc * 64 + (g8 & 3) * 16, o);
  }
  TS(14);
  constexpr int NA = 4 * (8 - LST), S0 = NA - 4 * SLAG, S1 = S0 + SPRE;
  wait_vm<NA - S1 + 1>();
  if (w == 0) wait_vm<1>();
  {
#pragma unroll
    for (int i = 0; i < 8; i++) tie(t2[i]);
    u32x4 o0;
#pragma unroll
    for (int j = 0; j < 4; j++) o0[j] = __builtin_amdgcn_perm(t2[2 * j + 1], t2[2 * j], 0x05040100u);
    st16(ws + WS_W2F + w * 229376 + (b2 >> 2) * 4096 + (b2 & 3) * 256, g * 1024 + r * 16, o0);
  }
  f32x2* raw = (f32x2*)(smem + C_RAW);
  u32x4 W2x[12];
  u32 fh;
  const u32 lds0 = (u32)(size_t)(LDS_AS char*)smem;
  const char* xa = ws + WS_X1 + tt * 131072 + ((w + rot) & 7) * 4096;
  const u32 lpc = lds0 + w * 4096;
  const int rt2 = dt + 2 * (idx >> 3), rot2 = rt2 < 7 ? rt2 : rt2 - 7;
#define kof(j) ((j) + rot2 < 7 ? (j) + rot2 : (j) + rot2 - 7)
  const char* w2b = ws + WS_W2F + dt * 229376 + w * 28672;
  const char* fhb = ws + WS_FH + tt2 * 2048 + w * 256;
  const u32 fho = ((lane >> 3) & 1) * 128 + g * 32 + (lane & 7) * 4;
  const char* fpb = ws + WS_FH + (tt * 16 + fs) * 128 + w * 4;
  auto ldw2 = [&](int j) NOPK {
    const char* q = w2b + kof(j >> 2) * 4096;
    if (j < 12) ld16k(W2x[j], q, j & 3, ao); else ld16k(A[j - 12], q, j & 3, ao);
  };
  if (w == 0) {
    f32x4 s = {0.f, 0.f, 0.f, 0.f};
#pragma unroll
    for (int q = 0; q < 8; q++) s += *(const f32x4*)(smem + B_RED + q * 1024 + lane * 16);
    st16(ws + WS_CD + ((dq * 16 + fb) * 2 + mat) * 1024, lane * 16, __builtin_bit_cast(u32x4, s));
    st8m<1>(ws + WS_GB + p * 8, 0, u32x2{f2u(rdl(mv, 22)), f2u(rdl(mv, 23))});
  }
  scl_run(S0, S1);
  wait_vm<0>();
  TS(15);
  bar();
  if (w == 0) st4one(ws + WS_FX + (b & 63) * 128 + (b >> 6) * 4, tag);
  TS(16);
  scl_run(S1, S1 + 2);
#pragma unroll
  for (int i = NA; i < 32; i++) lda(i);
  scl_run(S1 + 2, NA);
  wait_vm<0>();
  scl_run(NA, 32);
  if (w == 0) {
    __builtin_amdgcn_s_setprio(3);
#pragma unroll 1
    for (int it = 0; it < SPIN_CAP; it++) {
      u32x4 f;
      ld16(f, ws + WS_FX, lane * 128); wait_vm<0>(); tie(f);
      if (all_lanes(f[0] == tag && f[1] == tag && f[2] == tag && f[3] == tag)) break;
    }
    TS(17);
    bar();
    TS(18);
    glds4k(xa, ao, lpc); glds4k(xa + 32768, ao, lpc + 32768);
    const char* rb = smem + (lane >> 2) * 64 + (lane & 3) * 8;
    wait_vm<4>(); bar();
    TS(19); TC(20);
    TSF(14, 20);
    glds4k(xa + 65536, ao, lpc + 65536); glds4k(xa + 98304, ao, lpc + 98304);
#pragma unroll
    for (int q = 0; q < 4; q++) {
      const char* xr_ = ws + WS_X1 + (tt2 * 4 + q) * 32768 + dt * 4096 + sel * 32;
      const u32 xo = r * 256 + h * 64 + (g & 1) * 16, la = lds0 + D_RES + q * 2048;
      glds1k<0>(xr_, xo, la); glds1k<0>(xr_ + 128, xo, la + 1024);
    }
    asm volatile("s_mov_b32 m0, %2\n\ts_nop 0\n\tglobal_load_lds_dword %0, %1 sc1"
                 ::"v"(lane * 4), "s"(ws + WS_GB + dt * 256), "s"(lds0 + D_GB) : "memory");
    if (b == 0) st4one(ws + WS_E, tag);
    ln2_tile(rb, raw, raw + 132, lane);
    TS(21);
    wait_vm<17>(); lds_barrier();
    TS(22);
    st4one(fpb, tag); st4one(fpb + 32, tag);
#pragma unroll
    for (int i = 0; i < 14; i++) ldw2(i);
    ln2_tile(rb + 32768, raw + 32, raw + 132, lane);
    TS(23);
    wait_vm<16>(); lds_barrier();
    TS(24);
#pragma unroll
    for (int i = 14; i < 28; i++) ldw2(i);
    rb = far(rb + 65536);
    ln2_tile(rb, raw + 64, raw + 132, lane);
    asm volatile("" ::: "memory");
    *(volatile LDS_AS u32*)(smem + L_CNT + 16) = 1;
    TS(25);
    ln2_tile(rb + 32768, raw + 96, raw + 132, lane);
    asm volatile("" ::: "memory");
    *(volatile LDS_AS u32*)(smem + L_CNT + 16) = 2;
    __builtin_amdgcn_s_setprio(0);
    TS(26); TS(27); TC(28);
    TSF(21, 28);
    ld4(fh, fhb, fho);
    wait_vm<0>(); tie(fh);
#pragma unroll
    for (int i = 0; i < 28; i++) { if (i < 12) tie(W2x[i]); else tie(A[i - 12]); }
    TS(29);
  }
  int w_ = w;
  asm volatile("" : "+s"(w_));
  if (w_ != 0) {
    const char* rb = smem + h * 512 + ((lane >> 2) & 3) * 64 + ((lane >> 4) & 1) * 32 + (lane & 3) * 8;
    const char* rb2 = far(rb + 65536);
    u32 cdv[8];
    f32x16 acc[2], zero;
#pragma unroll
    for (int i = 0; i < 16; i++) zero[i] = 0.f;
    TS(17);
    lds_barrier();
    TS(18);
    glds4k(xa, ao, lpc); glds4k(xa + 32768, ao, lpc + 32768);
    wait_vm<4>();
    bar();
    TS(19); TC(20);
    TSF(14, 20);
    __builtin_amdgcn_s_setprio(3);
    u32x4 Bq[3], Ax, op;
    Bq[0] = tr8(rb); Bq[1] = tr8(rb + 1024);
    f32x2 rw;
    Ep q[2];
    float ev, gu, oa;
    const char* hb = ws + WS_HF + (size_t)(tt * 8 * 56 + (ft >> 1)) * 1024 + (ft & 1) * 512;
    const u32 ho = (c >> 4) * 57344 + h * 256 + r * 16;
    auto stat_wait = [&](u32 need) NOPK {
#pragma unroll 1
      for (int it = 0; it < SPIN_CAP * 16; it++)
        if (__builtin_amdgcn_readfirstlane(*(volatile LDS_AS u32*)(smem + L_CNT + 16)) >= need) break;
      asm volatile("" ::: "memory");
    };
    auto tile = [&](auto tc) NOPK {
    constexpr int T = decltype(tc)::v, E = T - 1;
#pragma unroll
    for (int ks = 0; ks < 32; ks++) {
      const int n = T * 32 + ks, m = n + 2;
      if (n == 30) {
        TS(21);
        wait_vm<8>();
#pragma unroll
        for (int i = 0; i < 8; i++) tie(cdv[i]);
        lds_barrier();
        TS(22);
      } else if (n == 62) {
        TS(23);
        wait_vm<11>();
        lds_barrier();
        TS(24);
      }
      if (n == 30 || n == 62) __builtin_amdgcn_s_setprio(3);
      if (n == 14 || n == 46 || n == 100) __builtin_amdgcn_s_setprio(2);
      if (n == 22 || n == 54 || n == 118) __builtin_amdgcn_s_setprio(1);
      if (n == 27 || n == 59) __builtin_amdgcn_s_setprio(0);
      if (n == 96) { TS(25); tie(acc[0]); stat_wait(1); }
      if (n == 118) { tie(acc[1]); stat_wait(2); }
      if (n == 120) wait_vm<10>();
      if (m < 128) Bq[m % 3] = tr8((m < 64 ? rb : rb2) + (m & 63) * 1024);
      if (T > 0 && ks == 0) rw = raw[E * 32 + c];
      if (n == 118) rw = raw[96 + c];
      if (n >= 120) A[n - 112] = *(const u32x4*)(smem + ((n >> 2) & 1) * 32768 + w * 4096 + (n & 3) * 1024 + lane * 16);
      __builtin_amdgcn_sched_barrier(0);
      acc[T & 1] = mfma32(A[ks], Bq[n % 3], ks == 0 ? zero : acc[T & 1]);
      if (n == 31) {
        const float cd = ((u2f(cdv[0]) + u2f(cdv[1])) + (u2f(cdv[2]) + u2f(cdv[3]))) + ((u2f(cdv[4]) + u2f(cdv[5])) + (u2f(cdv[6]) + u2f(cdv[7])));
        const float chi = rbf(cd);
        Ax = u32x4{pk(chi, chi), pk(cd - chi, cd - chi), 0, 0};
      }
      if ((T > 0 && ks == 2) || n == 120) {
        const int X = (n == 120 ? 3 : E) & 1;
        const u32 be = ln2_fold(rw, h, q[X]);
        acc[X] = mfma32(Ax, u32x4{be, be, 0, 0}, acc[X]);
      }
      if (T > 0 && ks >= 4 && ks < 20) {
        const int k = (ks - 4) >> 1, i = (k >> 1) + 4 * (k & 1);
        if ((ks & 1) == 0) {
          ev = fmaf(__builtin_amdgcn_exp2f(accg(acc[E & 1], i) * q[E & 1].q0), q[E & 1].v, q[E & 1].v);
          gu = accg(acc[E & 1], i) * accu(acc[E & 1], i);
        } else {
          const float o = gu * __builtin_amdgcn_rcpf(ev);
          if (k & 1) op[k >> 1] = pk(oa, o); else oa = o;
        }
      }
      __builtin_amdgcn_sched_barrier(0);
      if (n == 1) {
        const u32 co = ((c >> 3) & 1) * 1024 + ((w - 1) * 8 + (c & 7)) * 16 + h * 8 + (c >> 4) * 4;
#pragma unroll
        for (int i = 0; i < 8; i++) ld4(cdv[i], ws + WS_CD + (i * 16 + fs) * 2048, co);
      }
      if (T == 0 && ks >= 4 && ks < 28 && ks % 3 == 1) {
        const int i = (ks - 4) / 3, o = 65536 + (i >> 2) * 32768;
        if ((i & 3) == 0) glds1k<0>(xa + o, ao, lpc + o); else if ((i & 3) == 1) glds1k<1024>(xa + o, ao, lpc + o);
        else if ((i & 3) == 2) glds1k<2048>(xa + o, ao, lpc + o); else glds1k<3072>(xa + o, ao, lpc + o);
      }
      if (T > 1 && ks == 9) { wait_vm<6>(); st4one(fpb + (T - 2) * 32, tag); }
      if (T > 0 && ks == 20) st16(hb + E * 114688, ho, op);
      if (T > 0 && ks % 3 == 1 && ks < 30) {
        const int i = ks / 3;
        if (T == 3) { if (i < 8) ldw2(12 + i); }
        else if (i < 6) ldw2(6 * E + i);
        else if (i == 6) glds1k<0>(w2b + kof(5 + E) * 4096, ao, lpc + E * 32768);
        else if (i == 7) glds1k<1024>(w2b + kof(5 + E) * 4096, ao, lpc + E * 32768);
        else if (i == 8) glds1k<2048>(w2b + kof(5 + E) * 4096, ao, lpc + E * 32768);
        else glds1k<3072>(w2b + kof(5 + E) * 4096, ao, lpc + E * 32768);
      }
      if (n == 122) ld4(fh, fhb, fho);
    }
    };
    tile(IC<0>{}); tile(IC<1>{}); tile(IC<2>{}); tile(IC<3>{});
    TS(26); TS(27); TC(28);
    TSF(21, 28);
#pragma unroll
    for (int i = 0; i < 4; i++) op[i] = pk(swi(acc[1], i, q[1]), swi(acc[1], i + 4, q[1]));
    st16(hb + 3 * 114688, ho, op);
    __builtin_amdgcn_s_setprio(0);
    wait_vm<1>(); tie(fh);
#pragma unroll
    for (int i = 0; i < 20; i++) { if (i < 12) tie(W2x[i]); else tie(A[i - 12]); }
    TS(29);
  }
  {
    const char* hq = ws + WS_HF + (size_t)((tt2 * 8 + sel) * 56 + w * 7) * 1024;
    u32x4 H[3][7];
    auto need = [&](unsigned long long mask) NOPK {
#pragma unroll 1
      for (int it = 0; it < SPIN_CAP && (__builtin_amdgcn_ballot_w64(fh != tag) & mask) != 0; it++) {
        __builtin_amdgcn_s_sleep(2);
        ld4(fh, fhb, fho); wait_vm<0>(); tie(fh);
      }
    };
    auto ldh = [&](int q) NOPK {
#pragma unroll
      for (int j = 0; j < 7; j++) ld16(H[q % 3][j], hq + q * 114688 + kof(j) * 1024, ao);
    };
    char* wr = smem + w * 4096 + r * 256;
    auto quarter = [&](int q, int yng) NOPK {
      f32x4 acc[4];
#pragma unroll
      for (int j = 0; j < 7; j++) {
        __builtin_amdgcn_sched_barrier(0);
        if (yng == 0) { if (j == 0) wait_vm<5>(); else if (j == 2) wait_vm<3>(); else if (j == 4) wait_vm<1>(); else if (j == 6) wait_vm<0>(); }
        if (yng == 8 && j == 0) wait_vm<8>();
        tie(H[q % 3][j]);
#pragma unroll
        for (int ds = 0; ds < 4; ds++) {
          const int f = 4 * j + ds;
          acc[ds] = mfma16(f < 12 ? W2x[f] : A[f - 12], H[q % 3][j], j == 0 ? f32x4{0.f, 0.f, 0.f, 0.f} : acc[ds]);
        }
      }
      __builtin_amdgcn_sched_barrier(0);
#pragma unroll
      for (int ds = 0; ds < 4; ds++) *(f32x4*)(wr + q * 32768 + (((ds * 4 + g) ^ r) * 16)) = acc[ds];
    };
    const int tk = (tid >> 4) & 15, qh = w >> 2;
    u32 xq[2][4];
    f32x4 gbv;
    auto finish = [&](int qp) NOPK {
      const char* rp = smem + (2 * qp + qh) * 32768 + tk * 256 + ((r ^ tk) * 16);
      f32x4 s = *(const f32x4*)rp;
#pragma unroll
      for (int i = 1; i < 8; i++) s += *(const f32x4*)(rp + i * 4096);
      float y[4];
#pragma unroll
      for (int j = 0; j < 4; j++) y[j] = fmaf(gbv[j], s[j], bflo(xq[qp][j]));
      asm volatile("global_store_dwordx2 %0, %1, %2 sc1" ::"v"((tk * D + r * 4) * 2), "v"(u32x2{pk(y[0], y[1]), pk(y[2], y[3])}),
                   "s"(out + (size_t)((tt2 * 4 + 2 * qp + qh) * 32 + sel * 16) * D + dt * 64) : "memory");
    };
    st4one(fpb + 64, tag);
    need(0xffffull);
    ldh(0);
    TS(30);
    {
      const char* xp = smem + D_RES + qh * 2048 + (tk >> 3) * 256 + r * 16 + (tk & 7) * 2;
#pragma unroll
      for (int i = 0; i < 8; i++) xq[i >> 2][i & 3] = *(const u16*)(xp + (i >> 2) * 4096 + (i & 3) * 512);
      gbv = *(const f32x4*)(smem + D_GB + r * 16);
    }
    wait_vm<8>();
    st4one(fpb + 96, tag);
    TS(31);
    need(0xffffull << 16);
    ld4(fh, fhb, fho);
    ldh(1);
    wait_vm<7>(); tie(fh);
    TS(32);
    need(0xffffull << 32);
    ld4(fh, fhb, fho);
    ldh(2);
    quarter(0, -1);
    TS(33);
    wait_vm<7>(); tie(fh);
    need(0xffffull << 48);
    TS(34);
    ldh(3);
    quarter(1, -1);
    TS(35);
    lds_barrier();
    finish(0);
    asm volatile("" ::: "memory");
    TS(36);
    TSF(29, 36);
    quarter(2, 8);
    TS(37);
    quarter(3, 0);
    TS(38);
    lds_barrier();
    asm volatile(".Lpf1:");
    finish(1);
    TS(39);
    TS(40);
    TSF(37, 40);
    TSEND();
  }
}

extern "C" void kernel_launch(void* const* d_in, const int* in_sizes, int n_in, void* d_out, int out_size, void* d_ws,
                              size_t ws_size, hipStream_t stream) {
  const u16* const* in = (const u16* const*)d_in;
  fused<<<256, 512, 0, stream>>>(in[0], in[1], in[2], in[3], in[4], in[5], in[6], in[7], in[8], in[10], in[11], in[12], in[13], in[14],
                                 (char*)d_ws, (u16*)d_out);
}
```

```cpp
#include <hip/hip_runtime.h>
#include <stdint.h>

typedef unsigned int u32;
typedef unsigned short u16;
typedef __attribute__((ext_vector_type(2))) float f32x2;
typedef __attribute__((ext_vector_type(4))) float f32x4;
typedef __attribute__((ext_vector_type(16))) float f32x16;
typedef __attribute__((ext_vector_type(2))) u32 u32x2;
typedef __attribute__((ext_vector_type(4))) u32 u32x4;
typedef __attribute__((ext_vector_type(2))) __bf16 bf16x2;
typedef __attribute__((ext_vector_type(8))) __bf16 bf16x8;
typedef __attribute__((ext_vector_type(4))) short s16x4;
#define LDS_AS __attribute__((address_space(3)))
#if defined(__HIP_DEVICE_COMPILE__)
#define NOPK __attribute__((target("no-packed-fp32-ops")))
#else
#define NOPK
#endif
#define DEVINL __device__ __forceinline__ NOPK

constexpr int L = 2048, D = 512, NS = 128, F = 1792;

constexpr size_t WS_E = 0;
constexpr size_t WS_FW = 4096;
constexpr size_t WS_FX = WS_FW + 32768;
constexpr size_t WS_FH = WS_FX + 32768;
constexpr size_t WS_MG = WS_FH + 65536;
constexpr size_t WS_GB = WS_MG + 98304;
constexpr size_t WS_XG = 262144;
constexpr size_t WS_X1 = WS_XG + (4u << 20);
constexpr size_t WS_W1F = WS_X1 + (2u << 20);
constexpr size_t WS_W2F = WS_W1F + 112 * 32 * 1024;
constexpr size_t WS_HF = WS_W2F + 16 * 112 * 1024;
constexpr size_t WS_CD = WS_HF + 64 * 112 * 1024;
constexpr int SPIN_CAP = 40000;
#define GP 1
#define RP 1
#define SCR 3
#define LBC 6
#define NF1 24
#define PFM 3
#define PFB 32768

#define u2f(x) __builtin_bit_cast(float, (u32)(x))
#define i2f(x) __builtin_bit_cast(float, (int)(x))
#define f2u(x) __builtin_bit_cast(u32, (float)(x))
#define f2i(x) __builtin_bit_cast(int, (float)(x))
#define TSDECL
#define TS(slot)
#define TC(slot)
#define TSF(lo, hi)
#define TSEND()
DEVINL float bflo(u32 u) { return u2f(u << 16); }
DEVINL float bfhi(u32 u) { return u2f(u & 0xffff0000u); }
DEVINL u32 pk(float a, float b) {
  f32x2 v = {a, b};
  return __builtin_bit_cast(u32, __builtin_convertvector(v, bf16x2));
}
DEVINL float rbf(float a) { return bflo(pk(a, a)); }
template <int CTRL> DEVINL float dpp_add(float v) {
  return v + i2f(__builtin_amdgcn_update_dpp(0, f2i(v), CTRL, 0xf, 0xf, true));
}
DEVINL float row4_sum(float v) {
  auto a = __builtin_amdgcn_permlane16_swap(f2u(v), f2u(v), false, false);
  v = u2f(a[0]) + u2f(a[1]);
  auto b = __builtin_amdgcn_permlane32_swap(f2u(v), f2u(v), false, false);
  return u2f(b[0]) + u2f(b[1]);
}
DEVINL float rs32(float a, float b) {
  auto r = __builtin_amdgcn_permlane32_swap(f2u(a), f2u(b), false, false);
  return u2f(r[0]) + u2f(r[1]);
}
DEVINL float rs16(float a, float b) {
  auto r = __builtin_amdgcn_permlane16_swap(f2u(a), f2u(b), false, false);
  return u2f(r[0]) + u2f(r[1]);
}
DEVINL float wave_sum(float v) { return row4_sum(dpp_add<0x140>(dpp_add<0x141>(dpp_add<0x4E>(dpp_add<0xB1>(v))))); }
DEVINL f32x4 mfma16(u32x4 a, u32x4 b, f32x4 c) {
  return __builtin_amdgcn_mfma_f32_16x16x32_bf16(__builtin_bit_cast(bf16x8, a), __builtin_bit_cast(bf16x8, b), c, 0, 0, 0);
}
DEVINL f32x16 mfma32(u32x4 a, u32x4 b, f32x16 c) {
  return __builtin_amdgcn_mfma_f32_32x32x16_bf16(__builtin_bit_cast(bf16x8, a), __builtin_bit_cast(bf16x8, b), c, 0, 0, 0);
}
DEVINL float dot2(u32 a, u32 b, float c) {
  return __builtin_amdgcn_fdot2_f32_bf16(__builtin_bit_cast(bf16x2, a), __builtin_bit_cast(bf16x2, b), c, false);
}
DEVINL void lds_barrier() {
  asm volatile("s_waitcnt lgkmcnt(0)" ::: "memory");
  __builtin_amdgcn_s_barrier();
  asm volatile("" ::: "memory");
}
DEVINL void bar() {
  asm volatile("" ::: "memory");
  __builtin_amdgcn_s_barrier();
  asm volatile("" ::: "memory");
}
DEVINL void st16(const void* sbase, u32 voff, u32x4 v) {
  asm volatile("global_store_dwordx4 %0, %1, %2 sc1\n\ts_nop 1" ::"v"(voff), "v"(v), "s"(sbase) : "memory");
}
DEVINL void st8(const void* sbase, u32 voff, u32x2 v) {
  asm volatile("global_store_dwordx2 %0, %1, %2 sc1\n\ts_nop 1" ::"v"(voff), "v"(v), "s"(sbase) : "memory");
}
DEVINL void st4(const void* sbase, u32 voff, u32 v) {
  asm volatile("global_store_dword %0, %1, %2 sc1\n\ts_nop 1" ::"v"(voff), "v"(v), "s"(sbase) : "memory");
}
template <int LANES> DEVINL void st8m(const void* sbase, u32 voff, u32x2 v) {
  asm volatile("s_mov_b64 exec, %3\n\tglobal_store_dwordx2 %0, %1, %2 sc1\n\ts_mov_b64 exec, -1\n\ts_nop 0"
               ::"v"(voff), "v"(v), "s"(sbase), "n"((1 << LANES) - 1) : "memory");
}
DEVINL void st4one(const void* sbase, u32 v) {
  asm volatile("s_mov_b64 exec, 1\n\tglobal_store_dword %0, %1, %2 sc1\n\ts_mov_b64 exec, -1\n\ts_nop 0"
               ::"v"(0), "v"(v), "s"(sbase) : "memory");
}
DEVINL void st16x2_56(const void* sbase, u32 voff, u32x4 a, u32x4 b) {
  asm volatile("s_mov_b32 exec_hi, 0xffffff\n\tglobal_store_dwordx4 %0, %1, %3 sc1\n\tglobal_store_dwordx4 %0, %2, %3 offset:256 sc1\n\t"
               "s_mov_b32 exec_hi, -1\n\ts_nop 0" ::"v"(voff), "v"(a), "v"(b), "s"(sbase) : "memory");
}
DEVINL void sld4(u32& d, const void* p) { asm volatile("s_load_dword %0, %1, 0" : "=s"(d) : "s"(p) : "memory"); }
template <int OFF = 0> DEVINL void ld16(u32x4& d, const void* sbase, u32 voff) {
  asm volatile("global_load_dwordx4 %0, %1, %2 offset:%3 sc1" : "=v"(d) : "v"(voff), "s"(sbase), "n"(OFF) : "memory");
}
DEVINL void ld16k(u32x4& d, const char* sbase, int kb, u32 voff) {
  const char* sb = sbase + (kb >> 2) * 4096;
  if ((kb & 3) == 0) ld16<0>(d, sb, voff); else if ((kb & 3) == 1) ld16<1024>(d, sb, voff);
  else if ((kb & 3) == 2) ld16<2048>(d, sb, voff); else ld16<3072>(d, sb, voff);
}
DEVINL void ld8(u32x2& d, const void* sbase, u32 voff) {
  asm volatile("global_load_dwordx2 %0, %1, %2 sc1" : "=v"(d) : "v"(voff), "s"(sbase) : "memory");
}
template <int OFF = 0> DEVINL void ld4(u32& d, const void* sbase, u32 voff) {
  asm volatile("global_load_dword %0, %1, %2 offset:%3 sc1" : "=v"(d) : "v"(voff), "s"(sbase), "n"(OFF) : "memory");
}
template <int OFF = 0> DEVINL void lp16(u32x4& d, const void* sbase, u32 voff) {
  asm volatile("global_load_dwordx4 %0, %1, %2 offset:%3" : "=v"(d) : "v"(voff), "s"(sbase), "n"(OFF) : "memory");
}
template <int OFF = 0> DEVINL void lp4(u32& d, const void* sbase, u32 voff) {
  asm volatile("global_load_dword %0, %1, %2 offset:%3" : "=v"(d) : "v"(voff), "s"(sbase), "n"(OFF) : "memory");
}
template <int OFF = 0> DEVINL void lp2(u32& d, const void* sbase, u32 voff) {
  asm volatile("global_load_ushort %0, %1, %2 offset:%3" : "=v"(d) : "v"(voff), "s"(sbase), "n"(OFF) : "memory");
}
template <class T> DEVINL void tie(T& a) { asm volatile("" : "+v"(a)); }
template <int N> DEVINL void wait_vm() { asm volatile("s_waitcnt vmcnt(%0)" ::"n"(N) : "memory"); }
#define wait_vmn(k) asm volatile("s_waitcnt vmcnt(%0)" ::"n"(k) : "memory")
DEVINL bool all_lanes(bool ok) { return __builtin_amdgcn_ballot_w64(!ok) == 0; }
DEVINL void spin4(u32 f, u32 tag, const void* sbase, u32 voff) {
#pragma unroll 1
  for (int it = 0; it < SPIN_CAP && !all_lanes(f == tag); it++) {
    __builtin_amdgcn_s_sleep(1);
    ld4(f, sbase, voff); wait_vm<0>(); tie(f);
  }
}
DEVINL bool arrive_last(char* smem_word, int n) {
  u32 old;
  asm volatile("s_mov_b64 exec, 1\n\tds_add_rtn_u32 %0, %1, %2\n\ts_waitcnt lgkmcnt(0)\n\ts_mov_b64 exec, -1"
               : "=&v"(old) : "v"((u32)(size_t)(LDS_AS char*)smem_word), "v"(1) : "memory");
  return __builtin_amdgcn_readfirstlane(old) == (u32)(n - 1);
}
DEVINL void scale8(u32x4& a, const u32x4& s, const u32x4& t, int on) {
  u32 a0 = a[0], a1 = a[1], a2 = a[2], a3 = a[3], x0, x1, x2, x3;
  asm("s_cmp_eq_u32 %16, 0\n\ts_cbranch_scc1 .Lsk%=\n\tv_dot2_f32_bf16 %4, %0, %8, 0\n\tv_dot2_f32_bf16 %0, %0, %9, 0\n\tv_dot2_f32_bf16 %5, %1, %10, 0\n\tv_dot2_f32_bf16 %1, %1, %11, 0\n\t"
      "v_dot2_f32_bf16 %6, %2, %12, 0\n\tv_dot2_f32_bf16 %2, %2, %13, 0\n\tv_dot2_f32_bf16 %7, %3, %14, 0\n\tv_dot2_f32_bf16 %3, %3, %15, 0\n\t"
      "v_cvt_pk_bf16_f32 %0, %4, %0\n\tv_cvt_pk_bf16_f32 %1, %5, %1\n\tv_cvt_pk_bf16_f32 %2, %6, %2\n\ts_nop 0\n\tv_cvt_pk_bf16_f32 %3, %7, %3\n.Lsk%=:"
      : "+v"(a0), "+v"(a1), "+v"(a2), "+v"(a3), "=&v"(x0), "=&v"(x1), "=&v"(x2), "=&v"(x3)
      : "v"(s[0]), "v"(s[1]), "v"(s[2]), "v"(s[3]), "v"(t[0]), "v"(t[1]), "v"(t[2]), "v"(t[3]), "s"(on) : "scc");
  a = u32x4{a0, a1, a2, a3};
}
DEVINL u32x2 tr_read(const char* p) {
  return __builtin_bit_cast(u32x2, __builtin_amdgcn_ds_read_tr16_b64_v4i16((LDS_AS s16x4*)p));
}
DEVINL float rdl(float v, int l) { return i2f(__builtin_amdgcn_readlane(f2i(v), l)); }
DEVINL float sel2(int c, float a, float b) { return c ? b : a; }
DEVINL u32 swap2(u32 v) { return (u32)__builtin_amdgcn_update_dpp(0, (int)v, 0x4E, 0xf, 0xf, true); }
DEVINL u32 swap1(u32 v) { return (u32)__builtin_amdgcn_update_dpp(0, (int)v, 0xB1, 0xf, 0xf, true); }
constexpr float LOG2E = 1.4426950408889634f, LN2 = 0.6931471805599453f, INV2PI = 0.15915494309189535f;

constexpr int A_TAB = 0, A_P = 8 * 1536, A_KC = A_P + 24576, A_KCS = 4112, A_GV = A_KC + 2 * A_KCS;
constexpr int B_Y1 = 0, B_RED = 40960, B_KC = 49152, B_KCS = 4160, B_KCH = 4 * 4160, B_U = B_KC + 2 * B_KCH, B_US = 5152, B_UCH = 2 * B_US;
constexpr int B_Y2 = B_U + 2 * B_UCH, A_XT = B_Y2;
constexpr int C_RAW = 148480, L_CNT = C_RAW + 1024, D_RES = L_CNT + 64, D_GB = D_RES + 8192, B_SC = D_GB + 256, LDS_BYTES = B_SC + 4096;
static_assert(A_GV + 2048 <= B_KC && B_Y2 + 40960 <= C_RAW && 131072 <= C_RAW && LDS_BYTES <= 163840, "lds map");

DEVINL void glds4k(const void* sbase, u32 voff, u32 ldsaddr) {
  asm volatile("s_mov_b32 m0, %2\n\ts_nop 0\n\t"
               "global_load_lds_dwordx4 %0, %1 sc1\n\tglobal_load_lds_dwordx4 %0, %1 offset:1024 sc1\n\t"
               "global_load_lds_dwordx4 %0, %1 offset:2048 sc1\n\tglobal_load_lds_dwordx4 %0, %1 offset:3072 sc1"
               ::"v"(voff), "s"(sbase), "s"(ldsaddr) : "memory");
}
DEVINL u32x4 tr8(const char* p) {
  const u32x2 lo = tr_read(p), hi = tr_read(p + 256);
  return u32x4{lo[0], lo[1], hi[0], hi[1]};
}
template <int OFF> DEVINL void glds1k(const void* sbase, u32 voff, u32 ldsaddr) {
  asm volatile("s_mov_b32 m0, %2\n\ts_nop 0\n\tglobal_load_lds_dwordx4 %0, %1 offset:%3 sc1" ::"v"(voff), "s"(sbase), "s"(ldsaddr), "n"(OFF) : "memory");
}
DEVINL const char* far(const char* p) {
  u32 a = (u32)(size_t)(LDS_AS const char*)p;
  asm("" : "+v"(a));
  return (const char*)(LDS_AS const char*)(size_t)a;
}
struct Ep { float q0, v; };
template <int N> struct IC { static constexpr int v = N; };
#define accg(a, i) a[((i) & 3) + 8 * ((i) >> 2)]
#define accu(a, i) a[((i) & 3) + 8 * ((i) >> 2) + 4]
DEVINL float swi(const f32x16& a, int i, const Ep& q) {
  const float e = __builtin_amdgcn_exp2f(accg(a, i) * q.q0);
  return (accg(a, i) * accu(a, i)) * __builtin_amdgcn_rcpf(fmaf(e, q.v, q.v));
}
DEVINL u32 ln2_fold(f32x2 s, int h, Ep& q) {
  const float m = s[0] * (1.f / D);
  const float v = fmaxf(fmaf(-m, m, s[1] * (1.f / D)), 0.f) + 1e-5f;
  const float rs = __builtin_amdgcn_rsqf(v);
  q.q0 = rs * -LOG2E; q.v = v;
  const float val = h ? v * rs : -m;
  const float vhi = rbf(val);
  return pk(vhi, val - vhi);
}
DEVINL void ln2_tile(const char* rb, f32x2* raw, f32x2* dummy, int lane) {
  const u32x4 ones = {0x3f803f80u, 0x3f803f80u, 0x3f803f80u, 0x3f803f80u};
  f32x4 sg[2], sx[2];
#pragma unroll
  for (int i = 0; i < 2; i++) { sg[i] = f32x4{0.f, 0.f, 0.f, 0.f}; sx[i] = f32x4{0.f, 0.f, 0.f, 0.f}; }
  u32x4 fr[4][2];
  auto rd = [&](int n) NOPK {
#pragma unroll
    for (int gi = 0; gi < 2; gi++) {
      const char* pp = rb + n * 2048 + gi * 32;
      const u32x2 lo = tr_read(pp), hi = tr_read(pp + 1024);
      fr[n & 3][gi] = u32x4{lo[0], lo[1], hi[0], hi[1]};
    }
  };
  rd(0); rd(1); rd(2);
#pragma unroll
  for (int n = 0; n < 16; n++) {
    if (n + 3 < 16) rd(n + 3);
    __builtin_amdgcn_sched_barrier(0);
#pragma unroll
    for (int gi = 0; gi < 2; gi++) {
      sg[gi] = mfma16(fr[n & 3][gi], fr[n & 3][gi], sg[gi]);
      sx[gi] = mfma16(ones, fr[n & 3][gi], sx[gi]);
    }
    __builtin_amdgcn_sched_barrier(0);
  }
#pragma unroll
  for (int gi = 0; gi < 2; gi++) {
    const float gsel = (lane & 2) ? ((lane & 1) ? sg[gi][3] : sg[gi][2]) : ((lane & 1) ? sg[gi][1] : sg[gi][0]);
    f32x2* dst = (lane >> 4) == ((lane & 15) >> 2) ? raw + gi * 16 + (lane & 15) : dummy;
    *dst = f32x2{sx[gi][0], gsel};
  }
}

__global__ NOPK __launch_bounds__(512, 2) void fused(
    const u16* __restrict__ x, const u16* __restrict__ Are, const u16* __restrict__ Aim, const u16* __restrict__ Bre,
    const u16* __restrict__ Bim, const u16* __restrict__ Cre, const u16* __restrict__ Cim, const u16* __restrict__ logdt,
    const u16* __restrict__ dskip, const u16* __restrict__ w1, const u16* __restrict__ w3, const u16* __restrict__ w2,
    const u16* __restrict__ cvec, const u16* __restrict__ wmod, char* ws, u16* __restrict__ out) {
  __shared__ __attribute__((aligned(16))) char smem[LDS_BYTES];
  const int b = blockIdx.x, tid = threadIdx.x, lane = tid & 63;
  const int w = __builtin_amdgcn_readfirstlane(tid >> 6);
  TSDECL; TS(0);
  const int xg = b & 7, idx = b >> 3;
  const int p = xg * 32 + idx;
  const int r = lane & 15, g = lane >> 4;
  asm volatile("" ::"s"(x), "s"(Are), "s"(Aim), "s"(Bre), "s"(Bim), "s"(Cre), "s"(Cim), "s"(logdt), "s"(dskip), "s"(w1), "s"(w3),
               "s"(w2), "s"(cvec), "s"(wmod), "s"(ws), "s"(out));
  u32x4 xr, wm0, wm1;
  u32 vare, vaim, vbre, vbim, vcre, vcim, c0, c1;
  const int cg = xg * 24 + (idx < 24 ? idx : 23);
  u32 e_prev, ldt2, dsk;
  lp16(xr, x + b * 4096, tid * 16);
  sld4(e_prev, ws + WS_E); sld4(ldt2, logdt + 2 * p); sld4(dsk, dskip + 2 * p);
  {
    const u32 so = (p * 256 + w * 32 + (lane & 31)) * 2;
    lp2(vare, Are, so); lp2(vaim, Aim, so); lp2(vbre, Bre, so); lp2(vbim, Bim, so); lp2(vcre, Cre, so); lp2(vcim, Cim, so);
    const u32 mo = (tid >> 3) * 6144 + (tid & 7) * 16;
    const u16* wmp = wmod + (cg & 3) * 128 * 3072 + (cg >> 2) * 64;
    lp16(wm0, wmp, mo); lp16(wm1, wmp + 64 * 3072, mo);
    lp2(c0, cvec + (cg & 3) * 128, (tid >> 3) * 2); lp2<128>(c1, cvec + (cg & 3) * 128, (tid >> 3) * 2);
  }
  u32 pf;
  {
    unsigned long long pc;
    u32 po = ((idx & PFM) * 512 + tid) * (PFB / (PFM + 1) / 512);
    asm volatile("s_getpc_b64 %1\n.Lpf0:\n\tv_min_u32 %2, .Lpf1-.Lpf0, %2\n\tglobal_load_dword %0, %2, %1"
                 : "=v"(pf), "=&s"(pc), "+v"(po)::"memory");
  }
  TS(1);
  if (tid < 32) *(u32x4*)(smem + B_KC + (tid >> 2) * B_KCS + 4096 + (tid & 3) * 16) = u32x4{0, 0, 0, 0};
  else if (tid < 36) *(u32x4*)(smem + L_CNT + (tid - 32) * 16) = u32x4{0, 0, 0, 0};
  else if (tid < 38) *(u32x2*)(smem + B_U + (tid & 1) * B_UCH + B_US + 1024) = u32x2{0, 0};
  else if (tid >= 256) *(u32x4*)(smem + B_U + (w - 4) * B_US + lane * 16) = u32x4{0, 0, 0, 0};
  asm volatile("s_waitcnt lgkmcnt(0)" : "+s"(e_prev), "+s"(ldt2), "+s"(dsk)::"memory");
  const u32 tag = e_prev * 1664525u + 1013904223u;
  wait_vm<11>(); tie(xr);
  {
    *(u32x4*)(smem + A_XT + w * 1056 + lane * 16) = xr;
    float f[8];
#pragma unroll
    for (int j = 0; j < 4; j++) { f[2 * j] = bflo(xr[j]); f[2 * j + 1] = bfhi(xr[j]); }
    const float m = wave_sum(((f[0] + f[1]) + (f[2] + f[3])) + ((f[4] + f[5]) + (f[6] + f[7]))) * (1.f / D);
    float s = 0.f;
#pragma unroll
    for (int j = 0; j < 8; j++) { f[j] -= m; s = fmaf(f[j], f[j], s); }
    const float sd = __builtin_amdgcn_sqrtf(rbf(rbf(wave_sum(s) * (1.f / D)) + 1e-5f));
    if (lane == 0) *(u32*)(smem + A_XT + w * 1056 + 1024) = pk(m, sd);
  }
  TS(2);
  wait_vm<5>(); tie(vare); tie(vaim); tie(vbre); tie(vbim); tie(vcre); tie(vcim);
  char* tab = smem + A_TAB + w * 1536;
  {
    const float dt = rbf(__builtin_amdgcn_exp2f((w >> 2 ? bfhi(ldt2) : bflo(ldt2)) * LOG2E));
    const float a = dt * bflo(vare) * LOG2E, bq = dt * bflo(vaim) * INV2PI;
    const float br = dt * bflo(vbre), bi = dt * bflo(vbim), cr = bflo(vcre), ci = bflo(vcim);
    const float m = lane < 32 ? 1.f : 256.f;
    const float e = __builtin_amdgcn_exp2f(a * m), ph = __builtin_amdgcn_fractf(-bq * m);
    const float ex = e * __builtin_amdgcn_cosf(ph), ey = e * __builtin_amdgcn_sinf(ph);
    char* t = tab + (lane & 31) * 48;
    *(f32x2*)(t + 16 + (lane >> 5) * 8) = f32x2{ex, ey};
    f32x4 o = {a, bq, cr * br - ci * bi, cr * bi + ci * br};
    if (lane >= 32) o = f32x4{ex, ey, ex * ex - ey * ey, (ex + ex) * ey};
    *(f32x4*)(t + (lane >> 5) * 32) = o;
  }
  TS(3);
  lds_barrier();
  {
    const char* xt = smem + A_XT + (lane & 7) * 1056;
    const u32 st = *(const u32*)(xt + 1024);
#pragma unroll
    for (int e = 0; e < 2; e++) {
      const u32x2 xq = *(const u32x2*)(xt + (e * 64 + w * 8 + (lane >> 3)) * 8);
      st16(ws + WS_XG + (size_t)(e * 64 + w * 8) * 32768 + b * 128, (lane >> 3) * 32768 + (lane & 7) * 16, u32x4{xq[0], xq[1], st, tag});
    }
  }
  TS(41);
  const int fb = (xg >> 1) * 4 + (idx & 3), mat = (idx >> 2) & 1, dq = (xg & 1) * 4 + (idx >> 3), d0 = dq * 64 + w * 8;
  const int b2 = b < 224 ? b : 223;
  u32 v[8], t2[8];
  {
    const u16* wp = (mat ? w3 : w1) + (size_t)d0 * F + fb * 112;
    const u32 vo = (lane < 56 ? lane : 55) * 4;
#pragma unroll
    for (int i = 0; i < 4; i++) { lp4<0>(v[2 * i], wp + 2 * i * F, vo); lp4<2 * F>(v[2 * i + 1], wp + 2 * i * F, vo); }
#pragma unroll
    for (int i = 0; i < 2; i++) {
      const u16* wq = w2 + (size_t)(b2 * 8 + i * 4) * D;
      const u32 to = tid * 2;
      lp2<0>(t2[4 * i], wq, to); lp2<1024>(t2[4 * i + 1], wq, to); lp2<2048>(t2[4 * i + 2], wq, to); lp2<3072>(t2[4 * i + 3], wq, to);
    }
  }
  auto retile = [&]() NOPK {
    wait_vm<0>();
#pragma unroll
    for (int i = 0; i < 8; i++) { tie(v[i]); tie(t2[i]); }
    u32x4 o0, o1, o2;
#pragma unroll
    for (int j = 0; j < 4; j++) {
      o0[j] = __builtin_amdgcn_perm(v[2 * j + 1], v[2 * j], 0x05040100u);
      o1[j] = __builtin_amdgcn_perm(v[2 * j + 1], v[2 * j], 0x07060302u);
      o2[j] = __builtin_amdgcn_perm(t2[2 * j + 1], t2[2 * j], 0x05040100u);
    }
    st16x2_56(ws + WS_W1F + (size_t)(fb * 7 * 32 + (d0 >> 4)) * 1024 + (w & 1) * 512 + mat * 128, (lane >> 3) * 32768 + (lane & 7) * 16, o0, o1);
    st16(ws + WS_W2F + w * 229376 + (b2 >> 2) * 4096 + (b2 & 3) * 256, g * 1024 + r * 16, o2);
  };
  auto gemv = [&]() NOPK
  {
    wait_vm<18>(); tie(pf); tie(wm0); tie(wm1); tie(c0); tie(c1);
    const float cx0 = bflo(c0), cx1 = bflo(c1);
    const float s0 = rbf(cx0 * __builtin_amdgcn_rcpf(1.f + __builtin_amdgcn_exp2f(cx0 * -LOG2E)));
    const float s1 = rbf(cx1 * __builtin_amdgcn_rcpf(1.f + __builtin_amdgcn_exp2f(cx1 * -LOG2E)));
    float acc[8];
#pragma unroll
    for (int j = 0; j < 4; j++) {
      acc[2 * j] = fmaf(s0, bflo(wm0[j]), s1 * bflo(wm1[j]));
      acc[2 * j + 1] = fmaf(s0, bfhi(wm0[j]), s1 * bfhi(wm1[j]));
    }
    const float q0 = dpp_add<0x128>(rs16(rs32(acc[0], acc[4]), rs32(acc[2], acc[6])));
    const float q1 = dpp_add<0x128>(rs16(rs32(acc[1], acc[5]), rs32(acc[3], acc[7])));
    *(float*)(smem + A_GV + w * 256 + (lane & 7) * 32 + (lane >> 3) * 4) = (lane & 8) ? q1 : q0;
    if (arrive_last(smem + L_CNT, 8) && idx < 24) {
      float s = 0.f;
#pragma unroll
      for (int i = 0; i < 8; i++) s += *(const float*)(smem + A_GV + i * 256 + lane * 4);
      st8(ws + WS_MG + (cg & 3) * 24576 + (cg >> 2) * 512, lane * 8, u32x2{f2u(s), tag});
    }
  };
  TS(4);
  if (GP == 0) { gemv(); TS(44); }
  const u32 gcode = __builtin_amdgcn_perm(0x400b0916u, 0x0a201408u, (r & 7) | 0x0c0c0c00u);
  const int gx = r < 8 ? 2 * r : (int)((gcode >> 2) * 256 + (gcode & 3) * 4);
  {
    asm volatile("" ::: "memory");
    const float mF = (float)(16 * r), mX = (float)gx;
    const char* tp = tab + g * 8 * 48;
    const char* tq = tp + (r < 8 ? 16 : 32);
    f32x4 acc[3];
#pragma unroll
    for (int s = 0; s < 3; s++) acc[s] = f32x4{0.f, 0.f, 0.f, 0.f};
#pragma unroll
    for (int ks = 0; ks < 2; ks++) {
      u32x4 Ff, Gf[3];
#pragma unroll
      for (int i = 0; i < 4; i++) {
        const f32x4 s0 = *(const f32x4*)(tp + (ks * 4 + i) * 48), s1 = *(const f32x4*)(tq + (ks * 4 + i) * 48);
        const float e0 = __builtin_amdgcn_exp2f(s0[0] * mF), p0 = __builtin_amdgcn_fractf(s0[1] * mF);
        const float zx = e0 * __builtin_amdgcn_cosf(p0), zy = e0 * __builtin_amdgcn_sinf(p0);
        Ff[i] = pk(s0[2] * zx - s0[3] * zy, s0[2] * zy + s0[3] * zx);
        const float e1 = __builtin_amdgcn_exp2f(s0[0] * mX), p1 = __builtin_amdgcn_fractf(s0[1] * -mX);
        const float xx = e1 * __builtin_amdgcn_cosf(p1), xy = e1 * __builtin_amdgcn_sinf(p1);
        Gf[0][i] = pk(xx, xy);
        Gf[1][i] = pk(xx * s1[0] - xy * s1[1], xx * s1[1] + xy * s1[0]);
        Gf[2][i] = pk(xx * s1[2] - xy * s1[3], xx * s1[3] + xy * s1[2]);
      }
#pragma unroll
      for (int s = 0; s < 3; s++) acc[s] = mfma16(Ff, Gf[s], acc[s]);
      if (ks == 0) { TS(45); if (RP == 0) retile(); if (GP == 1) { gemv(); TS(44); } }
    }
#pragma unroll
    for (int s = 0; s < 3; s++) *(f32x4*)(smem + A_P + w * 3072 + s * 1024 + lane * 16) = acc[s];
  }
  if (RP == 1) retile();
  TS(5);
  u32x4 xg4[4];
  u32x2 mg, mh, mc[4];
  const char* xgb = ws + WS_XG + (size_t)(p >> 1) * 32768;
  const u32 xgo = (tid >> 2) * 256 + (tid & 3) * 16;
  u32 mga = (4 - ((lane >> 3) & 1)) * 512 + d0 + (lane & 7), mgb = ((0x5210 >> ((lane & 6) * 2)) & 7) * 512 + 2 * p + (lane & 1);
  tie(mga); tie(mgb);
  const u32 mgo = ((lane & 16) ? mgb : mga) * 8 + (lane >> 5) * 24576;
  auto lookx = [&]() NOPK {
    ld16<0>(xg4[0], xgb, xgo); ld16<64>(xg4[1], xgb, xgo); ld16<128>(xg4[2], xgb, xgo); ld16<192>(xg4[3], xgb, xgo);
  };
  auto lookm = [&]() NOPK {
    ld8(mg, ws + WS_MG, mgo); ld8(mh, ws + WS_MG + 49152, mgo);
  };
  auto lookc = [&]() NOPK {
#pragma unroll
    for (int q = 0; q < 4; q++) ld8(mc[q], ws + WS_MG + q * 24576 + 16384, tid * 8);
  };
  lookx();
  lds_barrier();
  TS(6);
  TSF(0, 6); TSF(41, 41); TSF(44, 45);
  const int kch = tid >> 8;
  u16* Kc = (u16*)(smem + A_KC + kch * A_KCS);
  if ((w & 3) < 3) {
    const int s = w & 3;
    const char* pp = smem + A_P + kch * 12288 + s * 1024 + lane * 16;
    const f32x4 q = (*(const f32x4*)(pp) + *(const f32x4*)(pp + 3072)) + (*(const f32x4*)(pp + 6144) + *(const f32x4*)(pp + 9216));
    const int pos = gx + 64 * g + (r < 8 ? (s == 0 ? 0 : s == 1 ? 1 : 256) : s * 256);
    const u32 o0 = pk(q[0], q[1]), o1 = pk(q[2], q[3]);
    if (pos <= 2048) Kc[pos] = (u16)o0;
    if (pos < 2048) {
      Kc[pos + 16] = (u16)(o0 >> 16);
      Kc[pos + 32] = (u16)o1;
      Kc[pos + 48] = (u16)(o1 >> 16);
    }
  }
  wait_vm<4>();
  lds_barrier();
  TS(7);
  if (w == 7) st4one(ws + WS_FW + (fb * 16 + mat * 8 + dq) * 128, tag);
  const int tt = (xg & 1) * 8 + (idx & 7), fs = (xg >> 1) * 4 + (idx >> 3);
  const u32 fwo = fs * 2048 + (lane & 15) * 128;
  u32 fw;
  lookm(); lookc();
  {
    u32 e[6];
    const int ki = tid & 255;
#pragma unroll
    for (int j = 0; j < 6; j++) {
      const float l0 = (float)(2047 - 8 * ki - 2 * j);
      const u32 bits = pk(fmaxf(l0, 0.f), fmaxf(l0 - 1.f, 0.f));
      e[j] = (u32)Kc[(int)bflo(bits)];
      if (j < 5) e[j] |= (u32)Kc[(int)bfhi(bits)] << 16;
    }
    if (ki == 255) { e[4] = 0; e[5] = 0; }
    const u32 a0 = __builtin_amdgcn_alignbit(e[1], e[0], 16), a1 = __builtin_amdgcn_alignbit(e[2], e[1], 16);
    const u32 a2 = __builtin_amdgcn_alignbit(e[3], e[2], 16), a3 = __builtin_amdgcn_alignbit(e[4], e[3], 16);
    const u32 a4 = __builtin_amdgcn_alignbit(e[5], e[4], 16);
    char* kc = smem + B_KC + kch * B_KCH + ki * 16;
    *(u32x4*)(kc) = u32x4{e[0], e[1], e[2], e[3]};
    *(u32x4*)(kc + B_KCS) = u32x4{a0, a1, a2, a3};
    *(u32x4*)(kc + 2 * B_KCS) = u32x4{e[1], e[2], e[3], e[4]};
    *(u32x4*)(kc + 3 * B_KCS) = u32x4{a1, a2, a3, a4};
  }
  TS(8);
  wait_vm<0>(); tie(xg4[0]); tie(xg4[1]); tie(xg4[2]); tie(xg4[3]); tie(mg); tie(mh); tie(mc[0]); tie(mc[1]); tie(mc[2]); tie(mc[3]);
  TS(47);
#pragma unroll 1
  for (int it = 0; it < SPIN_CAP && !all_lanes(xg4[0][3] == tag && xg4[1][3] == tag && xg4[2][3] == tag && xg4[3][3] == tag); it++) {
    lookx();
    wait_vm<0>(); tie(xg4[0]); tie(xg4[1]); tie(xg4[2]); tie(xg4[3]);
  }
#pragma unroll 1
  for (int it = 0; it < SPIN_CAP && !all_lanes(mg[1] == tag && mh[1] == tag); it++) {
    lookm();
    wait_vm<0>(); tie(mg); tie(mh);
  }
  TS(9);
  const int h = lane >> 5, c = lane & 31;
  const int tt2 = (~xg & 1) * 8 + (xg & 6) + (idx >> 4), sel = (idx >> 3) & 1, dt = idx & 7;
  const int rot = (2 * (idx >> 3) + idx) & 7;
  const int ft = fs * 7 + w - 1;
  u32x4 A[32];
  const u32 ao = lane * 16;
  auto lda = [&](int i) NOPK {
    const char* wq = ws + WS_W1F + (size_t)ft * 32768 + (((i >> 2) + rot) & 7) * 4096;
    asm volatile("s_cmp_eq_u32 %4, 0\n\ts_cbranch_scc1 .Lna%=\n\tglobal_load_dwordx4 %0, %1, %2 offset:%3 sc1\n.Lna%=:"
                 : "=v"(A[i]) : "v"(ao), "s"(wq), "n"((i & 3) * 1024), "s"(w) : "memory", "scc");
  };
  ld4(fw, ws + WS_FW, fwo);
  const int g8 = tid >> 1, cc = tid & 1;
  const float mq = u2f(mg[0]) + u2f(mh[0]);
  const float mv = rbf(rs32(mq, mq)), mv1 = rbf(1.f + mv);
  const float ga = sel2(cc, rdl(mv, 20), rdl(mv, 21));
  u32x4 hp, xv;
  {
    const float sh0 = rdl(mv, 16), sh1 = rdl(mv, 17), sc0 = rdl(mv1, 18), sc1 = rdl(mv1, 19);
    const u32 psel = cc ? 0x03020706u : 0x05040100u;
    const bool up = tid & 2;
    u32 ph[4], px[4];
#pragma unroll
    for (int i = 0; i < 4; i++) {
      const u32 xq = (p & 1) ? xg4[i][1] : xg4[i][0];
      const float m = bflo(xg4[i][2]), rs = __builtin_amdgcn_rcpf(bfhi(xg4[i][2]));
      const u32 n = pk((bflo(xq) - m) * rs, (bfhi(xq) - m) * rs);
      const u32 hq = pk(fmaf(bflo(n), sc0, sh0), fmaf(bfhi(n), sc1, sh1));
      ph[i] = __builtin_amdgcn_perm(swap1(hq), hq, psel);
      px[i] = __builtin_amdgcn_perm(swap1(xq), xq, psel);
    }
#pragma unroll
    for (int j = 0; j < 2; j++) {
      const u32 rh = swap2(up ? ph[j] : ph[2 + j]), kh = up ? ph[2 + j] : ph[j];
      hp[2 * j] = up ? rh : kh; hp[2 * j + 1] = up ? kh : rh;
      const u32 rx = swap2(up ? px[j] : px[2 + j]), kx = up ? px[2 + j] : px[j];
      xv[2 * j] = up ? rx : kx; xv[2 * j + 1] = up ? kx : rx;
    }
    char* u = smem + B_U + cc * B_UCH + 1024 + g8 * 16;
    *(u32x4*)(u) = hp;
    *(u32x2*)(u + B_US + 8) = u32x2{hp[0], hp[1]};
    *(u32x2*)(u + B_US + 16) = u32x2{hp[2], hp[3]};
  }
#pragma unroll 1
  for (int it = 0; it < SPIN_CAP && !all_lanes(mc[0][1] == tag && mc[1][1] == tag && mc[2][1] == tag && mc[3][1] == tag); it++) {
    lookc();
    wait_vm<0>(); tie(mc[0]); tie(mc[1]); tie(mc[2]); tie(mc[3]);
  }
  {
    const float sc = 1.f + rbf((u2f(mc[0][0]) + u2f(mc[2][0])) + (u2f(mc[1][0]) + u2f(mc[3][0])));
    const u32 e = pk(sc, sc) & ((tid & 1) ? 0xffff0000u : 0xffffu);
    *(u32*)(smem + B_SC + tid * 4) = e;
    *(u32*)(smem + B_SC + 2048 + tid * 4) = e;
  }
  TS(10);
  lds_barrier();
  TS(11);
  const char* tb = smem + B_SC + rot * 256 + h * 32;
  u32x4 tq[3][2];
  auto ldt = [&](int i) NOPK { tq[i % 3][0] = *(const u32x4*)(tb + i * 64); tq[i % 3][1] = *(const u32x4*)(tb + i * 64 + 16); };
  auto scl = [&](int i) NOPK { tie(A[i]); scale8(A[i], tq[i % 3][0], tq[i % 3][1], w); tie(A[i]); };
  auto epi = [&](int yb) NOPK {
    const int gl = g8 & 127;
    const float* Y = (const float*)(smem + yb) + cc * 1280 + 8 * gl + 8 * (gl >> 2);
    const f32x4 ya = (*(const f32x4*)Y + *(const f32x4*)(Y + 2560)) + (*(const f32x4*)(Y + 5120) + *(const f32x4*)(Y + 7680));
    const f32x4 yb4 = (*(const f32x4*)(Y + 4) + *(const f32x4*)(Y + 2564)) + (*(const f32x4*)(Y + 5124) + *(const f32x4*)(Y + 7684));
    const float dk = cc ? bfhi(dsk) : bflo(dsk);
    u32x4 o;
#pragma unroll
    for (int j = 0; j < 4; j++) {
      const float y0 = j < 2 ? ya[2 * j] : yb4[2 * j - 4], y1 = j < 2 ? ya[2 * j + 1] : yb4[2 * j - 3];
      o[j] = pk(fmaf(ga, fmaf(bflo(hp[j]), dk, y0), bflo(xv[j])), fmaf(ga, fmaf(bfhi(hp[j]), dk, y1), bfhi(xv[j])));
    }
    st16(ws + WS_X1 + p * 128 + (w >> 1) * 32768, ((g8 >> 2) & 15) * 131072 + cc * 64 + (g8 & 3) * 16, o);
  };
  constexpr int LBT = LBC, NS2 = 51 / SCR + 1;
  {
    const int ch = w & 1, nq = w >> 1;
    const char* pa = smem + B_KC + ch * B_KCH + (3 - (r & 3)) * B_KCS + 2 * (2040 - 8 * (r >> 2) + 8 * g) - 512 * 7 - 64 * (2 * nq + 1);
    const char* pb = smem + B_U + ch * B_UCH + ((r & 1) ? 0 : B_US) + 2 * (512 + 32 * (r >> 1) + 8 * g) - 64 * (2 * nq + 1);
    const int yo = w * 5120 + 160 * (r >> 1) + 16 * (r & 1) + 32 * g;
    f32x4 acc[4];
    u32x4 la[2][4], lb[2][4], hf[4];
    auto ldhi = [&](int q) NOPK {
      const int e = 4 + ((q & 7) >> 1);
      hf[q & 3] = *(const u32x4*)(((q & 1) ? pa + 512 * (7 - e) : pb + 512 * e) + 64 * (1 - (q >> 3)));
    };
#pragma unroll
    for (int vv = 0; vv < 2; vv++)
#pragma unroll
      for (int n = 0; n < 8; n++) {
        const int e = (0x30212310 >> (4 * n)) & 3;
        if ((0xb1 >> n) & 1) la[vv][e] = *(const u32x4*)(pa + 512 * (7 - e) + 64 * (1 - vv));
        else lb[vv][e] = *(const u32x4*)(pb + 512 * e + 64 * (1 - vv));
      }
    __builtin_amdgcn_sched_barrier(0);
    {
      f32x4 sums = {0.f, 0.f, 0.f, 0.f};
#pragma unroll
      for (int j = 0; j < 4; j++) {
        const float k0 = rdl(mv1, 2 * j), k1 = rdl(mv1, 2 * j + 1), s0 = rdl(mv, 8 + 2 * j), s1 = rdl(mv, 8 + 2 * j + 1);
        const float a0 = bflo(v[2 * j]), a1 = bflo(v[2 * j + 1]), b0 = bfhi(v[2 * j]), b1 = bfhi(v[2 * j + 1]);
        sums[0] = dot2(pk(a0 * k0, a1 * k1), 0x3f803f80u, sums[0]);
        sums[1] = dot2(pk(b0 * k0, b1 * k1), 0x3f803f80u, sums[1]);
        sums[2] = fmaf(a0, s0, fmaf(a1, s1, sums[2]));
        sums[3] = fmaf(b0, s0, fmaf(b1, s1, sums[3]));
      }
      *(f32x4*)(smem + B_RED + tid * 16) = sums;
    }
#pragma unroll
    for (int i = 0; i < 4; i++) acc[i] = f32x4{0.f, 0.f, 0.f, 0.f};
    wait_vm<0>(); tie(fw);
    spin4(fw, tag, ws + WS_FW, fwo);
    TS(46);
    __builtin_amdgcn_sched_barrier(0);
#pragma unroll
    for (int n = 0; n < 20; n++) {
      const int vv = n / 10, k = n % 10, e = (0x0231211000ll >> (4 * k)) & 3, u = (0x0000112231ll >> (4 * k)) & 3;
      acc[e + u] = mfma16(la[vv][e], lb[vv][u], acc[e + u]);
      if (n % 3 == 0 && n / 3 < LBC) lda(n / 3);
      if (n >= 12 && n < 15) ldhi(n - 12);
      __builtin_amdgcn_sched_barrier(0);
    }
    TS(12);
#pragma unroll
    for (int i = 0; i < 4; i++) *(f32x4*)(smem + B_Y1 + yo + 1280 * i) = acc[i];
    lds_barrier();
    TS(13);
    TSF(7, 13); TSF(46, 47);
    int w_ = w;
    asm volatile("" : "+s"(w_));
    if (w_ < 4) {
      __builtin_amdgcn_s_setprio(3);
      epi(B_Y1);
      __builtin_amdgcn_s_setprio(0);
    }
    asm volatile("" : "+s"(w_));
    if (w_ == 0) {
      f32x4 s = {0.f, 0.f, 0.f, 0.f};
#pragma unroll
      for (int q = 0; q < 8; q++) s += *(const f32x4*)(smem + B_RED + q * 1024 + lane * 16);
      st16(ws + WS_CD + ((dq * 16 + fb) * 2 + mat) * 1024, lane * 16, __builtin_bit_cast(u32x4, s));
      st8m<1>(ws + WS_GB + p * 8, 0, u32x2{f2u(rdl(mv, 22)), f2u(rdl(mv, 23))});
    }
    TS(14);
    ldt(0);
#pragma unroll
    for (int n = 0; n < 52; n++) {
      const int vv = n / 26, k = n % 26;
      const int e = (k < 13 ? 0x2210144443210ll >> (4 * k) : 0x7036631023555ll >> (4 * (k - 13))) & 7;
      const int u = (k < 13 ? 0x2555332104444ll >> (4 * k) : 0x0731026631210ll >> (4 * (k - 13))) & 7;
      const int q = vv * 8 + (e > 3 ? 2 * e - 7 : 2 * u - 8);
      if (n == NF1) {
        asm volatile("" : "+s"(w_));
        if (w_ < 4) {
          wait_vmn((n + SCR - 1) / SCR);
          if (w_ == 0) wait_vm<0>();
          if (arrive_last(smem + L_CNT + 8, 4)) st4one(ws + WS_FX + (b & 63) * 128 + (b >> 6) * 4, tag);
        }
        TS(15);
      }
      if (k == 0 || k == 4 || k == 8 || k == 12 || k == 16 || k == 20 || k == 23 || k == 25) {
        const int qn = vv * 8 + (k < 23 ? k / 4 : k == 23 ? 6 : 7) + 3;
        if (qn < 16) ldhi(qn);
      }
      if (n % SCR == 0) ldt(n / SCR + 1);
      __builtin_amdgcn_sched_barrier(0);
      acc[e + u - 4] = mfma16(e > 3 ? hf[q & 3] : la[vv][e], u > 3 ? hf[q & 3] : lb[vv][u], n < 4 ? f32x4{0.f, 0.f, 0.f, 0.f} : acc[e + u - 4]);
      if (n % SCR == 0) { wait_vm<LBT - 1>(); lda(LBT + n / SCR); scl(n / SCR); }
      __builtin_amdgcn_sched_barrier(0);
    }
    TS(16);
#pragma unroll
    for (int i = 0; i < 4; i++) *(f32x4*)(smem + B_Y2 + yo + 1280 * i) = acc[i];
    ldt(NS2 + 1);
    lds_barrier();
    TS(42);
    asm volatile("" : "+s"(w_));
    if (w_ >= 4) {
      __builtin_amdgcn_s_setprio(3);
      epi(B_Y2);
      __builtin_amdgcn_s_setprio(0);
    }
    TS(43);
    asm volatile("" : "+s"(w_));
    if (w_ != 0) {
#pragma unroll
      for (int i = NS2; i < 32; i++) {
        if (i + 2 < 32) ldt(i + 2);
        wait_vmn(i + LBT < 32 ? LBT - 1 : 31 - i);
        if (i + LBT < 32) lda(i + LBT);
        __builtin_amdgcn_sched_barrier(0);
        scl(i);
        __builtin_amdgcn_sched_barrier(0);
      }
    }
    wait_vm<0>();
  }
  f32x2* raw = (f32x2*)(smem + C_RAW);
  u32x4 W2x[12];
  u32 fh;
  const u32 lds0 = (u32)(size_t)(LDS_AS char*)smem;
  const char* xa = ws + WS_X1 + tt * 131072 + ((w + rot) & 7) * 4096;
  const u32 lpc = lds0 + w * 4096;
  const int rt2 = dt + 2 * (idx >> 3), rot2 = rt2 < 7 ? rt2 : rt2 - 7;
#define kof(j) ((j) + rot2 < 7 ? (j) + rot2 : (j) + rot2 - 7)
  const char* w2b = ws + WS_W2F + dt * 229376 + w * 28672;
  const char* fhb = ws + WS_FH + tt2 * 2048 + w * 256;
  const u32 fho = ((lane >> 3) & 1) * 128 + g * 32 + (lane & 7) * 4;
  const char* fpb = ws + WS_FH + (tt * 16 + fs) * 128 + w * 4;
  auto ldw2 = [&](int j) NOPK {
    const char* q = w2b + kof(j >> 2) * 4096;
    if (j < 12) ld16k(W2x[j], q, j & 3, ao); else ld16k(A[j - 12], q, j & 3, ao);
  };
  auto poll_x = [&](int part) NOPK {
#pragma unroll 1
    for (int it = 0; it < SPIN_CAP; it++) {
      u32x4 f;
      ld16(f, ws + WS_FX + part * 8192, lane * 128); wait_vm<0>(); tie(f);
      if (all_lanes(f[0] == tag && f[1] == tag && f[2] == tag && f[3] == tag)) break;
    }
  };
  if (w == 0) {
    __builtin_amdgcn_s_setprio(3);
    poll_x(0);
    TS(17);
#pragma unroll
    for (int q = 0; q < 8; q++) glds4k(ws + WS_X1 + tt * 131072 + ((q + rot) & 7) * 4096, ao, lds0 + q * 4096);
    const char* rb = smem + (lane >> 2) * 64 + (lane & 3) * 8;
    wait_vm<0>();
    TS(18);
    bar();
    TS(19); TC(20);
    TSF(14, 20); TSF(42, 43);
    st4one(ws + WS_FX + 8192 + (b & 63) * 128 + (b >> 6) * 4, tag);
    glds4k(xa + 32768, ao, lpc + 32768);
    if (b == 0) st4one(ws + WS_E, tag);
    st4one(fpb, tag); st4one(fpb + 32, tag);
#pragma unroll
    for (int i = 0; i < 14; i++) ldw2(i);
    ln2_tile(rb, raw, raw + 132, lane);
    TS(21);
    poll_x(1);
    lds_barrier();
    TS(22);
    glds4k(xa + 65536, ao, lpc + 65536); glds4k(xa + 98304, ao, lpc + 98304);
#pragma unroll
    for (int q = 0; q < 4; q++) {
      const char* xr_ = ws + WS_X1 + (tt2 * 4 + q) * 32768 + dt * 4096 + sel * 32;
      const u32 xo = r * 256 + h * 64 + (g & 1) * 16, la = lds0 + D_RES + q * 2048;
      glds1k<0>(xr_, xo, la); glds1k<0>(xr_ + 128, xo, la + 1024);
    }
    asm volatile("s_mov_b32 m0, %2\n\ts_nop 0\n\tglobal_load_lds_dword %0, %1 sc1"
                 ::"v"(lane * 4), "s"(ws + WS_GB + dt * 256), "s"(lds0 + D_GB) : "memory");
    ln2_tile(rb + 32768, raw + 32, raw + 132, lane);
    TS(23);
    wait_vm<0>(); lds_barrier();
    TS(24);
#pragma unroll
    for (int i = 14; i < 28; i++) ldw2(i);
    rb = far(rb + 65536);
    ln2_tile(rb, raw + 64, raw + 132, lane);
    asm volatile("" ::: "memory");
    *(volatile LDS_AS u32*)(smem + L_CNT + 16) = 1;
    TS(25);
    ln2_tile(rb + 32768, raw + 96, raw + 132, lane);
    asm volatile("" ::: "memory");
    *(volatile LDS_AS u32*)(smem + L_CNT + 16) = 2;
    __builtin_amdgcn_s_setprio(0);
    TS(26); TS(27); TC(28);
    TSF(21, 28);
    ld4(fh, fhb, fho);
    wait_vm<0>(); tie(fh);
#pragma unroll
    for (int i = 0; i < 28; i++) { if (i < 12) tie(W2x[i]); else tie(A[i - 12]); }
    TS(29);
  }
  int w_ = w;
  asm volatile("" : "+s"(w_));
  if (w_ != 0) {
    const char* rb = smem + h * 512 + ((lane >> 2) & 3) * 64 + ((lane >> 4) & 1) * 32 + (lane & 3) * 8;
    const char* rb2 = far(rb + 65536);
    u32 cdv[8];
    f32x16 acc[2], zero;
#pragma unroll
    for (int i = 0; i < 16; i++) zero[i] = 0.f;
    TS(17);
    lds_barrier();
    TS(18);
    TS(19); TC(20);
    TSF(14, 20); TSF(42, 43);
    __builtin_amdgcn_s_setprio(3);
    u32x4 Bq[3], Ax, op;
    Bq[0] = tr8(rb); Bq[1] = tr8(rb + 1024);
    f32x2 rw;
    Ep q[2];
    float ev, gu, oa;
    const char* hb = ws + WS_HF + (size_t)(tt * 8 * 56 + (ft >> 1)) * 1024 + (ft & 1) * 512;
    const u32 ho = (c >> 4) * 57344 + h * 256 + r * 16;
    auto stat_wait = [&](u32 need) NOPK {
#pragma unroll 1
      for (int it = 0; it < SPIN_CAP * 16; it++)
        if (__builtin_amdgcn_readfirstlane(*(volatile LDS_AS u32*)(smem + L_CNT + 16)) >= need) break;
      asm volatile("" ::: "memory");
    };
    auto tile = [&](auto tc) NOPK {
    constexpr int T = decltype(tc)::v, E = T - 1;
#pragma unroll
    for (int ks = 0; ks < 32; ks++) {
      const int n = T * 32 + ks, m = n + 2;
      if (n == 30) {
        TS(21);
        wait_vm<8>();
#pragma unroll
        for (int i = 0; i < 8; i++) tie(cdv[i]);
        lds_barrier();
        TS(22);
      } else if (n == 62) {
        TS(23);
        wait_vm<5>();
        lds_barrier();
        TS(24);
      }
      if (n == 30 || n == 62) __builtin_amdgcn_s_setprio(3);
      if (n == 14 || n == 46 || n == 100) __builtin_amdgcn_s_setprio(2);
      if (n == 22 || n == 54 || n == 118) __builtin_amdgcn_s_setprio(1);
      if (n == 27 || n == 59) __builtin_amdgcn_s_setprio(0);
      if (n == 96) { TS(25); tie(acc[0]); stat_wait(1); }
      if (n == 118) { tie(acc[1]); stat_wait(2); }
      if (n == 120) wait_vm<10>();
      if (m < 128) Bq[m % 3] = tr8((m < 64 ? rb : rb2) + (m & 63) * 1024);
      if (T > 0 && ks == 0) rw = raw[E * 32 + c];
      if (n == 118) rw = raw[96 + c];
      if (n >= 120) A[n - 112] = *(const u32x4*)(smem + ((n >> 2) & 1) * 32768 + w * 4096 + (n & 3) * 1024 + lane * 16);
      __builtin_amdgcn_sched_barrier(0);
      acc[T & 1] = mfma32(A[ks], Bq[n % 3], ks == 0 ? zero : acc[T & 1]);
      if (n == 31) {
        const float cd = ((u2f(cdv[0]) + u2f(cdv[1])) + (u2f(cdv[2]) + u2f(cdv[3]))) + ((u2f(cdv[4]) + u2f(cdv[5])) + (u2f(cdv[6]) + u2f(cdv[7])));
        const float chi = rbf(cd);
        Ax = u32x4{pk(chi, chi), pk(cd - chi, cd - chi), 0, 0};
      }
      if ((T > 0 && ks == 2) || n == 120) {
        const int X = (n == 120 ? 3 : E) & 1;
        const u32 be = ln2_fold(rw, h, q[X]);
        acc[X] = mfma32(Ax, u32x4{be, be, 0, 0}, acc[X]);
      }
      if (T > 0 && ks >= 4 && ks < 20) {
        const int k = (ks - 4) >> 1, i = (k >> 1) + 4 * (k & 1);
        if ((ks & 1) == 0) {
          ev = fmaf(__builtin_amdgcn_exp2f(accg(acc[E & 1], i) * q[E & 1].q0), q[E & 1].v, q[E & 1].v);
          gu = accg(acc[E & 1], i) * accu(acc[E & 1], i);
        } else {
          const float o = gu * __builtin_amdgcn_rcpf(ev);
          if (k & 1) op[k >> 1] = pk(oa, o); else oa = o;
        }
      }
      __builtin_amdgcn_sched_barrier(0);
      if (n == 1) {
        const u32 co = ((c >> 3) & 1) * 1024 + ((w - 1) * 8 + (c & 7)) * 16 + h * 8 + (c >> 4) * 4;
#pragma unroll
        for (int i = 0; i < 8; i++) ld4(cdv[i], ws + WS_CD + (i * 16 + fs) * 2048, co);
      }
      if (n == 0) glds4k(xa + 32768, ao, lpc + 32768);
      if (T == 0 && ks >= 4 && ks < 28 && ks % 3 == 1) ldw2((ks - 4) / 3);
      if (T == 1 && ks < 16 && (ks & 1)) {
        const int i = ks >> 1, o = 65536 + (i >> 2) * 32768;
        if ((i & 3) == 0) glds1k<0>(xa + o, ao, lpc + o); else if ((i & 3) == 1) glds1k<1024>(xa + o, ao, lpc + o);
        else if ((i & 3) == 2) glds1k<2048>(xa + o, ao, lpc + o); else glds1k<3072>(xa + o, ao, lpc + o);
      }
      if (T == 1 && (ks == 18 || ks == 22 || ks == 24 || ks == 26)) ldw2(ks == 18 ? 8 : ks / 2 - 2);
      if (T > 1 && ks == 9) { wait_vm<T == 2 ? 6 : 4>(); st4one(fpb + (T - 2) * 32, tag); }
      if (T > 0 && ks == 20) st16(hb + E * 114688, ho, op);
      if (T > 1 && ks % 3 == 1 && ks < 24) {
        const int i = ks / 3;
        const char* q = w2b + kof(5 + (i >> 2)) * 4096;
        const u32 la = lpc + (i >> 2) * 32768;
        if (T == 3) ldw2(12 + i);
        else if ((i & 3) == 0) glds1k<0>(q, ao, la); else if ((i & 3) == 1) glds1k<1024>(q, ao, la);
        else if ((i & 3) == 2) glds1k<2048>(q, ao, la); else glds1k<3072>(q, ao, la);
      }
      if (n == 122) ld4(fh, fhb, fho);
    }
    };
    tile(IC<0>{}); tile(IC<1>{}); tile(IC<2>{}); tile(IC<3>{});
    TS(26); TS(27); TC(28);
    TSF(21, 28);
#pragma unroll
    for (int i = 0; i < 4; i++) op[i] = pk(swi(acc[1], i, q[1]), swi(acc[1], i + 4, q[1]));
    st16(hb + 3 * 114688, ho, op);
    __builtin_amdgcn_s_setprio(0);
    wait_vm<1>(); tie(fh);
#pragma unroll
    for (int i = 0; i < 20; i++) { if (i < 12) tie(W2x[i]); else tie(A[i - 12]); }
    TS(29);
  }
  {
    const char* hq = ws + WS_HF + (size_t)((tt2 * 8 + sel) * 56 + w * 7) * 1024;
    u32x4 H[3][7];
    auto need = [&](unsigned long long mask) NOPK {
#pragma unroll 1
      for (int it = 0; it < SPIN_CAP && (__builtin_amdgcn_ballot_w64(fh != tag) & mask) != 0; it++) {
        __builtin_amdgcn_s_sleep(2);
        ld4(fh, fhb, fho); wait_vm<0>(); tie(fh);
      }
    };
    auto ldh = [&](int q) NOPK {
#pragma unroll
      for (int j = 0; j < 7; j++) ld16(H[q % 3][j], hq + q * 114688 + kof(j) * 1024, ao);
    };
    char* wr = smem + w * 4096 + r * 256;
    auto quarter = [&](int q, int yng) NOPK {
      f32x4 acc[4];
#pragma unroll
      for (int j = 0; j < 7; j++) {
        __builtin_amdgcn_sched_barrier(0);
        if (yng == 0) { if (j == 0) wait_vm<5>(); else if (j == 2) wait_vm<3>(); else if (j == 4) wait_vm<1>(); else if (j == 6) wait_vm<0>(); }
        if (yng == 8 && j == 0) wait_vm<8>();
        tie(H[q % 3][j]);
#pragma unroll
        for (int ds = 0; ds < 4; ds++) {
          const int f = 4 * j + ds;
          acc[ds] = mfma16(f < 12 ? W2x[f] : A[f - 12], H[q % 3][j], j == 0 ? f32x4{0.f, 0.f, 0.f, 0.f} : acc[ds]);
        }
      }
      __builtin_amdgcn_sched_barrier(0);
#pragma unroll
      for (int ds = 0; ds < 4; ds++) *(f32x4*)(wr + q * 32768 + (((ds * 4 + g) ^ r) * 16)) = acc[ds];
    };
    const int tk = (tid >> 4) & 15, qh = w >> 2;
    u32 xq[2][4];
    f32x4 gbv;
    auto finish = [&](int qp) NOPK {
      const char* rp = smem + (2 * qp + qh) * 32768 + tk * 256 + ((r ^ tk) * 16);
      f32x4 s = *(const f32x4*)rp;
#pragma unroll
      for (int i = 1; i < 8; i++) s += *(const f32x4*)(rp + i * 4096);
      float y[4];
#pragma unroll
      for (int j = 0; j < 4; j++) y[j] = fmaf(gbv[j], s[j], bflo(xq[qp][j]));
      asm volatile("global_store_dwordx2 %0, %1, %2 sc1" ::"v"((tk * D + r * 4) * 2), "v"(u32x2{pk(y[0], y[1]), pk(y[2], y[3])}),
                   "s"(out + (size_t)((2 * qp + qh) * 512 + tt2 * 32 + sel * 16) * D + dt * 64) : "memory");
    };
    st4one(fpb + 64, tag);
    need(0xffffull);
    ldh(0);
    TS(30);
    {
      const char* xp = smem + D_RES + qh * 2048 + (tk >> 3) * 256 + r * 16 + (tk & 7) * 2;
#pragma unroll
      for (int i = 0; i < 8; i++) xq[i >> 2][i & 3] = *(const u16*)(xp + (i >> 2) * 4096 + (i & 3) * 512);
      gbv = *(const f32x4*)(smem + D_GB + r * 16);
    }
    wait_vm<8>();
    st4one(fpb + 96, tag);
    TS(31);
    need(0xffffull << 16);
    ld4(fh, fhb, fho);
    ldh(1);
    wait_vm<7>(); tie(fh);
    TS(32);
    need(0xffffull << 32);
    ld4(fh, fhb, fho);
    ldh(2);
    quarter(0, -1);
    TS(33);
    wait_vm<7>(); tie(fh);
    need(0xffffull << 48);
    TS(34);
    ldh(3);
    quarter(1, -1);
    TS(35);
    lds_barrier();
    finish(0);
    asm volatile("" ::: "memory");
    TS(36);
    TSF(29, 36);
    quarter(2, 8);
    TS(37);
    quarter(3, 0);
    TS(38);
    lds_barrier();
    asm volatile(".Lpf1:");
    finish(1);
    TS(39);
    TS(40);
    TSF(37, 40);
    TSEND();
  }
}

extern "C" void kernel_launch(void* const* d_in, const int* in_sizes, int n_in, void* d_out, int out_size, void* d_ws,
                              size_t ws_size, hipStream_t stream) {
  const u16* const* in = (const u16* const*)d_in;
  fused<<<256, 512, 0, stream>>>(in[0], in[1], in[2], in[3], in[4], in[5], in[6], in[7], in[8], in[10], in[11], in[12], in[13], in[14],
                                 (char*)d_ws, (u16*)d_out);
}
```

```cpp
#include <hip/hip_runtime.h>
#include <stdint.h>

typedef unsigned int u32;
typedef unsigned short u16;
typedef __attribute__((ext_vector_type(2))) float f32x2;
typedef __attribute__((ext_vector_type(4))) float f32x4;
typedef __attribute__((ext_vector_type(16))) float f32x16;
typedef __attribute__((ext_vector_type(2))) u32 u32x2;
typedef __attribute__((ext_vector_type(4))) u32 u32x4;
typedef __attribute__((ext_vector_type(2))) __bf16 bf16x2;
typedef __attribute__((ext_vector_type(8))) __bf16 bf16x8;
typedef __attribute__((ext_vector_type(4))) short s16x4;
#define LDS_AS __attribute__((address_space(3)))
#if defined(__HIP_DEVICE_COMPILE__)
#define NOPK __attribute__((target("no-packed-fp32-ops")))
#else
#define NOPK
#endif
#define DEVINL __device__ __forceinline__ NOPK

constexpr int L = 2048, D = 512, NS = 128, F = 1792;

constexpr size_t WS_E = 0;
constexpr size_t WS_FW = 4096;
constexpr size_t WS_FX = WS_FW + 32768;
constexpr size_t WS_FH = WS_FX + 32768;
constexpr size_t WS_MG = WS_FH + 65536;
constexpr size_t WS_GB = WS_MG + 98304;
constexpr size_t WS_XG = 262144;
constexpr size_t WS_X1 = WS_XG + (4u << 20);
constexpr size_t WS_W1F = WS_X1 + (2u << 20);
constexpr size_t WS_W2F = WS_W1F + 112 * 32 * 1024;
constexpr size_t WS_HF = WS_W2F + 16 * 112 * 1024;
constexpr size_t WS_CD = WS_HF + 64 * 112 * 1024;
constexpr int SPIN_CAP = 40000;
#define GP 1
#define RP 1
#define SCR 3
#define LBC 6
#define NF1 24
#define PFM 3
#define PFB 32768

#define u2f(x) __builtin_bit_cast(float, (u32)(x))
#define i2f(x) __builtin_bit_cast(float, (int)(x))
#define f2u(x) __builtin_bit_cast(u32, (float)(x))
#define f2i(x) __builtin_bit_cast(int, (float)(x))
#define TSDECL
#define TS(slot)
#define TC(slot)
#define TSF(lo, hi)
#define TSEND()
DEVINL float bflo(u32 u) { return u2f(u << 16); }
DEVINL float bfhi(u32 u) { return u2f(u & 0xffff0000u); }
DEVINL u32 pk(float a, float b) {
  f32x2 v = {a, b};
  return __builtin_bit_cast(u32, __builtin_convertvector(v, bf16x2));
}
DEVINL float rbf(float a) { return bflo(pk(a, a)); }
template <int CTRL> DEVINL float dpp_add(float v) {
  return v + i2f(__builtin_amdgcn_update_dpp(0, f2i(v), CTRL, 0xf, 0xf, true));
}
DEVINL float row4_sum(float v) {
  auto a = __builtin_amdgcn_permlane16_swap(f2u(v), f2u(v), false, false);
  v = u2f(a[0]) + u2f(a[1]);
  auto b = __builtin_amdgcn_permlane32_swap(f2u(v), f2u(v), false, false);
  return u2f(b[0]) + u2f(b[1]);
}
DEVINL float rs32(float a, float b) {
  auto r = __builtin_amdgcn_permlane32_swap(f2u(a), f2u(b), false, false);
  return u2f(r[0]) + u2f(r[1]);
}
DEVINL float rs16(float a, float b) {
  auto r = __builtin_amdgcn_permlane16_swap(f2u(a), f2u(b), false, false);
  return u2f(r[0]) + u2f(r[1]);
}
DEVINL float wave_sum(float v) { return row4_sum(dpp_add<0x140>(dpp_add<0x141>(dpp_add<0x4E>(dpp_add<0xB1>(v))))); }
DEVINL f32x4 mfma16(u32x4 a, u32x4 b, f32x4 c) {
  return __builtin_amdgcn_mfma_f32_16x16x32_bf16(__builtin_bit_cast(bf16x8, a), __builtin_bit_cast(bf16x8, b), c, 0, 0, 0);
}
DEVINL f32x16 mfma32(u32x4 a, u32x4 b, f32x16 c) {
  return __builtin_amdgcn_mfma_f32_32x32x16_bf16(__builtin_bit_cast(bf16x8, a), __builtin_bit_cast(bf16x8, b), c, 0, 0, 0);
}
DEVINL float dot2(u32 a, u32 b, float c) {
  return __builtin_amdgcn_fdot2_f32_bf16(__builtin_bit_cast(bf16x2, a), __builtin_bit_cast(bf16x2, b), c, false);
}
DEVINL void lds_barrier() {
  asm volatile("s_waitcnt lgkmcnt(0)" ::: "memory");
  __builtin_amdgcn_s_barrier();
  asm volatile("" ::: "memory");
}
DEVINL void bar() {
  asm volatile("" ::: "memory");
  __builtin_amdgcn_s_barrier();
  asm volatile("" ::: "memory");
}
DEVINL void st16(const void* sbase, u32 voff, u32x4 v) {
  asm volatile("global_store_dwordx4 %0, %1, %2 sc1\n\ts_nop 1" ::"v"(voff), "v"(v), "s"(sbase) : "memory");
}
DEVINL void st8(const void* sbase, u32 voff, u32x2 v) {
  asm volatile("global_store_dwordx2 %0, %1, %2 sc1\n\ts_nop 1" ::"v"(voff), "v"(v), "s"(sbase) : "memory");
}
DEVINL void st4(const void* sbase, u32 voff, u32 v) {
  asm volatile("global_store_dword %0, %1, %2 sc1\n\ts_nop 1" ::"v"(voff), "v"(v), "s"(sbase) : "memory");
}
template <int LANES> DEVINL void st8m(const void* sbase, u32 voff, u32x2 v) {
  asm volatile("s_mov_b64 exec, %3\n\tglobal_store_dwordx2 %0, %1, %2 sc1\n\ts_mov_b64 exec, -1\n\ts_nop 0"
               ::"v"(voff), "v"(v), "s"(sbase), "n"((1 << LANES) - 1) : "memory");
}
DEVINL void st4one(const void* sbase, u32 v) {
  asm volatile("s_mov_b64 exec, 1\n\tglobal_store_dword %0, %1, %2 sc1\n\ts_mov_b64 exec, -1\n\ts_nop 0"
               ::"v"(0), "v"(v), "s"(sbase) : "memory");
}
DEVINL void st16x2_56(const void* sbase, u32 voff, u32x4 a, u32x4 b) {
  asm volatile("s_mov_b32 exec_hi, 0xffffff\n\tglobal_store_dwordx4 %0, %1, %3 sc1\n\tglobal_store_dwordx4 %0, %2, %3 offset:256 sc1\n\t"
               "s_mov_b32 exec_hi, -1\n\ts_nop 0" ::"v"(voff), "v"(a), "v"(b), "s"(sbase) : "memory");
}
DEVINL void sld4(u32& d, const void* p) { asm volatile("s_load_dword %0, %1, 0" : "=s"(d) : "s"(p) : "memory"); }
template <int OFF = 0> DEVINL void ld16(u32x4& d, const void* sbase, u32 voff) {
  asm volatile("global_load_dwordx4 %0, %1, %2 offset:%3 sc1" : "=v"(d) : "v"(voff), "s"(sbase), "n"(OFF) : "memory");
}
DEVINL void ld16k(u32x4& d, const char* sbase, int kb, u32 voff) {
  const char* sb = sbase + (kb >> 2) * 4096;
  if ((kb & 3) == 0) ld16<0>(d, sb, voff); else if ((kb & 3) == 1) ld16<1024>(d, sb, voff);
  else if ((kb & 3) == 2) ld16<2048>(d, sb, voff); else ld16<3072>(d, sb, voff);
}
DEVINL void ld8(u32x2& d, const void* sbase, u32 voff) {
  asm volatile("global_load_dwordx2 %0, %1, %2 sc1" : "=v"(d) : "v"(voff), "s"(sbase) : "memory");
}
template <int OFF = 0> DEVINL void ld4(u32& d, const void* sbase, u32 voff) {
  asm volatile("global_load_dword %0, %1, %2 offset:%3 sc1" : "=v"(d) : "v"(voff), "s"(sbase), "n"(OFF) : "memory");
}
template <int OFF = 0> DEVINL void lp16(u32x4& d, const void* sbase, u32 voff) {
  asm volatile("global_load_dwordx4 %0, %1, %2 offset:%3" : "=v"(d) : "v"(voff), "s"(sbase), "n"(OFF) : "memory");
}
template <int OFF = 0> DEVINL void lp4(u32& d, const void* sbase, u32 voff) {
  asm volatile("global_load_dword %0, %1, %2 offset:%3" : "=v"(d) : "v"(voff), "s"(sbase), "n"(OFF) : "memory");
}
template <int OFF = 0> DEVINL void lp2(u32& d, const void* sbase, u32 voff) {
  asm volatile("global_load_ushort %0, %1, %2 offset:%3" : "=v"(d) : "v"(voff), "s"(sbase), "n"(OFF) : "memory");
}
template <class T> DEVINL void tie(T& a) { asm volatile("" : "+v"(a)); }
template <int N> DEVINL void wait_vm() { asm volatile("s_waitcnt vmcnt(%0)" ::"n"(N) : "memory"); }
#define wait_vmn(k) asm volatile("s_waitcnt vmcnt(%0)" ::"n"(k) : "memory")
DEVINL bool all_lanes(bool ok) { return __builtin_amdgcn_ballot_w64(!ok) == 0; }
DEVINL void spin4(u32 f, u32 tag, const void* sbase, u32 voff) {
#pragma unroll 1
  for (int it = 0; it < SPIN_CAP && !all_lanes(f == tag); it++) {
    __builtin_amdgcn_s_sleep(1);
    ld4(f, sbase, voff); wait_vm<0>(); tie(f);
  }
}
DEVINL bool arrive_last(char* smem_word, int n) {
  u32 old;
  asm volatile("s_mov_b64 exec, 1\n\tds_add_rtn_u32 %0, %1, %2\n\ts_waitcnt lgkmcnt(0)\n\ts_mov_b64 exec, -1"
               : "=&v"(old) : "v"((u32)(size_t)(LDS_AS char*)smem_word), "v"(1) : "memory");
  return __builtin_amdgcn_readfirstlane(old) == (u32)(n - 1);
}
DEVINL void scale8(u32x4& a, const u32x4& s, const u32x4& t, int on) {
  u32 a0 = a[0], a1 = a[1], a2 = a[2], a3 = a[3], x0, x1, x2, x3;
  asm("s_cmp_eq_u32 %16, 0\n\ts_cbranch_scc1 .Lsk%=\n\tv_dot2_f32_bf16 %4, %0, %8, 0\n\tv_dot2_f32_bf16 %0, %0, %9, 0\n\tv_dot2_f32_bf16 %5, %1, %10, 0\n\tv_dot2_f32_bf16 %1, %1, %11, 0\n\t"
      "v_dot2_f32_bf16 %6, %2, %12, 0\n\tv_dot2_f32_bf16 %2, %2, %13, 0\n\tv_dot2_f32_bf16 %7, %3, %14, 0\n\tv_dot2_f32_bf16 %3, %3, %15, 0\n\t"
      "v_cvt_pk_bf16_f32 %0, %4, %0\n\tv_cvt_pk_bf16_f32 %1, %5, %1\n\tv_cvt_pk_bf16_f32 %2, %6, %2\n\ts_nop 0\n\tv_cvt_pk_bf16_f32 %3, %7, %3\n.Lsk%=:"
      : "+v"(a0), "+v"(a1), "+v"(a2), "+v"(a3), "=&v"(x0), "=&v"(x1), "=&v"(x2), "=&v"(x3)
      : "v"(s[0]), "v"(s[1]), "v"(s[2]), "v"(s[3]), "v"(t[0]), "v"(t[1]), "v"(t[2]), "v"(t[3]), "s"(on) : "scc");
  a = u32x4{a0, a1, a2, a3};
}
DEVINL u32x2 tr_read(const char* p) {
  return __builtin_bit_cast(u32x2, __builtin_amdgcn_ds_read_tr16_b64_v4i16((LDS_AS s16x4*)p));
}
DEVINL float rdl(float v, int l) { return i2f(__builtin_amdgcn_readlane(f2i(v), l)); }
DEVINL float sel2(int c, float a, float b) { return c ? b : a; }
DEVINL u32 swap2(u32 v) { return (u32)__builtin_amdgcn_update_dpp(0, (int)v, 0x4E, 0xf, 0xf, true); }
DEVINL u32 swap1(u32 v) { return (u32)__builtin_amdgcn_update_dpp(0, (int)v, 0xB1, 0xf, 0xf, true); }
constexpr float LOG2E = 1.4426950408889634f, LN2 = 0.6931471805599453f, INV2PI = 0.15915494309189535f;

constexpr int A_TAB = 0, A_P = 8 * 1536, A_KC = A_P + 24576, A_KCS = 4112, A_GV = A_KC + 2 * A_KCS;
constexpr int B_Y1 = 0, B_RED = 40960, B_KC = 49152, B_KCS = 4160, B_KCH = 4 * 4160, B_U = B_KC + 2 * B_KCH, B_US = 5152, B_UCH = 2 * B_US;
constexpr int B_Y2 = B_U + 2 * B_UCH, A_XT = B_Y2;
constexpr int C_RAW = 148480, L_CNT = C_RAW + 1024, D_RES = L_CNT + 64, D_GB = D_RES + 8192, B_SC = D_GB + 256, LDS_BYTES = B_SC + 4096;
static_assert(A_GV + 2048 <= B_KC && B_Y2 + 40960 <= C_RAW && 131072 <= C_RAW && LDS_BYTES <= 163840, "lds map");

DEVINL void glds4k(const void* sbase, u32 voff, u32 ldsaddr) {
  asm volatile("s_mov_b32 m0, %2\n\ts_nop 0\n\t"
               "global_load_lds_dwordx4 %0, %1 sc1\n\tglobal_load_lds_dwordx4 %0, %1 offset:1024 sc1\n\t"
               "global_load_lds_dwordx4 %0, %1 offset:2048 sc1\n\tglobal_load_lds_dwordx4 %0, %1 offset:3072 sc1"
               ::"v"(voff), "s"(sbase), "s"(ldsaddr) : "memory");
}
DEVINL u32x4 tr8(const char* p) {
  const u32x2 lo = tr_read(p), hi = tr_read(p + 256);
  return u32x4{lo[0], lo[1], hi[0], hi[1]};
}
template <int OFF> DEVINL void glds1k(const void* sbase, u32 voff, u32 ldsaddr) {
  asm volatile("s_mov_b32 m0, %2\n\ts_nop 0\n\tglobal_load_lds_dwordx4 %0, %1 offset:%3 sc1" ::"v"(voff), "s"(sbase), "s"(ldsaddr), "n"(OFF) : "memory");
}
DEVINL const char* far(const char* p) {
  u32 a = (u32)(size_t)(LDS_AS const char*)p;
  asm("" : "+v"(a));
  return (const char*)(LDS_AS const char*)(size_t)a;
}
struct Ep { float q0, v; };
template <int N> struct IC { static constexpr int v = N; };
#define accg(a, i) a[((i) & 3) + 8 * ((i) >> 2)]
#define accu(a, i) a[((i) & 3) + 8 * ((i) >> 2) + 4]
DEVINL float swi(const f32x16& a, int i, const Ep& q) {
  const float e = __builtin_amdgcn_exp2f(accg(a, i) * q.q0);
  return (accg(a, i) * accu(a, i)) * __builtin_amdgcn_rcpf(fmaf(e, q.v, q.v));
}
DEVINL u32 ln2_fold(f32x2 s, int h, Ep& q) {
  const float m = s[0] * (1.f / D);
  const float v = fmaxf(fmaf(-m, m, s[1] * (1.f / D)), 0.f) + 1e-5f;
  const float rs = __builtin_amdgcn_rsqf(v);
  q.q0 = rs * -LOG2E; q.v = v;
  const float val = h ? v * rs : -m;
  const float vhi = rbf(val);
  return pk(vhi, val - vhi);
}
DEVINL void ln2_tile(const char* rb, f32x2* raw, f32x2* dummy, int lane) {
  const u32x4 ones = {0x3f803f80u, 0x3f803f80u, 0x3f803f80u, 0x3f803f80u};
  f32x4 sg[2], sx[2];
#pragma unroll
  for (int i = 0; i < 2; i++) { sg[i] = f32x4{0.f, 0.f, 0.f, 0.f}; sx[i] = f32x4{0.f, 0.f, 0.f, 0.f}; }
  u32x4 fr[4][2];
  auto rd = [&](int n) NOPK {
#pragma unroll
    for (int gi = 0; gi < 2; gi++) {
      const char* pp = rb + n * 2048 + gi * 32;
      const u32x2 lo = tr_read(pp), hi = tr_read(pp + 1024);
      fr[n & 3][gi] = u32x4{lo[0], lo[1], hi[0], hi[1]};
    }
  };
  rd(0); rd(1); rd(2);
#pragma unroll
  for (int n = 0; n < 16; n++) {
    if (n + 3 < 16) rd(n + 3);
    __builtin_amdgcn_sched_barrier(0);
#pragma unroll
    for (int gi = 0; gi < 2; gi++) {
      sg[gi] = mfma16(fr[n & 3][gi], fr[n & 3][gi], sg[gi]);
      sx[gi] = mfma16(ones, fr[n & 3][gi], sx[gi]);
    }
    __builtin_amdgcn_sched_barrier(0);
  }
#pragma unroll
  for (int gi = 0; gi < 2; gi++) {
    const float gsel = (lane & 2) ? ((lane & 1) ? sg[gi][3] : sg[gi][2]) : ((lane & 1) ? sg[gi][1] : sg[gi][0]);
    f32x2* dst = (lane >> 4) == ((lane & 15) >> 2) ? raw + gi * 16 + (lane & 15) : dummy;
    *dst = f32x2{sx[gi][0], gsel};
  }
}

__global__ NOPK __launch_bounds__(512, 2) void fused(
    const u16* __restrict__ x, const u16* __restrict__ Are, const u16* __restrict__ Aim, const u16* __restrict__ Bre,
    const u16* __restrict__ Bim, const u16* __restrict__ Cre, const u16* __restrict__ Cim, const u16* __restrict__ logdt,
    const u16* __restrict__ dskip, const u16* __restrict__ w1, const u16* __restrict__ w3, const u16* __restrict__ w2,
    const u16* __restrict__ cvec, const u16* __restrict__ wmod, char* ws, u16* __restrict__ out) {
  __shared__ __attribute__((aligned(16))) char smem[LDS_BYTES];
  const int b = blockIdx.x, tid = threadIdx.x, lane = tid & 63;
  const int w = __builtin_amdgcn_readfirstlane(tid >> 6);
  TSDECL; TS(0);
  const int xg = b & 7, idx = b >> 3;
  const unsigned long long t00_ = __builtin_amdgcn_s_memrealtime();
  const int p = xg * 32 + idx;
  const int r = lane & 15, g = lane >> 4;
  asm volatile("" ::"s"(x), "s"(Are), "s"(Aim), "s"(Bre), "s"(Bim), "s"(Cre), "s"(Cim), "s"(logdt), "s"(dskip), "s"(w1), "s"(w3),
               "s"(w2), "s"(cvec), "s"(wmod), "s"(ws), "s"(out));
  u32x4 xr, wm0, wm1;
  u32 vare, vaim, vbre, vbim, vcre, vcim, c0, c1;
  const int cg = xg * 24 + (idx < 24 ? idx : 23);
  u32 e_prev, ldt2, dsk;
  lp16(xr, x + b * 4096, tid * 16);
  sld4(e_prev, ws + WS_E); sld4(ldt2, logdt + 2 * p); sld4(dsk, dskip + 2 * p);
  {
    const u32 so = (p * 256 + w * 32 + (lane & 31)) * 2;
    lp2(vare, Are, so); lp2(vaim, Aim, so); lp2(vbre, Bre, so); lp2(vbim, Bim, so); lp2(vcre, Cre, so); lp2(vcim, Cim, so);
    const u32 mo = (tid >> 3) * 6144 + (tid & 7) * 16;
    const u16* wmp = wmod + (cg & 3) * 128 * 3072 + (cg >> 2) * 64;
    lp16(wm0, wmp, mo); lp16(wm1, wmp + 64 * 3072, mo);
    lp2(c0, cvec + (cg & 3) * 128, (tid >> 3) * 2); lp2<128>(c1, cvec + (cg & 3) * 128, (tid >> 3) * 2);
  }
  u32 pf;
  {
    unsigned long long pc;
    u32 po = ((idx & PFM) * 512 + tid) * (PFB / (PFM + 1) / 512);
    asm volatile("s_getpc_b64 %1\n.Lpf0:\n\tv_min_u32 %2, .Lpf1-.Lpf0, %2\n\tglobal_load_dword %0, %2, %1"
                 : "=v"(pf), "=&s"(pc), "+v"(po)::"memory");
  }
  TS(1);
  if (tid < 32) *(u32x4*)(smem + B_KC + (tid >> 2) * B_KCS + 4096 + (tid & 3) * 16) = u32x4{0, 0, 0, 0};
  else if (tid < 36) *(u32x4*)(smem + L_CNT + (tid - 32) * 16) = u32x4{0, 0, 0, 0};
  else if (tid < 38) *(u32x2*)(smem + B_U + (tid & 1) * B_UCH + B_US + 1024) = u32x2{0, 0};
  else if (tid >= 256) *(u32x4*)(smem + B_U + (w - 4) * B_US + lane * 16) = u32x4{0, 0, 0, 0};
  asm volatile("s_waitcnt lgkmcnt(0)" : "+s"(e_prev), "+s"(ldt2), "+s"(dsk)::"memory");
  const u32 tag = e_prev * 1664525u + 1013904223u;
  wait_vm<11>(); tie(xr);
  {
    *(u32x4*)(smem + A_XT + w * 1056 + lane * 16) = xr;
    float f[8];
#pragma unroll
    for (int j = 0; j < 4; j++) { f[2 * j] = bflo(xr[j]); f[2 * j + 1] = bfhi(xr[j]); }
    const float m = wave_sum(((f[0] + f[1]) + (f[2] + f[3])) + ((f[4] + f[5]) + (f[6] + f[7]))) * (1.f / D);
    float s = 0.f;
#pragma unroll
    for (int j = 0; j < 8; j++) { f[j] -= m; s = fmaf(f[j], f[j], s); }
    const float sd = __builtin_amdgcn_sqrtf(rbf(rbf(wave_sum(s) * (1.f / D)) + 1e-5f));
    if (lane == 0) *(u32*)(smem + A_XT + w * 1056 + 1024) = pk(m, sd);
  }
  TS(2);
  wait_vm<5>(); tie(vare); tie(vaim); tie(vbre); tie(vbim); tie(vcre); tie(vcim);
  char* tab = smem + A_TAB + w * 1536;
  {
    const float dt = rbf(__builtin_amdgcn_exp2f((w >> 2 ? bfhi(ldt2) : bflo(ldt2)) * LOG2E));
    const float a = dt * bflo(vare) * LOG2E, bq = dt * bflo(vaim) * INV2PI;
    const float br = dt * bflo(vbre), bi = dt * bflo(vbim), cr = bflo(vcre), ci = bflo(vcim);
    const float m = lane < 32 ? 1.f : 256.f;
    const float e = __builtin_amdgcn_exp2f(a * m), ph = __builtin_amdgcn_fractf(-bq * m);
    const float ex = e * __builtin_amdgcn_cosf(ph), ey = e * __builtin_amdgcn_sinf(ph);
    char* t = tab + (lane & 31) * 48;
    *(f32x2*)(t + 16 + (lane >> 5) * 8) = f32x2{ex, ey};
    f32x4 o = {a, bq, cr * br - ci * bi, cr * bi + ci * br};
    if (lane >= 32) o = f32x4{ex, ey, ex * ex - ey * ey, (ex + ex) * ey};
    *(f32x4*)(t + (lane >> 5) * 32) = o;
  }
  TS(3);
  lds_barrier();
  {
    const char* xt = smem + A_XT + (lane & 7) * 1056;
    const u32 st = *(const u32*)(xt + 1024);
#pragma unroll
    for (int e = 0; e < 2; e++) {
      const u32x2 xq = *(const u32x2*)(xt + (e * 64 + w * 8 + (lane >> 3)) * 8);
      st16(ws + WS_XG + (size_t)(e * 64 + w * 8) * 32768 + b * 128, (lane >> 3) * 32768 + (lane & 7) * 16, u32x4{xq[0], xq[1], st, tag});
    }
  }
  TS(41);
  if (w == 0) st4one(ws + (32u << 20) + b * 4, (u32)t00_);
  wait_vm<0>();
  const int fb = (xg >> 1) * 4 + (idx & 3), mat = (idx >> 2) & 1, dq = (xg & 1) * 4 + (idx >> 3), d0 = dq * 64 + w * 8;
  const int b2 = b < 224 ? b : 223;
  u32 v[8], t2[8];
  {
    const u16* wp = (mat ? w3 : w1) + (size_t)d0 * F + fb * 112;
    const u32 vo = (lane < 56 ? lane : 55) * 4;
#pragma unroll
    for (int i = 0; i < 4; i++) { lp4<0>(v[2 * i], wp + 2 * i * F, vo); lp4<2 * F>(v[2 * i + 1], wp + 2 * i * F, vo); }
#pragma unroll
    for (int i = 0; i < 2; i++) {
      const u16* wq = w2 + (size_t)(b2 * 8 + i * 4) * D;
      const u32 to = tid * 2;
      lp2<0>(t2[4 * i], wq, to); lp2<1024>(t2[4 * i + 1], wq, to); lp2<2048>(t2[4 * i + 2], wq, to); lp2<3072>(t2[4 * i + 3], wq, to);
    }
  }
  auto retile = [&]() NOPK {
    wait_vm<0>();
#pragma unroll
    for (int i = 0; i < 8; i++) { tie(v[i]); tie(t2[i]); }
    u32x4 o0, o1, o2;
#pragma unroll
    for (int j = 0; j < 4; j++) {
      o0[j] = __builtin_amdgcn_perm(v[2 * j + 1], v[2 * j], 0x05040100u);
      o1[j] = __builtin_amdgcn_perm(v[2 * j + 1], v[2 * j], 0x07060302u);
      o2[j] = __builtin_amdgcn_perm(t2[2 * j + 1], t2[2 * j], 0x05040100u);
    }
    st16x2_56(ws + WS_W1F + (size_t)(fb * 7 * 32 + (d0 >> 4)) * 1024 + (w & 1) * 512 + mat * 128, (lane >> 3) * 32768 + (lane & 7) * 16, o0, o1);
    st16(ws + WS_W2F + w * 229376 + (b2 >> 2) * 4096 + (b2 & 3) * 256, g * 1024 + r * 16, o2);
  };
  auto gemv = [&]() NOPK
  {
    wait_vm<18>(); tie(pf); tie(wm0); tie(wm1); tie(c0); tie(c1);
    const float cx0 = bflo(c0), cx1 = bflo(c1);
    const float s0 = rbf(cx0 * __builtin_amdgcn_rcpf(1.f + __builtin_amdgcn_exp2f(cx0 * -LOG2E)));
    const float s1 = rbf(cx1 * __builtin_amdgcn_rcpf(1.f + __builtin_amdgcn_exp2f(cx1 * -LOG2E)));
    float acc[8];
#pragma unroll
    for (int j = 0; j < 4; j++) {
      acc[2 * j] = fmaf(s0, bflo(wm0[j]), s1 * bflo(wm1[j]));
      acc[2 * j + 1] = fmaf(s0, bfhi(wm0[j]), s1 * bfhi(wm1[j]));
    }
    const float q0 = dpp_add<0x128>(rs16(rs32(acc[0], acc[4]), rs32(acc[2], acc[6])));
    const float q1 = dpp_add<0x128>(rs16(rs32(acc[1], acc[5]), rs32(acc[3], acc[7])));
    *(float*)(smem + A_GV + w * 256 + (lane & 7) * 32 + (lane >> 3) * 4) = (lane & 8) ? q1 : q0;
    if (arrive_last(smem + L_CNT, 8) && idx < 24) {
      float s = 0.f;
#pragma unroll
      for (int i = 0; i < 8; i++) s += *(const float*)(smem + A_GV + i * 256 + lane * 4);
      st8(ws + WS_MG + (cg & 3) * 24576 + (cg >> 2) * 512, lane * 8, u32x2{f2u(s), tag});
    }
  };
  TS(4);
  if (GP == 0) { gemv(); TS(44); }
  const u32 gcode = __builtin_amdgcn_perm(0x400b0916u, 0x0a201408u, (r & 7) | 0x0c0c0c00u);
  const int gx = r < 8 ? 2 * r : (int)((gcode >> 2) * 256 + (gcode & 3) * 4);
  {
    asm volatile("" ::: "memory");
    const float mF = (float)(16 * r), mX = (float)gx;
    const char* tp = tab + g * 8 * 48;
    const char* tq = tp + (r < 8 ? 16 : 32);
    f32x4 acc[3];
#pragma unroll
    for (int s = 0; s < 3; s++) acc[s] = f32x4{0.f, 0.f, 0.f, 0.f};
#pragma unroll
    for (int ks = 0; ks < 2; ks++) {
      u32x4 Ff, Gf[3];
#pragma unroll
      for (int i = 0; i < 4; i++) {
        const f32x4 s0 = *(const f32x4*)(tp + (ks * 4 + i) * 48), s1 = *(const f32x4*)(tq + (ks * 4 + i) * 48);
        const float e0 = __builtin_amdgcn_exp2f(s0[0] * mF), p0 = __builtin_amdgcn_fractf(s0[1] * mF);
        const float zx = e0 * __builtin_amdgcn_cosf(p0), zy = e0 * __builtin_amdgcn_sinf(p0);
        Ff[i] = pk(s0[2] * zx - s0[3] * zy, s0[2] * zy + s0[3] * zx);
        const float e1 = __builtin_amdgcn_exp2f(s0[0] * mX), p1 = __builtin_amdgcn_fractf(s0[1] * -mX);
        const float xx = e1 * __builtin_amdgcn_cosf(p1), xy = e1 * __builtin_amdgcn_sinf(p1);
        Gf[0][i] = pk(xx, xy);
        Gf[1][i] = pk(xx * s1[0] - xy * s1[1], xx * s1[1] + xy * s1[0]);
        Gf[2][i] = pk(xx * s1[2] - xy * s1[3], xx * s1[3] + xy * s1[2]);
      }
#pragma unroll
      for (int s = 0; s < 3; s++) acc[s] = mfma16(Ff, Gf[s], acc[s]);
      if (ks == 0) { TS(45); if (RP == 0) retile(); if (GP == 1) { gemv(); TS(44); } }
    }
#pragma unroll
    for (int s = 0; s < 3; s++) *(f32x4*)(smem + A_P + w * 3072 + s * 1024 + lane * 16) = acc[s];
  }
  if (RP == 1) retile();
  TS(5);
  u32x4 xg4[4];
  u32x2 mg, mh, mc[4];
  const char* xgb = ws + WS_XG + (size_t)(p >> 1) * 32768;
  const u32 xgo = (tid >> 2) * 256 + (tid & 3) * 16;
  u32 mga = (4 - ((lane >> 3) & 1)) * 512 + d0 + (lane & 7), mgb = ((0x5210 >> ((lane & 6) * 2)) & 7) * 512 + 2 * p + (lane & 1);
  tie(mga); tie(mgb);
  const u32 mgo = ((lane & 16) ? mgb : mga) * 8 + (lane >> 5) * 24576;
  auto lookx = [&]() NOPK {
    ld16<0>(xg4[0], xgb, xgo); ld16<64>(xg4[1], xgb, xgo); ld16<128>(xg4[2], xgb, xgo); ld16<192>(xg4[3], xgb, xgo);
  };
  auto lookm = [&]() NOPK {
    ld8(mg, ws + WS_MG, mgo); ld8(mh, ws + WS_MG + 49152, mgo);
  };
  auto lookc = [&]() NOPK {
#pragma unroll
    for (int q = 0; q < 4; q++) ld8(mc[q], ws + WS_MG + q * 24576 + 16384, tid * 8);
  };
  lookx();
  lds_barrier();
  TS(6);
  TSF(0, 6); TSF(41, 41); TSF(44, 45);
  const int kch = tid >> 8;
  u16* Kc = (u16*)(smem + A_KC + kch * A_KCS);
  if ((w & 3) < 3) {
    const int s = w & 3;
    const char* pp = smem + A_P + kch * 12288 + s * 1024 + lane * 16;
    const f32x4 q = (*(const f32x4*)(pp) + *(const f32x4*)(pp + 3072)) + (*(const f32x4*)(pp + 6144) + *(const f32x4*)(pp + 9216));
    const int pos = gx + 64 * g + (r < 8 ? (s == 0 ? 0 : s == 1 ? 1 : 256) : s * 256);
    const u32 o0 = pk(q[0], q[1]), o1 = pk(q[2], q[3]);
    if (pos <= 2048) Kc[pos] = (u16)o0;
    if (pos < 2048) {
      Kc[pos + 16] = (u16)(o0 >> 16);
      Kc[pos + 32] = (u16)o1;
      Kc[pos + 48] = (u16)(o1 >> 16);
    }
  }
  wait_vm<4>();
  lds_barrier();
  TS(7);
  if (w == 7) st4one(ws + WS_FW + (fb * 16 + mat * 8 + dq) * 128, tag);
  const int tt = (xg & 1) * 8 + (idx & 7), fs = (xg >> 1) * 4 + (idx >> 3);
  const u32 fwo = fs * 2048 + (lane & 15) * 128;
  u32 fw;
  lookm(); lookc();
  {
    u32 e[6];
    const int ki = tid & 255;
#pragma unroll
    for (int j = 0; j < 6; j++) {
      const float l0 = (float)(2047 - 8 * ki - 2 * j);
      const u32 bits = pk(fmaxf(l0, 0.f), fmaxf(l0 - 1.f, 0.f));
      e[j] = (u32)Kc[(int)bflo(bits)];
      if (j < 5) e[j] |= (u32)Kc[(int)bfhi(bits)] << 16;
    }
    if (ki == 255) { e[4] = 0; e[5] = 0; }
    const u32 a0 = __builtin_amdgcn_alignbit(e[1], e[0], 16), a1 = __builtin_amdgcn_alignbit(e[2], e[1], 16);
    const u32 a2 = __builtin_amdgcn_alignbit(e[3], e[2], 16), a3 = __builtin_amdgcn_alignbit(e[4], e[3], 16);
    const u32 a4 = __builtin_amdgcn_alignbit(e[5], e[4], 16);
    char* kc = smem + B_KC + kch * B_KCH + ki * 16;
    *(u32x4*)(kc) = u32x4{e[0], e[1], e[2], e[3]};
    *(u32x4*)(kc + B_KCS) = u32x4{a0, a1, a2, a3};
    *(u32x4*)(kc + 2 * B_KCS) = u32x4{e[1], e[2], e[3], e[4]};
    *(u32x4*)(kc + 3 * B_KCS) = u32x4{a1, a2, a3, a4};
  }
  TS(8);
  wait_vm<0>(); tie(xg4[0]); tie(xg4[1]); tie(xg4[2]); tie(xg4[3]); tie(mg); tie(mh); tie(mc[0]); tie(mc[1]); tie(mc[2]); tie(mc[3]);
  TS(47);
#pragma unroll 1
  for (int it = 0; it < SPIN_CAP && !all_lanes(xg4[0][3] == tag && xg4[1][3] == tag && xg4[2][3] == tag && xg4[3][3] == tag); it++) {
    lookx();
    wait_vm<0>(); tie(xg4[0]); tie(xg4[1]); tie(xg4[2]); tie(xg4[3]);
  }
#pragma unroll 1
  for (int it = 0; it < SPIN_CAP && !all_lanes(mg[1] == tag && mh[1] == tag); it++) {
    lookm();
    wait_vm<0>(); tie(mg); tie(mh);
  }
  TS(9);
  const int h = lane >> 5, c = lane & 31;
  const int tt2 = (~xg & 1) * 8 + (xg & 6) + (idx >> 4), sel = (idx >> 3) & 1, dt = idx & 7;
  const int rot = (2 * (idx >> 3) + idx) & 7;
  const int ft = fs * 7 + w - 1;
  u32x4 A[32];
  const u32 ao = lane * 16;
  auto lda = [&](int i) NOPK {
    const char* wq = ws + WS_W1F + (size_t)ft * 32768 + (((i >> 2) + rot) & 7) * 4096;
    asm volatile("s_cmp_eq_u32 %4, 0\n\ts_cbranch_scc1 .Lna%=\n\tglobal_load_dwordx4 %0, %1, %2 offset:%3 sc1\n.Lna%=:"
                 : "=v"(A[i]) : "v"(ao), "s"(wq), "n"((i & 3) * 1024), "s"(w) : "memory", "scc");
  };
  ld4(fw, ws + WS_FW, fwo);
  const int g8 = tid >> 1, cc = tid & 1;
  const float mq = u2f(mg[0]) + u2f(mh[0]);
  const float mv = rbf(rs32(mq, mq)), mv1 = rbf(1.f + mv);
  const float ga = sel2(cc, rdl(mv, 20), rdl(mv, 21));
  u32x4 hp, xv;
  {
    const float sh0 = rdl(mv, 16), sh1 = rdl(mv, 17), sc0 = rdl(mv1, 18), sc1 = rdl(mv1, 19);
    const u32 psel = cc ? 0x03020706u : 0x05040100u;
    const bool up = tid & 2;
    u32 ph[4], px[4];
#pragma unroll
    for (int i = 0; i < 4; i++) {
      const u32 xq = (p & 1) ? xg4[i][1] : xg4[i][0];
      const float m = bflo(xg4[i][2]), rs = __builtin_amdgcn_rcpf(bfhi(xg4[i][2]));
      const u32 n = pk((bflo(xq) - m) * rs, (bfhi(xq) - m) * rs);
      const u32 hq = pk(fmaf(bflo(n), sc0, sh0), fmaf(bfhi(n), sc1, sh1));
      ph[i] = __builtin_amdgcn_perm(swap1(hq), hq, psel);
      px[i] = __builtin_amdgcn_perm(swap1(xq), xq, psel);
    }
#pragma unroll
    for (int j = 0; j < 2; j++) {
      const u32 rh = swap2(up ? ph[j] : ph[2 + j]), kh = up ? ph[2 + j] : ph[j];
      hp[2 * j] = up ? rh : kh; hp[2 * j + 1] = up ? kh : rh;
      const u32 rx = swap2(up ? px[j] : px[2 + j]), kx = up ? px[2 + j] : px[j];
      xv[2 * j] = up ? rx : kx; xv[2 * j + 1] = up ? kx : rx;
    }
    char* u = smem + B_U + cc * B_UCH + 1024 + g8 * 16;
    *(u32x4*)(u) = hp;
    *(u32x2*)(u + B_US + 8) = u32x2{hp[0], hp[1]};
    *(u32x2*)(u + B_US + 16) = u32x2{hp[2], hp[3]};
  }
#pragma unroll 1
  for (int it = 0; it < SPIN_CAP && !all_lanes(mc[0][1] == tag && mc[1][1] == tag && mc[2][1] == tag && mc[3][1] == tag); it++) {
    lookc();
    wait_vm<0>(); tie(mc[0]); tie(mc[1]); tie(mc[2]); tie(mc[3]);
  }
  {
    const float sc = 1.f + rbf((u2f(mc[0][0]) + u2f(mc[2][0])) + (u2f(mc[1][0]) + u2f(mc[3][0])));
    const u32 e = pk(sc, sc) & ((tid & 1) ? 0xffff0000u : 0xffffu);
    *(u32*)(smem + B_SC + tid * 4) = e;
    *(u32*)(smem + B_SC + 2048 + tid * 4) = e;
  }
  TS(10);
  lds_barrier();
  TS(11);
  const char* tb = smem + B_SC + rot * 256 + h * 32;
  u32x4 tq[3][2];
  auto ldt = [&](int i) NOPK { tq[i % 3][0] = *(const u32x4*)(tb + i * 64); tq[i % 3][1] = *(const u32x4*)(tb + i * 64 + 16); };
  auto scl = [&](int i) NOPK { tie(A[i]); scale8(A[i], tq[i % 3][0], tq[i % 3][1], w); tie(A[i]); };
  auto epi = [&](int yb) NOPK {
    const int gl = g8 & 127;
    const float* Y = (const float*)(smem + yb) + cc * 1280 + 8 * gl + 8 * (gl >> 2);
    const f32x4 ya = (*(const f32x4*)Y + *(const f32x4*)(Y + 2560)) + (*(const f32x4*)(Y + 5120) + *(const f32x4*)(Y + 7680));
    const f32x4 yb4 = (*(const f32x4*)(Y + 4) + *(const f32x4*)(Y + 2564)) + (*(const f32x4*)(Y + 5124) + *(const f32x4*)(Y + 7684));
    const float dk = cc ? bfhi(dsk) : bflo(dsk);
    u32x4 o;
#pragma unroll
    for (int j = 0; j < 4; j++) {
      const float y0 = j < 2 ? ya[2 * j] : yb4[2 * j - 4], y1 = j < 2 ? ya[2 * j + 1] : yb4[2 * j - 3];
      o[j] = pk(fmaf(ga, fmaf(bflo(hp[j]), dk, y0), bflo(xv[j])), fmaf(ga, fmaf(bfhi(hp[j]), dk, y1), bfhi(xv[j])));
    }
    st16(ws + WS_X1 + p * 128 + (w >> 1) * 32768, ((g8 >> 2) & 15) * 131072 + cc * 64 + (g8 & 3) * 16, o);
  };
  constexpr int LBT = LBC, NS2 = 51 / SCR + 1;
  {
    const int ch = w & 1, nq = w >> 1;
    const char* pa = smem + B_KC + ch * B_KCH + (3 - (r & 3)) * B_KCS + 2 * (2040 - 8 * (r >> 2) + 8 * g) - 512 * 7 - 64 * (2 * nq + 1);
    const char* pb = smem + B_U + ch * B_UCH + ((r & 1) ? 0 : B_US) + 2 * (512 + 32 * (r >> 1) + 8 * g) - 64 * (2 * nq + 1);
    const int yo = w * 5120 + 160 * (r >> 1) + 16 * (r & 1) + 32 * g;
    f32x4 acc[4];
    u32x4 la[2][4], lb[2][4], hf[4];
    auto ldhi = [&](int q) NOPK {
      const int e = 4 + ((q & 7) >> 1);
      hf[q & 3] = *(const u32x4*)(((q & 1) ? pa + 512 * (7 - e) : pb + 512 * e) + 64 * (1 - (q >> 3)));
    };
#pragma unroll
    for (int vv = 0; vv < 2; vv++)
#pragma unroll
      for (int n = 0; n < 8; n++) {
        const int e = (0x30212310 >> (4 * n)) & 3;
        if ((0xb1 >> n) & 1) la[vv][e] = *(const u32x4*)(pa + 512 * (7 - e) + 64 * (1 - vv));
        else lb[vv][e] = *(const u32x4*)(pb + 512 * e + 64 * (1 - vv));
      }
    __builtin_amdgcn_sched_barrier(0);
    {
      f32x4 sums = {0.f, 0.f, 0.f, 0.f};
#pragma unroll
      for (int j = 0; j < 4; j++) {
        const float k0 = rdl(mv1, 2 * j), k1 = rdl(mv1, 2 * j + 1), s0 = rdl(mv, 8 + 2 * j), s1 = rdl(mv, 8 + 2 * j + 1);
        const float a0 = bflo(v[2 * j]), a1 = bflo(v[2 * j + 1]), b0 = bfhi(v[2 * j]), b1 = bfhi(v[2 * j + 1]);
        sums[0] = dot2(pk(a0 * k0, a1 * k1), 0x3f803f80u, sums[0]);
        sums[1] = dot2(pk(b0 * k0, b1 * k1), 0x3f803f80u, sums[1]);
        sums[2] = fmaf(a0, s0, fmaf(a1, s1, sums[2]));
        sums[3] = fmaf(b0, s0, fmaf(b1, s1, sums[3]));
      }
      *(f32x4*)(smem + B_RED + tid * 16) = sums;
    }
#pragma unroll
    for (int i = 0; i < 4; i++) acc[i] = f32x4{0.f, 0.f, 0.f, 0.f};
    wait_vm<0>(); tie(fw);
    spin4(fw, tag, ws + WS_FW, fwo);
    TS(46);
    __builtin_amdgcn_sched_barrier(0);
#pragma unroll
    for (int n = 0; n < 20; n++) {
      const int vv = n / 10, k = n % 10, e = (0x0231211000ll >> (4 * k)) & 3, u = (0x0000112231ll >> (4 * k)) & 3;
      acc[e + u] = mfma16(la[vv][e], lb[vv][u], acc[e + u]);
      if (n % 3 == 0 && n / 3 < LBC) lda(n / 3);
      if (n >= 12 && n < 15) ldhi(n - 12);
      __builtin_amdgcn_sched_barrier(0);
    }
    TS(12);
#pragma unroll
    for (int i = 0; i < 4; i++) *(f32x4*)(smem + B_Y1 + yo + 1280 * i) = acc[i];
    lds_barrier();
    TS(13);
    TSF(7, 13); TSF(46, 47);
    int w_ = w;
    asm volatile("" : "+s"(w_));
    if (w_ < 4) {
      __builtin_amdgcn_s_setprio(3);
      epi(B_Y1);
      __builtin_amdgcn_s_setprio(0);
    }
    asm volatile("" : "+s"(w_));
    if (w_ == 0) {
      f32x4 s = {0.f, 0.f, 0.f, 0.f};
#pragma unroll
      for (int q = 0; q < 8; q++) s += *(const f32x4*)(smem + B_RED + q * 1024 + lane * 16);
      st16(ws + WS_CD + ((dq * 16 + fb) * 2 + mat) * 1024, lane * 16, __builtin_bit_cast(u32x4, s));
      st8m<1>(ws + WS_GB + p * 8, 0, u32x2{f2u(rdl(mv, 22)), f2u(rdl(mv, 23))});
    }
    TS(14);
    ldt(0);
#pragma unroll
    for (int n = 0; n < 52; n++) {
      const int vv = n / 26, k = n % 26;
      const int e = (k < 13 ? 0x2210144443210ll >> (4 * k) : 0x7036631023555ll >> (4 * (k - 13))) & 7;
      const int u = (k < 13 ? 0x2555332104444ll >> (4 * k) : 0x0731026631210ll >> (4 * (k - 13))) & 7;
      const int q = vv * 8 + (e > 3 ? 2 * e - 7 : 2 * u - 8);
      if (n == NF1) {
        asm volatile("" : "+s"(w_));
        if (w_ < 4) {
          wait_vmn((n + SCR - 1) / SCR);
          if (w_ == 0) wait_vm<0>();
          if (arrive_last(smem + L_CNT + 8, 4)) st4one(ws + WS_FX + (b & 63) * 128 + (b >> 6) * 4, tag);
        }
        TS(15);
      }
      if (k == 0 || k == 4 || k == 8 || k == 12 || k == 16 || k == 20 || k == 23 || k == 25) {
        const int qn = vv * 8 + (k < 23 ? k / 4 : k == 23 ? 6 : 7) + 3;
        if (qn < 16) ldhi(qn);
      }
      if (n % SCR == 0) ldt(n / SCR + 1);
      __builtin_amdgcn_sched_barrier(0);
      acc[e + u - 4] = mfma16(e > 3 ? hf[q & 3] : la[vv][e], u > 3 ? hf[q & 3] : lb[vv][u], n < 4 ? f32x4{0.f, 0.f, 0.f, 0.f} : acc[e + u - 4]);
      if (n % SCR == 0) { wait_vm<LBT - 1>(); lda(LBT + n / SCR); scl(n / SCR); }
      __builtin_amdgcn_sched_barrier(0);
    }
    TS(16);
#pragma unroll
    for (int i = 0; i < 4; i++) *(f32x4*)(smem + B_Y2 + yo + 1280 * i) = acc[i];
    ldt(NS2 + 1);
    lds_barrier();
    TS(42);
    asm volatile("" : "+s"(w_));
    if (w_ >= 4) {
      __builtin_amdgcn_s_setprio(3);
      epi(B_Y2);
      __builtin_amdgcn_s_setprio(0);
    }
    TS(43);
    asm volatile("" : "+s"(w_));
    if (w_ != 0) {
#pragma unroll
      for (int i = NS2; i < 32; i++) {
        if (i + 2 < 32) ldt(i + 2);
        wait_vmn(i + LBT < 32 ? LBT - 1 : 31 - i);
        if (i + LBT < 32) lda(i + LBT);
        __builtin_amdgcn_sched_barrier(0);
        scl(i);
        __builtin_amdgcn_sched_barrier(0);
      }
    }
    wait_vm<0>();
  }
  f32x2* raw = (f32x2*)(smem + C_RAW);
  u32x4 W2x[12];
  u32 fh;
  const u32 lds0 = (u32)(size_t)(LDS_AS char*)smem;
  const char* xa = ws + WS_X1 + tt * 131072 + ((w + rot) & 7) * 4096;
  const u32 lpc = lds0 + w * 4096;
  const int rt2 = dt + 2 * (idx >> 3), rot2 = rt2 < 7 ? rt2 : rt2 - 7;
#define kof(j) ((j) + rot2 < 7 ? (j) + rot2 : (j) + rot2 - 7)
  const char* w2b = ws + WS_W2F + dt * 229376 + w * 28672;
  const char* fhb = ws + WS_FH + tt2 * 2048 + w * 256;
  const u32 fho = ((lane >> 3) & 1) * 128 + g * 32 + (lane & 7) * 4;
  const char* fpb = ws + WS_FH + (tt * 16 + fs) * 128 + w * 4;
  auto ldw2 = [&](int j) NOPK {
    const char* q = w2b + kof(j >> 2) * 4096;
    if (j < 12) ld16k(W2x[j], q, j & 3, ao); else ld16k(A[j - 12], q, j & 3, ao);
  };
  auto poll_x = [&](int part) NOPK {
#pragma unroll 1
    for (int it = 0; it < SPIN_CAP; it++) {
      u32x4 f;
      ld16(f, ws + WS_FX + part * 8192, lane * 128); wait_vm<0>(); tie(f);
      if (all_lanes(f[0] == tag && f[1] == tag && f[2] == tag && f[3] == tag)) break;
    }
  };
  if (w == 0) {
    __builtin_amdgcn_s_setprio(3);
    poll_x(0);
    TS(17);
#pragma unroll
    for (int q = 0; q < 8; q++) glds4k(ws + WS_X1 + tt * 131072 + ((q + rot) & 7) * 4096, ao, lds0 + q * 4096);
    const char* rb = smem + (lane >> 2) * 64 + (lane & 3) * 8;
    wait_vm<0>();
    TS(18);
    bar();
    TS(19); TC(20);
    TSF(14, 20); TSF(42, 43);
    st4one(ws + WS_FX + 8192 + (b & 63) * 128 + (b >> 6) * 4, tag);
    glds4k(xa + 32768, ao, lpc + 32768);
    if (b == 0) st4one(ws + WS_E, tag);
    st4one(fpb, tag); st4one(fpb + 32, tag);
#pragma unroll
    for (int i = 0; i < 14; i++) ldw2(i);
    ln2_tile(rb, raw, raw + 132, lane);
    TS(21);
    poll_x(1);
    lds_barrier();
    TS(22);
    glds4k(xa + 65536, ao, lpc + 65536); glds4k(xa + 98304, ao, lpc + 98304);
#pragma unroll
    for (int q = 0; q < 4; q++) {
      const char* xr_ = ws + WS_X1 + (tt2 * 4 + q) * 32768 + dt * 4096 + sel * 32;
      const u32 xo = r * 256 + h * 64 + (g & 1) * 16, la = lds0 + D_RES + q * 2048;
      glds1k<0>(xr_, xo, la); glds1k<0>(xr_ + 128, xo, la + 1024);
    }
    asm volatile("s_mov_b32 m0, %2\n\ts_nop 0\n\tglobal_load_lds_dword %0, %1 sc1"
                 ::"v"(lane * 4), "s"(ws + WS_GB + dt * 256), "s"(lds0 + D_GB) : "memory");
    ln2_tile(rb + 32768, raw + 32, raw + 132, lane);
    TS(23);
    wait_vm<0>(); lds_barrier();
    TS(24);
#pragma unroll
    for (int i = 14; i < 28; i++) ldw2(i);
    rb = far(rb + 65536);
    ln2_tile(rb, raw + 64, raw + 132, lane);
    asm volatile("" ::: "memory");
    *(volatile LDS_AS u32*)(smem + L_CNT + 16) = 1;
    TS(25);
    ln2_tile(rb + 32768, raw + 96, raw + 132, lane);
    asm volatile("" ::: "memory");
    *(volatile LDS_AS u32*)(smem + L_CNT + 16) = 2;
    __builtin_amdgcn_s_setprio(0);
    TS(26); TS(27); TC(28);
    TSF(21, 28);
    ld4(fh, fhb, fho);
    wait_vm<0>(); tie(fh);
#pragma unroll
    for (int i = 0; i < 28; i++) { if (i < 12) tie(W2x[i]); else tie(A[i - 12]); }
    TS(29);
  }
  int w_ = w;
  asm volatile("" : "+s"(w_));
  if (w_ != 0) {
    const char* rb = smem + h * 512 + ((lane >> 2) & 3) * 64 + ((lane >> 4) & 1) * 32 + (lane & 3) * 8;
    const char* rb2 = far(rb + 65536);
    u32 cdv[8];
    f32x16 acc[2], zero;
#pragma unroll
    for (int i = 0; i < 16; i++) zero[i] = 0.f;
    TS(17);
    lds_barrier();
    TS(18);
    TS(19); TC(20);
    TSF(14, 20); TSF(42, 43);
    __builtin_amdgcn_s_setprio(3);
    u32x4 Bq[3], Ax, op;
    Bq[0] = tr8(rb); Bq[1] = tr8(rb + 1024);
    f32x2 rw;
    Ep q[2];
    float ev, gu, oa;
    const char* hb = ws + WS_HF + (size_t)(tt * 8 * 56 + (ft >> 1)) * 1024 + (ft & 1) * 512;
    const u32 ho = (c >> 4) * 57344 + h * 256 + r * 16;
    auto stat_wait = [&](u32 need) NOPK {
#pragma unroll 1
      for (int it = 0; it < SPIN_CAP * 16; it++)
        if (__builtin_amdgcn_readfirstlane(*(volatile LDS_AS u32*)(smem + L_CNT + 16)) >= need) break;
      asm volatile("" ::: "memory");
    };
    auto tile = [&](auto tc) NOPK {
    constexpr int T = decltype(tc)::v, E = T - 1;
#pragma unroll
    for (int ks = 0; ks < 32; ks++) {
      const int n = T * 32 + ks, m = n + 2;
      if (n == 30) {
        TS(21);
        wait_vm<8>();
#pragma unroll
        for (int i = 0; i < 8; i++) tie(cdv[i]);
        lds_barrier();
        TS(22);
      } else if (n == 62) {
        TS(23);
        wait_vm<5>();
        lds_barrier();
        TS(24);
      }
      if (n == 30 || n == 62) __builtin_amdgcn_s_setprio(3);
      if (n == 14 || n == 46 || n == 100) __builtin_amdgcn_s_setprio(2);
      if (n == 22 || n == 54 || n == 118) __builtin_amdgcn_s_setprio(1);
      if (n == 27 || n == 59) __builtin_amdgcn_s_setprio(0);
      if (n == 96) { TS(25); tie(acc[0]); stat_wait(1); }
      if (n == 118) { tie(acc[1]); stat_wait(2); }
      if (n == 120) wait_vm<10>();
      if (m < 128) Bq[m % 3] = tr8((m < 64 ? rb : rb2) + (m & 63) * 1024);
      if (T > 0 && ks == 0) rw = raw[E * 32 + c];
      if (n == 118) rw = raw[96 + c];
      if (n >= 120) A[n - 112] = *(const u32x4*)(smem + ((n >> 2) & 1) * 32768 + w * 4096 + (n & 3) * 1024 + lane * 16);
      __builtin_amdgcn_sched_barrier(0);
      acc[T & 1] = mfma32(A[ks], Bq[n % 3], ks == 0 ? zero : acc[T & 1]);
      if (n == 31) {
        const float cd = ((u2f(cdv[0]) + u2f(cdv[1])) + (u2f(cdv[2]) + u2f(cdv[3]))) + ((u2f(cdv[4]) + u2f(cdv[5])) + (u2f(cdv[6]) + u2f(cdv[7])));
        const float chi = rbf(cd);
        Ax = u32x4{pk(chi, chi), pk(cd - chi, cd - chi), 0, 0};
      }
      if ((T > 0 && ks == 2) || n == 120) {
        const int X = (n == 120 ? 3 : E) & 1;
        const u32 be = ln2_fold(rw, h, q[X]);
        acc[X] = mfma32(Ax, u32x4{be, be, 0, 0}, acc[X]);
      }
      if (T > 0 && ks >= 4 && ks < 20) {
        const int k = (ks - 4) >> 1, i = (k >> 1) + 4 * (k & 1);
        if ((ks & 1) == 0) {
          ev = fmaf(__builtin_amdgcn_exp2f(accg(acc[E & 1], i) * q[E & 1].q0), q[E & 1].v, q[E & 1].v);
          gu = accg(acc[E & 1], i) * accu(acc[E & 1], i);
        } else {
          const float o = gu * __builtin_amdgcn_rcpf(ev);
          if (k & 1) op[k >> 1] = pk(oa, o); else oa = o;
        }
      }
      __builtin_amdgcn_sched_barrier(0);
      if (n == 1) {
        const u32 co = ((c >> 3) & 1) * 1024 + ((w - 1) * 8 + (c & 7)) * 16 + h * 8 + (c >> 4) * 4;
#pragma unroll
        for (int i = 0; i < 8; i++) ld4(cdv[i], ws + WS_CD + (i * 16 + fs) * 2048, co);
      }
      if (n == 0) glds4k(xa + 32768, ao, lpc + 32768);
      if (T == 0 && ks >= 4 && ks < 28 && ks % 3 == 1) ldw2((ks - 4) / 3);
      if (T == 1 && ks < 16 && (ks & 1)) {
        const int i = ks >> 1, o = 65536 + (i >> 2) * 32768;
        if ((i & 3) == 0) glds1k<0>(xa + o, ao, lpc + o); else if ((i & 3) == 1) glds1k<1024>(xa + o, ao, lpc + o);
        else if ((i & 3) == 2) glds1k<2048>(xa + o, ao, lpc + o); else glds1k<3072>(xa + o, ao, lpc + o);
      }
      if (T == 1 && (ks == 18 || ks == 22 || ks == 24 || ks == 26)) ldw2(ks == 18 ? 8 : ks / 2 - 2);
      if (T > 1 && ks == 9) { wait_vm<T == 2 ? 6 : 4>(); st4one(fpb + (T - 2) * 32, tag); }
      if (T > 0 && ks == 20) st16(hb + E * 114688, ho, op);
      if (T > 1 && ks % 3 == 1 && ks < 24) {
        const int i = ks / 3;
        const char* q = w2b + kof(5 + (i >> 2)) * 4096;
        const u32 la = lpc + (i >> 2) * 32768;
        if (T == 3) ldw2(12 + i);
        else if ((i & 3) == 0) glds1k<0>(q, ao, la); else if ((i & 3) == 1) glds1k<1024>(q, ao, la);
        else if ((i & 3) == 2) glds1k<2048>(q, ao, la); else glds1k<3072>(q, ao, la);
      }
      if (n == 122) ld4(fh, fhb, fho);
    }
    };
    tile(IC<0>{}); tile(IC<1>{}); tile(IC<2>{}); tile(IC<3>{});
    TS(26); TS(27); TC(28);
    TSF(21, 28);
#pragma unroll
    for (int i = 0; i < 4; i++) op[i] = pk(swi(acc[1], i, q[1]), swi(acc[1], i + 4, q[1]));
    st16(hb + 3 * 114688, ho, op);
    __builtin_amdgcn_s_setprio(0);
    wait_vm<1>(); tie(fh);
#pragma unroll
    for (int i = 0; i < 20; i++) { if (i < 12) tie(W2x[i]); else tie(A[i - 12]); }
    TS(29);
  }
  {
    const char* hq = ws + WS_HF + (size_t)((tt2 * 8 + sel) * 56 + w * 7) * 1024;
    u32x4 H[3][7];
    auto need = [&](unsigned long long mask) NOPK {
#pragma unroll 1
      for (int it = 0; it < SPIN_CAP && (__builtin_amdgcn_ballot_w64(fh != tag) & mask) != 0; it++) {
        __builtin_amdgcn_s_sleep(2);
        ld4(fh, fhb, fho); wait_vm<0>(); tie(fh);
      }
    };
    auto ldh = [&](int q) NOPK {
#pragma unroll
      for (int j = 0; j < 7; j++) ld16(H[q % 3][j], hq + q * 114688 + kof(j) * 1024, ao);
    };
    char* wr = smem + w * 4096 + r * 256;
    auto quarter = [&](int q, int yng) NOPK {
      f32x4 acc[4];
#pragma unroll
      for (int j = 0; j < 7; j++) {
        __builtin_amdgcn_sched_barrier(0);
        if (yng == 0) { if (j == 0) wait_vm<5>(); else if (j == 2) wait_vm<3>(); else if (j == 4) wait_vm<1>(); else if (j == 6) wait_vm<0>(); }
        if (yng == 8 && j == 0) wait_vm<8>();
        tie(H[q % 3][j]);
#pragma unroll
        for (int ds = 0; ds < 4; ds++) {
          const int f = 4 * j + ds;
          acc[ds] = mfma16(f < 12 ? W2x[f] : A[f - 12], H[q % 3][j], j == 0 ? f32x4{0.f, 0.f, 0.f, 0.f} : acc[ds]);
        }
      }
      __builtin_amdgcn_sched_barrier(0);
#pragma unroll
      for (int ds = 0; ds < 4; ds++) *(f32x4*)(wr + q * 32768 + (((ds * 4 + g) ^ r) * 16)) = acc[ds];
    };
    const int tk = (tid >> 4) & 15, qh = w >> 2;
    u32 xq[2][4];
    f32x4 gbv;
    auto finish = [&](int qp) NOPK {
      const char* rp = smem + (2 * qp + qh) * 32768 + tk * 256 + ((r ^ tk) * 16);
      f32x4 s = *(const f32x4*)rp;
#pragma unroll
      for (int i = 1; i < 8; i++) s += *(const f32x4*)(rp + i * 4096);
      float y[4];
#pragma unroll
      for (int j = 0; j < 4; j++) y[j] = fmaf(gbv[j], s[j], bflo(xq[qp][j]));
      asm volatile("global_store_dwordx2 %0, %1, %2 sc1" ::"v"((tk * D + r * 4) * 2), "v"(u32x2{pk(y[0], y[1]), pk(y[2], y[3])}),
                   "s"(out + (size_t)((2 * qp + qh) * 512 + tt2 * 32 + sel * 16) * D + dt * 64) : "memory");
    };
    st4one(fpb + 64, tag);
    need(0xffffull);
    ldh(0);
    TS(30);
    {
      const char* xp = smem + D_RES + qh * 2048 + (tk >> 3) * 256 + r * 16 + (tk & 7) * 2;
#pragma unroll
      for (int i = 0; i < 8; i++) xq[i >> 2][i & 3] = *(const u16*)(xp + (i >> 2) * 4096 + (i & 3) * 512);
      gbv = *(const f32x4*)(smem + D_GB + r * 16);
    }
    wait_vm<8>();
    st4one(fpb + 96, tag);
    TS(31);
    need(0xffffull << 16);
    ld4(fh, fhb, fho);
    ldh(1);
    wait_vm<7>(); tie(fh);
    TS(32);
    need(0xffffull << 32);
    ld4(fh, fhb, fho);
    ldh(2);
    quarter(0, -1);
    TS(33);
    wait_vm<7>(); tie(fh);
    need(0xffffull << 48);
    TS(34);
    ldh(3);
    quarter(1, -1);
    TS(35);
    lds_barrier();
    finish(0);
    asm volatile("" ::: "memory");
    TS(36);
    TSF(29, 36);
    quarter(2, 8);
    TS(37);
    quarter(3, 0);
    TS(38);
    lds_barrier();
    asm volatile(".Lpf1:");
    finish(1);
    TS(39);
    TS(40);
    {
      wait_vm<0>();
      u32x4 tv; ld16(tv, ws + (32u << 20), lane * 16); wait_vm<0>(); tie(tv);
      const u32 me = (u32)t00_;
      int d = min(min((int)(tv[0] - me), (int)(tv[1] - me)), min((int)(tv[2] - me), (int)(tv[3] - me)));
      int e = __builtin_amdgcn_readlane(d, 0);
#pragma unroll 1
      for (int i = 1; i < 64; i++) { int q = __builtin_amdgcn_readlane(d, i); e = min(e, q); }
      const u32 base = me + e;
      while ((u32)__builtin_amdgcn_s_memrealtime() - base < 4000u) __builtin_amdgcn_s_sleep(1);
    }
    TSF(37, 40);
    TSEND();
  }
}

extern "C" void kernel_launch(void* const* d_in, const int* in_sizes, int n_in, void* d_out, int out_size, void* d_ws,
                              size_t ws_size, hipStream_t stream) {
  const u16* const* in = (const u16* const*)d_in;
  fused<<<256, 512, 0, stream>>>(in[0], in[1], in[2], in[3], in[4], in[5], in[6], in[7], in[8], in[10], in[11], in[12], in[13], in[14],
                                 (char*)d_ws, (u16*)d_out);
}
```

```cpp
#include <hip/hip_runtime.h>
#include <stdint.h>

typedef unsigned int u32;
typedef unsigned short u16;
typedef __attribute__((ext_vector_type(2))) float f32x2;
typedef __attribute__((ext_vector_type(4))) float f32x4;
typedef __attribute__((ext_vector_type(16))) float f32x16;
typedef __attribute__((ext_vector_type(2))) u32 u32x2;
typedef __attribute__((ext_vector_type(4))) u32 u32x4;
typedef __attribute__((ext_vector_type(2))) __bf16 bf16x2;
typedef __attribute__((ext_vector_type(8))) __bf16 bf16x8;
typedef __attribute__((ext_vector_type(4))) short s16x4;
#define LDS_AS __attribute__((address_space(3)))
#if defined(__HIP_DEVICE_COMPILE__)
#define NOPK __attribute__((target("no-packed-fp32-ops")))
#else
#define NOPK
#endif
#define DEVINL __device__ __forceinline__ NOPK

constexpr int L = 2048, D = 512, NS = 128, F = 1792;

constexpr size_t WS_E = 0;
constexpr size_t WS_FW = 4096;
constexpr size_t WS_FX = WS_FW + 32768;
constexpr size_t WS_FH = WS_FX + 32768;
constexpr size_t WS_MG = WS_FH + 65536;
constexpr size_t WS_GB = WS_MG + 98304;
constexpr size_t WS_XG = 262144;
constexpr size_t WS_X1 = WS_XG + (4u << 20);
constexpr size_t WS_W1F = WS_X1 + (2u << 20);
constexpr size_t WS_W2F = WS_W1F + 112 * 32 * 1024;
constexpr size_t WS_HF = WS_W2F + 16 * 112 * 1024;
constexpr size_t WS_CD = WS_HF + 64 * 112 * 1024;
constexpr int SPIN_CAP = 40000;
#define GP 1
#define RP 1
#define SCR 3
#define LBC 6
#define NF1 24
#define PFM 3
#define PFB 32768

#define u2f(x) __builtin_bit_cast(float, (u32)(x))
#define i2f(x) __builtin_bit_cast(float, (int)(x))
#define f2u(x) __builtin_bit_cast(u32, (float)(x))
#define f2i(x) __builtin_bit_cast(int, (float)(x))
#define TSDECL
#define TS(slot)
#define TC(slot)
#define TSF(lo, hi)
#define TSEND()
DEVINL float bflo(u32 u) { return u2f(u << 16); }
DEVINL float bfhi(u32 u) { return u2f(u & 0xffff0000u); }
DEVINL u32 pk(float a, float b) {
  f32x2 v = {a, b};
  return __builtin_bit_cast(u32, __builtin_convertvector(v, bf16x2));
}
DEVINL float rbf(float a) { return bflo(pk(a, a)); }
template <int CTRL> DEVINL float dpp_add(float v) {
  return v + i2f(__builtin_amdgcn_update_dpp(0, f2i(v), CTRL, 0xf, 0xf, true));
}
DEVINL float row4_sum(float v) {
  auto a = __builtin_amdgcn_permlane16_swap(f2u(v), f2u(v), false, false);
  v = u2f(a[0]) + u2f(a[1]);
  auto b = __builtin_amdgcn_permlane32_swap(f2u(v), f2u(v), false, false);
  return u2f(b[0]) + u2f(b[1]);
}
DEVINL float rs32(float a, float b) {
  auto r = __builtin_amdgcn_permlane32_swap(f2u(a), f2u(b), false, false);
  return u2f(r[0]) + u2f(r[1]);
}
DEVINL float rs16(float a, float b) {
  auto r = __builtin_amdgcn_permlane16_swap(f2u(a), f2u(b), false, false);
  return u2f(r[0]) + u2f(r[1]);
}
DEVINL float wave_sum(float v) { return row4_sum(dpp_add<0x140>(dpp_add<0x141>(dpp_add<0x4E>(dpp_add<0xB1>(v))))); }
DEVINL f32x4 mfma16(u32x4 a, u32x4 b, f32x4 c) {
  return __builtin_amdgcn_mfma_f32_16x16x32_bf16(__builtin_bit_cast(bf16x8, a), __builtin_bit_cast(bf16x8, b), c, 0, 0, 0);
}
DEVINL f32x16 mfma32(u32x4 a, u32x4 b, f32x16 c) {
  return __builtin_amdgcn_mfma_f32_32x32x16_bf16(__builtin_bit_cast(bf16x8, a), __builtin_bit_cast(bf16x8, b), c, 0, 0, 0);
}
DEVINL float dot2(u32 a, u32 b, float c) {
  return __builtin_amdgcn_fdot2_f32_bf16(__builtin_bit_cast(bf16x2, a), __builtin_bit_cast(bf16x2, b), c, false);
}
DEVINL void lds_barrier() {
  asm volatile("s_waitcnt lgkmcnt(0)" ::: "memory");
  __builtin_amdgcn_s_barrier();
  asm volatile("" ::: "memory");
}
DEVINL void bar() {
  asm volatile("" ::: "memory");
  __builtin_amdgcn_s_barrier();
  asm volatile("" ::: "memory");
}
DEVINL void st16(const void* sbase, u32 voff, u32x4 v) {
  asm volatile("global_store_dwordx4 %0, %1, %2 sc1\n\ts_nop 1" ::"v"(voff), "v"(v), "s"(sbase) : "memory");
}
DEVINL void st8(const void* sbase, u32 voff, u32x2 v) {
  asm volatile("global_store_dwordx2 %0, %1, %2 sc1\n\ts_nop 1" ::"v"(voff), "v"(v), "s"(sbase) : "memory");
}
DEVINL void st4(const void* sbase, u32 voff, u32 v) {
  asm volatile("global_store_dword %0, %1, %2 sc1\n\ts_nop 1" ::"v"(voff), "v"(v), "s"(sbase) : "memory");
}
template <int LANES> DEVINL void st8m(const void* sbase, u32 voff, u32x2 v) {
  asm volatile("s_mov_b64 exec, %3\n\tglobal_store_dwordx2 %0, %1, %2 sc1\n\ts_mov_b64 exec, -1\n\ts_nop 0"
               ::"v"(voff), "v"(v), "s"(sbase), "n"((1 << LANES) - 1) : "memory");
}
DEVINL void st4one(const void* sbase, u32 v) {
  asm volatile("s_mov_b64 exec, 1\n\tglobal_store_dword %0, %1, %2 sc1\n\ts_mov_b64 exec, -1\n\ts_nop 0"
               ::"v"(0), "v"(v), "s"(sbase) : "memory");
}
DEVINL void st16x2_56(const void* sbase, u32 voff, u32x4 a, u32x4 b) {
  asm volatile("s_mov_b32 exec_hi, 0xffffff\n\tglobal_store_dwordx4 %0, %1, %3 sc1\n\tglobal_store_dwordx4 %0, %2, %3 offset:256 sc1\n\t"
               "s_mov_b32 exec_hi, -1\n\ts_nop 0" ::"v"(voff), "v"(a), "v"(b), "s"(sbase) : "memory");
}
DEVINL void sld4(u32& d, const void* p) { asm volatile("s_load_dword %0, %1, 0" : "=s"(d) : "s"(p) : "memory"); }
template <int OFF = 0> DEVINL void ld16(u32x4& d, const void* sbase, u32 voff) {
  asm volatile("global_load_dwordx4 %0, %1, %2 offset:%3 sc1" : "=v"(d) : "v"(voff), "s"(sbase), "n"(OFF) : "memory");
}
DEVINL void ld16k(u32x4& d, const char* sbase, int kb, u32 voff) {
  const char* sb = sbase + (kb >> 2) * 4096;
  if ((kb & 3) == 0) ld16<0>(d, sb, voff); else if ((kb & 3) == 1) ld16<1024>(d, sb, voff);
  else if ((kb & 3) == 2) ld16<2048>(d, sb, voff); else ld16<3072>(d, sb, voff);
}
DEVINL void ld8(u32x2& d, const void* sbase, u32 voff) {
  asm volatile("global_load_dwordx2 %0, %1, %2 sc1" : "=v"(d) : "v"(voff), "s"(sbase) : "memory");
}
template <int OFF = 0> DEVINL void ld4(u32& d, const void* sbase, u32 voff) {
  asm volatile("global_load_dword %0, %1, %2 offset:%3 sc1" : "=v"(d) : "v"(voff), "s"(sbase), "n"(OFF) : "memory");
}
template <int OFF = 0> DEVINL void lp16(u32x4& d, const void* sbase, u32 voff) {
  asm volatile("global_load_dwordx4 %0, %1, %2 offset:%3" : "=v"(d) : "v"(voff), "s"(sbase), "n"(OFF) : "memory");
}
template <int OFF = 0> DEVINL void lp4(u32& d, const void* sbase, u32 voff) {
  asm volatile("global_load_dword %0, %1, %2 offset:%3" : "=v"(d) : "v"(voff), "s"(sbase), "n"(OFF) : "memory");
}
template <int OFF = 0> DEVINL void lp2(u32& d, const void* sbase, u32 voff) {
  asm volatile("global_load_ushort %0, %1, %2 offset:%3" : "=v"(d) : "v"(voff), "s"(sbase), "n"(OFF) : "memory");
}
template <class T> DEVINL void tie(T& a) { asm volatile("" : "+v"(a)); }
template <int N> DEVINL void wait_vm() { asm volatile("s_waitcnt vmcnt(%0)" ::"n"(N) : "memory"); }
#define wait_vmn(k) asm volatile("s_waitcnt vmcnt(%0)" ::"n"(k) : "memory")
DEVINL bool all_lanes(bool ok) { return __builtin_amdgcn_ballot_w64(!ok) == 0; }
DEVINL void spin4(u32 f, u32 tag, const void* sbase, u32 voff) {
#pragma unroll 1
  for (int it = 0; it < SPIN_CAP && !all_lanes(f == tag); it++) {
    __builtin_amdgcn_s_sleep(1);
    ld4(f, sbase, voff); wait_vm<0>(); tie(f);
  }
}
DEVINL bool arrive_last(char* smem_word, int n) {
  u32 old;
  asm volatile("s_mov_b64 exec, 1\n\tds_add_rtn_u32 %0, %1, %2\n\ts_waitcnt lgkmcnt(0)\n\ts_mov_b64 exec, -1"
               : "=&v"(old) : "v"((u32)(size_t)(LDS_AS char*)smem_word), "v"(1) : "memory");
  return __builtin_amdgcn_readfirstlane(old) == (u32)(n - 1);
}
DEVINL void scale8(u32x4& a, const u32x4& s, const u32x4& t, int on) {
  u32 a0 = a[0], a1 = a[1], a2 = a[2], a3 = a[3], x0, x1, x2, x3;
  asm("s_cmp_eq_u32 %16, 0\n\ts_cbranch_scc1 .Lsk%=\n\tv_dot2_f32_bf16 %4, %0, %8, 0\n\tv_dot2_f32_bf16 %0, %0, %9, 0\n\tv_dot2_f32_bf16 %5, %1, %10, 0\n\tv_dot2_f32_bf16 %1, %1, %11, 0\n\t"
      "v_dot2_f32_bf16 %6, %2, %12, 0\n\tv_dot2_f32_bf16 %2, %2, %13, 0\n\tv_dot2_f32_bf16 %7, %3, %14, 0\n\tv_dot2_f32_bf16 %3, %3, %15, 0\n\t"
      "v_cvt_pk_bf16_f32 %0, %4, %0\n\tv_cvt_pk_bf16_f32 %1, %5, %1\n\tv_cvt_pk_bf16_f32 %2, %6, %2\n\ts_nop 0\n\tv_cvt_pk_bf16_f32 %3, %7, %3\n.Lsk%=:"
      : "+v"(a0), "+v"(a1), "+v"(a2), "+v"(a3), "=&v"(x0), "=&v"(x1), "=&v"(x2), "=&v"(x3)
      : "v"(s[0]), "v"(s[1]), "v"(s[2]), "v"(s[3]), "v"(t[0]), "v"(t[1]), "v"(t[2]), "v"(t[3]), "s"(on) : "scc");
  a = u32x4{a0, a1, a2, a3};
}
DEVINL u32x2 tr_read(const char* p) {
  return __builtin_bit_cast(u32x2, __builtin_amdgcn_ds_read_tr16_b64_v4i16((LDS_AS s16x4*)p));
}
DEVINL float rdl(float v, int l) { return i2f(__builtin_amdgcn_readlane(f2i(v), l)); }
DEVINL float sel2(int c, float a, float b) { return c ? b : a; }
DEVINL u32 swap2(u32 v) { return (u32)__builtin_amdgcn_update_dpp(0, (int)v, 0x4E, 0xf, 0xf, true); }
DEVINL u32 swap1(u32 v) { return (u32)__builtin_amdgcn_update_dpp(0, (int)v, 0xB1, 0xf, 0xf, true); }
constexpr float LOG2E = 1.4426950408889634f, LN2 = 0.6931471805599453f, INV2PI = 0.15915494309189535f;

constexpr int A_TAB = 0, A_P = 8 * 1536, A_KC = A_P + 24576, A_KCS = 4112, A_GV = A_KC + 2 * A_KCS;
constexpr int B_Y1 = 0, B_RED = 40960, B_KC = 49152, B_KCS = 4160, B_KCH = 4 * 4160, B_U = B_KC + 2 * B_KCH, B_US = 5152, B_UCH = 2 * B_US;
constexpr int B_Y2 = B_U + 2 * B_UCH, A_XT = B_Y2;
constexpr int C_RAW = 148480, L_CNT = C_RAW + 1024, D_RES = L_CNT + 64, D_GB = D_RES + 8192, B_SC = D_GB + 256, LDS_BYTES = B_SC + 4096;
static_assert(A_GV + 2048 <= B_KC && B_Y2 + 40960 <= C_RAW && 131072 <= C_RAW && LDS_BYTES <= 163840, "lds map");

DEVINL void glds4k(const void* sbase, u32 voff, u32 ldsaddr) {
  asm volatile("s_mov_b32 m0, %2\n\ts_nop 0\n\t"
               "global_load_lds_dwordx4 %0, %1 sc1\n\tglobal_load_lds_dwordx4 %0, %1 offset:1024 sc1\n\t"
               "global_load_lds_dwordx4 %0, %1 offset:2048 sc1\n\tglobal_load_lds_dwordx4 %0, %1 offset:3072 sc1"
               ::"v"(voff), "s"(sbase), "s"(ldsaddr) : "memory");
}
DEVINL u32x4 tr8(const char* p) {
  const u32x2 lo = tr_read(p), hi = tr_read(p + 256);
  return u32x4{lo[0], lo[1], hi[0], hi[1]};
}
template <int OFF> DEVINL void glds1k(const void* sbase, u32 voff, u32 ldsaddr) {
  asm volatile("s_mov_b32 m0, %2\n\ts_nop 0\n\tglobal_load_lds_dwordx4 %0, %1 offset:%3 sc1" ::"v"(voff), "s"(sbase), "s"(ldsaddr), "n"(OFF) : "memory");
}
DEVINL const char* far(const char* p) {
  u32 a = (u32)(size_t)(LDS_AS const char*)p;
  asm("" : "+v"(a));
  return (const char*)(LDS_AS const char*)(size_t)a;
}
struct Ep { float q0, v; };
template <int N> struct IC { static constexpr int v = N; };
#define accg(a, i) a[((i) & 3) + 8 * ((i) >> 2)]
#define accu(a, i) a[((i) & 3) + 8 * ((i) >> 2) + 4]
DEVINL float swi(const f32x16& a, int i, const Ep& q) {
  const float e = __builtin_amdgcn_exp2f(accg(a, i) * q.q0);
  return (accg(a, i) * accu(a, i)) * __builtin_amdgcn_rcpf(fmaf(e, q.v, q.v));
}
DEVINL u32 ln2_fold(f32x2 s, int h, Ep& q) {
  const float m = s[0] * (1.f / D);
  const float v = fmaxf(fmaf(-m, m, s[1] * (1.f / D)), 0.f) + 1e-5f;
  const float rs = __builtin_amdgcn_rsqf(v);
  q.q0 = rs * -LOG2E; q.v = v;
  const float val = h ? v * rs : -m;
  const float vhi = rbf(val);
  return pk(vhi, val - vhi);
}
DEVINL void ln2_tile(const char* rb, f32x2* raw, f32x2* dummy, int lane) {
  const u32x4 ones = {0x3f803f80u, 0x3f803f80u, 0x3f803f80u, 0x3f803f80u};
  f32x4 sg[2], sx[2];
#pragma unroll
  for (int i = 0; i < 2; i++) { sg[i] = f32x4{0.f, 0.f, 0.f, 0.f}; sx[i] = f32x4{0.f, 0.f, 0.f, 0.f}; }
  u32x4 fr[4][2];
  auto rd = [&](int n) NOPK {
#pragma unroll
    for (int gi = 0; gi < 2; gi++) {
      const char* pp = rb + n * 2048 + gi * 32;
      const u32x2 lo = tr_read(pp), hi = tr_read(pp + 1024);
      fr[n & 3][gi] = u32x4{lo[0], lo[1], hi[0], hi[1]};
    }
  };
  rd(0); rd(1); rd(2);
#pragma unroll
  for (int n = 0; n < 16; n++) {
    if (n + 3 < 16) rd(n + 3);
    __builtin_amdgcn_sched_barrier(0);
#pragma unroll
    for (int gi = 0; gi < 2; gi++) {
      sg[gi] = mfma16(fr[n & 3][gi], fr[n & 3][gi], sg[gi]);
      sx[gi] = mfma16(ones, fr[n & 3][gi], sx[gi]);
    }
    __builtin_amdgcn_sched_barrier(0);
  }
#pragma unroll
  for (int gi = 0; gi < 2; gi++) {
    const float gsel = (lane & 2) ? ((lane & 1) ? sg[gi][3] : sg[gi][2]) : ((lane & 1) ? sg[gi][1] : sg[gi][0]);
    f32x2* dst = (lane >> 4) == ((lane & 15) >> 2) ? raw + gi * 16 + (lane & 15) : dummy;
    *dst = f32x2{sx[gi][0], gsel};
  }
}

__global__ NOPK __launch_bounds__(512, 2) void fused(
    const u16* a0, const u16* a1, const u16* a2, const u16* a3, const u16* a4, const u16* a5, const u16* a6, const u16* a7,
    const u16* a8, const u16* a9, const u16* a10, const u16* a11, const u16* a12, const u16* a13, char* a14, u16* a15) {
  typedef __attribute__((ext_vector_type(8))) unsigned long long u64x8;
  unsigned long long t00_;
  u64x8 ka, kb;
  asm volatile("s_memrealtime %0\n\ts_waitcnt lgkmcnt(0)" : "=s"(t00_) :: "memory");
  asm volatile("s_load_dwordx16 %0, %2, 0\n\ts_load_dwordx16 %1, %2, 64\n\ts_waitcnt lgkmcnt(0)" : "=&s"(ka), "=&s"(kb) : "s"(__builtin_amdgcn_kernarg_segment_ptr()) : "memory");

  const u16 *x = (const u16*)ka[0], *Are = (const u16*)ka[1], *Aim = (const u16*)ka[2], *Bre = (const u16*)ka[3], *Bim = (const u16*)ka[4], *Cre = (const u16*)ka[5],
            *Cim = (const u16*)ka[6], *logdt = (const u16*)ka[7], *dskip = (const u16*)kb[0], *w1 = (const u16*)kb[1], *w3 = (const u16*)kb[2], *w2 = (const u16*)kb[3],
            *cvec = (const u16*)kb[4], *wmod = (const u16*)kb[5];
  char* ws = (char*)kb[6];
  u16* out = (u16*)kb[7];

  __shared__ __attribute__((aligned(16))) char smem[LDS_BYTES];
  const int b = blockIdx.x, tid = threadIdx.x, lane = tid & 63;
  const int w = __builtin_amdgcn_readfirstlane(tid >> 6);
  TSDECL; TS(0);
  const int xg = b & 7, idx = b >> 3;
  const int p = xg * 32 + idx;
  const int r = lane & 15, g = lane >> 4;
  asm volatile("" ::"s"(x), "s"(Are), "s"(Aim), "s"(Bre), "s"(Bim), "s"(Cre), "s"(Cim), "s"(logdt), "s"(dskip), "s"(w1), "s"(w3),
               "s"(w2), "s"(cvec), "s"(wmod), "s"(ws), "s"(out));
  u32x4 xr, wm0, wm1;
  u32 vare, vaim, vbre, vbim, vcre, vcim, c0, c1;
  const int cg = xg * 24 + (idx < 24 ? idx : 23);
  u32 e_prev, ldt2, dsk;
  lp16(xr, x + b * 4096, tid * 16);
  sld4(e_prev, ws + WS_E); sld4(ldt2, logdt + 2 * p); sld4(dsk, dskip + 2 * p);
  {
    const u32 so = (p * 256 + w * 32 + (lane & 31)) * 2;
    lp2(vare, Are, so); lp2(vaim, Aim, so); lp2(vbre, Bre, so); lp2(vbim, Bim, so); lp2(vcre, Cre, so); lp2(vcim, Cim, so);
    const u32 mo = (tid >> 3) * 6144 + (tid & 7) * 16;
    const u16* wmp = wmod + (cg & 3) * 128 * 3072 + (cg >> 2) * 64;
    lp16(wm0, wmp, mo); lp16(wm1, wmp + 64 * 3072, mo);
    lp2(c0, cvec + (cg & 3) * 128, (tid >> 3) * 2); lp2<128>(c1, cvec + (cg & 3) * 128, (tid >> 3) * 2);
  }
  u32 pf;
  {
    unsigned long long pc;
    u32 po = ((idx & PFM) * 512 + tid) * (PFB / (PFM + 1) / 512);
    asm volatile("s_getpc_b64 %1\n.Lpf0:\n\tv_min_u32 %2, .Lpf1-.Lpf0, %2\n\tglobal_load_dword %0, %2, %1"
                 : "=v"(pf), "=&s"(pc), "+v"(po)::"memory");
  }
  TS(1);
  if (tid < 32) *(u32x4*)(smem + B_KC + (tid >> 2) * B_KCS + 4096 + (tid & 3) * 16) = u32x4{0, 0, 0, 0};
  else if (tid < 36) *(u32x4*)(smem + L_CNT + (tid - 32) * 16) = u32x4{0, 0, 0, 0};
  else if (tid < 38) *(u32x2*)(smem + B_U + (tid & 1) * B_UCH + B_US + 1024) = u32x2{0, 0};
  else if (tid >= 256) *(u32x4*)(smem + B_U + (w - 4) * B_US + lane * 16) = u32x4{0, 0, 0, 0};
  asm volatile("s_waitcnt lgkmcnt(0)" : "+s"(e_prev), "+s"(ldt2), "+s"(dsk)::"memory");
  const u32 tag = e_prev * 1664525u + 1013904223u;
  wait_vm<11>(); tie(xr);
  {
    *(u32x4*)(smem + A_XT + w * 1056 + lane * 16) = xr;
    float f[8];
#pragma unroll
    for (int j = 0; j < 4; j++) { f[2 * j] = bflo(xr[j]); f[2 * j + 1] = bfhi(xr[j]); }
    const float m = wave_sum(((f[0] + f[1]) + (f[2] + f[3])) + ((f[4] + f[5]) + (f[6] + f[7]))) * (1.f / D);
    float s = 0.f;
#pragma unroll
    for (int j = 0; j < 8; j++) { f[j] -= m; s = fmaf(f[j], f[j], s); }
    const float sd = __builtin_amdgcn_sqrtf(rbf(rbf(wave_sum(s) * (1.f / D)) + 1e-5f));
    if (lane == 0) *(u32*)(smem + A_XT + w * 1056 + 1024) = pk(m, sd);
  }
  TS(2);
  wait_vm<5>(); tie(vare); tie(vaim); tie(vbre); tie(vbim); tie(vcre); tie(vcim);
  char* tab = smem + A_TAB + w * 1536;
  {
    const float dt = rbf(__builtin_amdgcn_exp2f((w >> 2 ? bfhi(ldt2) : bflo(ldt2)) * LOG2E));
    const float a = dt * bflo(vare) * LOG2E, bq = dt * bflo(vaim) * INV2PI;
    const float br = dt * bflo(vbre), bi = dt * bflo(vbim), cr = bflo(vcre), ci = bflo(vcim);
    const float m = lane < 32 ? 1.f : 256.f;
    const float e = __builtin_amdgcn_exp2f(a * m), ph = __builtin_amdgcn_fractf(-bq * m);
    const float ex = e * __builtin_amdgcn_cosf(ph), ey = e * __builtin_amdgcn_sinf(ph);
    char* t = tab + (lane & 31) * 48;
    *(f32x2*)(t + 16 + (lane >> 5) * 8) = f32x2{ex, ey};
    f32x4 o = {a, bq, cr * br - ci * bi, cr * bi + ci * br};
    if (lane >= 32) o = f32x4{ex, ey, ex * ex - ey * ey, (ex + ex) * ey};
    *(f32x4*)(t + (lane >> 5) * 32) = o;
  }
  TS(3);
  lds_barrier();
  {
    const char* xt = smem + A_XT + (lane & 7) * 1056;
    const u32 st = *(const u32*)(xt + 1024);
#pragma unroll
    for (int e = 0; e < 2; e++) {
      const u32x2 xq = *(const u32x2*)(xt + (e * 64 + w * 8 + (lane >> 3)) * 8);
      st16(ws + WS_XG + (size_t)(e * 64 + w * 8) * 32768 + b * 128, (lane >> 3) * 32768 + (lane & 7) * 16, u32x4{xq[0], xq[1], st, tag});
    }
  }
  TS(41);
  const int fb = (xg >> 1) * 4 + (idx & 3), mat = (idx >> 2) & 1, dq = (xg & 1) * 4 + (idx >> 3), d0 = dq * 64 + w * 8;
  const int b2 = b < 224 ? b : 223;
  u32 v[8], t2[8];
  {
    const u16* wp = (mat ? w3 : w1) + (size_t)d0 * F + fb * 112;
    const u32 vo = (lane < 56 ? lane : 55) * 4;
#pragma unroll
    for (int i = 0; i < 4; i++) { lp4<0>(v[2 * i], wp + 2 * i * F, vo); lp4<2 * F>(v[2 * i + 1], wp + 2 * i * F, vo); }
#pragma unroll
    for (int i = 0; i < 2; i++) {
      const u16* wq = w2 + (size_t)(b2 * 8 + i * 4) * D;
      const u32 to = tid * 2;
      lp2<0>(t2[4 * i], wq, to); lp2<1024>(t2[4 * i + 1], wq, to); lp2<2048>(t2[4 * i + 2], wq, to); lp2<3072>(t2[4 * i + 3], wq, to);
    }
  }
  auto retile = [&]() NOPK {
    wait_vm<0>();
#pragma unroll
    for (int i = 0; i < 8; i++) { tie(v[i]); tie(t2[i]); }
    u32x4 o0, o1, o2;
#pragma unroll
    for (int j = 0; j < 4; j++) {
      o0[j] = __builtin_amdgcn_perm(v[2 * j + 1], v[2 * j], 0x05040100u);
      o1[j] = __builtin_amdgcn_perm(v[2 * j + 1], v[2 * j], 0x07060302u);
      o2[j] = __builtin_amdgcn_perm(t2[2 * j + 1], t2[2 * j], 0x05040100u);
    }
    st16x2_56(ws + WS_W1F + (size_t)(fb * 7 * 32 + (d0 >> 4)) * 1024 + (w & 1) * 512 + mat * 128, (lane >> 3) * 32768 + (lane & 7) * 16, o0, o1);
    st16(ws + WS_W2F + w * 229376 + (b2 >> 2) * 4096 + (b2 & 3) * 256, g * 1024 + r * 16, o2);
  };
  auto gemv = [&]() NOPK
  {
    wait_vm<18>(); tie(pf); tie(wm0); tie(wm1); tie(c0); tie(c1);
    const float cx0 = bflo(c0), cx1 = bflo(c1);
    const float s0 = rbf(cx0 * __builtin_amdgcn_rcpf(1.f + __builtin_amdgcn_exp2f(cx0 * -LOG2E)));
    const float s1 = rbf(cx1 * __builtin_amdgcn_rcpf(1.f + __builtin_amdgcn_exp2f(cx1 * -LOG2E)));
    float acc[8];
#pragma unroll
    for (int j = 0; j < 4; j++) {
      acc[2 * j] = fmaf(s0, bflo(wm0[j]), s1 * bflo(wm1[j]));
      acc[2 * j + 1] = fmaf(s0, bfhi(wm0[j]), s1 * bfhi(wm1[j]));
    }
    const float q0 = dpp_add<0x128>(rs16(rs32(acc[0], acc[4]), rs32(acc[2], acc[6])));
    const float q1 = dpp_add<0x128>(rs16(rs32(acc[1], acc[5]), rs32(acc[3], acc[7])));
    *(float*)(smem + A_GV + w * 256 + (lane & 7) * 32 + (lane >> 3) * 4) = (lane & 8) ? q1 : q0;
    if (arrive_last(smem + L_CNT, 8) && idx < 24) {
      float s = 0.f;
#pragma unroll
      for (int i = 0; i < 8; i++) s += *(const float*)(smem + A_GV + i * 256 + lane * 4);
      st8(ws + WS_MG + (cg & 3) * 24576 + (cg >> 2) * 512, lane * 8, u32x2{f2u(s), tag});
    }
  };
  TS(4);
  if (GP == 0) { gemv(); TS(44); }
  const u32 gcode = __builtin_amdgcn_perm(0x400b0916u, 0x0a201408u, (r & 7) | 0x0c0c0c00u);
  const int gx = r < 8 ? 2 * r : (int)((gcode >> 2) * 256 + (gcode & 3) * 4);
  {
    asm volatile("" ::: "memory");
    const float mF = (float)(16 * r), mX = (float)gx;
    const char* tp = tab + g * 8 * 48;
    const char* tq = tp + (r < 8 ? 16 : 32);
    f32x4 acc[3];
#pragma unroll
    for (int s = 0; s < 3; s++) acc[s] = f32x4{0.f, 0.f, 0.f, 0.f};
#pragma unroll
    for (int ks = 0; ks < 2; ks++) {
      u32x4 Ff, Gf[3];
#pragma unroll
      for (int i = 0; i < 4; i++) {
        const f32x4 s0 = *(const f32x4*)(tp + (ks * 4 + i) * 48), s1 = *(const f32x4*)(tq + (ks * 4 + i) * 48);
        const float e0 = __builtin_amdgcn_exp2f(s0[0] * mF), p0 = __builtin_amdgcn_fractf(s0[1] * mF);
        const float zx = e0 * __builtin_amdgcn_cosf(p0), zy = e0 * __builtin_amdgcn_sinf(p0);
        Ff[i] = pk(s0[2] * zx - s0[3] * zy, s0[2] * zy + s0[3] * zx);
        const float e1 = __builtin_amdgcn_exp2f(s0[0] * mX), p1 = __builtin_amdgcn_fractf(s0[1] * -mX);
        const float xx = e1 * __builtin_amdgcn_cosf(p1), xy = e1 * __builtin_amdgcn_sinf(p1);
        Gf[0][i] = pk(xx, xy);
        Gf[1][i] = pk(xx * s1[0] - xy * s1[1], xx * s1[1] + xy * s1[0]);
        Gf[2][i] = pk(xx * s1[2] - xy * s1[3], xx * s1[3] + xy * s1[2]);
      }
#pragma unroll
      for (int s = 0; s < 3; s++) acc[s] = mfma16(Ff, Gf[s], acc[s]);
      if (ks == 0) { TS(45); if (RP == 0) retile(); if (GP == 1) { gemv(); TS(44); } }
    }
#pragma unroll
    for (int s = 0; s < 3; s++) *(f32x4*)(smem + A_P + w * 3072 + s * 1024 + lane * 16) = acc[s];
  }
  if (RP == 1) retile();
  TS(5);
  u32x4 xg4[4];
  u32x2 mg, mh, mc[4];
  const char* xgb = ws + WS_XG + (size_t)(p >> 1) * 32768;
  const u32 xgo = (tid >> 2) * 256 + (tid & 3) * 16;
  u32 mga = (4 - ((lane >> 3) & 1)) * 512 + d0 + (lane & 7), mgb = ((0x5210 >> ((lane & 6) * 2)) & 7) * 512 + 2 * p + (lane & 1);
  tie(mga); tie(mgb);
  const u32 mgo = ((lane & 16) ? mgb : mga) * 8 + (lane >> 5) * 24576;
  auto lookx = [&]() NOPK {
    ld16<0>(xg4[0], xgb, xgo); ld16<64>(xg4[1], xgb, xgo); ld16<128>(xg4[2], xgb, xgo); ld16<192>(xg4[3], xgb, xgo);
  };
  auto lookm = [&]() NOPK {
    ld8(mg, ws + WS_MG, mgo); ld8(mh, ws + WS_MG + 49152, mgo);
  };
  auto lookc = [&]() NOPK {
#pragma unroll
    for (int q = 0; q < 4; q++) ld8(mc[q], ws + WS_MG + q * 24576 + 16384, tid * 8);
  };
  lookx();
  lds_barrier();
  TS(6);
  TSF(0, 6); TSF(41, 41); TSF(44, 45);
  const int kch = tid >> 8;
  u16* Kc = (u16*)(smem + A_KC + kch * A_KCS);
  if ((w & 3) < 3) {
    const int s = w & 3;
    const char* pp = smem + A_P + kch * 12288 + s * 1024 + lane * 16;
    const f32x4 q = (*(const f32x4*)(pp) + *(const f32x4*)(pp + 3072)) + (*(const f32x4*)(pp + 6144) + *(const f32x4*)(pp + 9216));
    const int pos = gx + 64 * g + (r < 8 ? (s == 0 ? 0 : s == 1 ? 1 : 256) : s * 256);
    const u32 o0 = pk(q[0], q[1]), o1 = pk(q[2], q[3]);
    if (pos <= 2048) Kc[pos] = (u16)o0;
    if (pos < 2048) {
      Kc[pos + 16] = (u16)(o0 >> 16);
      Kc[pos + 32] = (u16)o1;
      Kc[pos + 48] = (u16)(o1 >> 16);
    }
  }
  wait_vm<4>();
  lds_barrier();
  TS(7);
  if (w == 7) st4one(ws + WS_FW + (fb * 16 + mat * 8 + dq) * 128, tag);
  const int tt = (xg & 1) * 8 + (idx & 7), fs = (xg >> 1) * 4 + (idx >> 3);
  const u32 fwo = fs * 2048 + (lane & 15) * 128;
  u32 fw;
  lookm(); lookc();
  {
    u32 e[6];
    const int ki = tid & 255;
#pragma unroll
    for (int j = 0; j < 6; j++) {
      const float l0 = (float)(2047 - 8 * ki - 2 * j);
      const u32 bits = pk(fmaxf(l0, 0.f), fmaxf(l0 - 1.f, 0.f));
      e[j] = (u32)Kc[(int)bflo(bits)];
      if (j < 5) e[j] |= (u32)Kc[(int)bfhi(bits)] << 16;
    }
    if (ki == 255) { e[4] = 0; e[5] = 0; }
    const u32 a0 = __builtin_amdgcn_alignbit(e[1], e[0], 16), a1 = __builtin_amdgcn_alignbit(e[2], e[1], 16);
    const u32 a2 = __builtin_amdgcn_alignbit(e[3], e[2], 16), a3 = __builtin_amdgcn_alignbit(e[4], e[3], 16);
    const u32 a4 = __builtin_amdgcn_alignbit(e[5], e[4], 16);
    char* kc = smem + B_KC + kch * B_KCH + ki * 16;
    *(u32x4*)(kc) = u32x4{e[0], e[1], e[2], e[3]};
    *(u32x4*)(kc + B_KCS) = u32x4{a0, a1, a2, a3};
    *(u32x4*)(kc + 2 * B_KCS) = u32x4{e[1], e[2], e[3], e[4]};
    *(u32x4*)(kc + 3 * B_KCS) = u32x4{a1, a2, a3, a4};
  }
  TS(8);
  wait_vm<0>(); tie(xg4[0]); tie(xg4[1]); tie(xg4[2]); tie(xg4[3]); tie(mg); tie(mh); tie(mc[0]); tie(mc[1]); tie(mc[2]); tie(mc[3]);
  TS(47);
#pragma unroll 1
  for (int it = 0; it < SPIN_CAP && !all_lanes(xg4[0][3] == tag && xg4[1][3] == tag && xg4[2][3] == tag && xg4[3][3] == tag); it++) {
    lookx();
    wait_vm<0>(); tie(xg4[0]); tie(xg4[1]); tie(xg4[2]); tie(xg4[3]);
  }
#pragma unroll 1
  for (int it = 0; it < SPIN_CAP && !all_lanes(mg[1] == tag && mh[1] == tag); it++) {
    lookm();
    wait_vm<0>(); tie(mg); tie(mh);
  }
  TS(9);
  const int h = lane >> 5, c = lane & 31;
  const int tt2 = (~xg & 1) * 8 + (xg & 6) + (idx >> 4), sel = (idx >> 3) & 1, dt = idx & 7;
  const int rot = (2 * (idx >> 3) + idx) & 7;
  const int ft = fs * 7 + w - 1;
  u32x4 A[32];
  const u32 ao = lane * 16;
  auto lda = [&](int i) NOPK {
    const char* wq = ws + WS_W1F + (size_t)ft * 32768 + (((i >> 2) + rot) & 7) * 4096;
    asm volatile("s_cmp_eq_u32 %4, 0\n\ts_cbranch_scc1 .Lna%=\n\tglobal_load_dwordx4 %0, %1, %2 offset:%3 sc1\n.Lna%=:"
                 : "=v"(A[i]) : "v"(ao), "s"(wq), "n"((i & 3) * 1024), "s"(w) : "memory", "scc");
  };
  ld4(fw, ws + WS_FW, fwo);
  const int g8 = tid >> 1, cc = tid & 1;
  const float mq = u2f(mg[0]) + u2f(mh[0]);
  const float mv = rbf(rs32(mq, mq)), mv1 = rbf(1.f + mv);
  const float ga = sel2(cc, rdl(mv, 20), rdl(mv, 21));
  u32x4 hp, xv;
  {
    const float sh0 = rdl(mv, 16), sh1 = rdl(mv, 17), sc0 = rdl(mv1, 18), sc1 = rdl(mv1, 19);
    const u32 psel = cc ? 0x03020706u : 0x05040100u;
    const bool up = tid & 2;
    u32 ph[4], px[4];
#pragma unroll
    for (int i = 0; i < 4; i++) {
      const u32 xq = (p & 1) ? xg4[i][1] : xg4[i][0];
      const float m = bflo(xg4[i][2]), rs = __builtin_amdgcn_rcpf(bfhi(xg4[i][2]));
      const u32 n = pk((bflo(xq) - m) * rs, (bfhi(xq) - m) * rs);
      const u32 hq = pk(fmaf(bflo(n), sc0, sh0), fmaf(bfhi(n), sc1, sh1));
      ph[i] = __builtin_amdgcn_perm(swap1(hq), hq, psel);
      px[i] = __builtin_amdgcn_perm(swap1(xq), xq, psel);
    }
#pragma unroll
    for (int j = 0; j < 2; j++) {
      const u32 rh = swap2(up ? ph[j] : ph[2 + j]), kh = up ? ph[2 + j] : ph[j];
      hp[2 * j] = up ? rh : kh; hp[2 * j + 1] = up ? kh : rh;
      const u32 rx = swap2(up ? px[j] : px[2 + j]), kx = up ? px[2 + j] : px[j];
      xv[2 * j] = up ? rx : kx; xv[2 * j + 1] = up ? kx : rx;
    }
    char* u = smem + B_U + cc * B_UCH + 1024 + g8 * 16;
    *(u32x4*)(u) = hp;
    *(u32x2*)(u + B_US + 8) = u32x2{hp[0], hp[1]};
    *(u32x2*)(u + B_US + 16) = u32x2{hp[2], hp[3]};
  }
#pragma unroll 1
  for (int it = 0; it < SPIN_CAP && !all_lanes(mc[0][1] == tag && mc[1][1] == tag && mc[2][1] == tag && mc[3][1] == tag); it++) {
    lookc();
    wait_vm<0>(); tie(mc[0]); tie(mc[1]); tie(mc[2]); tie(mc[3]);
  }
  {
    const float sc = 1.f + rbf((u2f(mc[0][0]) + u2f(mc[2][0])) + (u2f(mc[1][0]) + u2f(mc[3][0])));
    const u32 e = pk(sc, sc) & ((tid & 1) ? 0xffff0000u : 0xffffu);
    *(u32*)(smem + B_SC + tid * 4) = e;
    *(u32*)(smem + B_SC + 2048 + tid * 4) = e;
  }
  TS(10);
  lds_barrier();
  TS(11);
  const char* tb = smem + B_SC + rot * 256 + h * 32;
  u32x4 tq[3][2];
  auto ldt = [&](int i) NOPK { tq[i % 3][0] = *(const u32x4*)(tb + i * 64); tq[i % 3][1] = *(const u32x4*)(tb + i * 64 + 16); };
  auto scl = [&](int i) NOPK { tie(A[i]); scale8(A[i], tq[i % 3][0], tq[i % 3][1], w); tie(A[i]); };
  auto epi = [&](int yb) NOPK {
    const int gl = g8 & 127;
    const float* Y = (const float*)(smem + yb) + cc * 1280 + 8 * gl + 8 * (gl >> 2);
    const f32x4 ya = (*(const f32x4*)Y + *(const f32x4*)(Y + 2560)) + (*(const f32x4*)(Y + 5120) + *(const f32x4*)(Y + 7680));
    const f32x4 yb4 = (*(const f32x4*)(Y + 4) + *(const f32x4*)(Y + 2564)) + (*(const f32x4*)(Y + 5124) + *(const f32x4*)(Y + 7684));
    const float dk = cc ? bfhi(dsk) : bflo(dsk);
    u32x4 o;
#pragma unroll
    for (int j = 0; j < 4; j++) {
      const float y0 = j < 2 ? ya[2 * j] : yb4[2 * j - 4], y1 = j < 2 ? ya[2 * j + 1] : yb4[2 * j - 3];
      o[j] = pk(fmaf(ga, fmaf(bflo(hp[j]), dk, y0), bflo(xv[j])), fmaf(ga, fmaf(bfhi(hp[j]), dk, y1), bfhi(xv[j])));
    }
    st16(ws + WS_X1 + p * 128 + (w >> 1) * 32768, ((g8 >> 2) & 15) * 131072 + cc * 64 + (g8 & 3) * 16, o);
  };
  constexpr int LBT = LBC, NS2 = 51 / SCR + 1;
  {
    const int ch = w & 1, nq = w >> 1;
    const char* pa = smem + B_KC + ch * B_KCH + (3 - (r & 3)) * B_KCS + 2 * (2040 - 8 * (r >> 2) + 8 * g) - 512 * 7 - 64 * (2 * nq + 1);
    const char* pb = smem + B_U + ch * B_UCH + ((r & 1) ? 0 : B_US) + 2 * (512 + 32 * (r >> 1) + 8 * g) - 64 * (2 * nq + 1);
    const int yo = w * 5120 + 160 * (r >> 1) + 16 * (r & 1) + 32 * g;
    f32x4 acc[4];
    u32x4 la[2][4], lb[2][4], hf[4];
    auto ldhi = [&](int q) NOPK {
      const int e = 4 + ((q & 7) >> 1);
      hf[q & 3] = *(const u32x4*)(((q & 1) ? pa + 512 * (7 - e) : pb + 512 * e) + 64 * (1 - (q >> 3)));
    };
#pragma unroll
    for (int vv = 0; vv < 2; vv++)
#pragma unroll
      for (int n = 0; n < 8; n++) {
        const int e = (0x30212310 >> (4 * n)) & 3;
        if ((0xb1 >> n) & 1) la[vv][e] = *(const u32x4*)(pa + 512 * (7 - e) + 64 * (1 - vv));
        else lb[vv][e] = *(const u32x4*)(pb + 512 * e + 64 * (1 - vv));
      }
    __builtin_amdgcn_sched_barrier(0);
    {
      f32x4 sums = {0.f, 0.f, 0.f, 0.f};
#pragma unroll
      for (int j = 0; j < 4; j++) {
        const float k0 = rdl(mv1, 2 * j), k1 = rdl(mv1, 2 * j + 1), s0 = rdl(mv, 8 + 2 * j), s1 = rdl(mv, 8 + 2 * j + 1);
        const float a0 = bflo(v[2 * j]), a1 = bflo(v[2 * j + 1]), b0 = bfhi(v[2 * j]), b1 = bfhi(v[2 * j + 1]);
        sums[0] = dot2(pk(a0 * k0, a1 * k1), 0x3f803f80u, sums[0]);
        sums[1] = dot2(pk(b0 * k0, b1 * k1), 0x3f803f80u, sums[1]);
        sums[2] = fmaf(a0, s0, fmaf(a1, s1, sums[2]));
        sums[3] = fmaf(b0, s0, fmaf(b1, s1, sums[3]));
      }
      *(f32x4*)(smem + B_RED + tid * 16) = sums;
    }
#pragma unroll
    for (int i = 0; i < 4; i++) acc[i] = f32x4{0.f, 0.f, 0.f, 0.f};
    wait_vm<0>(); tie(fw);
    spin4(fw, tag, ws + WS_FW, fwo);
    TS(46);
    __builtin_amdgcn_sched_barrier(0);
#pragma unroll
    for (int n = 0; n < 20; n++) {
      const int vv = n / 10, k = n % 10, e = (0x0231211000ll >> (4 * k)) & 3, u = (0x0000112231ll >> (4 * k)) & 3;
      acc[e + u] = mfma16(la[vv][e], lb[vv][u], acc[e + u]);
      if (n % 3 == 0 && n / 3 < LBC) lda(n / 3);
      if (n >= 12 && n < 15) ldhi(n - 12);
      __builtin_amdgcn_sched_barrier(0);
    }
    TS(12);
#pragma unroll
    for (int i = 0; i < 4; i++) *(f32x4*)(smem + B_Y1 + yo + 1280 * i) = acc[i];
    lds_barrier();
    TS(13);
    TSF(7, 13); TSF(46, 47);
    int w_ = w;
    asm volatile("" : "+s"(w_));
    if (w_ < 4) {
      __builtin_amdgcn_s_setprio(3);
      epi(B_Y1);
      __builtin_amdgcn_s_setprio(0);
    }
    asm volatile("" : "+s"(w_));
    if (w_ == 0) {
      f32x4 s = {0.f, 0.f, 0.f, 0.f};
#pragma unroll
      for (int q = 0; q < 8; q++) s += *(const f32x4*)(smem + B_RED + q * 1024 + lane * 16);
      st16(ws + WS_CD + ((dq * 16 + fb) * 2 + mat) * 1024, lane * 16, __builtin_bit_cast(u32x4, s));
      st8m<1>(ws + WS_GB + p * 8, 0, u32x2{f2u(rdl(mv, 22)), f2u(rdl(mv, 23))});
    }
    TS(14);
    ldt(0);
#pragma unroll
    for (int n = 0; n < 52; n++) {
      const int vv = n / 26, k = n % 26;
      const int e = (k < 13 ? 0x2210144443210ll >> (4 * k) : 0x7036631023555ll >> (4 * (k - 13))) & 7;
      const int u = (k < 13 ? 0x2555332104444ll >> (4 * k) : 0x0731026631210ll >> (4 * (k - 13))) & 7;
      const int q = vv * 8 + (e > 3 ? 2 * e - 7 : 2 * u - 8);
      if (n == NF1) {
        asm volatile("" : "+s"(w_));
        if (w_ < 4) {
          wait_vmn((n + SCR - 1) / SCR);
          if (w_ == 0) wait_vm<0>();
          if (arrive_last(smem + L_CNT + 8, 4)) st4one(ws + WS_FX + (b & 63) * 128 + (b >> 6) * 4, tag);
        }
        TS(15);
      }
      if (k == 0 || k == 4 || k == 8 || k == 12 || k == 16 || k == 20 || k == 23 || k == 25) {
        const int qn = vv * 8 + (k < 23 ? k / 4 : k == 23 ? 6 : 7) + 3;
        if (qn < 16) ldhi(qn);
      }
      if (n % SCR == 0) ldt(n / SCR + 1);
      __builtin_amdgcn_sched_barrier(0);
      acc[e + u - 4] = mfma16(e > 3 ? hf[q & 3] : la[vv][e], u > 3 ? hf[q & 3] : lb[vv][u], n < 4 ? f32x4{0.f, 0.f, 0.f, 0.f} : acc[e + u - 4]);
      if (n % SCR == 0) { wait_vm<LBT - 1>(); lda(LBT + n / SCR); scl(n / SCR); }
      __builtin_amdgcn_sched_barrier(0);
    }
    TS(16);
#pragma unroll
    for (int i = 0; i < 4; i++) *(f32x4*)(smem + B_Y2 + yo + 1280 * i) = acc[i];
    ldt(NS2 + 1);
    lds_barrier();
    TS(42);
    asm volatile("" : "+s"(w_));
    if (w_ >= 4) {
      __builtin_amdgcn_s_setprio(3);
      epi(B_Y2);
      __builtin_amdgcn_s_setprio(0);
    }
    TS(43);
    asm volatile("" : "+s"(w_));
    if (w_ != 0) {
#pragma unroll
      for (int i = NS2; i < 32; i++) {
        if (i + 2 < 32) ldt(i + 2);
        wait_vmn(i + LBT < 32 ? LBT - 1 : 31 - i);
        if (i + LBT < 32) lda(i + LBT);
        __builtin_amdgcn_sched_barrier(0);
        scl(i);
        __builtin_amdgcn_sched_barrier(0);
      }
    }
    wait_vm<0>();
  }
  f32x2* raw = (f32x2*)(smem + C_RAW);
  u32x4 W2x[12];
  u32 fh;
  const u32 lds0 = (u32)(size_t)(LDS_AS char*)smem;
  const char* xa = ws + WS_X1 + tt * 131072 + ((w + rot) & 7) * 4096;
  const u32 lpc = lds0 + w * 4096;
  const int rt2 = dt + 2 * (idx >> 3), rot2 = rt2 < 7 ? rt2 : rt2 - 7;
#define kof(j) ((j) + rot2 < 7 ? (j) + rot2 : (j) + rot2 - 7)
  const char* w2b = ws + WS_W2F + dt * 229376 + w * 28672;
  const char* fhb = ws + WS_FH + tt2 * 2048 + w * 256;
  const u32 fho = ((lane >> 3) & 1) * 128 + g * 32 + (lane & 7) * 4;
  const char* fpb = ws + WS_FH + (tt * 16 + fs) * 128 + w * 4;
  auto ldw2 = [&](int j) NOPK {
    const char* q = w2b + kof(j >> 2) * 4096;
    if (j < 12) ld16k(W2x[j], q, j & 3, ao); else ld16k(A[j - 12], q, j & 3, ao);
  };
  auto poll_x = [&](int part) NOPK {
#pragma unroll 1
    for (int it = 0; it < SPIN_CAP; it++) {
      u32x4 f;
      ld16(f, ws + WS_FX + part * 8192, lane * 128); wait_vm<0>(); tie(f);
      if (all_lanes(f[0] == tag && f[1] == tag && f[2] == tag && f[3] == tag)) break;
    }
  };
  if (w == 0) {
    __builtin_amdgcn_s_setprio(3);
    poll_x(0);
    TS(17);
#pragma unroll
    for (int q = 0; q < 8; q++) glds4k(ws + WS_X1 + tt * 131072 + ((q + rot) & 7) * 4096, ao, lds0 + q * 4096);
    const char* rb = smem + (lane >> 2) * 64 + (lane & 3) * 8;
    wait_vm<0>();
    TS(18);
    bar();
    TS(19); TC(20);
    TSF(14, 20); TSF(42, 43);
    st4one(ws + WS_FX + 8192 + (b & 63) * 128 + (b >> 6) * 4, tag);
    glds4k(xa + 32768, ao, lpc + 32768);
    if (b == 0) st4one(ws + WS_E, tag);
    st4one(fpb, tag); st4one(fpb + 32, tag);
#pragma unroll
    for (int i = 0; i < 14; i++) ldw2(i);
    ln2_tile(rb, raw, raw + 132, lane);
    TS(21);
    poll_x(1);
    lds_barrier();
    TS(22);
    glds4k(xa + 65536, ao, lpc + 65536); glds4k(xa + 98304, ao, lpc + 98304);
#pragma unroll
    for (int q = 0; q < 4; q++) {
      const char* xr_ = ws + WS_X1 + (tt2 * 4 + q) * 32768 + dt * 4096 + sel * 32;
      const u32 xo = r * 256 + h * 64 + (g & 1) * 16, la = lds0 + D_RES + q * 2048;
      glds1k<0>(xr_, xo, la); glds1k<0>(xr_ + 128, xo, la + 1024);
    }
    asm volatile("s_mov_b32 m0, %2\n\ts_nop 0\n\tglobal_load_lds_dword %0, %1 sc1"
                 ::"v"(lane * 4), "s"(ws + WS_GB + dt * 256), "s"(lds0 + D_GB) : "memory");
    ln2_tile(rb + 32768, raw + 32, raw + 132, lane);
    TS(23);
    wait_vm<0>(); lds_barrier();
    TS(24);
#pragma unroll
    for (int i = 14; i < 28; i++) ldw2(i);
    rb = far(rb + 65536);
    ln2_tile(rb, raw + 64, raw + 132, lane);
    asm volatile("" ::: "memory");
    *(volatile LDS_AS u32*)(smem + L_CNT + 16) = 1;
    TS(25);
    ln2_tile(rb + 32768, raw + 96, raw + 132, lane);
    asm volatile("" ::: "memory");
    *(volatile LDS_AS u32*)(smem + L_CNT + 16) = 2;
    __builtin_amdgcn_s_setprio(0);
    TS(26); TS(27); TC(28);
    TSF(21, 28);
    ld4(fh, fhb, fho);
    wait_vm<0>(); tie(fh);
#pragma unroll
    for (int i = 0; i < 28; i++) { if (i < 12) tie(W2x[i]); else tie(A[i - 12]); }
    TS(29);
  }
  int w_ = w;
  asm volatile("" : "+s"(w_));
  if (w_ != 0) {
    const char* rb = smem + h * 512 + ((lane >> 2) & 3) * 64 + ((lane >> 4) & 1) * 32 + (lane & 3) * 8;
    const char* rb2 = far(rb + 65536);
    u32 cdv[8];
    f32x16 acc[2], zero;
#pragma unroll
    for (int i = 0; i < 16; i++) zero[i] = 0.f;
    TS(17);
    lds_barrier();
    TS(18);
    TS(19); TC(20);
    TSF(14, 20); TSF(42, 43);
    __builtin_amdgcn_s_setprio(3);
    u32x4 Bq[3], Ax, op;
    Bq[0] = tr8(rb); Bq[1] = tr8(rb + 1024);
    f32x2 rw;
    Ep q[2];
    float ev, gu, oa;
    const char* hb = ws + WS_HF + (size_t)(tt * 8 * 56 + (ft >> 1)) * 1024 + (ft & 1) * 512;
    const u32 ho = (c >> 4) * 57344 + h * 256 + r * 16;
    auto stat_wait = [&](u32 need) NOPK {
#pragma unroll 1
      for (int it = 0; it < SPIN_CAP * 16; it++)
        if (__builtin_amdgcn_readfirstlane(*(volatile LDS_AS u32*)(smem + L_CNT + 16)) >= need) break;
      asm volatile("" ::: "memory");
    };
    auto tile = [&](auto tc) NOPK {
    constexpr int T = decltype(tc)::v, E = T - 1;
#pragma unroll
    for (int ks = 0; ks < 32; ks++) {
      const int n = T * 32 + ks, m = n + 2;
      if (n == 30) {
        TS(21);
        wait_vm<8>();
#pragma unroll
        for (int i = 0; i < 8; i++) tie(cdv[i]);
        lds_barrier();
        TS(22);
      } else if (n == 62) {
        TS(23);
        wait_vm<5>();
        lds_barrier();
        TS(24);
      }
      if (n == 30 || n == 62) __builtin_amdgcn_s_setprio(3);
      if (n == 14 || n == 46 || n == 100) __builtin_amdgcn_s_setprio(2);
      if (n == 22 || n == 54 || n == 118) __builtin_amdgcn_s_setprio(1);
      if (n == 27 || n == 59) __builtin_amdgcn_s_setprio(0);
      if (n == 96) { TS(25); tie(acc[0]); stat_wait(1); }
      if (n == 118) { tie(acc[1]); stat_wait(2); }
      if (n == 120) wait_vm<10>();
      if (m < 128) Bq[m % 3] = tr8((m < 64 ? rb : rb2) + (m & 63) * 1024);
      if (T > 0 && ks == 0) rw = raw[E * 32 + c];
      if (n == 118) rw = raw[96 + c];
      if (n >= 120) A[n - 112] = *(const u32x4*)(smem + ((n >> 2) & 1) * 32768 + w * 4096 + (n & 3) * 1024 + lane * 16);
      __builtin_amdgcn_sched_barrier(0);
      acc[T & 1] = mfma32(A[ks], Bq[n % 3], ks == 0 ? zero : acc[T & 1]);
      if (n == 31) {
        const float cd = ((u2f(cdv[0]) + u2f(cdv[1])) + (u2f(cdv[2]) + u2f(cdv[3]))) + ((u2f(cdv[4]) + u2f(cdv[5])) + (u2f(cdv[6]) + u2f(cdv[7])));
        const float chi = rbf(cd);
        Ax = u32x4{pk(chi, chi), pk(cd - chi, cd - chi), 0, 0};
      }
      if ((T > 0 && ks == 2) || n == 120) {
        const int X = (n == 120 ? 3 : E) & 1;
        const u32 be = ln2_fold(rw, h, q[X]);
        acc[X] = mfma32(Ax, u32x4{be, be, 0, 0}, acc[X]);
      }
      if (T > 0 && ks >= 4 && ks < 20) {
        const int k = (ks - 4) >> 1, i = (k >> 1) + 4 * (k & 1);
        if ((ks & 1) == 0) {
          ev = fmaf(__builtin_amdgcn_exp2f(accg(acc[E & 1], i) * q[E & 1].q0), q[E & 1].v, q[E & 1].v);
          gu = accg(acc[E & 1], i) * accu(acc[E & 1], i);
        } else {
          const float o = gu * __builtin_amdgcn_rcpf(ev);
          if (k & 1) op[k >> 1] = pk(oa, o); else oa = o;
        }
      }
      __builtin_amdgcn_sched_barrier(0);
      if (n == 1) {
        const u32 co = ((c >> 3) & 1) * 1024 + ((w - 1) * 8 + (c & 7)) * 16 + h * 8 + (c >> 4) * 4;
#pragma unroll
        for (int i = 0; i < 8; i++) ld4(cdv[i], ws + WS_CD + (i * 16 + fs) * 2048, co);
      }
      if (n == 0) glds4k(xa + 32768, ao, lpc + 32768);
      if (T == 0 && ks >= 4 && ks < 28 && ks % 3 == 1) ldw2((ks - 4) / 3);
      if (T == 1 && ks < 16 && (ks & 1)) {
        const int i = ks >> 1, o = 65536 + (i >> 2) * 32768;
        if ((i & 3) == 0) glds1k<0>(xa + o, ao, lpc + o); else if ((i & 3) == 1) glds1k<1024>(xa + o, ao, lpc + o);
        else if ((i & 3) == 2) glds1k<2048>(xa + o, ao, lpc + o); else glds1k<3072>(xa + o, ao, lpc + o);
      }
      if (T == 1 && (ks == 18 || ks == 22 || ks == 24 || ks == 26)) ldw2(ks == 18 ? 8 : ks / 2 - 2);
      if (T > 1 && ks == 9) { wait_vm<T == 2 ? 6 : 4>(); st4one(fpb + (T - 2) * 32, tag); }
      if (T > 0 && ks == 20) st16(hb + E * 114688, ho, op);
      if (T > 1 && ks % 3 == 1 && ks < 24) {
        const int i = ks / 3;
        const char* q = w2b + kof(5 + (i >> 2)) * 4096;
        const u32 la = lpc + (i >> 2) * 32768;
        if (T == 3) ldw2(12 + i);
        else if ((i & 3) == 0) glds1k<0>(q, ao, la); else if ((i & 3) == 1) glds1k<1024>(q, ao, la);
        else if ((i & 3) == 2) glds1k<2048>(q, ao, la); else glds1k<3072>(q, ao, la);
      }
      if (n == 122) ld4(fh, fhb, fho);
    }
    };
    tile(IC<0>{}); tile(IC<1>{}); tile(IC<2>{}); tile(IC<3>{});
    TS(26); TS(27); TC(28);
    TSF(21, 28);
#pragma unroll
    for (int i = 0; i < 4; i++) op[i] = pk(swi(acc[1], i, q[1]), swi(acc[1], i + 4, q[1]));
    st16(hb + 3 * 114688, ho, op);
    __builtin_amdgcn_s_setprio(0);
    wait_vm<1>(); tie(fh);
#pragma unroll
    for (int i = 0; i < 20; i++) { if (i < 12) tie(W2x[i]); else tie(A[i - 12]); }
    TS(29);
  }
  {
    const char* hq = ws + WS_HF + (size_t)((tt2 * 8 + sel) * 56 + w * 7) * 1024;
    u32x4 H[3][7];
    auto need = [&](unsigned long long mask) NOPK {
#pragma unroll 1
      for (int it = 0; it < SPIN_CAP && (__builtin_amdgcn_ballot_w64(fh != tag) & mask) != 0; it++) {
        __builtin_amdgcn_s_sleep(2);
        ld4(fh, fhb, fho); wait_vm<0>(); tie(fh);
      }
    };
    auto ldh = [&](int q) NOPK {
#pragma unroll
      for (int j = 0; j < 7; j++) ld16(H[q % 3][j], hq + q * 114688 + kof(j) * 1024, ao);
    };
    char* wr = smem + w * 4096 + r * 256;
    auto quarter = [&](int q, int yng) NOPK {
      f32x4 acc[4];
#pragma unroll
      for (int j = 0; j < 7; j++) {
        __builtin_amdgcn_sched_barrier(0);
        if (yng == 0) { if (j == 0) wait_vm<5>(); else if (j == 2) wait_vm<3>(); else if (j == 4) wait_vm<1>(); else if (j == 6) wait_vm<0>(); }
        if (yng == 8 && j == 0) wait_vm<8>();
        tie(H[q % 3][j]);
#pragma unroll
        for (int ds = 0; ds < 4; ds++) {
          const int f = 4 * j + ds;
          acc[ds] = mfma16(f < 12 ? W2x[f] : A[f - 12], H[q % 3][j], j == 0 ? f32x4{0.f, 0.f, 0.f, 0.f} : acc[ds]);
        }
      }
      __builtin_amdgcn_sched_barrier(0);
#pragma unroll
      for (int ds = 0; ds < 4; ds++) *(f32x4*)(wr + q * 32768 + (((ds * 4 + g) ^ r) * 16)) = acc[ds];
    };
    const int tk = (tid >> 4) & 15, qh = w >> 2;
    u32 xq[2][4];
    f32x4 gbv;
    auto finish = [&](int qp) NOPK {
      const char* rp = smem + (2 * qp + qh) * 32768 + tk * 256 + ((r ^ tk) * 16);
      f32x4 s = *(const f32x4*)rp;
#pragma unroll
      for (int i = 1; i < 8; i++) s += *(const f32x4*)(rp + i * 4096);
      float y[4];
#pragma unroll
      for (int j = 0; j < 4; j++) y[j] = fmaf(gbv[j], s[j], bflo(xq[qp][j]));
      asm volatile("global_store_dwordx2 %0, %1, %2 sc1" ::"v"((tk * D + r * 4) * 2), "v"(u32x2{pk(y[0], y[1]), pk(y[2], y[3])}),
                   "s"(out + (size_t)((2 * qp + qh) * 512 + tt2 * 32 + sel * 16) * D + dt * 64) : "memory");
    };
    st4one(fpb + 64, tag);
    need(0xffffull);
    ldh(0);
    TS(30);
    {
      const char* xp = smem + D_RES + qh * 2048 + (tk >> 3) * 256 + r * 16 + (tk & 7) * 2;
#pragma unroll
      for (int i = 0; i < 8; i++) xq[i >> 2][i & 3] = *(const u16*)(xp + (i >> 2) * 4096 + (i & 3) * 512);
      gbv = *(const f32x4*)(smem + D_GB + r * 16);
    }
    wait_vm<8>();
    st4one(fpb + 96, tag);
    TS(31);
    need(0xffffull << 16);
    ld4(fh, fhb, fho);
    ldh(1);
    wait_vm<7>(); tie(fh);
    TS(32);
    need(0xffffull << 32);
    ld4(fh, fhb, fho);
    ldh(2);
    quarter(0, -1);
    TS(33);
    wait_vm<7>(); tie(fh);
    need(0xffffull << 48);
    TS(34);
    ldh(3);
    quarter(1, -1);
    TS(35);
    lds_barrier();
    finish(0);
    asm volatile("" ::: "memory");
    TS(36);
    TSF(29, 36);
    quarter(2, 8);
    TS(37);
    quarter(3, 0);
    TS(38);
    lds_barrier();
    asm volatile(".Lpf1:");
    finish(1);
    TS(39);
    TS(40); while (__builtin_amdgcn_s_memrealtime() - t00_ < 4000) __builtin_amdgcn_s_sleep(1);
    TSF(37, 40);
    TSEND();
  }
}

extern "C" void kernel_launch(void* const* d_in, const int* in_sizes, int n_in, void* d_out, int out_size, void* d_ws,
                              size_t ws_size, hipStream_t stream) {
  const u16* const* in = (const u16* const*)d_in;
  fused<<<256, 512, 0, stream>>>(in[0], in[1], in[2], in[3], in[4], in[5], in[6], in[7], in[8], in[10], in[11], in[12], in[13], in[14],
                                 (char*)d_ws, (u16*)d_out);
}
```

```cpp
#include <hip/hip_runtime.h>
#include <stdint.h>

typedef unsigned int u32;
typedef unsigned short u16;
typedef __attribute__((ext_vector_type(2))) float f32x2;
typedef __attribute__((ext_vector_type(4))) float f32x4;
typedef __attribute__((ext_vector_type(16))) float f32x16;
typedef __attribute__((ext_vector_type(2))) u32 u32x2;
typedef __attribute__((ext_vector_type(4))) u32 u32x4;
typedef __attribute__((ext_vector_type(2))) __bf16 bf16x2;
typedef __attribute__((ext_vector_type(8))) __bf16 bf16x8;
typedef __attribute__((ext_vector_type(4))) short s16x4;
#define LDS_AS __attribute__((address_space(3)))
#if defined(__HIP_DEVICE_COMPILE__)
#define NOPK __attribute__((target("no-packed-fp32-ops")))
#else
#define NOPK
#endif
#define DEVINL __device__ __forceinline__ NOPK

constexpr int L = 2048, D = 512, NS = 128, F = 1792;

constexpr size_t WS_E = 0;
constexpr size_t WS_FW = 4096;
constexpr size_t WS_FX = WS_FW + 32768;
constexpr size_t WS_FH = WS_FX + 32768;
constexpr size_t WS_MG = WS_FH + 65536;
constexpr size_t WS_GB = WS_MG + 98304;
constexpr size_t WS_XG = 262144;
constexpr size_t WS_X1 = WS_XG + (4u << 20);
constexpr size_t WS_W1F = WS_X1 + (2u << 20);
constexpr size_t WS_W2F = WS_W1F + 112 * 32 * 1024;
constexpr size_t WS_HF = WS_W2F + 16 * 112 * 1024;
constexpr size_t WS_CD = WS_HF + 64 * 112 * 1024;
constexpr int SPIN_CAP = 40000;
#define GP 1
#define RP 1
#define SCR 3
#define LBC 6
#define NF1 24
#define PFM 3
#define PFB 32768

#define u2f(x) __builtin_bit_cast(float, (u32)(x))
#define i2f(x) __builtin_bit_cast(float, (int)(x))
#define f2u(x) __builtin_bit_cast(u32, (float)(x))
#define f2i(x) __builtin_bit_cast(int, (float)(x))
#define TSDECL
#define TS(slot)
#define TC(slot)
#define TSF(lo, hi)
#define TSEND()
DEVINL float bflo(u32 u) { return u2f(u << 16); }
DEVINL float bfhi(u32 u) { return u2f(u & 0xffff0000u); }
DEVINL u32 pk(float a, float b) {
  f32x2 v = {a, b};
  return __builtin_bit_cast(u32, __builtin_convertvector(v, bf16x2));
}
DEVINL float rbf(float a) { return bflo(pk(a, a)); }
template <int CTRL> DEVINL float dpp_add(float v) {
  return v + i2f(__builtin_amdgcn_update_dpp(0, f2i(v), CTRL, 0xf, 0xf, true));
}
DEVINL float row4_sum(float v) {
  auto a = __builtin_amdgcn_permlane16_swap(f2u(v), f2u(v), false, false);
  v = u2f(a[0]) + u2f(a[1]);
  auto b = __builtin_amdgcn_permlane32_swap(f2u(v), f2u(v), false, false);
  return u2f(b[0]) + u2f(b[1]);
}
DEVINL float rs32(float a, float b) {
  auto r = __builtin_amdgcn_permlane32_swap(f2u(a), f2u(b), false, false);
  return u2f(r[0]) + u2f(r[1]);
}
DEVINL float rs16(float a, float b) {
  auto r = __builtin_amdgcn_permlane16_swap(f2u(a), f2u(b), false, false);
  return u2f(r[0]) + u2f(r[1]);
}
DEVINL float wave_sum(float v) { return row4_sum(dpp_add<0x140>(dpp_add<0x141>(dpp_add<0x4E>(dpp_add<0xB1>(v))))); }
DEVINL f32x4 mfma16(u32x4 a, u32x4 b, f32x4 c) {
  return __builtin_amdgcn_mfma_f32_16x16x32_bf16(__builtin_bit_cast(bf16x8, a), __builtin_bit_cast(bf16x8, b), c, 0, 0, 0);
}
DEVINL f32x16 mfma32(u32x4 a, u32x4 b, f32x16 c) {
  return __builtin_amdgcn_mfma_f32_32x32x16_bf16(__builtin_bit_cast(bf16x8, a), __builtin_bit_cast(bf16x8, b), c, 0, 0, 0);
}
DEVINL float dot2(u32 a, u32 b, float c) {
  return __builtin_amdgcn_fdot2_f32_bf16(__builtin_bit_cast(bf16x2, a), __builtin_bit_cast(bf16x2, b), c, false);
}
DEVINL void lds_barrier() {
  asm volatile("s_waitcnt lgkmcnt(0)" ::: "memory");
  __builtin_amdgcn_s_barrier();
  asm volatile("" ::: "memory");
}
DEVINL void bar() {
  asm volatile("" ::: "memory");
  __builtin_amdgcn_s_barrier();
  asm volatile("" ::: "memory");
}
DEVINL void st16(const void* sbase, u32 voff, u32x4 v) {
  asm volatile("global_store_dwordx4 %0, %1, %2 sc1\n\ts_nop 1" ::"v"(voff), "v"(v), "s"(sbase) : "memory");
}
DEVINL void st8(const void* sbase, u32 voff, u32x2 v) {
  asm volatile("global_store_dwordx2 %0, %1, %2 sc1\n\ts_nop 1" ::"v"(voff), "v"(v), "s"(sbase) : "memory");
}
DEVINL void st4(const void* sbase, u32 voff, u32 v) {
  asm volatile("global_store_dword %0, %1, %2 sc1\n\ts_nop 1" ::"v"(voff), "v"(v), "s"(sbase) : "memory");
}
template <int LANES> DEVINL void st8m(const void* sbase, u32 voff, u32x2 v) {
  asm volatile("s_mov_b64 exec, %3\n\tglobal_store_dwordx2 %0, %1, %2 sc1\n\ts_mov_b64 exec, -1\n\ts_nop 0"
               ::"v"(voff), "v"(v), "s"(sbase), "n"((1 << LANES) - 1) : "memory");
}
DEVINL void st4one(const void* sbase, u32 v) {
  asm volatile("s_mov_b64 exec, 1\n\tglobal_store_dword %0, %1, %2 sc1\n\ts_mov_b64 exec, -1\n\ts_nop 0"
               ::"v"(0), "v"(v), "s"(sbase) : "memory");
}
DEVINL void st16x2_56(const void* sbase, u32 voff, u32x4 a, u32x4 b) {
  asm volatile("s_mov_b32 exec_hi, 0xffffff\n\tglobal_store_dwordx4 %0, %1, %3 sc1\n\tglobal_store_dwordx4 %0, %2, %3 offset:256 sc1\n\t"
               "s_mov_b32 exec_hi, -1\n\ts_nop 0" ::"v"(voff), "v"(a), "v"(b), "s"(sbase) : "memory");
}
DEVINL void sld4(u32& d, const void* p) { asm volatile("s_load_dword %0, %1, 0" : "=s"(d) : "s"(p) : "memory"); }
template <int OFF = 0> DEVINL void ld16(u32x4& d, const void* sbase, u32 voff) {
  asm volatile("global_load_dwordx4 %0, %1, %2 offset:%3 sc1" : "=v"(d) : "v"(voff), "s"(sbase), "n"(OFF) : "memory");
}
DEVINL void ld16k(u32x4& d, const char* sbase, int kb, u32 voff) {
  const char* sb = sbase + (kb >> 2) * 4096;
  if ((kb & 3) == 0) ld16<0>(d, sb, voff); else if ((kb & 3) == 1) ld16<1024>(d, sb, voff);
  else if ((kb & 3) == 2) ld16<2048>(d, sb, voff); else ld16<3072>(d, sb, voff);
}
DEVINL void ld8(u32x2& d, const void* sbase, u32 voff) {
  asm volatile("global_load_dwordx2 %0, %1, %2 sc1" : "=v"(d) : "v"(voff), "s"(sbase) : "memory");
}
template <int OFF = 0> DEVINL void ld4(u32& d, const void* sbase, u32 voff) {
  asm volatile("global_load_dword %0, %1, %2 offset:%3 sc1" : "=v"(d) : "v"(voff), "s"(sbase), "n"(OFF) : "memory");
}
template <int OFF = 0> DEVINL void lp16(u32x4& d, const void* sbase, u32 voff) {
  asm volatile("global_load_dwordx4 %0, %1, %2 offset:%3" : "=v"(d) : "v"(voff), "s"(sbase), "n"(OFF) : "memory");
}
template <int OFF = 0> DEVINL void lp4(u32& d, const void* sbase, u32 voff) {
  asm volatile("global_load_dword %0, %1, %2 offset:%3" : "=v"(d) : "v"(voff), "s"(sbase), "n"(OFF) : "memory");
}
template <int OFF = 0> DEVINL void lp2(u32& d, const void* sbase, u32 voff) {
  asm volatile("global_load_ushort %0, %1, %2 offset:%3" : "=v"(d) : "v"(voff), "s"(sbase), "n"(OFF) : "memory");
}
template <class T> DEVINL void tie(T& a) { asm volatile("" : "+v"(a)); }
template <int N> DEVINL void wait_vm() { asm volatile("s_waitcnt vmcnt(%0)" ::"n"(N) : "memory"); }
#define wait_vmn(k) asm volatile("s_waitcnt vmcnt(%0)" ::"n"(k) : "memory")
DEVINL bool all_lanes(bool ok) { return __builtin_amdgcn_ballot_w64(!ok) == 0; }
DEVINL void spin4(u32 f, u32 tag, const void* sbase, u32 voff) {
#pragma unroll 1
  for (int it = 0; it < SPIN_CAP && !all_lanes(f == tag); it++) {
    __builtin_amdgcn_s_sleep(1);
    ld4(f, sbase, voff); wait_vm<0>(); tie(f);
  }
}
DEVINL bool arrive_last(char* smem_word, int n) {
  u32 old;
  asm volatile("s_mov_b64 exec, 1\n\tds_add_rtn_u32 %0, %1, %2\n\ts_waitcnt lgkmcnt(0)\n\ts_mov_b64 exec, -1"
               : "=&v"(old) : "v"((u32)(size_t)(LDS_AS char*)smem_word), "v"(1) : "memory");
  return __builtin_amdgcn_readfirstlane(old) == (u32)(n - 1);
}
DEVINL void scale8(u32x4& a, const u32x4& s, const u32x4& t, int on) {
  u32 a0 = a[0], a1 = a[1], a2 = a[2], a3 = a[3], x0, x1, x2, x3;
  asm("s_cmp_eq_u32 %16, 0\n\ts_cbranch_scc1 .Lsk%=\n\tv_dot2_f32_bf16 %4, %0, %8, 0\n\tv_dot2_f32_bf16 %0, %0, %9, 0\n\tv_dot2_f32_bf16 %5, %1, %10, 0\n\tv_dot2_f32_bf16 %1, %1, %11, 0\n\t"
      "v_dot2_f32_bf16 %6, %2, %12, 0\n\tv_dot2_f32_bf16 %2, %2, %13, 0\n\tv_dot2_f32_bf16 %7, %3, %14, 0\n\tv_dot2_f32_bf16 %3, %3, %15, 0\n\t"
      "v_cvt_pk_bf16_f32 %0, %4, %0\n\tv_cvt_pk_bf16_f32 %1, %5, %1\n\tv_cvt_pk_bf16_f32 %2, %6, %2\n\ts_nop 0\n\tv_cvt_pk_bf16_f32 %3, %7, %3\n.Lsk%=:"
      : "+v"(a0), "+v"(a1), "+v"(a2), "+v"(a3), "=&v"(x0), "=&v"(x1), "=&v"(x2), "=&v"(x3)
      : "v"(s[0]), "v"(s[1]), "v"(s[2]), "v"(s[3]), "v"(t[0]), "v"(t[1]), "v"(t[2]), "v"(t[3]), "s"(on) : "scc");
  a = u32x4{a0, a1, a2, a3};
}
DEVINL u32x2 tr_read(const char* p) {
  return __builtin_bit_cast(u32x2, __builtin_amdgcn_ds_read_tr16_b64_v4i16((LDS_AS s16x4*)p));
}
DEVINL float rdl(float v, int l) { return i2f(__builtin_amdgcn_readlane(f2i(v), l)); }
DEVINL float sel2(int c, float a, float b) { return c ? b : a; }
DEVINL u32 swap2(u32 v) { return (u32)__builtin_amdgcn_update_dpp(0, (int)v, 0x4E, 0xf, 0xf, true); }
DEVINL u32 swap1(u32 v) { return (u32)__builtin_amdgcn_update_dpp(0, (int)v, 0xB1, 0xf, 0xf, true); }
constexpr float LOG2E = 1.4426950408889634f, LN2 = 0.6931471805599453f, INV2PI = 0.15915494309189535f;

constexpr int A_TAB = 0, A_P = 8 * 1536, A_KC = A_P + 24576, A_KCS = 4112, A_GV = A_KC + 2 * A_KCS;
constexpr int B_Y1 = 0, B_RED = 40960, B_KC = 49152, B_KCS = 4160, B_KCH = 4 * 4160, B_U = B_KC + 2 * B_KCH, B_US = 5152, B_UCH = 2 * B_US;
constexpr int B_Y2 = B_U + 2 * B_UCH, A_XT = B_Y2;
constexpr int C_RAW = 148480, L_CNT = C_RAW + 1024, D_RES = L_CNT + 64, D_GB = D_RES + 8192, B_SC = D_GB + 256, LDS_BYTES = B_SC + 4096;
static_assert(A_GV + 2048 <= B_KC && B_Y2 + 40960 <= C_RAW && 131072 <= C_RAW && LDS_BYTES <= 163840, "lds map");

DEVINL void glds4k(const void* sbase, u32 voff, u32 ldsaddr) {
  asm volatile("s_mov_b32 m0, %2\n\ts_nop 0\n\t"
               "global_load_lds_dwordx4 %0, %1 sc1\n\tglobal_load_lds_dwordx4 %0, %1 offset:1024 sc1\n\t"
               "global_load_lds_dwordx4 %0, %1 offset:2048 sc1\n\tglobal_load_lds_dwordx4 %0, %1 offset:3072 sc1"
               ::"v"(voff), "s"(sbase), "s"(ldsaddr) : "memory");
}
DEVINL u32x4 tr8(const char* p) {
  const u32x2 lo = tr_read(p), hi = tr_read(p + 256);
  return u32x4{lo[0], lo[1], hi[0], hi[1]};
}
template <int OFF> DEVINL void glds1k(const void* sbase, u32 voff, u32 ldsaddr) {
  asm volatile("s_mov_b32 m0, %2\n\ts_nop 0\n\tglobal_load_lds_dwordx4 %0, %1 offset:%3 sc1" ::"v"(voff), "s"(sbase), "s"(ldsaddr), "n"(OFF) : "memory");
}
DEVINL const char* far(const char* p) {
  u32 a = (u32)(size_t)(LDS_AS const char*)p;
  asm("" : "+v"(a));
  return (const char*)(LDS_AS const char*)(size_t)a;
}
struct Ep { float q0, v; };
template <int N> struct IC { static constexpr int v = N; };
#define accg(a, i) a[((i) & 3) + 8 * ((i) >> 2)]
#define accu(a, i) a[((i) & 3) + 8 * ((i) >> 2) + 4]
DEVINL float swi(const f32x16& a, int i, const Ep& q) {
  const float e = __builtin_amdgcn_exp2f(accg(a, i) * q.q0);
  return (accg(a, i) * accu(a, i)) * __builtin_amdgcn_rcpf(fmaf(e, q.v, q.v));
}
DEVINL u32 ln2_fold(f32x2 s, int h, Ep& q) {
  const float m = s[0] * (1.f / D);
  const float v = fmaxf(fmaf(-m, m, s[1] * (1.f / D)), 0.f) + 1e-5f;
  const float rs = __builtin_amdgcn_rsqf(v);
  q.q0 = rs * -LOG2E; q.v = v;
  const float val = h ? v * rs : -m;
  const float vhi = rbf(val);
  return pk(vhi, val - vhi);
}
DEVINL void ln2_tile(const char* rb, f32x2* raw, f32x2* dummy, int lane) {
  const u32x4 ones = {0x3f803f80u, 0x3f803f80u, 0x3f803f80u, 0x3f803f80u};
  f32x4 sg[2], sx[2];
#pragma unroll
  for (int i = 0; i < 2; i++) { sg[i] = f32x4{0.f, 0.f, 0.f, 0.f}; sx[i] = f32x4{0.f, 0.f, 0.f, 0.f}; }
  u32x4 fr[4][2];
  auto rd = [&](int n) NOPK {
#pragma unroll
    for (int gi = 0; gi < 2; gi++) {
      const char* pp = rb + n * 2048 + gi * 32;
      const u32x2 lo = tr_read(pp), hi = tr_read(pp + 1024);
      fr[n & 3][gi] = u32x4{lo[0], lo[1], hi[0], hi[1]};
    }
  };
  rd(0); rd(1); rd(2);
#pragma unroll
  for (int n = 0; n < 16; n++) {
    if (n + 3 < 16) rd(n + 3);
    __builtin_amdgcn_sched_barrier(0);
#pragma unroll
    for (int gi = 0; gi < 2; gi++) {
      sg[gi] = mfma16(fr[n & 3][gi], fr[n & 3][gi], sg[gi]);
      sx[gi] = mfma16(ones, fr[n & 3][gi], sx[gi]);
    }
    __builtin_amdgcn_sched_barrier(0);
  }
#pragma unroll
  for (int gi = 0; gi < 2; gi++) {
    const float gsel = (lane & 2) ? ((lane & 1) ? sg[gi][3] : sg[gi][2]) : ((lane & 1) ? sg[gi][1] : sg[gi][0]);
    f32x2* dst = (lane >> 4) == ((lane & 15) >> 2) ? raw + gi * 16 + (lane & 15) : dummy;
    *dst = f32x2{sx[gi][0], gsel};
  }
}

__global__ NOPK __launch_bounds__(512, 2) void fused(
    const u16* __restrict__ x, const u16* __restrict__ Are, const u16* __restrict__ Aim, const u16* __restrict__ Bre,
    const u16* __restrict__ Bim, const u16* __restrict__ Cre, const u16* __restrict__ Cim, const u16* __restrict__ logdt,
    const u16* __restrict__ dskip, const u16* __restrict__ w1, const u16* __restrict__ w3, const u16* __restrict__ w2,
    const u16* __restrict__ cvec, const u16* __restrict__ wmod, char* ws, u16* __restrict__ out) {
  __shared__ __attribute__((aligned(16))) char smem[LDS_BYTES];
  const int b = blockIdx.x, tid = threadIdx.x, lane = tid & 63;
  const int w = __builtin_amdgcn_readfirstlane(tid >> 6);
  TSDECL; TS(0);
  const int xg = b & 7, idx = b >> 3;
  const unsigned long long t00_ = __builtin_amdgcn_s_memrealtime();
  const int p = xg * 32 + idx;
  const int r = lane & 15, g = lane >> 4;
  asm volatile("" ::"s"(x), "s"(Are), "s"(Aim), "s"(Bre), "s"(Bim), "s"(Cre), "s"(Cim), "s"(logdt), "s"(dskip), "s"(w1), "s"(w3),
               "s"(w2), "s"(cvec), "s"(wmod), "s"(ws), "s"(out));
  u32x4 xr, wm0, wm1;
  u32 vare, vaim, vbre, vbim, vcre, vcim, c0, c1;
  const int cg = xg * 24 + (idx < 24 ? idx : 23);
  u32 e_prev, ldt2, dsk;
  lp16(xr, x + b * 4096, tid * 16);
  sld4(e_prev, ws + WS_E); sld4(ldt2, logdt + 2 * p); sld4(dsk, dskip + 2 * p);
  {
    const u32 so = (p * 256 + w * 32 + (lane & 31)) * 2;
    lp2(vare, Are, so); lp2(vaim, Aim, so); lp2(vbre, Bre, so); lp2(vbim, Bim, so); lp2(vcre, Cre, so); lp2(vcim, Cim, so);
    const u32 mo = (tid >> 3) * 6144 + (tid & 7) * 16;
    const u16* wmp = wmod + (cg & 3) * 128 * 3072 + (cg >> 2) * 64;
    lp16(wm0, wmp, mo); lp16(wm1, wmp + 64 * 3072, mo);
    lp2(c0, cvec + (cg & 3) * 128, (tid >> 3) * 2); lp2<128>(c1, cvec + (cg & 3) * 128, (tid >> 3) * 2);
  }
  u32 pf;
  {
    unsigned long long pc;
    u32 po = ((idx & PFM) * 512 + tid) * (PFB / (PFM + 1) / 512);
    asm volatile("s_getpc_b64 %1\n.Lpf0:\n\tv_min_u32 %2, .Lpf1-.Lpf0, %2\n\tglobal_load_dword %0, %2, %1"
                 : "=v"(pf), "=&s"(pc), "+v"(po)::"memory");
  }
  TS(1);
  if (tid < 32) *(u32x4*)(smem + B_KC + (tid >> 2) * B_KCS + 4096 + (tid & 3) * 16) = u32x4{0, 0, 0, 0};
  else if (tid < 36) *(u32x4*)(smem + L_CNT + (tid - 32) * 16) = u32x4{0, 0, 0, 0};
  else if (tid < 38) *(u32x2*)(smem + B_U + (tid & 1) * B_UCH + B_US + 1024) = u32x2{0, 0};
  else if (tid >= 256) *(u32x4*)(smem + B_U + (w - 4) * B_US + lane * 16) = u32x4{0, 0, 0, 0};
  asm volatile("s_waitcnt lgkmcnt(0)" : "+s"(e_prev), "+s"(ldt2), "+s"(dsk)::"memory");
  const u32 tag = e_prev * 1664525u + 1013904223u;
  wait_vm<11>(); tie(xr);
  {
    *(u32x4*)(smem + A_XT + w * 1056 + lane * 16) = xr;
    float f[8];
#pragma unroll
    for (int j = 0; j < 4; j++) { f[2 * j] = bflo(xr[j]); f[2 * j + 1] = bfhi(xr[j]); }
    const float m = wave_sum(((f[0] + f[1]) + (f[2] + f[3])) + ((f[4] + f[5]) + (f[6] + f[7]))) * (1.f / D);
    float s = 0.f;
#pragma unroll
    for (int j = 0; j < 8; j++) { f[j] -= m; s = fmaf(f[j], f[j], s); }
    const float sd = __builtin_amdgcn_sqrtf(rbf(rbf(wave_sum(s) * (1.f / D)) + 1e-5f));
    if (lane == 0) *(u32*)(smem + A_XT + w * 1056 + 1024) = pk(m, sd);
  }
  TS(2);
  wait_vm<5>(); tie(vare); tie(vaim); tie(vbre); tie(vbim); tie(vcre); tie(vcim);
  char* tab = smem + A_TAB + w * 1536;
  {
    const float dt = rbf(__builtin_amdgcn_exp2f((w >> 2 ? bfhi(ldt2) : bflo(ldt2)) * LOG2E));
    const float a = dt * bflo(vare) * LOG2E, bq = dt * bflo(vaim) * INV2PI;
    const float br = dt * bflo(vbre), bi = dt * bflo(vbim), cr = bflo(vcre), ci = bflo(vcim);
    const float m = lane < 32 ? 1.f : 256.f;
    const float e = __builtin_amdgcn_exp2f(a * m), ph = __builtin_amdgcn_fractf(-bq * m);
    const float ex = e * __builtin_amdgcn_cosf(ph), ey = e * __builtin_amdgcn_sinf(ph);
    char* t = tab + (lane & 31) * 48;
    *(f32x2*)(t + 16 + (lane >> 5) * 8) = f32x2{ex, ey};
    f32x4 o = {a, bq, cr * br - ci * bi, cr * bi + ci * br};
    if (lane >= 32) o = f32x4{ex, ey, ex * ex - ey * ey, (ex + ex) * ey};
    *(f32x4*)(t + (lane >> 5) * 32) = o;
  }
  TS(3);
  lds_barrier();
  {
    const char* xt = smem + A_XT + (lane & 7) * 1056;
    const u32 st = *(const u32*)(xt + 1024);
#pragma unroll
    for (int e = 0; e < 2; e++) {
      const u32x2 xq = *(const u32x2*)(xt + (e * 64 + w * 8 + (lane >> 3)) * 8);
      st16(ws + WS_XG + (size_t)(e * 64 + w * 8) * 32768 + b * 128, (lane >> 3) * 32768 + (lane & 7) * 16, u32x4{xq[0], xq[1], st, tag});
    }
  }
  TS(41);
  const int fb = (xg >> 1) * 4 + (idx & 3), mat = (idx >> 2) & 1, dq = (xg & 1) * 4 + (idx >> 3), d0 = dq * 64 + w * 8;
  const int b2 = b < 224 ? b : 223;
  u32 v[8], t2[8];
  {
    const u16* wp = (mat ? w3 : w1) + (size_t)d0 * F + fb * 112;
    const u32 vo = (lane < 56 ? lane : 55) * 4;
#pragma unroll
    for (int i = 0; i < 4; i++) { lp4<0>(v[2 * i], wp + 2 * i * F, vo); lp4<2 * F>(v[2 * i + 1], wp + 2 * i * F, vo); }
#pragma unroll
    for (int i = 0; i < 2; i++) {
      const u16* wq = w2 + (size_t)(b2 * 8 + i * 4) * D;
      const u32 to = tid * 2;
      lp2<0>(t2[4 * i], wq, to); lp2<1024>(t2[4 * i + 1], wq, to); lp2<2048>(t2[4 * i + 2], wq, to); lp2<3072>(t2[4 * i + 3], wq, to);
    }
  }
  auto retile = [&]() NOPK {
    wait_vm<0>();
#pragma unroll
    for (int i = 0; i < 8; i++) { tie(v[i]); tie(t2[i]); }
    u32x4 o0, o1, o2;
#pragma unroll
    for (int j = 0; j < 4; j++) {
      o0[j] = __builtin_amdgcn_perm(v[2 * j + 1], v[2 * j], 0x05040100u);
      o1[j] = __builtin_amdgcn_perm(v[2 * j + 1], v[2 * j], 0x07060302u);
      o2[j] = __builtin_amdgcn_perm(t2[2 * j + 1], t2[2 * j], 0x05040100u);
    }
    st16x2_56(ws + WS_W1F + (size_t)(fb * 7 * 32 + (d0 >> 4)) * 1024 + (w & 1) * 512 + mat * 128, (lane >> 3) * 32768 + (lane & 7) * 16, o0, o1);
    st16(ws + WS_W2F + w * 229376 + (b2 >> 2) * 4096 + (b2 & 3) * 256, g * 1024 + r * 16, o2);
  };
  auto gemv = [&]() NOPK
  {
    wait_vm<18>(); tie(pf); tie(wm0); tie(wm1); tie(c0); tie(c1);
    const float cx0 = bflo(c0), cx1 = bflo(c1);
    const float s0 = rbf(cx0 * __builtin_amdgcn_rcpf(1.f + __builtin_amdgcn_exp2f(cx0 * -LOG2E)));
    const float s1 = rbf(cx1 * __builtin_amdgcn_rcpf(1.f + __builtin_amdgcn_exp2f(cx1 * -LOG2E)));
    float acc[8];
#pragma unroll
    for (int j = 0; j < 4; j++) {
      acc[2 * j] = fmaf(s0, bflo(wm0[j]), s1 * bflo(wm1[j]));
      acc[2 * j + 1] = fmaf(s0, bfhi(wm0[j]), s1 * bfhi(wm1[j]));
    }
    const float q0 = dpp_add<0x128>(rs16(rs32(acc[0], acc[4]), rs32(acc[2], acc[6])));
    const float q1 = dpp_add<0x128>(rs16(rs32(acc[1], acc[5]), rs32(acc[3], acc[7])));
    *(float*)(smem + A_GV + w * 256 + (lane & 7) * 32 + (lane >> 3) * 4) = (lane & 8) ? q1 : q0;
    if (arrive_last(smem + L_CNT, 8) && idx < 24) {
      float s = 0.f;
#pragma unroll
      for (int i = 0; i < 8; i++) s += *(const float*)(smem + A_GV + i * 256 + lane * 4);
      st8(ws + WS_MG + (cg & 3) * 24576 + (cg >> 2) * 512, lane * 8, u32x2{f2u(s), tag});
    }
  };
  TS(4);
  if (GP == 0) { gemv(); TS(44); }
  const u32 gcode = __builtin_amdgcn_perm(0x400b0916u, 0x0a201408u, (r & 7) | 0x0c0c0c00u);
  const int gx = r < 8 ? 2 * r : (int)((gcode >> 2) * 256 + (gcode & 3) * 4);
  {
    asm volatile("" ::: "memory");
    const float mF = (float)(16 * r), mX = (float)gx;
    const char* tp = tab + g * 8 * 48;
    const char* tq = tp + (r < 8 ? 16 : 32);
    f32x4 acc[3];
#pragma unroll
    for (int s = 0; s < 3; s++) acc[s] = f32x4{0.f, 0.f, 0.f, 0.f};
#pragma unroll
    for (int ks = 0; ks < 2; ks++) {
      u32x4 Ff, Gf[3];
#pragma unroll
      for (int i = 0; i < 4; i++) {
        const f32x4 s0 = *(const f32x4*)(tp + (ks * 4 + i) * 48), s1 = *(const f32x4*)(tq + (ks * 4 + i) * 48);
        const float e0 = __builtin_amdgcn_exp2f(s0[0] * mF), p0 = __builtin_amdgcn_fractf(s0[1] * mF);
        const float zx = e0 * __builtin_amdgcn_cosf(p0), zy = e0 * __builtin_amdgcn_sinf(p0);
        Ff[i] = pk(s0[2] * zx - s0[3] * zy, s0[2] * zy + s0[3] * zx);
        const float e1 = __builtin_amdgcn_exp2f(s0[0] * mX), p1 = __builtin_amdgcn_fractf(s0[1] * -mX);
        const float xx = e1 * __builtin_amdgcn_cosf(p1), xy = e1 * __builtin_amdgcn_sinf(p1);
        Gf[0][i] = pk(xx, xy);
        Gf[1][i] = pk(xx * s1[0] - xy * s1[1], xx * s1[1] + xy * s1[0]);
        Gf[2][i] = pk(xx * s1[2] - xy * s1[3], xx * s1[3] + xy * s1[2]);
      }
#pragma unroll
      for (int s = 0; s < 3; s++) acc[s] = mfma16(Ff, Gf[s], acc[s]);
      if (ks == 0) { TS(45); if (RP == 0) retile(); if (GP == 1) { gemv(); TS(44); } }
    }
#pragma unroll
    for (int s = 0; s < 3; s++) *(f32x4*)(smem + A_P + w * 3072 + s * 1024 + lane * 16) = acc[s];
  }
  if (RP == 1) retile();
  TS(5);
  u32x4 xg4[4];
  u32x2 mg, mh, mc[4];
  const char* xgb = ws + WS_XG + (size_t)(p >> 1) * 32768;
  const u32 xgo = (tid >> 2) * 256 + (tid & 3) * 16;
  u32 mga = (4 - ((lane >> 3) & 1)) * 512 + d0 + (lane & 7), mgb = ((0x5210 >> ((lane & 6) * 2)) & 7) * 512 + 2 * p + (lane & 1);
  tie(mga); tie(mgb);
  const u32 mgo = ((lane & 16) ? mgb : mga) * 8 + (lane >> 5) * 24576;
  auto lookx = [&]() NOPK {
    ld16<0>(xg4[0], xgb, xgo); ld16<64>(xg4[1], xgb, xgo); ld16<128>(xg4[2], xgb, xgo); ld16<192>(xg4[3], xgb, xgo);
  };
  auto lookm = [&]() NOPK {
    ld8(mg, ws + WS_MG, mgo); ld8(mh, ws + WS_MG + 49152, mgo);
  };
  auto lookc = [&]() NOPK {
#pragma unroll
    for (int q = 0; q < 4; q++) ld8(mc[q], ws + WS_MG + q * 24576 + 16384, tid * 8);
  };
  lookx();
  lds_barrier();
  TS(6);
  TSF(0, 6); TSF(41, 41); TSF(44, 45);
  const int kch = tid >> 8;
  u16* Kc = (u16*)(smem + A_KC + kch * A_KCS);
  if ((w & 3) < 3) {
    const int s = w & 3;
    const char* pp = smem + A_P + kch * 12288 + s * 1024 + lane * 16;
    const f32x4 q = (*(const f32x4*)(pp) + *(const f32x4*)(pp + 3072)) + (*(const f32x4*)(pp + 6144) + *(const f32x4*)(pp + 9216));
    const int pos = gx + 64 * g + (r < 8 ? (s == 0 ? 0 : s == 1 ? 1 : 256) : s * 256);
    const u32 o0 = pk(q[0], q[1]), o1 = pk(q[2], q[3]);
    if (pos <= 2048) Kc[pos] = (u16)o0;
    if (pos < 2048) {
      Kc[pos + 16] = (u16)(o0 >> 16);
      Kc[pos + 32] = (u16)o1;
      Kc[pos + 48] = (u16)(o1 >> 16);
    }
  }
  wait_vm<4>();
  lds_barrier();
  TS(7);
  if (w == 7) st4one(ws + WS_FW + (fb * 16 + mat * 8 + dq) * 128, tag);
  const int tt = (xg & 1) * 8 + (idx & 7), fs = (xg >> 1) * 4 + (idx >> 3);
  const u32 fwo = fs * 2048 + (lane & 15) * 128;
  u32 fw;
  lookm(); lookc();
  {
    u32 e[6];
    const int ki = tid & 255;
#pragma unroll
    for (int j = 0; j < 6; j++) {
      const float l0 = (float)(2047 - 8 * ki - 2 * j);
      const u32 bits = pk(fmaxf(l0, 0.f), fmaxf(l0 - 1.f, 0.f));
      e[j] = (u32)Kc[(int)bflo(bits)];
      if (j < 5) e[j] |= (u32)Kc[(int)bfhi(bits)] << 16;
    }
    if (ki == 255) { e[4] = 0; e[5] = 0; }
    const u32 a0 = __builtin_amdgcn_alignbit(e[1], e[0], 16), a1 = __builtin_amdgcn_alignbit(e[2], e[1], 16);
    const u32 a2 = __builtin_amdgcn_alignbit(e[3], e[2], 16), a3 = __builtin_amdgcn_alignbit(e[4], e[3], 16);
    const u32 a4 = __builtin_amdgcn_alignbit(e[5], e[4], 16);
    char* kc = smem + B_KC + kch * B_KCH + ki * 16;
    *(u32x4*)(kc) = u32x4{e[0], e[1], e[2], e[3]};
    *(u32x4*)(kc + B_KCS) = u32x4{a0, a1, a2, a3};
    *(u32x4*)(kc + 2 * B_KCS) = u32x4{e[1], e[2], e[3], e[4]};
    *(u32x4*)(kc + 3 * B_KCS) = u32x4{a1, a2, a3, a4};
  }
  TS(8);
  wait_vm<0>(); tie(xg4[0]); tie(xg4[1]); tie(xg4[2]); tie(xg4[3]); tie(mg); tie(mh); tie(mc[0]); tie(mc[1]); tie(mc[2]); tie(mc[3]);
  TS(47);
#pragma unroll 1
  for (int it = 0; it < SPIN_CAP && !all_lanes(xg4[0][3] == tag && xg4[1][3] == tag && xg4[2][3] == tag && xg4[3][3] == tag); it++) {
    lookx();
    wait_vm<0>(); tie(xg4[0]); tie(xg4[1]); tie(xg4[2]); tie(xg4[3]);
  }
#pragma unroll 1
  for (int it = 0; it < SPIN_CAP && !all_lanes(mg[1] == tag && mh[1] == tag); it++) {
    lookm();
    wait_vm<0>(); tie(mg); tie(mh);
  }
  TS(9);
  const int h = lane >> 5, c = lane & 31;
  const int tt2 = (~xg & 1) * 8 + (xg & 6) + (idx >> 4), sel = (idx >> 3) & 1, dt = idx & 7;
  const int rot = (2 * (idx >> 3) + idx) & 7;
  const int ft = fs * 7 + w - 1;
  u32x4 A[32];
  const u32 ao = lane * 16;
  auto lda = [&](int i) NOPK {
    const char* wq = ws + WS_W1F + (size_t)ft * 32768 + (((i >> 2) + rot) & 7) * 4096;
    asm volatile("s_cmp_eq_u32 %4, 0\n\ts_cbranch_scc1 .Lna%=\n\tglobal_load_dwordx4 %0, %1, %2 offset:%3 sc1\n.Lna%=:"
                 : "=v"(A[i]) : "v"(ao), "s"(wq), "n"((i & 3) * 1024), "s"(w) : "memory", "scc");
  };
  ld4(fw, ws + WS_FW, fwo);
  const int g8 = tid >> 1, cc = tid & 1;
  const float mq = u2f(mg[0]) + u2f(mh[0]);
  const float mv = rbf(rs32(mq, mq)), mv1 = rbf(1.f + mv);
  const float ga = sel2(cc, rdl(mv, 20), rdl(mv, 21));
  u32x4 hp, xv;
  {
    const float sh0 = rdl(mv, 16), sh1 = rdl(mv, 17), sc0 = rdl(mv1, 18), sc1 = rdl(mv1, 19);
    const u32 psel = cc ? 0x03020706u : 0x05040100u;
    const bool up = tid & 2;
    u32 ph[4], px[4];
#pragma unroll
    for (int i = 0; i < 4; i++) {
      const u32 xq = (p & 1) ? xg4[i][1] : xg4[i][0];
      const float m = bflo(xg4[i][2]), rs = __builtin_amdgcn_rcpf(bfhi(xg4[i][2]));
      const u32 n = pk((bflo(xq) - m) * rs, (bfhi(xq) - m) * rs);
      const u32 hq = pk(fmaf(bflo(n), sc0, sh0), fmaf(bfhi(n), sc1, sh1));
      ph[i] = __builtin_amdgcn_perm(swap1(hq), hq, psel);
      px[i] = __builtin_amdgcn_perm(swap1(xq), xq, psel);
    }
#pragma unroll
    for (int j = 0; j < 2; j++) {
      const u32 rh = swap2(up ? ph[j] : ph[2 + j]), kh = up ? ph[2 + j] : ph[j];
      hp[2 * j] = up ? rh : kh; hp[2 * j + 1] = up ? kh : rh;
      const u32 rx = swap2(up ? px[j] : px[2 + j]), kx = up ? px[2 + j] : px[j];
      xv[2 * j] = up ? rx : kx; xv[2 * j + 1] = up ? kx : rx;
    }
    char* u = smem + B_U + cc * B_UCH + 1024 + g8 * 16;
    *(u32x4*)(u) = hp;
    *(u32x2*)(u + B_US + 8) = u32x2{hp[0], hp[1]};
    *(u32x2*)(u + B_US + 16) = u32x2{hp[2], hp[3]};
  }
#pragma unroll 1
  for (int it = 0; it < SPIN_CAP && !all_lanes(mc[0][1] == tag && mc[1][1] == tag && mc[2][1] == tag && mc[3][1] == tag); it++) {
    lookc();
    wait_vm<0>(); tie(mc[0]); tie(mc[1]); tie(mc[2]); tie(mc[3]);
  }
  {
    const float sc = 1.f + rbf((u2f(mc[0][0]) + u2f(mc[2][0])) + (u2f(mc[1][0]) + u2f(mc[3][0])));
    const u32 e = pk(sc, sc) & ((tid & 1) ? 0xffff0000u : 0xffffu);
    *(u32*)(smem + B_SC + tid * 4) = e;
    *(u32*)(smem + B_SC + 2048 + tid * 4) = e;
  }
  TS(10);
  lds_barrier();
  TS(11);
  const char* tb = smem + B_SC + rot * 256 + h * 32;
  u32x4 tq[3][2];
  auto ldt = [&](int i) NOPK { tq[i % 3][0] = *(const u32x4*)(tb + i * 64); tq[i % 3][1] = *(const u32x4*)(tb + i * 64 + 16); };
  auto scl = [&](int i) NOPK { tie(A[i]); scale8(A[i], tq[i % 3][0], tq[i % 3][1], w); tie(A[i]); };
  auto epi = [&](int yb) NOPK {
    const int gl = g8 & 127;
    const float* Y = (const float*)(smem + yb) + cc * 1280 + 8 * gl + 8 * (gl >> 2);
    const f32x4 ya = (*(const f32x4*)Y + *(const f32x4*)(Y + 2560)) + (*(const f32x4*)(Y + 5120) + *(const f32x4*)(Y + 7680));
    const f32x4 yb4 = (*(const f32x4*)(Y + 4) + *(const f32x4*)(Y + 2564)) + (*(const f32x4*)(Y + 5124) + *(const f32x4*)(Y + 7684));
    const float dk = cc ? bfhi(dsk) : bflo(dsk);
    u32x4 o;
#pragma unroll
    for (int j = 0; j < 4; j++) {
      const float y0 = j < 2 ? ya[2 * j] : yb4[2 * j - 4], y1 = j < 2 ? ya[2 * j + 1] : yb4[2 * j - 3];
      o[j] = pk(fmaf(ga, fmaf(bflo(hp[j]), dk, y0), bflo(xv[j])), fmaf(ga, fmaf(bfhi(hp[j]), dk, y1), bfhi(xv[j])));
    }
    st16(ws + WS_X1 + p * 128 + (w >> 1) * 32768, ((g8 >> 2) & 15) * 131072 + cc * 64 + (g8 & 3) * 16, o);
  };
  constexpr int LBT = LBC, NS2 = 51 / SCR + 1;
  {
    const int ch = w & 1, nq = w >> 1;
    const char* pa = smem + B_KC + ch * B_KCH + (3 - (r & 3)) * B_KCS + 2 * (2040 - 8 * (r >> 2) + 8 * g) - 512 * 7 - 64 * (2 * nq + 1);
    const char* pb = smem + B_U + ch * B_UCH + ((r & 1) ? 0 : B_US) + 2 * (512 + 32 * (r >> 1) + 8 * g) - 64 * (2 * nq + 1);
    const int yo = w * 5120 + 160 * (r >> 1) + 16 * (r & 1) + 32 * g;
    f32x4 acc[4];
    u32x4 la[2][4], lb[2][4], hf[4];
    auto ldhi = [&](int q) NOPK {
      const int e = 4 + ((q & 7) >> 1);
      hf[q & 3] = *(const u32x4*)(((q & 1) ? pa + 512 * (7 - e) : pb + 512 * e) + 64 * (1 - (q >> 3)));
    };
#pragma unroll
    for (int vv = 0; vv < 2; vv++)
#pragma unroll
      for (int n = 0; n < 8; n++) {
        const int e = (0x30212310 >> (4 * n)) & 3;
        if ((0xb1 >> n) & 1) la[vv][e] = *(const u32x4*)(pa + 512 * (7 - e) + 64 * (1 - vv));
        else lb[vv][e] = *(const u32x4*)(pb + 512 * e + 64 * (1 - vv));
      }
    __builtin_amdgcn_sched_barrier(0);
    {
      f32x4 sums = {0.f, 0.f, 0.f, 0.f};
#pragma unroll
      for (int j = 0; j < 4; j++) {
        const float k0 = rdl(mv1, 2 * j), k1 = rdl(mv1, 2 * j + 1), s0 = rdl(mv, 8 + 2 * j), s1 = rdl(mv, 8 + 2 * j + 1);
        const float a0 = bflo(v[2 * j]), a1 = bflo(v[2 * j + 1]), b0 = bfhi(v[2 * j]), b1 = bfhi(v[2 * j + 1]);
        sums[0] = dot2(pk(a0 * k0, a1 * k1), 0x3f803f80u, sums[0]);
        sums[1] = dot2(pk(b0 * k0, b1 * k1), 0x3f803f80u, sums[1]);
        sums[2] = fmaf(a0, s0, fmaf(a1, s1, sums[2]));
        sums[3] = fmaf(b0, s0, fmaf(b1, s1, sums[3]));
      }
      *(f32x4*)(smem + B_RED + tid * 16) = sums;
    }
#pragma unroll
    for (int i = 0; i < 4; i++) acc[i] = f32x4{0.f, 0.f, 0.f, 0.f};
    wait_vm<0>(); tie(fw);
    spin4(fw, tag, ws + WS_FW, fwo);
    TS(46);
    __builtin_amdgcn_sched_barrier(0);
#pragma unroll
    for (int n = 0; n < 20; n++) {
      const int vv = n / 10, k = n % 10, e = (0x0231211000ll >> (4 * k)) & 3, u = (0x0000112231ll >> (4 * k)) & 3;
      acc[e + u] = mfma16(la[vv][e], lb[vv][u], acc[e + u]);
      if (n % 3 == 0 && n / 3 < LBC) lda(n / 3);
      if (n >= 12 && n < 15) ldhi(n - 12);
      __builtin_amdgcn_sched_barrier(0);
    }
    TS(12);
#pragma unroll
    for (int i = 0; i < 4; i++) *(f32x4*)(smem + B_Y1 + yo + 1280 * i) = acc[i];
    lds_barrier();
    TS(13);
    TSF(7, 13); TSF(46, 47);
    int w_ = w;
    asm volatile("" : "+s"(w_));
    if (w_ < 4) {
      __builtin_amdgcn_s_setprio(3);
      epi(B_Y1);
      __builtin_amdgcn_s_setprio(0);
    }
    asm volatile("" : "+s"(w_));
    if (w_ == 0) {
      f32x4 s = {0.f, 0.f, 0.f, 0.f};
#pragma unroll
      for (int q = 0; q < 8; q++) s += *(const f32x4*)(smem + B_RED + q * 1024 + lane * 16);
      st16(ws + WS_CD + ((dq * 16 + fb) * 2 + mat) * 1024, lane * 16, __builtin_bit_cast(u32x4, s));
      st8m<1>(ws + WS_GB + p * 8, 0, u32x2{f2u(rdl(mv, 22)), f2u(rdl(mv, 23))});
    }
    TS(14);
    ldt(0);
#pragma unroll
    for (int n = 0; n < 52; n++) {
      const int vv = n / 26, k = n % 26;
      const int e = (k < 13 ? 0x2210144443210ll >> (4 * k) : 0x7036631023555ll >> (4 * (k - 13))) & 7;
      const int u = (k < 13 ? 0x2555332104444ll >> (4 * k) : 0x0731026631210ll >> (4 * (k - 13))) & 7;
      const int q = vv * 8 + (e > 3 ? 2 * e - 7 : 2 * u - 8);
      if (n == NF1) {
        asm volatile("" : "+s"(w_));
        if (w_ < 4) {
          wait_vmn((n + SCR - 1) / SCR);
          if (w_ == 0) wait_vm<0>();
          if (arrive_last(smem + L_CNT + 8, 4)) st4one(ws + WS_FX + (b & 63) * 128 + (b >> 6) * 4, tag);
        }
        TS(15);
      }
      if (k == 0 || k == 4 || k == 8 || k == 12 || k == 16 || k == 20 || k == 23 || k == 25) {
        const int qn = vv * 8 + (k < 23 ? k / 4 : k == 23 ? 6 : 7) + 3;
        if (qn < 16) ldhi(qn);
      }
      if (n % SCR == 0) ldt(n / SCR + 1);
      __builtin_amdgcn_sched_barrier(0);
      acc[e + u - 4] = mfma16(e > 3 ? hf[q & 3] : la[vv][e], u > 3 ? hf[q & 3] : lb[vv][u], n < 4 ? f32x4{0.f, 0.f, 0.f, 0.f} : acc[e + u - 4]);
      if (n % SCR == 0) { wait_vm<LBT - 1>(); lda(LBT + n / SCR); scl(n / SCR); }
      __builtin_amdgcn_sched_barrier(0);
    }
    TS(16);
#pragma unroll
    for (int i = 0; i < 4; i++) *(f32x4*)(smem + B_Y2 + yo + 1280 * i) = acc[i];
    ldt(NS2 + 1);
    lds_barrier();
    TS(42);
    asm volatile("" : "+s"(w_));
    if (w_ >= 4) {
      __builtin_amdgcn_s_setprio(3);
      epi(B_Y2);
      __builtin_amdgcn_s_setprio(0);
    }
    TS(43);
    asm volatile("" : "+s"(w_));
    if (w_ != 0) {
#pragma unroll
      for (int i = NS2; i < 32; i++) {
        if (i + 2 < 32) ldt(i + 2);
        wait_vmn(i + LBT < 32 ? LBT - 1 : 31 - i);
        if (i + LBT < 32) lda(i + LBT);
        __builtin_amdgcn_sched_barrier(0);
        scl(i);
        __builtin_amdgcn_sched_barrier(0);
      }
    }
    wait_vm<0>();
  }
  f32x2* raw = (f32x2*)(smem + C_RAW);
  u32x4 W2x[12];
  u32 fh;
  const u32 lds0 = (u32)(size_t)(LDS_AS char*)smem;
  const char* xa = ws + WS_X1 + tt * 131072 + ((w + rot) & 7) * 4096;
  const u32 lpc = lds0 + w * 4096;
  const int rt2 = dt + 2 * (idx >> 3), rot2 = rt2 < 7 ? rt2 : rt2 - 7;
#define kof(j) ((j) + rot2 < 7 ? (j) + rot2 : (j) + rot2 - 7)
  const char* w2b = ws + WS_W2F + dt * 229376 + w * 28672;
  const char* fhb = ws + WS_FH + tt2 * 2048 + w * 256;
  const u32 fho = ((lane >> 3) & 1) * 128 + g * 32 + (lane & 7) * 4;
  const char* fpb = ws + WS_FH + (tt * 16 + fs) * 128 + w * 4;
  auto ldw2 = [&](int j) NOPK {
    const char* q = w2b + kof(j >> 2) * 4096;
    if (j < 12) ld16k(W2x[j], q, j & 3, ao); else ld16k(A[j - 12], q, j & 3, ao);
  };
  auto poll_x = [&](int part) NOPK {
#pragma unroll 1
    for (int it = 0; it < SPIN_CAP; it++) {
      u32x4 f;
      ld16(f, ws + WS_FX + part * 8192, lane * 128); wait_vm<0>(); tie(f);
      if (all_lanes(f[0] == tag && f[1] == tag && f[2] == tag && f[3] == tag)) break;
    }
  };
  if (w == 0) {
    __builtin_amdgcn_s_setprio(3);
    poll_x(0);
    TS(17);
#pragma unroll
    for (int q = 0; q < 8; q++) glds4k(ws + WS_X1 + tt * 131072 + ((q + rot) & 7) * 4096, ao, lds0 + q * 4096);
    const char* rb = smem + (lane >> 2) * 64 + (lane & 3) * 8;
    wait_vm<0>();
    TS(18);
    bar();
    TS(19); while (__builtin_amdgcn_s_memrealtime() - t00_ < 2000) __builtin_amdgcn_s_sleep(1); TC(20);
    TSF(14, 20); TSF(42, 43);
    st4one(ws + WS_FX + 8192 + (b & 63) * 128 + (b >> 6) * 4, tag);
    glds4k(xa + 32768, ao, lpc + 32768);
    if (b == 0) st4one(ws + WS_E, tag);
    st4one(fpb, tag); st4one(fpb + 32, tag);
#pragma unroll
    for (int i = 0; i < 14; i++) ldw2(i);
    ln2_tile(rb, raw, raw + 132, lane);
    TS(21);
    poll_x(1);
    lds_barrier();
    TS(22);
    glds4k(xa + 65536, ao, lpc + 65536); glds4k(xa + 98304, ao, lpc + 98304);
#pragma unroll
    for (int q = 0; q < 4; q++) {
      const char* xr_ = ws + WS_X1 + (tt2 * 4 + q) * 32768 + dt * 4096 + sel * 32;
      const u32 xo = r * 256 + h * 64 + (g & 1) * 16, la = lds0 + D_RES + q * 2048;
      glds1k<0>(xr_, xo, la); glds1k<0>(xr_ + 128, xo, la + 1024);
    }
    asm volatile("s_mov_b32 m0, %2\n\ts_nop 0\n\tglobal_load_lds_dword %0, %1 sc1"
                 ::"v"(lane * 4), "s"(ws + WS_GB + dt * 256), "s"(lds0 + D_GB) : "memory");
    ln2_tile(rb + 32768, raw + 32, raw + 132, lane);
    TS(23);
    wait_vm<0>(); lds_barrier();
    TS(24);
#pragma unroll
    for (int i = 14; i < 28; i++) ldw2(i);
    rb = far(rb + 65536);
    ln2_tile(rb, raw + 64, raw + 132, lane);
    asm volatile("" ::: "memory");
    *(volatile LDS_AS u32*)(smem + L_CNT + 16) = 1;
    TS(25);
    ln2_tile(rb + 32768, raw + 96, raw + 132, lane);
    asm volatile("" ::: "memory");
    *(volatile LDS_AS u32*)(smem + L_CNT + 16) = 2;
    __builtin_amdgcn_s_setprio(0);
    TS(26); TS(27); TC(28);
    TSF(21, 28);
    ld4(fh, fhb, fho);
    wait_vm<0>(); tie(fh);
#pragma unroll
    for (int i = 0; i < 28; i++) { if (i < 12) tie(W2x[i]); else tie(A[i - 12]); }
    TS(29);
  }
  int w_ = w;
  asm volatile("" : "+s"(w_));
  if (w_ != 0) {
    const char* rb = smem + h * 512 + ((lane >> 2) & 3) * 64 + ((lane >> 4) & 1) * 32 + (lane & 3) * 8;
    const char* rb2 = far(rb + 65536);
    u32 cdv[8];
    f32x16 acc[2], zero;
#pragma unroll
    for (int i = 0; i < 16; i++) zero[i] = 0.f;
    TS(17);
    lds_barrier();
    TS(18);
    TS(19); while (__builtin_amdgcn_s_memrealtime() - t00_ < 2000) __builtin_amdgcn_s_sleep(1); TC(20);
    TSF(14, 20); TSF(42, 43);
    __builtin_amdgcn_s_setprio(3);
    u32x4 Bq[3], Ax, op;
    Bq[0] = tr8(rb); Bq[1] = tr8(rb + 1024);
    f32x2 rw;
    Ep q[2];
    float ev, gu, oa;
    const char* hb = ws + WS_HF + (size_t)(tt * 8 * 56 + (ft >> 1)) * 1024 + (ft & 1) * 512;
    const u32 ho = (c >> 4) * 57344 + h * 256 + r * 16;
    auto stat_wait = [&](u32 need) NOPK {
#pragma unroll 1
      for (int it = 0; it < SPIN_CAP * 16; it++)
        if (__builtin_amdgcn_readfirstlane(*(volatile LDS_AS u32*)(smem + L_CNT + 16)) >= need) break;
      asm volatile("" ::: "memory");
    };
    auto tile = [&](auto tc) NOPK {
    constexpr int T = decltype(tc)::v, E = T - 1;
#pragma unroll
    for (int ks = 0; ks < 32; ks++) {
      const int n = T * 32 + ks, m = n + 2;
      if (n == 30) {
        TS(21);
        wait_vm<8>();
#pragma unroll
        for (int i = 0; i < 8; i++) tie(cdv[i]);
        lds_barrier();
        TS(22);
      } else if (n == 62) {
        TS(23);
        wait_vm<5>();
        lds_barrier();
        TS(24);
      }
      if (n == 30 || n == 62) __builtin_amdgcn_s_setprio(3);
      if (n == 14 || n == 46 || n == 100) __builtin_amdgcn_s_setprio(2);
      if (n == 22 || n == 54 || n == 118) __builtin_amdgcn_s_setprio(1);
      if (n == 27 || n == 59) __builtin_amdgcn_s_setprio(0);
      if (n == 96) { TS(25); tie(acc[0]); stat_wait(1); }
      if (n == 118) { tie(acc[1]); stat_wait(2); }
      if (n == 120) wait_vm<10>();
      if (m < 128) Bq[m % 3] = tr8((m < 64 ? rb : rb2) + (m & 63) * 1024);
      if (T > 0 && ks == 0) rw = raw[E * 32 + c];
      if (n == 118) rw = raw[96 + c];
      if (n >= 120) A[n - 112] = *(const u32x4*)(smem + ((n >> 2) & 1) * 32768 + w * 4096 + (n & 3) * 1024 + lane * 16);
      __builtin_amdgcn_sched_barrier(0);
      acc[T & 1] = mfma32(A[ks], Bq[n % 3], ks == 0 ? zero : acc[T & 1]);
      if (n == 31) {
        const float cd = ((u2f(cdv[0]) + u2f(cdv[1])) + (u2f(cdv[2]) + u2f(cdv[3]))) + ((u2f(cdv[4]) + u2f(cdv[5])) + (u2f(cdv[6]) + u2f(cdv[7])));
        const float chi = rbf(cd);
        Ax = u32x4{pk(chi, chi), pk(cd - chi, cd - chi), 0, 0};
      }
      if ((T > 0 && ks == 2) || n == 120) {
        const int X = (n == 120 ? 3 : E) & 1;
        const u32 be = ln2_fold(rw, h, q[X]);
        acc[X] = mfma32(Ax, u32x4{be, be, 0, 0}, acc[X]);
      }
      if (T > 0 && ks >= 4 && ks < 20) {
        const int k = (ks - 4) >> 1, i = (k >> 1) + 4 * (k & 1);
        if ((ks & 1) == 0) {
          ev = fmaf(__builtin_amdgcn_exp2f(accg(acc[E & 1], i) * q[E & 1].q0), q[E & 1].v, q[E & 1].v);
          gu = accg(acc[E & 1], i) * accu(acc[E & 1], i);
        } else {
          const float o = gu * __builtin_amdgcn_rcpf(ev);
          if (k & 1) op[k >> 1] = pk(oa, o); else oa = o;
        }
      }
      __builtin_amdgcn_sched_barrier(0);
      if (n == 1) {
        const u32 co = ((c >> 3) & 1) * 1024 + ((w - 1) * 8 + (c & 7)) * 16 + h * 8 + (c >> 4) * 4;
#pragma unroll
        for (int i = 0; i < 8; i++) ld4(cdv[i], ws + WS_CD + (i * 16 + fs) * 2048, co);
      }
      if (n == 0) glds4k(xa + 32768, ao, lpc + 32768);
      if (T == 0 && ks >= 4 && ks < 28 && ks % 3 == 1) ldw2((ks - 4) / 3);
      if (T == 1 && ks < 16 && (ks & 1)) {
        const int i = ks >> 1, o = 65536 + (i >> 2) * 32768;
        if ((i & 3) == 0) glds1k<0>(xa + o, ao, lpc + o); else if ((i & 3) == 1) glds1k<1024>(xa + o, ao, lpc + o);
        else if ((i & 3) == 2) glds1k<2048>(xa + o, ao, lpc + o); else glds1k<3072>(xa + o, ao, lpc + o);
      }
      if (T == 1 && (ks == 18 || ks == 22 || ks == 24 || ks == 26)) ldw2(ks == 18 ? 8 : ks / 2 - 2);
      if (T > 1 && ks == 9) { wait_vm<T == 2 ? 6 : 4>(); st4one(fpb + (T - 2) * 32, tag); }
      if (T > 0 && ks == 20) st16(hb + E * 114688, ho, op);
      if (T > 1 && ks % 3 == 1 && ks < 24) {
        const int i = ks / 3;
        const char* q = w2b + kof(5 + (i >> 2)) * 4096;
        const u32 la = lpc + (i >> 2) * 32768;
        if (T == 3) ldw2(12 + i);
        else if ((i & 3) == 0) glds1k<0>(q, ao, la); else if ((i & 3) == 1) glds1k<1024>(q, ao, la);
        else if ((i & 3) == 2) glds1k<2048>(q, ao, la); else glds1k<3072>(q, ao, la);
      }
      if (n == 122) ld4(fh, fhb, fho);
    }
    };
    tile(IC<0>{}); tile(IC<1>{}); tile(IC<2>{}); tile(IC<3>{});
    TS(26); TS(27); TC(28);
    TSF(21, 28);
#pragma unroll
    for (int i = 0; i < 4; i++) op[i] = pk(swi(acc[1], i, q[1]), swi(acc[1], i + 4, q[1]));
    st16(hb + 3 * 114688, ho, op);
    __builtin_amdgcn_s_setprio(0);
    wait_vm<1>(); tie(fh);
#pragma unroll
    for (int i = 0; i < 20; i++) { if (i < 12) tie(W2x[i]); else tie(A[i - 12]); }
    TS(29);
  }
  {
    const char* hq = ws + WS_HF + (size_t)((tt2 * 8 + sel) * 56 + w * 7) * 1024;
    u32x4 H[3][7];
    auto need = [&](unsigned long long mask) NOPK {
#pragma unroll 1
      for (int it = 0; it < SPIN_CAP && (__builtin_amdgcn_ballot_w64(fh != tag) & mask) != 0; it++) {
        __builtin_amdgcn_s_sleep(2);
        ld4(fh, fhb, fho); wait_vm<0>(); tie(fh);
      }
    };
    auto ldh = [&](int q) NOPK {
#pragma unroll
      for (int j = 0; j < 7; j++) ld16(H[q % 3][j], hq + q * 114688 + kof(j) * 1024, ao);
    };
    char* wr = smem + w * 4096 + r * 256;
    auto quarter = [&](int q, int yng) NOPK {
      f32x4 acc[4];
#pragma unroll
      for (int j = 0; j < 7; j++) {
        __builtin_amdgcn_sched_barrier(0);
        if (yng == 0) { if (j == 0) wait_vm<5>(); else if (j == 2) wait_vm<3>(); else if (j == 4) wait_vm<1>(); else if (j == 6) wait_vm<0>(); }
        if (yng == 8 && j == 0) wait_vm<8>();
        tie(H[q % 3][j]);
#pragma unroll
        for (int ds = 0; ds < 4; ds++) {
          const int f = 4 * j + ds;
          acc[ds] = mfma16(f < 12 ? W2x[f] : A[f - 12], H[q % 3][j], j == 0 ? f32x4{0.f, 0.f, 0.f, 0.f} : acc[ds]);
        }
      }
      __builtin_amdgcn_sched_barrier(0);
#pragma unroll
      for (int ds = 0; ds < 4; ds++) *(f32x4*)(wr + q * 32768 + (((ds * 4 + g) ^ r) * 16)) = acc[ds];
    };
    const int tk = (tid >> 4) & 15, qh = w >> 2;
    u32 xq[2][4];
    f32x4 gbv;
    auto finish = [&](int qp) NOPK {
      const char* rp = smem + (2 * qp + qh) * 32768 + tk * 256 + ((r ^ tk) * 16);
      f32x4 s = *(const f32x4*)rp;
#pragma unroll
      for (int i = 1; i < 8; i++) s += *(const f32x4*)(rp + i * 4096);
      float y[4];
#pragma unroll
      for (int j = 0; j < 4; j++) y[j] = fmaf(gbv[j], s[j], bflo(xq[qp][j]));
      asm volatile("global_store_dwordx2 %0, %1, %2 sc1" ::"v"((tk * D + r * 4) * 2), "v"(u32x2{pk(y[0], y[1]), pk(y[2], y[3])}),
                   "s"(out + (size_t)((2 * qp + qh) * 512 + tt2 * 32 + sel * 16) * D + dt * 64) : "memory");
    };
    st4one(fpb + 64, tag);
    need(0xffffull);
    ldh(0);
    TS(30);
    {
      const char* xp = smem + D_RES + qh * 2048 + (tk >> 3) * 256 + r * 16 + (tk & 7) * 2;
#pragma unroll
      for (int i = 0; i < 8; i++) xq[i >> 2][i & 3] = *(const u16*)(xp + (i >> 2) * 4096 + (i & 3) * 512);
      gbv = *(const f32x4*)(smem + D_GB + r * 16);
    }
    wait_vm<8>();
    st4one(fpb + 96, tag);
    TS(31);
    need(0xffffull << 16);
    ld4(fh, fhb, fho);
    ldh(1);
    wait_vm<7>(); tie(fh);
    TS(32);
    need(0xffffull << 32);
    ld4(fh, fhb, fho);
    ldh(2);
    quarter(0, -1);
    TS(33);
    wait_vm<7>(); tie(fh);
    need(0xffffull << 48);
    TS(34);
    ldh(3);
    quarter(1, -1);
    TS(35);
    lds_barrier();
    finish(0);
    asm volatile("" ::: "memory");
    TS(36);
    TSF(29, 36);
    quarter(2, 8);
    TS(37);
    quarter(3, 0);
    TS(38);
    lds_barrier();
    asm volatile(".Lpf1:");
    finish(1);
    TS(39);
    TS(40);
    TSF(37, 40);
    TSEND();
  }
}

extern "C" void kernel_launch(void* const* d_in, const int* in_sizes, int n_in, void* d_out, int out_size, void* d_ws,
                              size_t ws_size, hipStream_t stream) {
  const u16* const* in = (const u16* const*)d_in;
  fused<<<256, 512, 0, stream>>>(in[0], in[1], in[2], in[3], in[4], in[5], in[6], in[7], in[8], in[10], in[11], in[12], in[13], in[14],
                                 (char*)d_ws, (u16*)d_out);
}
```

```cpp
#include <hip/hip_runtime.h>
#include <stdint.h>

typedef unsigned int u32;
typedef unsigned short u16;
typedef __attribute__((ext_vector_type(2))) float f32x2;
typedef __attribute__((ext_vector_type(4))) float f32x4;
typedef __attribute__((ext_vector_type(16))) float f32x16;
typedef __attribute__((ext_vector_type(2))) u32 u32x2;
typedef __attribute__((ext_vector_type(4))) u32 u32x4;
typedef __attribute__((ext_vector_type(2))) __bf16 bf16x2;
typedef __attribute__((ext_vector_type(8))) __bf16 bf16x8;
typedef __attribute__((ext_vector_type(4))) short s16x4;
#define LDS_AS __attribute__((address_space(3)))
#if defined(__HIP_DEVICE_COMPILE__)
#define NOPK __attribute__((target("no-packed-fp32-ops")))
#else
#define NOPK
#endif
#define DEVINL __device__ __forceinline__ NOPK

constexpr int L = 2048, D = 512, NS = 128, F = 1792;

constexpr size_t WS_E = 0;
constexpr size_t WS_FW = 4096;
constexpr size_t WS_FX = WS_FW + 32768;
constexpr size_t WS_FH = WS_FX + 32768;
constexpr size_t WS_MG = WS_FH + 65536;
constexpr size_t WS_GB = WS_MG + 98304;
constexpr size_t WS_XG = 262144;
constexpr size_t WS_X1 = WS_XG + (4u << 20);
constexpr size_t WS_W1F = WS_X1 + (2u << 20);
constexpr size_t WS_W2F = WS_W1F + 112 * 32 * 1024;
constexpr size_t WS_HF = WS_W2F + 16 * 112 * 1024;
constexpr size_t WS_CD = WS_HF + 64 * 112 * 1024;
constexpr int SPIN_CAP = 40000;
#define VP 0
#define LX 1
#define GP 1
#define RP 1
#define SCR 3
#define LBC 6
#define NF1 24
#define PFM 3
#define PFB 32768

#define u2f(x) __builtin_bit_cast(float, (u32)(x))
#define i2f(x) __builtin_bit_cast(float, (int)(x))
#define f2u(x) __builtin_bit_cast(u32, (float)(x))
#define f2i(x) __builtin_bit_cast(int, (float)(x))
#define TSDECL
#define TS(slot)
#define TC(slot)
#define TSF(lo, hi)
#define TSEND()
DEVINL float bflo(u32 u) { return u2f(u << 16); }
DEVINL float bfhi(u32 u) { return u2f(u & 0xffff0000u); }
DEVINL u32 pk(float a, float b) {
  f32x2 v = {a, b};
  return __builtin_bit_cast(u32, __builtin_convertvector(v, bf16x2));
}
DEVINL float rbf(float a) { return bflo(pk(a, a)); }
template <int CTRL> DEVINL float dpp_add(float v) {
  return v + i2f(__builtin_amdgcn_update_dpp(0, f2i(v), CTRL, 0xf, 0xf, true));
}
DEVINL float row4_sum(float v) {
  auto a = __builtin_amdgcn_permlane16_swap(f2u(v), f2u(v), false, false);
  v = u2f(a[0]) + u2f(a[1]);
  auto b = __builtin_amdgcn_permlane32_swap(f2u(v), f2u(v), false, false);
  return u2f(b[0]) + u2f(b[1]);
}
DEVINL float rs32(float a, float b) {
  auto r = __builtin_amdgcn_permlane32_swap(f2u(a), f2u(b), false, false);
  return u2f(r[0]) + u2f(r[1]);
}
DEVINL float rs16(float a, float b) {
  auto r = __builtin_amdgcn_permlane16_swap(f2u(a), f2u(b), false, false);
  return u2f(r[0]) + u2f(r[1]);
}
DEVINL float wave_sum(float v) { return row4_sum(dpp_add<0x140>(dpp_add<0x141>(dpp_add<0x4E>(dpp_add<0xB1>(v))))); }
DEVINL f32x4 mfma16(u32x4 a, u32x4 b, f32x4 c) {
  return __builtin_amdgcn_mfma_f32_16x16x32_bf16(__builtin_bit_cast(bf16x8, a), __builtin_bit_cast(bf16x8, b), c, 0, 0, 0);
}
DEVINL f32x16 mfma32(u32x4 a, u32x4 b, f32x16 c) {
  return __builtin_amdgcn_mfma_f32_32x32x16_bf16(__builtin_bit_cast(bf16x8, a), __builtin_bit_cast(bf16x8, b), c, 0, 0, 0);
}
DEVINL float dot2(u32 a, u32 b, float c) {
  return __builtin_amdgcn_fdot2_f32_bf16(__builtin_bit_cast(bf16x2, a), __builtin_bit_cast(bf16x2, b), c, false);
}
DEVINL void lds_barrier() {
  asm volatile("s_waitcnt lgkmcnt(0)" ::: "memory");
  __builtin_amdgcn_s_barrier();
  asm volatile("" ::: "memory");
}
DEVINL void bar() {
  asm volatile("" ::: "memory");
  __builtin_amdgcn_s_barrier();
  asm volatile("" ::: "memory");
}
DEVINL void st16(const void* sbase, u32 voff, u32x4 v) {
  asm volatile("global_store_dwordx4 %0, %1, %2 sc1\n\ts_nop 1" ::"v"(voff), "v"(v), "s"(sbase) : "memory");
}
DEVINL void st8(const void* sbase, u32 voff, u32x2 v) {
  asm volatile("global_store_dwordx2 %0, %1, %2 sc1\n\ts_nop 1" ::"v"(voff), "v"(v), "s"(sbase) : "memory");
}
DEVINL void st4(const void* sbase, u32 voff, u32 v) {
  asm volatile("global_store_dword %0, %1, %2 sc1\n\ts_nop 1" ::"v"(voff), "v"(v), "s"(sbase) : "memory");
}
template <int LANES> DEVINL void st8m(const void* sbase, u32 voff, u32x2 v) {
  asm volatile("s_mov_b64 exec, %3\n\tglobal_store_dwordx2 %0, %1, %2 sc1\n\ts_mov_b64 exec, -1\n\ts_nop 0"
               ::"v"(voff), "v"(v), "s"(sbase), "n"((1 << LANES) - 1) : "memory");
}
DEVINL void st4one(const void* sbase, u32 v) {
  asm volatile("s_mov_b64 exec, 1\n\tglobal_store_dword %0, %1, %2 sc1\n\ts_mov_b64 exec, -1\n\ts_nop 0"
               ::"v"(0), "v"(v), "s"(sbase) : "memory");
}
DEVINL void st16x2_56(const void* sbase, u32 voff, u32x4 a, u32x4 b) {
  asm volatile("s_mov_b32 exec_hi, 0xffffff\n\tglobal_store_dwordx4 %0, %1, %3 sc1\n\tglobal_store_dwordx4 %0, %2, %3 offset:256 sc1\n\t"
               "s_mov_b32 exec_hi, -1\n\ts_nop 0" ::"v"(voff), "v"(a), "v"(b), "s"(sbase) : "memory");
}
DEVINL void sld4(u32& d, const void* p) { asm volatile("s_load_dword %0, %1, 0" : "=s"(d) : "s"(p) : "memory"); }
template <int OFF = 0> DEVINL void ld16(u32x4& d, const void* sbase, u32 voff) {
  asm volatile("global_load_dwordx4 %0, %1, %2 offset:%3 sc1" : "=v"(d) : "v"(voff), "s"(sbase), "n"(OFF) : "memory");
}
DEVINL void ld16k(u32x4& d, const char* sbase, int kb, u32 voff) {
  const char* sb = sbase + (kb >> 2) * 4096;
  if ((kb & 3) == 0) ld16<0>(d, sb, voff); else if ((kb & 3) == 1) ld16<1024>(d, sb, voff);
  else if ((kb & 3) == 2) ld16<2048>(d, sb, voff); else ld16<3072>(d, sb, voff);
}
DEVINL void ld8(u32x2& d, const void* sbase, u32 voff) {
  asm volatile("global_load_dwordx2 %0, %1, %2 sc1" : "=v"(d) : "v"(voff), "s"(sbase) : "memory");
}
template <int OFF = 0> DEVINL void ld4(u32& d, const void* sbase, u32 voff) {
  asm volatile("global_load_dword %0, %1, %2 offset:%3 sc1" : "=v"(d) : "v"(voff), "s"(sbase), "n"(OFF) : "memory");
}
template <int OFF = 0> DEVINL void lp16(u32x4& d, const void* sbase, u32 voff) {
  asm volatile("global_load_dwordx4 %0, %1, %2 offset:%3" : "=v"(d) : "v"(voff), "s"(sbase), "n"(OFF) : "memory");
}
template <int OFF = 0> DEVINL void lp4(u32& d, const void* sbase, u32 voff) {
  asm volatile("global_load_dword %0, %1, %2 offset:%3" : "=v"(d) : "v"(voff), "s"(sbase), "n"(OFF) : "memory");
}
template <int OFF = 0> DEVINL void lp2(u32& d, const void* sbase, u32 voff) {
  asm volatile("global_load_ushort %0, %1, %2 offset:%3" : "=v"(d) : "v"(voff), "s"(sbase), "n"(OFF) : "memory");
}
template <class T> DEVINL void tie(T& a) { asm volatile("" : "+v"(a)); }
template <int N> DEVINL void wait_vm() { asm volatile("s_waitcnt vmcnt(%0)" ::"n"(N) : "memory"); }
#define wait_vmn(k) asm volatile("s_waitcnt vmcnt(%0)" ::"n"(k) : "memory")
DEVINL bool all_lanes(bool ok) { return __builtin_amdgcn_ballot_w64(!ok) == 0; }
DEVINL void spin4(u32 f, u32 tag, const void* sbase, u32 voff) {
#pragma unroll 1
  for (int it = 0; it < SPIN_CAP && !all_lanes(f == tag); it++) {
    __builtin_amdgcn_s_sleep(1);
    ld4(f, sbase, voff); wait_vm<0>(); tie(f);
  }
}
DEVINL bool arrive_last(char* smem_word, int n) {
  u32 old;
  asm volatile("s_mov_b64 exec, 1\n\tds_add_rtn_u32 %0, %1, %2\n\ts_waitcnt lgkmcnt(0)\n\ts_mov_b64 exec, -1"
               : "=&v"(old) : "v"((u32)(size_t)(LDS_AS char*)smem_word), "v"(1) : "memory");
  return __builtin_amdgcn_readfirstlane(old) == (u32)(n - 1);
}
DEVINL void scale8(u32x4& a, const u32x4& s, const u32x4& t, int on) {
  u32 a0 = a[0], a1 = a[1], a2 = a[2], a3 = a[3], x0, x1, x2, x3;
  asm("s_cmp_eq_u32 %16, 0\n\ts_cbranch_scc1 .Lsk%=\n\tv_dot2_f32_bf16 %4, %0, %8, 0\n\tv_dot2_f32_bf16 %0, %0, %9, 0\n\tv_dot2_f32_bf16 %5, %1, %10, 0\n\tv_dot2_f32_bf16 %1, %1, %11, 0\n\t"
      "v_dot2_f32_bf16 %6, %2, %12, 0\n\tv_dot2_f32_bf16 %2, %2, %13, 0\n\tv_dot2_f32_bf16 %7, %3, %14, 0\n\tv_dot2_f32_bf16 %3, %3, %15, 0\n\t"
      "v_cvt_pk_bf16_f32 %0, %4, %0\n\tv_cvt_pk_bf16_f32 %1, %5, %1\n\tv_cvt_pk_bf16_f32 %2, %6, %2\n\ts_nop 0\n\tv_cvt_pk_bf16_f32 %3, %7, %3\n.Lsk%=:"
      : "+v"(a0), "+v"(a1), "+v"(a2), "+v"(a3), "=&v"(x0), "=&v"(x1), "=&v"(x2), "=&v"(x3)
      : "v"(s[0]), "v"(s[1]), "v"(s[2]), "v"(s[3]), "v"(t[0]), "v"(t[1]), "v"(t[2]), "v"(t[3]), "s"(on) : "scc");
  a = u32x4{a0, a1, a2, a3};
}
DEVINL u32x2 tr_read(const char* p) {
  return __builtin_bit_cast(u32x2, __builtin_amdgcn_ds_read_tr16_b64_v4i16((LDS_AS s16x4*)p));
}
DEVINL float rdl(float v, int l) { return i2f(__builtin_amdgcn_readlane(f2i(v), l)); }
DEVINL float sel2(int c, float a, float b) { return c ? b : a; }
DEVINL u32 swap2(u32 v) { return (u32)__builtin_amdgcn_update_dpp(0, (int)v, 0x4E, 0xf, 0xf, true); }
DEVINL u32 swap1(u32 v) { return (u32)__builtin_amdgcn_update_dpp(0, (int)v, 0xB1, 0xf, 0xf, true); }
constexpr float LOG2E = 1.4426950408889634f, LN2 = 0.6931471805599453f, INV2PI = 0.15915494309189535f;

constexpr int A_TAB = 0, A_P = 8 * 1536, A_KC = A_P + 24576, A_KCS = 4112, A_GV = A_KC + 2 * A_KCS;
constexpr int B_Y1 = 0, B_RED = 40960, B_KC = 49152, B_KCS = 4160, B_KCH = 4 * 4160, B_U = B_KC + 2 * B_KCH, B_US = 5152, B_UCH = 2 * B_US;
constexpr int B_Y2 = B_U + 2 * B_UCH, A_XT = B_Y2;
constexpr int C_RAW = 148480, L_CNT = C_RAW + 1024, D_RES = L_CNT + 64, D_GB = D_RES + 8192, B_SC = D_GB + 256, LDS_BYTES = B_SC + 4096;
static_assert(A_GV + 2048 <= B_KC && B_Y2 + 40960 <= C_RAW && 131072 <= C_RAW && LDS_BYTES <= 163840, "lds map");

DEVINL void glds4k(const void* sbase, u32 voff, u32 ldsaddr) {
  asm volatile("s_mov_b32 m0, %2\n\ts_nop 0\n\t"
               "global_load_lds_dwordx4 %0, %1 sc1\n\tglobal_load_lds_dwordx4 %0, %1 offset:1024 sc1\n\t"
               "global_load_lds_dwordx4 %0, %1 offset:2048 sc1\n\tglobal_load_lds_dwordx4 %0, %1 offset:3072 sc1"
               ::"v"(voff), "s"(sbase), "s"(ldsaddr) : "memory");
}
DEVINL u32x4 tr8(const char* p) {
  const u32x2 lo = tr_read(p), hi = tr_read(p + 256);
  return u32x4{lo[0], lo[1], hi[0], hi[1]};
}
template <int OFF> DEVINL void glds1k(const void* sbase, u32 voff, u32 ldsaddr) {
  asm volatile("s_mov_b32 m0, %2\n\ts_nop 0\n\tglobal_load_lds_dwordx4 %0, %1 offset:%3 sc1" ::"v"(voff), "s"(sbase), "s"(ldsaddr), "n"(OFF) : "memory");
}
DEVINL const char* far(const char* p) {
  u32 a = (u32)(size_t)(LDS_AS const char*)p;
  asm("" : "+v"(a));
  return (const char*)(LDS_AS const char*)(size_t)a;
}
struct Ep { float q0, v; };
template <int N> struct IC { static constexpr int v = N; };
#define accg(a, i) a[((i) & 3) + 8 * ((i) >> 2)]
#define accu(a, i) a[((i) & 3) + 8 * ((i) >> 2) + 4]
DEVINL float swi(const f32x16& a, int i, const Ep& q) {
  const float e = __builtin_amdgcn_exp2f(accg(a, i) * q.q0);
  return (accg(a, i) * accu(a, i)) * __builtin_amdgcn_rcpf(fmaf(e, q.v, q.v));
}
DEVINL u32 ln2_fold(f32x2 s, int h, Ep& q) {
  const float m = s[0] * (1.f / D);
  const float v = fmaxf(fmaf(-m, m, s[1] * (1.f / D)), 0.f) + 1e-5f;
  const float rs = __builtin_amdgcn_rsqf(v);
  q.q0 = rs * -LOG2E; q.v = v;
  const float val = h ? v * rs : -m;
  const float vhi = rbf(val);
  return pk(vhi, val - vhi);
}
DEVINL void ln2_tile(const char* rb, f32x2* raw, f32x2* dummy, int lane) {
  const u32x4 ones = {0x3f803f80u, 0x3f803f80u, 0x3f803f80u, 0x3f803f80u};
  f32x4 sg[2], sx[2];
#pragma unroll
  for (int i = 0; i < 2; i++) { sg[i] = f32x4{0.f, 0.f, 0.f, 0.f}; sx[i] = f32x4{0.f, 0.f, 0.f, 0.f}; }
  u32x4 fr[4][2];
  auto rd = [&](int n) NOPK {
#pragma unroll
    for (int gi = 0; gi < 2; gi++) {
      const char* pp = rb + n * 2048 + gi * 32;
      const u32x2 lo = tr_read(pp), hi = tr_read(pp + 1024);
      fr[n & 3][gi] = u32x4{lo[0], lo[1], hi[0], hi[1]};
    }
  };
  rd(0); rd(1); rd(2);
#pragma unroll
  for (int n = 0; n < 16; n++) {
    if (n + 3 < 16) rd(n + 3);
    __builtin_amdgcn_sched_barrier(0);
#pragma unroll
    for (int gi = 0; gi < 2; gi++) {
      sg[gi] = mfma16(fr[n & 3][gi], fr[n & 3][gi], sg[gi]);
      sx[gi] = mfma16(ones, fr[n & 3][gi], sx[gi]);
    }
    __builtin_amdgcn_sched_barrier(0);
  }
#pragma unroll
  for (int gi = 0; gi < 2; gi++) {
    const float gsel = (lane & 2) ? ((lane & 1) ? sg[gi][3] : sg[gi][2]) : ((lane & 1) ? sg[gi][1] : sg[gi][0]);
    f32x2* dst = (lane >> 4) == ((lane & 15) >> 2) ? raw + gi * 16 + (lane & 15) : dummy;
    *dst = f32x2{sx[gi][0], gsel};
  }
}

__global__ NOPK __launch_bounds__(512, 2) void fused(
    const u16* __restrict__ x, const u16* __restrict__ Are, const u16* __restrict__ Aim, const u16* __restrict__ Bre,
    const u16* __restrict__ Bim, const u16* __restrict__ Cre, const u16* __restrict__ Cim, const u16* __restrict__ logdt,
    const u16* __restrict__ dskip, const u16* __restrict__ w1, const u16* __restrict__ w3, const u16* __restrict__ w2,
    const u16* __restrict__ cvec, const u16* __restrict__ wmod, char* ws, u16* __restrict__ out) {
  __shared__ __attribute__((aligned(16))) char smem[LDS_BYTES];
  const int b = blockIdx.x, tid = threadIdx.x, lane = tid & 63;
  const int w = __builtin_amdgcn_readfirstlane(tid >> 6);
  TSDECL; TS(0);
  const int xg = b & 7, idx = b >> 3;
  const int p = xg * 32 + idx;
  const int r = lane & 15, g = lane >> 4;
  asm volatile("" ::"s"(x), "s"(Are), "s"(Aim), "s"(Bre), "s"(Bim), "s"(Cre), "s"(Cim), "s"(logdt), "s"(dskip), "s"(w1), "s"(w3),
               "s"(w2), "s"(cvec), "s"(wmod), "s"(ws), "s"(out));
  u32x4 xr, wm0, wm1;
  u32 vare, vaim, vbre, vbim, vcre, vcim, c0, c1;
  const int cg = ((xg & 1) * 24 + (idx < 24 ? idx : 23)) * 4 + (xg >> 1);
  u32 e_prev, ldt2, dsk;
  lp16(xr, x + p * 4096, tid * 16);
  sld4(e_prev, ws + WS_E); sld4(ldt2, logdt + 2 * p); sld4(dsk, dskip + 2 * p);
  {
    const u32 so = (p * 256 + w * 32 + (lane & 31)) * 2;
    lp2(vare, Are, so); lp2(vaim, Aim, so); lp2(vbre, Bre, so); lp2(vbim, Bim, so); lp2(vcre, Cre, so); lp2(vcim, Cim, so);
    const u32 mo = (tid >> 3) * 6144 + (tid & 7) * 16;
    const u16* wmp = wmod + (cg & 3) * 128 * 3072 + (cg >> 2) * 64;
    lp16(wm0, wmp, mo); lp16(wm1, wmp + 64 * 3072, mo);
    lp2(c0, cvec + (cg & 3) * 128, (tid >> 3) * 2); lp2<128>(c1, cvec + (cg & 3) * 128, (tid >> 3) * 2);
  }
  u32 pf;
  {
    unsigned long long pc;
    u32 po = ((idx & PFM) * 512 + tid) * (PFB / (PFM + 1) / 512);
    asm volatile("s_getpc_b64 %1\n.Lpf0:\n\tv_min_u32 %2, .Lpf1-.Lpf0, %2\n\tglobal_load_dword %0, %2, %1"
                 : "=v"(pf), "=&s"(pc), "+v"(po)::"memory");
  }
  TS(1);
  if (tid < 32) *(u32x4*)(smem + B_KC + (tid >> 2) * B_KCS + 4096 + (tid & 3) * 16) = u32x4{0, 0, 0, 0};
  else if (tid < 36) *(u32x4*)(smem + L_CNT + (tid - 32) * 16) = u32x4{0, 0, 0, 0};
  else if (tid < 38) *(u32x2*)(smem + B_U + (tid & 1) * B_UCH + B_US + 1024) = u32x2{0, 0};
  else if (tid >= 256) *(u32x4*)(smem + B_U + (w - 4) * B_US + lane * 16) = u32x4{0, 0, 0, 0};
  asm volatile("s_waitcnt lgkmcnt(0)" : "+s"(e_prev), "+s"(ldt2), "+s"(dsk)::"memory");
  const u32 tag = e_prev * 1664525u + 1013904223u;
  wait_vm<11>(); tie(xr);
  {
    *(u32x4*)(smem + A_XT + w * 1056 + lane * 16) = xr;
    float f[8];
#pragma unroll
    for (int j = 0; j < 4; j++) { f[2 * j] = bflo(xr[j]); f[2 * j + 1] = bfhi(xr[j]); }
    const float m = wave_sum(((f[0] + f[1]) + (f[2] + f[3])) + ((f[4] + f[5]) + (f[6] + f[7]))) * (1.f / D);
    float s = 0.f;
#pragma unroll
    for (int j = 0; j < 8; j++) { f[j] -= m; s = fmaf(f[j], f[j], s); }
    const float sd = __builtin_amdgcn_sqrtf(rbf(rbf(wave_sum(s) * (1.f / D)) + 1e-5f));
    if (lane == 0) *(u32*)(smem + A_XT + w * 1056 + 1024) = pk(m, sd);
  }
  TS(2);
  const int fb = (xg >> 1) * 4 + (idx & 3), mat = (idx >> 2) & 1, dq = (xg & 1) * 4 + (idx >> 3), d0 = dq * 64 + w * 8;
  const int b2 = (idx < 28 ? (idx >> 2) * 32 : 192) + xg * 4 + (idx & 3);
  u32 v[8], t2[8];
  auto ldv = [&]() NOPK
  {
    const u16* wp = (mat ? w3 : w1) + (size_t)d0 * F + fb * 112;
    const u32 vo = (lane < 56 ? lane : 55) * 4;
#pragma unroll
    for (int i = 0; i < 4; i++) { lp4<0>(v[2 * i], wp + 2 * i * F, vo); lp4<2 * F>(v[2 * i + 1], wp + 2 * i * F, vo); }
#pragma unroll
    for (int i = 0; i < 2; i++) {
      const u16* wq = w2 + (size_t)(b2 * 8 + i * 4) * D;
      const u32 to = tid * 2;
      lp2<0>(t2[4 * i], wq, to); lp2<1024>(t2[4 * i + 1], wq, to); lp2<2048>(t2[4 * i + 2], wq, to); lp2<3072>(t2[4 * i + 3], wq, to);
    }
  };
  if (VP == 0) ldv();
  wait_vm<VP == 0 ? 21 : 5>(); tie(vare); tie(vaim); tie(vbre); tie(vbim); tie(vcre); tie(vcim);
  char* tab = smem + A_TAB + w * 1536;
  {
    const float dt = rbf(__builtin_amdgcn_exp2f((w >> 2 ? bfhi(ldt2) : bflo(ldt2)) * LOG2E));
    const float a = dt * bflo(vare) * LOG2E, bq = dt * bflo(vaim) * INV2PI;
    const float br = dt * bflo(vbre), bi = dt * bflo(vbim), cr = bflo(vcre), ci = bflo(vcim);
    const float m = lane < 32 ? 1.f : 256.f;
    const float e = __builtin_amdgcn_exp2f(a * m), ph = __builtin_amdgcn_fractf(-bq * m);
    const float ex = e * __builtin_amdgcn_cosf(ph), ey = e * __builtin_amdgcn_sinf(ph);
    char* t = tab + (lane & 31) * 48;
    *(f32x2*)(t + 16 + (lane >> 5) * 8) = f32x2{ex, ey};
    f32x4 o = {a, bq, cr * br - ci * bi, cr * bi + ci * br};
    if (lane >= 32) o = f32x4{ex, ey, ex * ex - ey * ey, (ex + ex) * ey};
    *(f32x4*)(t + (lane >> 5) * 32) = o;
  }
  if (VP == 1) ldv();
  TS(3);
  lds_barrier();
  {
    const char* xt = smem + A_XT + (lane & 7) * 1056;
    const u32 st = *(const u32*)(xt + 1024);
#pragma unroll
    for (int e = 0; e < 2; e++) {
      const u32x2 xq = *(const u32x2*)(xt + (e * 64 + w * 8 + (lane >> 3)) * 8);
      st16(ws + WS_XG + (size_t)(e * 64 + w * 8) * 32768 + p * 128, (lane >> 3) * 32768 + (lane & 7) * 16, u32x4{xq[0], xq[1], st, tag});
    }
  }
  TS(41);
  if (VP == 2) ldv();
  auto retile = [&]() NOPK {
    wait_vm<VP == 2 ? 0 : 2>();
#pragma unroll
    for (int i = 0; i < 8; i++) { tie(v[i]); tie(t2[i]); }
    u32x4 o0, o1, o2;
#pragma unroll
    for (int j = 0; j < 4; j++) {
      o0[j] = __builtin_amdgcn_perm(v[2 * j + 1], v[2 * j], 0x05040100u);
      o1[j] = __builtin_amdgcn_perm(v[2 * j + 1], v[2 * j], 0x07060302u);
      o2[j] = __builtin_amdgcn_perm(t2[2 * j + 1], t2[2 * j], 0x05040100u);
    }
    st16x2_56(ws + WS_W1F + (size_t)(fb * 7 * 32 + (d0 >> 4)) * 1024 + (w & 1) * 512 + mat * 128, (lane >> 3) * 32768 + (lane & 7) * 16, o0, o1);
    st16(ws + WS_W2F + w * 229376 + (b2 >> 2) * 4096 + (b2 & 3) * 256, g * 1024 + r * 16, o2);
  };
  auto gemv = [&]() NOPK
  {
    wait_vm<18>(); tie(pf); tie(wm0); tie(wm1); tie(c0); tie(c1);
    const float cx0 = bflo(c0), cx1 = bflo(c1);
    const float s0 = rbf(cx0 * __builtin_amdgcn_rcpf(1.f + __builtin_amdgcn_exp2f(cx0 * -LOG2E)));
    const float s1 = rbf(cx1 * __builtin_amdgcn_rcpf(1.f + __builtin_amdgcn_exp2f(cx1 * -LOG2E)));
    float acc[8];
#pragma unroll
    for (int j = 0; j < 4; j++) {
      acc[2 * j] = fmaf(s0, bflo(wm0[j]), s1 * bflo(wm1[j]));
      acc[2 * j + 1] = fmaf(s0, bfhi(wm0[j]), s1 * bfhi(wm1[j]));
    }
    const float q0 = dpp_add<0x128>(rs16(rs32(acc[0], acc[4]), rs32(acc[2], acc[6])));
    const float q1 = dpp_add<0x128>(rs16(rs32(acc[1], acc[5]), rs32(acc[3], acc[7])));
    *(float*)(smem + A_GV + w * 256 + (lane & 7) * 32 + (lane >> 3) * 4) = (lane & 8) ? q1 : q0;
    if (arrive_last(smem + L_CNT, 8) && idx < 24) {
      float s = 0.f;
#pragma unroll
      for (int i = 0; i < 8; i++) s += *(const float*)(smem + A_GV + i * 256 + lane * 4);
      st8(ws + WS_MG + (cg & 3) * 24576 + (cg >> 2) * 512, lane * 8, u32x2{f2u(s), tag});
    }
  };
  TS(4);
  if (GP == 0) { gemv(); TS(44); }
  const u32 gcode = __builtin_amdgcn_perm(0x400b0916u, 0x0a201408u, (r & 7) | 0x0c0c0c00u);
  const int gx = r < 8 ? 2 * r : (int)((gcode >> 2) * 256 + (gcode & 3) * 4);
  {
    asm volatile("" ::: "memory");
    const float mF = (float)(16 * r), mX = (float)gx;
    const char* tp = tab + g * 8 * 48;
    const char* tq = tp + (r < 8 ? 16 : 32);
    f32x4 acc[3];
#pragma unroll
    for (int s = 0; s < 3; s++) acc[s] = f32x4{0.f, 0.f, 0.f, 0.f};
#pragma unroll
    for (int ks = 0; ks < 2; ks++) {
      u32x4 Ff, Gf[3];
#pragma unroll
      for (int i = 0; i < 4; i++) {
        const f32x4 s0 = *(const f32x4*)(tp + (ks * 4 + i) * 48), s1 = *(const f32x4*)(tq + (ks * 4 + i) * 48);
        const float e0 = __builtin_amdgcn_exp2f(s0[0] * mF), p0 = __builtin_amdgcn_fractf(s0[1] * mF);
        const float zx = e0 * __builtin_amdgcn_cosf(p0), zy = e0 * __builtin_amdgcn_sinf(p0);
        Ff[i] = pk(s0[2] * zx - s0[3] * zy, s0[2] * zy + s0[3] * zx);
        const float e1 = __builtin_amdgcn_exp2f(s0[0] * mX), p1 = __builtin_amdgcn_fractf(s0[1] * -mX);
        const float xx = e1 * __builtin_amdgcn_cosf(p1), xy = e1 * __builtin_amdgcn_sinf(p1);
        Gf[0][i] = pk(xx, xy);
        Gf[1][i] = pk(xx * s1[0] - xy * s1[1], xx * s1[1] + xy * s1[0]);
        Gf[2][i] = pk(xx * s1[2] - xy * s1[3], xx * s1[3] + xy * s1[2]);
      }
#pragma unroll
      for (int s = 0; s < 3; s++) acc[s] = mfma16(Ff, Gf[s], acc[s]);
      if (ks == 0) { TS(45); if (RP == 0) retile(); if (GP == 1) { gemv(); TS(44); } }
    }
#pragma unroll
    for (int s = 0; s < 3; s++) *(f32x4*)(smem + A_P + w * 3072 + s * 1024 + lane * 16) = acc[s];
  }
  if (RP == 1) retile();
  TS(5);
  u32x4 xg4[4];
  u32x2 mg, mh, mc[4];
  const char* xgb = ws + WS_XG + (size_t)(p >> 1) * 32768;
  const u32 xgo = (tid >> 2) * 256 + (tid & 3) * 16;
  u32 mga = (4 - ((lane >> 3) & 1)) * 512 + d0 + (lane & 7), mgb = ((0x5210 >> ((lane & 6) * 2)) & 7) * 512 + 2 * p + (lane & 1);
  tie(mga); tie(mgb);
  const u32 mgo = ((lane & 16) ? mgb : mga) * 8 + (lane >> 5) * 24576;
  auto lookx = [&]() NOPK {
    ld16<0>(xg4[0], xgb, xgo); ld16<64>(xg4[1], xgb, xgo); ld16<128>(xg4[2], xgb, xgo); ld16<192>(xg4[3], xgb, xgo);
  };
  auto lookm = [&]() NOPK {
    ld8(mg, ws + WS_MG, mgo); ld8(mh, ws + WS_MG + 49152, mgo);
  };
  auto lookc = [&]() NOPK {
#pragma unroll
    for (int q = 0; q < 4; q++) ld8(mc[q], ws + WS_MG + q * 24576 + 16384, tid * 8);
  };
  if (LX == 0) lookx();
  lds_barrier();
  TS(6);
  TSF(0, 6); TSF(41, 41); TSF(44, 45); TSF(55, 56); TSF(58, 59);
  const int kch = tid >> 8;
  u16* Kc = (u16*)(smem + A_KC + kch * A_KCS);
  if ((w & 3) < 3) {
    const int s = w & 3;
    const char* pp = smem + A_P + kch * 12288 + s * 1024 + lane * 16;
    const f32x4 q = (*(const f32x4*)(pp) + *(const f32x4*)(pp + 3072)) + (*(const f32x4*)(pp + 6144) + *(const f32x4*)(pp + 9216));
    const int pos = gx + 64 * g + (r < 8 ? (s == 0 ? 0 : s == 1 ? 1 : 256) : s * 256);
    const u32 o0 = pk(q[0], q[1]), o1 = pk(q[2], q[3]);
    if (pos <= 2048) Kc[pos] = (u16)o0;
    if (pos < 2048) {
      Kc[pos + 16] = (u16)(o0 >> 16);
      Kc[pos + 32] = (u16)o1;
      Kc[pos + 48] = (u16)(o1 >> 16);
    }
  }
  if (LX == 1) lookx();
  wait_vm<LX == 2 ? 0 : 4>();
  lds_barrier();
  TS(7);
  if (w == 7) st4one(ws + WS_FW + (fb * 16 + mat * 8 + dq) * 128, tag);
  const int tt = (xg & 1) * 8 + (idx & 7), fs = (xg >> 1) * 4 + (idx >> 3);
  const u32 fwo = fs * 2048 + (lane & 15) * 128;
  u32 fw;
  if (LX == 2) lookx();
  lookm(); lookc();
  {
    u32 e[6];
    const int ki = tid & 255;
#pragma unroll
    for (int j = 0; j < 6; j++) {
      const float l0 = (float)(2047 - 8 * ki - 2 * j);
      const u32 bits = pk(fmaxf(l0, 0.f), fmaxf(l0 - 1.f, 0.f));
      e[j] = (u32)Kc[(int)bflo(bits)];
      if (j < 5) e[j] |= (u32)Kc[(int)bfhi(bits)] << 16;
    }
    if (ki == 255) { e[4] = 0; e[5] = 0; }
    const u32 a0 = __builtin_amdgcn_alignbit(e[1], e[0], 16), a1 = __builtin_amdgcn_alignbit(e[2], e[1], 16);
    const u32 a2 = __builtin_amdgcn_alignbit(e[3], e[2], 16), a3 = __builtin_amdgcn_alignbit(e[4], e[3], 16);
    const u32 a4 = __builtin_amdgcn_alignbit(e[5], e[4], 16);
    char* kc = smem + B_KC + kch * B_KCH + ki * 16;
    *(u32x4*)(kc) = u32x4{e[0], e[1], e[2], e[3]};
    *(u32x4*)(kc + B_KCS) = u32x4{a0, a1, a2, a3};
    *(u32x4*)(kc + 2 * B_KCS) = u32x4{e[1], e[2], e[3], e[4]};
    *(u32x4*)(kc + 3 * B_KCS) = u32x4{a1, a2, a3, a4};
  }
  TS(8);
  wait_vm<0>(); tie(xg4[0]); tie(xg4[1]); tie(xg4[2]); tie(xg4[3]); tie(mg); tie(mh); tie(mc[0]); tie(mc[1]); tie(mc[2]); tie(mc[3]);
  TS(47);
#pragma unroll 1
  for (int it = 0; it < SPIN_CAP && !all_lanes(xg4[0][3] == tag && xg4[1][3] == tag && xg4[2][3] == tag && xg4[3][3] == tag); it++) {
    lookx();
    wait_vm<0>(); tie(xg4[0]); tie(xg4[1]); tie(xg4[2]); tie(xg4[3]);
  }
#pragma unroll 1
  for (int it = 0; it < SPIN_CAP && !all_lanes(mg[1] == tag && mh[1] == tag); it++) {
    lookm();
    wait_vm<0>(); tie(mg); tie(mh);
  }
  TS(9);
  const int h = lane >> 5, c = lane & 31;
  const int tt2 = (~xg & 1) * 8 + (xg & 6) + (idx >> 4), sel = (idx >> 3) & 1, dt = idx & 7;
  const int rot = (2 * (idx >> 3) + idx) & 7;
  const int ft = fs * 7 + w - 1;
  u32x4 A[32];
  const u32 ao = lane * 16;
  auto lda = [&](int i) NOPK {
    const char* wq = ws + WS_W1F + (size_t)ft * 32768 + (((i >> 2) + rot) & 7) * 4096;
    asm volatile("s_cmp_eq_u32 %4, 0\n\ts_cbranch_scc1 .Lna%=\n\tglobal_load_dwordx4 %0, %1, %2 offset:%3 sc1\n.Lna%=:"
                 : "=v"(A[i]) : "v"(ao), "s"(wq), "n"((i & 3) * 1024), "s"(w) : "memory", "scc");
  };
  ld4(fw, ws + WS_FW, fwo);
  const int g8 = tid >> 1, cc = tid & 1;
  const float mq = u2f(mg[0]) + u2f(mh[0]);
  const float mv = rbf(rs32(mq, mq)), mv1 = rbf(1.f + mv);
  const float ga = sel2(cc, rdl(mv, 20), rdl(mv, 21));
  u32x4 hp, xv;
  {
    const float sh0 = rdl(mv, 16), sh1 = rdl(mv, 17), sc0 = rdl(mv1, 18), sc1 = rdl(mv1, 19);
    const u32 psel = cc ? 0x03020706u : 0x05040100u;
    const bool up = tid & 2;
    u32 ph[4], px[4];
#pragma unroll
    for (int i = 0; i < 4; i++) {
      const u32 xq = (p & 1) ? xg4[i][1] : xg4[i][0];
      const float m = bflo(xg4[i][2]), rs = __builtin_amdgcn_rcpf(bfhi(xg4[i][2]));
      const u32 n = pk((bflo(xq) - m) * rs, (bfhi(xq) - m) * rs);
      const u32 hq = pk(fmaf(bflo(n), sc0, sh0), fmaf(bfhi(n), sc1, sh1));
      ph[i] = __builtin_amdgcn_perm(swap1(hq), hq, psel);
      px[i] = __builtin_amdgcn_perm(swap1(xq), xq, psel);
    }
#pragma unroll
    for (int j = 0; j < 2; j++) {
      const u32 rh = swap2(up ? ph[j] : ph[2 + j]), kh = up ? ph[2 + j] : ph[j];
      hp[2 * j] = up ? rh : kh; hp[2 * j + 1] = up ? kh : rh;
      const u32 rx = swap2(up ? px[j] : px[2 + j]), kx = up ? px[2 + j] : px[j];
      xv[2 * j] = up ? rx : kx; xv[2 * j + 1] = up ? kx : rx;
    }
    char* u = smem + B_U + cc * B_UCH + 1024 + g8 * 16;
    *(u32x4*)(u) = hp;
    *(u32x2*)(u + B_US + 8) = u32x2{hp[0], hp[1]};
    *(u32x2*)(u + B_US + 16) = u32x2{hp[2], hp[3]};
  }
#pragma unroll 1
  for (int it = 0; it < SPIN_CAP && !all_lanes(mc[0][1] == tag && mc[1][1] == tag && mc[2][1] == tag && mc[3][1] == tag); it++) {
    lookc();
    wait_vm<0>(); tie(mc[0]); tie(mc[1]); tie(mc[2]); tie(mc[3]);
  }
  {
    const float sc = 1.f + rbf((u2f(mc[0][0]) + u2f(mc[2][0])) + (u2f(mc[1][0]) + u2f(mc[3][0])));
    const u32 e = pk(sc, sc) & ((tid & 1) ? 0xffff0000u : 0xffffu);
    *(u32*)(smem + B_SC + tid * 4) = e;
    *(u32*)(smem + B_SC + 2048 + tid * 4) = e;
  }
  TS(10);
  lds_barrier();
  TS(11);
  const char* tb = smem + B_SC + rot * 256 + h * 32;
  u32x4 tq[3][2];
  auto ldt = [&](int i) NOPK { tq[i % 3][0] = *(const u32x4*)(tb + i * 64); tq[i % 3][1] = *(const u32x4*)(tb + i * 64 + 16); };
  auto scl = [&](int i) NOPK { tie(A[i]); scale8(A[i], tq[i % 3][0], tq[i % 3][1], w); tie(A[i]); };
  auto epi = [&](int yb) NOPK {
    const int gl = g8 & 127;
    const float* Y = (const float*)(smem + yb) + cc * 1280 + 8 * gl + 8 * (gl >> 2);
    const f32x4 ya = (*(const f32x4*)Y + *(const f32x4*)(Y + 2560)) + (*(const f32x4*)(Y + 5120) + *(const f32x4*)(Y + 7680));
    const f32x4 yb4 = (*(const f32x4*)(Y + 4) + *(const f32x4*)(Y + 2564)) + (*(const f32x4*)(Y + 5124) + *(const f32x4*)(Y + 7684));
    const float dk = cc ? bfhi(dsk) : bflo(dsk);
    u32x4 o;
#pragma unroll
    for (int j = 0; j < 4; j++) {
      const float y0 = j < 2 ? ya[2 * j] : yb4[2 * j - 4], y1 = j < 2 ? ya[2 * j + 1] : yb4[2 * j - 3];
      o[j] = pk(fmaf(ga, fmaf(bflo(hp[j]), dk, y0), bflo(xv[j])), fmaf(ga, fmaf(bfhi(hp[j]), dk, y1), bfhi(xv[j])));
    }
    st16(ws + WS_X1 + p * 128 + (w >> 1) * 32768, ((g8 >> 2) & 15) * 131072 + cc * 64 + (g8 & 3) * 16, o);
  };
  constexpr int LBT = LBC, NS2 = 51 / SCR + 1;
  {
    const int ch = w & 1, nq = w >> 1;
    const char* pa = smem + B_KC + ch * B_KCH + (3 - (r & 3)) * B_KCS + 2 * (2040 - 8 * (r >> 2) + 8 * g) - 512 * 7 - 64 * (2 * nq + 1);
    const char* pb = smem + B_U + ch * B_UCH + ((r & 1) ? 0 : B_US) + 2 * (512 + 32 * (r >> 1) + 8 * g) - 64 * (2 * nq + 1);
    const int yo = w * 5120 + 160 * (r >> 1) + 16 * (r & 1) + 32 * g;
    f32x4 acc[4];
    u32x4 la[2][4], lb[2][4], hf[4];
    auto ldhi = [&](int q) NOPK {
      const int e = 4 + ((q & 7) >> 1);
      hf[q & 3] = *(const u32x4*)(((q & 1) ? pa + 512 * (7 - e) : pb + 512 * e) + 64 * (1 - (q >> 3)));
    };
#pragma unroll
    for (int vv = 0; vv < 2; vv++)
#pragma unroll
      for (int n = 0; n < 8; n++) {
        const int e = (0x30212310 >> (4 * n)) & 3;
        if ((0xb1 >> n) & 1) la[vv][e] = *(const u32x4*)(pa + 512 * (7 - e) + 64 * (1 - vv));
        else lb[vv][e] = *(const u32x4*)(pb + 512 * e + 64 * (1 - vv));
      }
    __builtin_amdgcn_sched_barrier(0);
    {
      f32x4 sums = {0.f, 0.f, 0.f, 0.f};
#pragma unroll
      for (int j = 0; j < 4; j++) {
        const float k0 = rdl(mv1, 2 * j), k1 = rdl(mv1, 2 * j + 1), s0 = rdl(mv, 8 + 2 * j), s1 = rdl(mv, 8 + 2 * j + 1);
        const float a0 = bflo(v[2 * j]), a1 = bflo(v[2 * j + 1]), b0 = bfhi(v[2 * j]), b1 = bfhi(v[2 * j + 1]);
        sums[0] = dot2(pk(a0 * k0, a1 * k1), 0x3f803f80u, sums[0]);
        sums[1] = dot2(pk(b0 * k0, b1 * k1), 0x3f803f80u, sums[1]);
        sums[2] = fmaf(a0, s0, fmaf(a1, s1, sums[2]));
        sums[3] = fmaf(b0, s0, fmaf(b1, s1, sums[3]));
      }
      *(f32x4*)(smem + B_RED + tid * 16) = sums;
    }
#pragma unroll
    for (int i = 0; i < 4; i++) acc[i] = f32x4{0.f, 0.f, 0.f, 0.f};
    wait_vm<0>(); tie(fw);
    spin4(fw, tag, ws + WS_FW, fwo);
    TS(46);
    __builtin_amdgcn_sched_barrier(0);
#pragma unroll
    for (int n = 0; n < 20; n++) {
      const int vv = n / 10, k = n % 10, e = (0x0231211000ll >> (4 * k)) & 3, u = (0x0000112231ll >> (4 * k)) & 3;
      acc[e + u] = mfma16(la[vv][e], lb[vv][u], acc[e + u]);
      if (n % 3 == 0 && n / 3 < LBC) lda(n / 3);
      if (n >= 12 && n < 15) ldhi(n - 12);
      __builtin_amdgcn_sched_barrier(0);
    }
    TS(12);
#pragma unroll
    for (int i = 0; i < 4; i++) *(f32x4*)(smem + B_Y1 + yo + 1280 * i) = acc[i];
    lds_barrier();
    TS(13);
    TSF(7, 13); TSF(46, 47);
    int w_ = w;
    asm volatile("" : "+s"(w_));
    if (w_ < 4) {
      __builtin_amdgcn_s_setprio(3);
      epi(B_Y1);
      __builtin_amdgcn_s_setprio(0);
    }
    asm volatile("" : "+s"(w_));
    if (w_ == 0) {
      f32x4 s = {0.f, 0.f, 0.f, 0.f};
#pragma unroll
      for (int q = 0; q < 8; q++) s += *(const f32x4*)(smem + B_RED + q * 1024 + lane * 16);
      st16(ws + WS_CD + ((dq * 16 + fb) * 2 + mat) * 1024, lane * 16, __builtin_bit_cast(u32x4, s));
      st8m<1>(ws + WS_GB + p * 8, 0, u32x2{f2u(rdl(mv, 22)), f2u(rdl(mv, 23))});
    }
    TS(14);
    ldt(0);
#pragma unroll
    for (int n = 0; n < 52; n++) {
      const int vv = n / 26, k = n % 26;
      const int e = (k < 13 ? 0x2210144443210ll >> (4 * k) : 0x7036631023555ll >> (4 * (k - 13))) & 7;
      const int u = (k < 13 ? 0x2555332104444ll >> (4 * k) : 0x0731026631210ll >> (4 * (k - 13))) & 7;
      const int q = vv * 8 + (e > 3 ? 2 * e - 7 : 2 * u - 8);
      if (n == NF1) {
        asm volatile("" : "+s"(w_));
        if (w_ < 4) {
          wait_vmn((n + SCR - 1) / SCR);
          if (w_ == 0) wait_vm<0>();
          if (arrive_last(smem + L_CNT + 8, 4)) st4one(ws + WS_FX + (b & 63) * 128 + (b >> 6) * 4, tag);
        }
        TS(15);
      }
      if (k == 0 || k == 4 || k == 8 || k == 12 || k == 16 || k == 20 || k == 23 || k == 25) {
        const int qn = vv * 8 + (k < 23 ? k / 4 : k == 23 ? 6 : 7) + 3;
        if (qn < 16) ldhi(qn);
      }
      if (n % SCR == 0) ldt(n / SCR + 1);
      __builtin_amdgcn_sched_barrier(0);
      acc[e + u - 4] = mfma16(e > 3 ? hf[q & 3] : la[vv][e], u > 3 ? hf[q & 3] : lb[vv][u], n < 4 ? f32x4{0.f, 0.f, 0.f, 0.f} : acc[e + u - 4]);
      if (n % SCR == 0) { wait_vm<LBT - 1>(); lda(LBT + n / SCR); scl(n / SCR); }
      __builtin_amdgcn_sched_barrier(0);
    }
    TS(16);
#pragma unroll
    for (int i = 0; i < 4; i++) *(f32x4*)(smem + B_Y2 + yo + 1280 * i) = acc[i];
    ldt(NS2 + 1);
    lds_barrier();
    TS(42);
    asm volatile("" : "+s"(w_));
    if (w_ >= 4) {
      __builtin_amdgcn_s_setprio(3);
      epi(B_Y2);
      __builtin_amdgcn_s_setprio(0);
    }
    TS(43);
    asm volatile("" : "+s"(w_));
    if (w_ != 0) {
#pragma unroll
      for (int i = NS2; i < 32; i++) {
        if (i + 2 < 32) ldt(i + 2);
        wait_vmn(i + LBT < 32 ? LBT - 1 : 31 - i);
        if (i + LBT < 32) lda(i + LBT);
        __builtin_amdgcn_sched_barrier(0);
        scl(i);
        __builtin_amdgcn_sched_barrier(0);
      }
    }
    wait_vm<0>();
  }
  f32x2* raw = (f32x2*)(smem + C_RAW);
  u32x4 W2x[12];
  u32 fh;
  const u32 lds0 = (u32)(size_t)(LDS_AS char*)smem;
  const char* xa = ws + WS_X1 + tt * 131072 + ((w + rot) & 7) * 4096;
  const u32 lpc = lds0 + w * 4096;
  const int rt2 = dt + 2 * (idx >> 3), rot2 = rt2 < 7 ? rt2 : rt2 - 7;
#define kof(j) ((j) + rot2 < 7 ? (j) + rot2 : (j) + rot2 - 7)
  const char* w2b = ws + WS_W2F + dt * 229376 + w * 28672;
  const char* fhb = ws + WS_FH + tt2 * 2048 + w * 256;
  const u32 fho = ((lane >> 3) & 1) * 128 + g * 32 + (lane & 7) * 4;
  const char* fpb = ws + WS_FH + (tt * 16 + fs) * 128 + w * 4;
  auto ldw2 = [&](int j) NOPK {
    const char* q = w2b + kof(j >> 2) * 4096;
    if (j < 12) ld16k(W2x[j], q, j & 3, ao); else ld16k(A[j - 12], q, j & 3, ao);
  };
  auto poll_x = [&](int part) NOPK {
#pragma unroll 1
    for (int it = 0; it < SPIN_CAP; it++) {
      u32x4 f;
      ld16(f, ws + WS_FX + part * 8192, lane * 128); wait_vm<0>(); tie(f);
      if (all_lanes(f[0] == tag && f[1] == tag && f[2] == tag && f[3] == tag)) break;
    }
  };
  if (w == 0) {
    __builtin_amdgcn_s_setprio(3);
    poll_x(0);
    TS(17);
#pragma unroll
    for (int q = 0; q < 8; q++) glds4k(ws + WS_X1 + tt * 131072 + ((q + rot) & 7) * 4096, ao, lds0 + q * 4096);
    const char* rb = smem + (lane >> 2) * 64 + (lane & 3) * 8;
    wait_vm<0>();
    TS(18);
    bar();
    TS(19); TC(20);
    TSF(14, 20); TSF(42, 43);
    st4one(ws + WS_FX + 8192 + (b & 63) * 128 + (b >> 6) * 4, tag);
    glds4k(xa + 32768, ao, lpc + 32768);
    if (b == 0) st4one(ws + WS_E, tag);
    st4one(fpb, tag); st4one(fpb + 32, tag);
#pragma unroll
    for (int i = 0; i < 14; i++) ldw2(i);
    ln2_tile(rb, raw, raw + 132, lane);
    TS(21);
    poll_x(1);
    lds_barrier();
    TS(22);
    glds4k(xa + 65536, ao, lpc + 65536); glds4k(xa + 98304, ao, lpc + 98304);
#pragma unroll
    for (int q = 0; q < 4; q++) {
      const char* xr_ = ws + WS_X1 + (tt2 * 4 + q) * 32768 + dt * 4096 + sel * 32;
      const u32 xo = r * 256 + h * 64 + (g & 1) * 16, la = lds0 + D_RES + q * 2048;
      glds1k<0>(xr_, xo, la); glds1k<0>(xr_ + 128, xo, la + 1024);
    }
    asm volatile("s_mov_b32 m0, %2\n\ts_nop 0\n\tglobal_load_lds_dword %0, %1 sc1"
                 ::"v"(lane * 4), "s"(ws + WS_GB + dt * 256), "s"(lds0 + D_GB) : "memory");
    ln2_tile(rb + 32768, raw + 32, raw + 132, lane);
    TS(23);
    wait_vm<0>(); lds_barrier();
    TS(24);
#pragma unroll
    for (int i = 14; i < 28; i++) ldw2(i);
    rb = far(rb + 65536);
    ln2_tile(rb, raw + 64, raw + 132, lane);
    asm volatile("" ::: "memory");
    *(volatile LDS_AS u32*)(smem + L_CNT + 16) = 1;
    TS(25);
    ln2_tile(rb + 32768, raw + 96, raw + 132, lane);
    asm volatile("" ::: "memory");
    *(volatile LDS_AS u32*)(smem + L_CNT + 16) = 2;
    __builtin_amdgcn_s_setprio(0);
    TS(26); TS(27); TC(28);
    TSF(21, 28);
    ld4(fh, fhb, fho);
    wait_vm<0>(); tie(fh);
#pragma unroll
    for (int i = 0; i < 28; i++) { if (i < 12) tie(W2x[i]); else tie(A[i - 12]); }
    TS(29);
  }
  int w_ = w;
  asm volatile("" : "+s"(w_));
  if (w_ != 0) {
    const char* rb = smem + h * 512 + ((lane >> 2) & 3) * 64 + ((lane >> 4) & 1) * 32 + (lane & 3) * 8;
    const char* rb2 = far(rb + 65536);
    u32 cdv[8];
    f32x16 acc[2], zero;
#pragma unroll
    for (int i = 0; i < 16; i++) zero[i] = 0.f;
    TS(17);
    lds_barrier();
    TS(18);
    TS(19); TC(20);
    TSF(14, 20); TSF(42, 43);
    __builtin_amdgcn_s_setprio(3);
    u32x4 Bq[3], Ax, op;
    Bq[0] = tr8(rb); Bq[1] = tr8(rb + 1024);
    f32x2 rw;
    Ep q[2];
    float ev, gu, oa;
    const char* hb = ws + WS_HF + (size_t)(tt * 8 * 56 + (ft >> 1)) * 1024 + (ft & 1) * 512;
    const u32 ho = (c >> 4) * 57344 + h * 256 + r * 16;
    auto stat_wait = [&](u32 need) NOPK {
#pragma unroll 1
      for (int it = 0; it < SPIN_CAP * 16; it++)
        if (__builtin_amdgcn_readfirstlane(*(volatile LDS_AS u32*)(smem + L_CNT + 16)) >= need) break;
      asm volatile("" ::: "memory");
    };
    auto tile = [&](auto tc) NOPK {
    constexpr int T = decltype(tc)::v, E = T - 1;
#pragma unroll
    for (int ks = 0; ks < 32; ks++) {
      const int n = T * 32 + ks, m = n + 2;
      if (n == 30) {
        TS(21);
        wait_vm<8>();
#pragma unroll
        for (int i = 0; i < 8; i++) tie(cdv[i]);
        lds_barrier();
        TS(22);
      } else if (n == 62) {
        TS(23);
        wait_vm<5>();
        lds_barrier();
        TS(24);
      }
      if (n == 30 || n == 62) __builtin_amdgcn_s_setprio(3);
      if (n == 14 || n == 46 || n == 100) __builtin_amdgcn_s_setprio(2);
      if (n == 22 || n == 54 || n == 118) __builtin_amdgcn_s_setprio(1);
      if (n == 27 || n == 59) __builtin_amdgcn_s_setprio(0);
      if (n == 96) { TS(25); tie(acc[0]); stat_wait(1); }
      if (n == 118) { tie(acc[1]); stat_wait(2); }
      if (n == 120) wait_vm<10>();
      if (m < 128) Bq[m % 3] = tr8((m < 64 ? rb : rb2) + (m & 63) * 1024);
      if (T > 0 && ks == 0) rw = raw[E * 32 + c];
      if (n == 118) rw = raw[96 + c];
      if (n >= 120) A[n - 112] = *(const u32x4*)(smem + ((n >> 2) & 1) * 32768 + w * 4096 + (n & 3) * 1024 + lane * 16);
      __builtin_amdgcn_sched_barrier(0);
      acc[T & 1] = mfma32(A[ks], Bq[n % 3], ks == 0 ? zero : acc[T & 1]);
      if (n == 31) {
        const float cd = ((u2f(cdv[0]) + u2f(cdv[1])) + (u2f(cdv[2]) + u2f(cdv[3]))) + ((u2f(cdv[4]) + u2f(cdv[5])) + (u2f(cdv[6]) + u2f(cdv[7])));
        const float chi = rbf(cd);
        Ax = u32x4{pk(chi, chi), pk(cd - chi, cd - chi), 0, 0};
      }
      if ((T > 0 && ks == 2) || n == 120) {
        const int X = (n == 120 ? 3 : E) & 1;
        const u32 be = ln2_fold(rw, h, q[X]);
        acc[X] = mfma32(Ax, u32x4{be, be, 0, 0}, acc[X]);
      }
      if (T > 0 && ks >= 4 && ks < 20) {
        const int k = (ks - 4) >> 1, i = (k >> 1) + 4 * (k & 1);
        if ((ks & 1) == 0) {
          ev = fmaf(__builtin_amdgcn_exp2f(accg(acc[E & 1], i) * q[E & 1].q0), q[E & 1].v, q[E & 1].v);
          gu = accg(acc[E & 1], i) * accu(acc[E & 1], i);
        } else {
          const float o = gu * __builtin_amdgcn_rcpf(ev);
          if (k & 1) op[k >> 1] = pk(oa, o); else oa = o;
        }
      }
      __builtin_amdgcn_sched_barrier(0);
      if (n == 1) {
        const u32 co = ((c >> 3) & 1) * 1024 + ((w - 1) * 8 + (c & 7)) * 16 + h * 8 + (c >> 4) * 4;
#pragma unroll
        for (int i = 0; i < 8; i++) ld4(cdv[i], ws + WS_CD + (i * 16 + fs) * 2048, co);
      }
      if (n == 0) glds4k(xa + 32768, ao, lpc + 32768);
      if (T == 0 && ks >= 4 && ks < 28 && ks % 3 == 1) ldw2((ks - 4) / 3);
      if (T == 1 && ks < 16 && (ks & 1)) {
        const int i = ks >> 1, o = 65536 + (i >> 2) * 32768;
        if ((i & 3) == 0) glds1k<0>(xa + o, ao, lpc + o); else if ((i & 3) == 1) glds1k<1024>(xa + o, ao, lpc + o);
        else if ((i & 3) == 2) glds1k<2048>(xa + o, ao, lpc + o); else glds1k<3072>(xa + o, ao, lpc + o);
      }
      if (T == 1 && (ks == 18 || ks == 22 || ks == 24 || ks == 26)) ldw2(ks == 18 ? 8 : ks / 2 - 2);
      if (T > 1 && ks == 9) { wait_vm<T == 2 ? 6 : 4>(); st4one(fpb + (T - 2) * 32, tag); }
      if (T > 0 && ks == 20) st16(hb + E * 114688, ho, op);
      if (T > 1 && ks % 3 == 1 && ks < 24) {
        const int i = ks / 3;
        const char* q = w2b + kof(5 + (i >> 2)) * 4096;
        const u32 la = lpc + (i >> 2) * 32768;
        if (T == 3) ldw2(12 + i);
        else if ((i & 3) == 0) glds1k<0>(q, ao, la); else if ((i & 3) == 1) glds1k<1024>(q, ao, la);
        else if ((i & 3) == 2) glds1k<2048>(q, ao, la); else glds1k<3072>(q, ao, la);
      }
      if (n == 122) ld4(fh, fhb, fho);
    }
    };
    tile(IC<0>{}); tile(IC<1>{}); tile(IC<2>{}); tile(IC<3>{});
    TS(26); TS(27); TC(28);
    TSF(21, 28);
#pragma unroll
    for (int i = 0; i < 4; i++) op[i] = pk(swi(acc[1], i, q[1]), swi(acc[1], i + 4, q[1]));
    st16(hb + 3 * 114688, ho, op);
    __builtin_amdgcn_s_setprio(0);
    wait_vm<1>(); tie(fh);
#pragma unroll
    for (int i = 0; i < 20; i++) { if (i < 12) tie(W2x[i]); else tie(A[i - 12]); }
    TS(29);
  }
  {
    const char* hq = ws + WS_HF + (size_t)((tt2 * 8 + sel) * 56 + w * 7) * 1024;
    u32x4 H[3][7];
    auto need = [&](unsigned long long mask) NOPK {
#pragma unroll 1
      for (int it = 0; it < SPIN_CAP && (__builtin_amdgcn_ballot_w64(fh != tag) & mask) != 0; it++) {
        __builtin_amdgcn_s_sleep(2);
        ld4(fh, fhb, fho); wait_vm<0>(); tie(fh);
      }
    };
    auto ldh = [&](int q) NOPK {
#pragma unroll
      for (int j = 0; j < 7; j++) ld16(H[q % 3][j], hq + q * 114688 + kof(j) * 1024, ao);
    };
    char* wr = smem + w * 4096 + r * 256;
    auto quarter = [&](int q, int yng) NOPK {
      f32x4 acc[4];
#pragma unroll
      for (int j = 0; j < 7; j++) {
        __builtin_amdgcn_sched_barrier(0);
        if (yng == 0) { if (j == 0) wait_vm<5>(); else if (j == 2) wait_vm<3>(); else if (j == 4) wait_vm<1>(); else if (j == 6) wait_vm<0>(); }
        if (yng == 8 && j == 0) wait_vm<8>();
        tie(H[q % 3][j]);
#pragma unroll
        for (int ds = 0; ds < 4; ds++) {
          const int f = 4 * j + ds;
          acc[ds] = mfma16(f < 12 ? W2x[f] : A[f - 12], H[q % 3][j], j == 0 ? f32x4{0.f, 0.f, 0.f, 0.f} : acc[ds]);
        }
      }
      __builtin_amdgcn_sched_barrier(0);
#pragma unroll
      for (int ds = 0; ds < 4; ds++) *(f32x4*)(wr + q * 32768 + (((ds * 4 + g) ^ r) * 16)) = acc[ds];
    };
    const int tk = (tid >> 4) & 15, qh = w >> 2;
    u32 xq[2][4];
    f32x4 gbv;
    auto finish = [&](int qp) NOPK {
      const char* rp = smem + (2 * qp + qh) * 32768 + tk * 256 + ((r ^ tk) * 16);
      f32x4 s = *(const f32x4*)rp;
#pragma unroll
      for (int i = 1; i < 8; i++) s += *(const f32x4*)(rp + i * 4096);
      float y[4];
#pragma unroll
      for (int j = 0; j < 4; j++) y[j] = fmaf(gbv[j], s[j], bflo(xq[qp][j]));
      asm volatile("global_store_dwordx2 %0, %1, %2 sc1" ::"v"((tk * D + r * 4) * 2), "v"(u32x2{pk(y[0], y[1]), pk(y[2], y[3])}),
                   "s"(out + (size_t)((2 * qp + qh) * 512 + tt2 * 32 + sel * 16) * D + dt * 64) : "memory");
    };
    st4one(fpb + 64, tag);
    need(0xffffull);
    ldh(0);
    TS(30);
    {
      const char* xp = smem + D_RES + qh * 2048 + (tk >> 3) * 256 + r * 16 + (tk & 7) * 2;
#pragma unroll
      for (int i = 0; i < 8; i++) xq[i >> 2][i & 3] = *(const u16*)(xp + (i >> 2) * 4096 + (i & 3) * 512);
      gbv = *(const f32x4*)(smem + D_GB + r * 16);
    }
    wait_vm<8>();
    st4one(fpb + 96, tag);
    TS(31);
    need(0xffffull << 16);
    ld4(fh, fhb, fho);
    ldh(1);
    wait_vm<7>(); tie(fh);
    TS(32);
    need(0xffffull << 32);
    ld4(fh, fhb, fho);
    ldh(2);
    quarter(0, -1);
    TS(33);
    wait_vm<7>(); tie(fh);
    need(0xffffull << 48);
    TS(34);
    ldh(3);
    quarter(1, -1);
    TS(35);
    lds_barrier();
    finish(0);
    asm volatile("" ::: "memory");
    TS(36);
    TSF(29, 36);
    quarter(2, 8);
    TS(37);
    quarter(3, 0);
    TS(38);
    lds_barrier();
    asm volatile(".Lpf1:");
    finish(1);
    TS(39);
    TS(40);
    TSF(37, 40);
    TSEND();
  }
}

extern "C" void kernel_launch(void* const* d_in, const int* in_sizes, int n_in, void* d_out, int out_size, void* d_ws,
                              size_t ws_size, hipStream_t stream) {
  const u16* const* in = (const u16* const*)d_in;
  fused<<<256, 512, 0, stream>>>(in[0], in[1], in[2], in[3], in[4], in[5], in[6], in[7], in[8], in[10], in[11], in[12], in[13], in[14],
                                 (char*)d_ws, (u16*)d_out);
}
```

```cpp
#include <hip/hip_runtime.h>
#include <stdint.h>

typedef unsigned int u32;
typedef unsigned short u16;
typedef __attribute__((ext_vector_type(2))) float f32x2;
typedef __attribute__((ext_vector_type(4))) float f32x4;
typedef __attribute__((ext_vector_type(16))) float f32x16;
typedef __attribute__((ext_vector_type(2))) u32 u32x2;
typedef __attribute__((ext_vector_type(4))) u32 u32x4;
typedef __attribute__((ext_vector_type(2))) __bf16 bf16x2;
typedef __attribute__((ext_vector_type(8))) __bf16 bf16x8;
typedef __attribute__((ext_vector_type(4))) short s16x4;
#define LDS_AS __attribute__((address_space(3)))
#if defined(__HIP_DEVICE_COMPILE__)
#define NOPK __attribute__((target("no-packed-fp32-ops")))
#else
#define NOPK
#endif
#define DEVINL __device__ __forceinline__ NOPK

constexpr int L = 2048, D = 512, NS = 128, F = 1792;

constexpr size_t WS_E = 0;
constexpr size_t WS_FW = 4096;
constexpr size_t WS_FX = WS_FW + 32768;
constexpr size_t WS_FH = WS_FX + 32768;
constexpr size_t WS_MG = WS_FH + 65536;
constexpr size_t WS_GB = WS_MG + 98304;
constexpr size_t WS_XG = 262144;
constexpr size_t WS_X1 = WS_XG + (4u << 20);
constexpr size_t WS_XS = WS_X1 + (2u << 20);
constexpr size_t WS_W1F = WS_XS + (2u << 20);
constexpr size_t WS_W2F = WS_W1F + 112 * 32 * 1024;
constexpr size_t WS_HF = WS_W2F + 16 * 112 * 1024;
constexpr size_t WS_CD = WS_HF + 64 * 112 * 1024;
constexpr size_t WS_ST = WS_CD + 524288;
constexpr int SPIN_CAP = 40000;
#define VP 0
#define LX 1
#define GP 1
#define RP 1
#define FWP 0
#define LM 2
#define F1P 1
#define CDP 1
#define SO 1
#define DS 6
#define D1 0
#define NL1 16
#define SLP 0
#define CB 1
#define HF 2
#define D2 0
#define D3 0
#define FWN 0
#define NB0 0
#define W2I 4
#define SCR 2
#define LBC 7
#define NF1 40
#define LBA 4
#define NPRE 0
#define WIN 0
#define W2S 6
#define SH1 5
#define PFM 3
#define LP 1
#define R1 2
#define R2 2
#define WT 10
#define SQ 1
#define SQ2 1
#define FN 0
#define E2 0
#define F2N 26
#define GT 1
#define QO 1
#define FR 0
#define PQ 0
#define PFB 65536

#define u2f(x) __builtin_bit_cast(float, (u32)(x))
#define i2f(x) __builtin_bit_cast(float, (int)(x))
#define f2u(x) __builtin_bit_cast(u32, (float)(x))
#define f2i(x) __builtin_bit_cast(int, (float)(x))
#define TSDECL
#define TS(slot)
#define TC(slot)
#define TSF(lo, hi)
#define TSEND()
DEVINL float bflo(u32 u) { return u2f(u << 16); }
DEVINL float bfhi(u32 u) { return u2f(u & 0xffff0000u); }
DEVINL u32 pk(float a, float b) {
  f32x2 v = {a, b};
  return __builtin_bit_cast(u32, __builtin_convertvector(v, bf16x2));
}
DEVINL float rbf(float a) { return bflo(pk(a, a)); }
template <int CTRL> DEVINL float dpp_add(float v) {
  return v + i2f(__builtin_amdgcn_update_dpp(0, f2i(v), CTRL, 0xf, 0xf, true));
}
DEVINL float row4_sum(float v) {
  auto a = __builtin_amdgcn_permlane16_swap(f2u(v), f2u(v), false, false);
  v = u2f(a[0]) + u2f(a[1]);
  auto b = __builtin_amdgcn_permlane32_swap(f2u(v), f2u(v), false, false);
  return u2f(b[0]) + u2f(b[1]);
}
DEVINL float rs32(float a, float b) {
  auto r = __builtin_amdgcn_permlane32_swap(f2u(a), f2u(b), false, false);
  return u2f(r[0]) + u2f(r[1]);
}
DEVINL float rs16(float a, float b) {
  auto r = __builtin_amdgcn_permlane16_swap(f2u(a), f2u(b), false, false);
  return u2f(r[0]) + u2f(r[1]);
}
DEVINL float wave_sum(float v) { return row4_sum(dpp_add<0x140>(dpp_add<0x141>(dpp_add<0x4E>(dpp_add<0xB1>(v))))); }
DEVINL f32x4 mfma16(u32x4 a, u32x4 b, f32x4 c) {
  return __builtin_amdgcn_mfma_f32_16x16x32_bf16(__builtin_bit_cast(bf16x8, a), __builtin_bit_cast(bf16x8, b), c, 0, 0, 0);
}
DEVINL f32x16 mfma32(u32x4 a, u32x4 b, f32x16 c) {
  return __builtin_amdgcn_mfma_f32_32x32x16_bf16(__builtin_bit_cast(bf16x8, a), __builtin_bit_cast(bf16x8, b), c, 0, 0, 0);
}
DEVINL float dot2(u32 a, u32 b, float c) {
  return __builtin_amdgcn_fdot2_f32_bf16(__builtin_bit_cast(bf16x2, a), __builtin_bit_cast(bf16x2, b), c, false);
}
DEVINL void lds_barrier() {
  asm volatile("s_waitcnt lgkmcnt(0)" ::: "memory");
  __builtin_amdgcn_s_barrier();
  asm volatile("" ::: "memory");
}
DEVINL void bar() {
  asm volatile("" ::: "memory");
  __builtin_amdgcn_s_barrier();
  asm volatile("" ::: "memory");
}
DEVINL void st16(const void* sbase, u32 voff, u32x4 v) {
  asm volatile("global_store_dwordx4 %0, %1, %2 sc1\n\ts_nop 1" ::"v"(voff), "v"(v), "s"(sbase) : "memory");
}
DEVINL void st8(const void* sbase, u32 voff, u32x2 v) {
  asm volatile("global_store_dwordx2 %0, %1, %2 sc1\n\ts_nop 1" ::"v"(voff), "v"(v), "s"(sbase) : "memory");
}
DEVINL void st4(const void* sbase, u32 voff, u32 v) {
  asm volatile("global_store_dword %0, %1, %2 sc1\n\ts_nop 1" ::"v"(voff), "v"(v), "s"(sbase) : "memory");
}
template <int LANES> DEVINL void st8m(const void* sbase, u32 voff, u32x2 v) {
  asm volatile("s_mov_b64 exec, %3\n\tglobal_store_dwordx2 %0, %1, %2 sc1\n\ts_mov_b64 exec, -1\n\ts_nop 0"
               ::"v"(voff), "v"(v), "s"(sbase), "n"((1 << LANES) - 1) : "memory");
}
DEVINL void st4one(const void* sbase, u32 v) {
  asm volatile("s_mov_b64 exec, 1\n\tglobal_store_dword %0, %1, %2 sc1\n\ts_mov_b64 exec, -1\n\ts_nop 0"
               ::"v"(0), "v"(v), "s"(sbase) : "memory");
}
DEVINL void st16x2_56(const void* sbase, u32 voff, u32x4 a, u32x4 b) {
  asm volatile("s_mov_b32 exec_hi, 0xffffff\n\tglobal_store_dwordx4 %0, %1, %3 sc1\n\tglobal_store_dwordx4 %0, %2, %3 offset:256 sc1\n\t"
               "s_mov_b32 exec_hi, -1\n\ts_nop 0" ::"v"(voff), "v"(a), "v"(b), "s"(sbase) : "memory");
}
DEVINL void sld4(u32& d, const void* p) { asm volatile("s_load_dword %0, %1, 0" : "=s"(d) : "s"(p) : "memory"); }
template <int OFF = 0> DEVINL void ld16(u32x4& d, const void* sbase, u32 voff) {
  asm volatile("global_load_dwordx4 %0, %1, %2 offset:%3 sc1" : "=v"(d) : "v"(voff), "s"(sbase), "n"(OFF) : "memory");
}
DEVINL void ld16k(u32x4& d, const char* sbase, int kb, u32 voff) {
  const char* sb = sbase + (kb >> 2) * 4096;
  if ((kb & 3) == 0) ld16<0>(d, sb, voff); else if ((kb & 3) == 1) ld16<1024>(d, sb, voff);
  else if ((kb & 3) == 2) ld16<2048>(d, sb, voff); else ld16<3072>(d, sb, voff);
}
DEVINL void ld8(u32x2& d, const void* sbase, u32 voff) {
  asm volatile("global_load_dwordx2 %0, %1, %2 sc1" : "=v"(d) : "v"(voff), "s"(sbase) : "memory");
}
template <int OFF = 0> DEVINL void ld4(u32& d, const void* sbase, u32 voff) {
  asm volatile("global_load_dword %0, %1, %2 offset:%3 sc1" : "=v"(d) : "v"(voff), "s"(sbase), "n"(OFF) : "memory");
}
template <int OFF = 0> DEVINL void lp16(u32x4& d, const void* sbase, u32 voff) {
  asm volatile("global_load_dwordx4 %0, %1, %2 offset:%3" : "=v"(d) : "v"(voff), "s"(sbase), "n"(OFF) : "memory");
}
template <int OFF = 0> DEVINL void lp4(u32& d, const void* sbase, u32 voff) {
  asm volatile("global_load_dword %0, %1, %2 offset:%3" : "=v"(d) : "v"(voff), "s"(sbase), "n"(OFF) : "memory");
}
template <int OFF = 0> DEVINL void lp2(u32& d, const void* sbase, u32 voff) {
  asm volatile("global_load_ushort %0, %1, %2 offset:%3" : "=v"(d) : "v"(voff), "s"(sbase), "n"(OFF) : "memory");
}
template <class T> DEVINL void tie(T& a) { asm volatile("" : "+v"(a)); }
template <int N> DEVINL void wait_vm() { asm volatile("s_waitcnt vmcnt(%0)" ::"n"(N) : "memory"); }
#define wait_vmn(k) asm volatile("s_waitcnt vmcnt(%0)" ::"n"(k) : "memory")
DEVINL u32 dif(u32 d, u32 a, u32 b) { return __builtin_amdgcn_bitop3_b32(d, a, b, 0xF6); }
DEVINL u32 dif4(u32 a, u32 b, u32 c, u32 d, u32 t) { return dif(dif(a ^ b, c, d), a, t) | dif(0, c, t); }
DEVINL bool all_lanes(bool ok) { return __builtin_amdgcn_ballot_w64(!ok) == 0; }
DEVINL void spin4(u32 f, u32 tag, const void* sbase, u32 voff) {
#pragma unroll 1
  for (int it = 0; it < SPIN_CAP && !all_lanes(f == tag); it++) {
    __builtin_amdgcn_s_sleep(1);
    ld4(f, sbase, voff); wait_vm<0>(); tie(f);
  }
}
DEVINL bool arrive_last(char* smem_word, int n) {
  u32 old;
  asm volatile("s_mov_b64 exec, 1\n\tds_add_rtn_u32 %0, %1, %2\n\ts_waitcnt lgkmcnt(0)\n\ts_mov_b64 exec, -1"
               : "=&v"(old) : "v"((u32)(size_t)(LDS_AS char*)smem_word), "v"(1) : "memory");
  return __builtin_amdgcn_readfirstlane(old) == (u32)(n - 1);
}
DEVINL u32x2 tr_read(const char* p) {
  return __builtin_bit_cast(u32x2, __builtin_amdgcn_ds_read_tr16_b64_v4i16((LDS_AS s16x4*)p));
}
DEVINL float rdl(float v, int l) { return i2f(__builtin_amdgcn_readlane(f2i(v), l)); }
DEVINL float sel2(int c, float a, float b) { return c ? b : a; }
DEVINL u32 swap2(u32 v) { return (u32)__builtin_amdgcn_update_dpp(0, (int)v, 0x4E, 0xf, 0xf, true); }
DEVINL u32 swap1(u32 v) { return (u32)__builtin_amdgcn_update_dpp(0, (int)v, 0xB1, 0xf, 0xf, true); }
constexpr float LOG2E = 1.4426950408889634f, LN2 = 0.6931471805599453f, INV2PI = 0.15915494309189535f;

constexpr int A_TAB = 0, A_P = 8 * 1536, A_KC = A_P + 24576, A_KCS = 4112, A_GV = A_KC + 2 * A_KCS;
constexpr int B_Y1 = 0, B_RED = 32768, B_KC = 49152, B_KCS = 4160, B_KCH = 4 * 4160, B_U = B_KC + 2 * B_KCH, B_US = 5152, B_UCH = 2 * B_US;
constexpr int B_Y2 = B_U + 2 * B_UCH, A_XT = B_Y2, B_XS = B_Y2 + 32768;
constexpr int C_RAW = 148480, L_CNT = C_RAW + 2048, D_RES = L_CNT + 64, D_GB = D_RES + 8192, LDS_BYTES = D_GB + 256;
static_assert(GT || !QO, "order");
static_assert(A_GV + 2048 <= B_KC && B_Y2 + 40960 <= C_RAW && 131072 <= C_RAW && LDS_BYTES <= 163840, "lds map");

DEVINL void glds4k(const void* sbase, u32 voff, u32 ldsaddr) {
  asm volatile("s_mov_b32 m0, %2\n\ts_nop 0\n\t"
               "global_load_lds_dwordx4 %0, %1 sc1\n\tglobal_load_lds_dwordx4 %0, %1 offset:1024 sc1\n\t"
               "global_load_lds_dwordx4 %0, %1 offset:2048 sc1\n\tglobal_load_lds_dwordx4 %0, %1 offset:3072 sc1"
               ::"v"(voff), "s"(sbase), "s"(ldsaddr) : "memory");
}
DEVINL u32x4 tr8(const char* p) {
  const u32x2 lo = tr_read(p), hi = tr_read(p + 256);
  return u32x4{lo[0], lo[1], hi[0], hi[1]};
}
template <int OFF> DEVINL void glds1k(const void* sbase, u32 voff, u32 ldsaddr) {
  asm volatile("s_mov_b32 m0, %2\n\ts_nop 0\n\tglobal_load_lds_dwordx4 %0, %1 offset:%3 sc1" ::"v"(voff), "s"(sbase), "s"(ldsaddr), "n"(OFF) : "memory");
}
DEVINL const char* far(const char* p) {
  u32 a = (u32)(size_t)(LDS_AS const char*)p;
  asm("" : "+v"(a));
  return (const char*)(LDS_AS const char*)(size_t)a;
}
struct Ep { float q0, v; };
template <int N> struct IC { static constexpr int v = N; };
#define accg(a, i) a[((i) & 3) + 8 * ((i) >> 2)]
#define accu(a, i) a[((i) & 3) + 8 * ((i) >> 2) + 4]
DEVINL float swi(const f32x16& a, int i, const Ep& q) {
  const float e = __builtin_amdgcn_exp2f(accg(a, i) * q.q0);
  return (accg(a, i) * accu(a, i)) * __builtin_amdgcn_rcpf(fmaf(e, q.v, q.v));
}
DEVINL u32 split(float a) { const float hi = rbf(a); return pk(hi, a - hi); }

__global__ NOPK __launch_bounds__(512, 2) void fused(
    const u16* __restrict__ x, const u16* __restrict__ Are, const u16* __restrict__ Aim, const u16* __restrict__ Bre,
    const u16* __restrict__ Bim, const u16* __restrict__ Cre, const u16* __restrict__ Cim, const u16* __restrict__ logdt,
    const u16* __restrict__ dskip, const u16* __restrict__ w1, const u16* __restrict__ w3, const u16* __restrict__ w2,
    const u16* __restrict__ cvec, const u16* __restrict__ wmod, char* ws, u16* __restrict__ out) {
  __shared__ __attribute__((aligned(16))) char smem[LDS_BYTES];
  const int b = blockIdx.x, tid = threadIdx.x, lane = tid & 63;
  const int w = __builtin_amdgcn_readfirstlane(tid >> 6);
  TSDECL; TS(0);
  const int xg = b & 7, idx = b >> 3;
  const int p = xg * 32 + idx;
  const int r = lane & 15, g = lane >> 4;
  asm volatile("" ::"s"(x), "s"(Are), "s"(Aim), "s"(Bre), "s"(Bim), "s"(Cre), "s"(Cim), "s"(logdt), "s"(dskip), "s"(w1), "s"(w3),
               "s"(w2), "s"(cvec), "s"(wmod), "s"(ws), "s"(out));
  u32x4 xr, wm0, wm1;
  u32 vare, vaim, vbre, vbim, vcre, vcim, c0, c1;
  const int cg = ((xg & 1) * 24 + (idx < 24 ? idx : 23)) * 4 + (xg >> 1);
  u32 e_prev, ldt2, dsk;
  lp16(xr, x + ((p >> 2) * 32 + (p & 3) + 4 * w) * D, lane * 16);
  sld4(e_prev, ws + WS_E); sld4(ldt2, logdt + 2 * p); sld4(dsk, dskip + 2 * p);
  {
    const u32 so = (p * 256 + w * 32 + (lane & 31)) * 2;
    lp2(vare, Are, so); lp2(vaim, Aim, so); lp2(vbre, Bre, so); lp2(vbim, Bim, so); lp2(vcre, Cre, so); lp2(vcim, Cim, so);
    const u32 mo = (tid >> 3) * 6144 + (tid & 7) * 16;
    const u16* wmp = wmod + (cg & 3) * 128 * 3072 + (cg >> 2) * 64;
    lp16(wm0, wmp, mo); lp16(wm1, wmp + 64 * 3072, mo);
    lp2(c0, cvec + (cg & 3) * 128, (tid >> 3) * 2); lp2<128>(c1, cvec + (cg & 3) * 128, (tid >> 3) * 2);
  }
  u32 pf;
  {
    unsigned long long pc;
    u32 po = ((idx & PFM) * 512 + tid) * (PFB / (PFM + 1) / 512);
    asm volatile("s_getpc_b64 %1\n.Lpf0:\n\tv_min_u32 %2, .Lpf1-.Lpf0, %2\n\tglobal_load_dword %0, %2, %1"
                 : "=v"(pf), "=&s"(pc), "+v"(po)::"memory");
  }
  TS(1);
  if (tid < 32) *(u32x4*)(smem + B_KC + (tid >> 2) * B_KCS + 4096 + (tid & 3) * 16) = u32x4{0, 0, 0, 0};
  else if (tid < 36) *(u32x4*)(smem + L_CNT + (tid - 32) * 16) = u32x4{0, 0, 0, 0};
  else if (tid < 38) *(u32x2*)(smem + B_U + (tid & 1) * B_UCH + B_US + 1024) = u32x2{0, 0};
  else if (tid >= 256) *(u32x4*)(smem + B_U + (w - 4) * B_US + lane * 16) = u32x4{0, 0, 0, 0};
  asm volatile("s_waitcnt lgkmcnt(0)" : "+s"(e_prev), "+s"(ldt2), "+s"(dsk)::"memory");
  const u32 tag = e_prev * 1664525u + 1013904223u;
  const u32 lds0 = (u32)(size_t)(LDS_AS char*)smem;
  const u32 gcode = __builtin_amdgcn_perm(0x400b0916u, 0x0a201408u, (r & 7) | 0x0c0c0c00u);
  const int gx = r < 8 ? 2 * r : (int)((gcode >> 2) * 256 + (gcode & 3) * 4);
  const int kch = w >> 2;
  u32 kra[11], kwa[4];
  float dadd;
  {
    const u32 kb0 = lds0 + A_KC + kch * A_KCS - 0x7e00;
    float lf = (float)(2047 - 8 * (tid & 255));
    tie(lf);
#pragma unroll
    for (int j = 0; j < 6; j++) {
      const u32 bits = pk(lf - (float)(2 * j), lf - (float)(2 * j + 1));
      asm("v_mad_u32_u16 %0, %1, 2, %2" : "=v"(kra[2 * j]) : "v"(bits), "s"(kb0));
      if (j < 5) asm("v_mad_u32_u16 %0, %1, 2, %2 op_sel:[1,0,0,0]" : "=v"(kra[2 * j + 1]) : "v"(bits), "s"(kb0));
    }
#pragma unroll
    for (int j = 8; j < 11; j++) kra[j] = lf < 8.f ? lds0 + B_KC + 4096 : kra[j];
    const int s = w & 3, pos = gx + 64 * g + (r < 8 ? (s == 0 ? 0 : s == 1 ? 1 : 256) : s * 256), lim = (pos & 4) ? 1024 : 2048;
    const float p0 = pos <= lim ? (float)pos : 4096.f, p1 = pos < lim ? (float)pos : 4096.f;
    const u32 b0 = pk(p0, p1 + 16.f), b1 = pk(p1 + 32.f, p1 + 48.f);
    asm("v_mad_u32_u16 %0, %1, 2, %2" : "=v"(kwa[0]) : "v"(b0), "s"(kb0));
    asm("v_mad_u32_u16 %0, %1, 2, %2 op_sel:[1,0,0,0]" : "=v"(kwa[1]) : "v"(b0), "s"(kb0));
    asm("v_mad_u32_u16 %0, %1, 2, %2" : "=v"(kwa[2]) : "v"(b1), "s"(kb0));
    asm("v_mad_u32_u16 %0, %1, 2, %2 op_sel:[1,0,0,0]" : "=v"(kwa[3]) : "v"(b1), "s"(kb0));
    dadd = pos == 0 ? (kch ? bfhi(dsk) : bflo(dsk)) : 0.f;
#pragma unroll
    for (int j = 0; j < 11; j++) tie(kra[j]);
#pragma unroll
    for (int j = 0; j < 4; j++) tie(kwa[j]);
    tie(dadd);
  }
  wait_vm<11>(); tie(xr);
  {
    *(u32x4*)(smem + A_XT + w * 1056 + lane * 16) = xr;
    float S = 0.f, Q = 0.f;
#pragma unroll
    for (int j = 0; j < 4; j++) { S = dot2(xr[j], 0x3f803f80u, S); Q = dot2(xr[j], xr[j], Q); }
    float t = rs32(S, Q);
    t = dpp_add<0x140>(dpp_add<0x141>(dpp_add<0x4E>(dpp_add<0xB1>(rs16(t, t)))));
    const float m = rdl(t, 0) * (-1.f / D);
    const float sd = __builtin_amdgcn_sqrtf(rbf(rbf(fmaxf(fmaf(-m, m, rdl(t, 32) * (1.f / D)), 0.f)) + 1e-5f));
    if (lane == 0) *(u32*)(smem + A_XT + w * 1056 + 1024) = pk(m, sd);
  }
  TS(2);
  const int fb = (xg >> 1) * 4 + (idx & 3), mat = (idx >> 2) & 1, dq = (xg & 1) * 4 + (idx >> 3), d0 = dq * 64 + w * 8;
  const int b2 = (idx < 28 ? (idx >> 2) * 32 : 192) + xg * 4 + (idx & 3);
  u32 v[8], t2[8];
  auto ldv = [&]() NOPK
  {
    const u16* wp = (mat ? w3 : w1) + (size_t)d0 * F + fb * 112;
    const u32 vo = (lane < 56 ? lane : 55) * 4;
#pragma unroll
    for (int i = 0; i < 4; i++) { lp4<0>(v[2 * i], wp + 2 * i * F, vo); lp4<2 * F>(v[2 * i + 1], wp + 2 * i * F, vo); }
#pragma unroll
    for (int i = 0; i < 2; i++) {
      const u16* wq = w2 + (size_t)(b2 * 8 + i * 4) * D;
      const u32 to = tid * 2;
      lp2<0>(t2[4 * i], wq, to); lp2<1024>(t2[4 * i + 1], wq, to); lp2<2048>(t2[4 * i + 2], wq, to); lp2<3072>(t2[4 * i + 3], wq, to);
    }
  };
  if (VP == 0) ldv();
  wait_vm<VP == 0 ? 21 : 5>(); tie(vare); tie(vaim); tie(vbre); tie(vbim); tie(vcre); tie(vcim);
  char* tab = smem + A_TAB + w * 1536;
  {
    const float dt = rbf(__builtin_amdgcn_exp2f((w >> 2 ? bfhi(ldt2) : bflo(ldt2)) * LOG2E));
    const float a = dt * bflo(vare) * LOG2E, bq = dt * bflo(vaim) * INV2PI;
    const float br = dt * bflo(vbre), bi = dt * bflo(vbim), cr = bflo(vcre), ci = bflo(vcim);
    const float m = lane < 32 ? 1.f : 256.f;
    const float e = __builtin_amdgcn_exp2f(a * m), ph = __builtin_amdgcn_fractf(-bq * m);
    const float ex = e * __builtin_amdgcn_cosf(ph), ey = e * __builtin_amdgcn_sinf(ph);
    char* t = tab + (lane & 31) * 48;
    *(f32x2*)(t + 16 + (lane >> 5) * 8) = f32x2{ex, ey};
    f32x4 o = {a, bq, cr * br - ci * bi, cr * bi + ci * br};
    if (lane >= 32) o = f32x4{ex, ey, ex * ex - ey * ey, (ex + ex) * ey};
    *(f32x4*)(t + (lane >> 5) * 32) = o;
  }
  if (VP == 1) ldv();
  TS(3);
  lds_barrier();
  {
    const char* xt = smem + A_XT + (lane & 7) * 1056;
    const u32 st = *(const u32*)(xt + 1024);
#pragma unroll
    for (int e = 0; e < 2; e++) {
      const u32x2 xq = *(const u32x2*)(xt + (e * 64 + w * 8 + (lane >> 3)) * 8);
      st16(ws + WS_XG + (size_t)(e * 64 + w * 8) * 32768 + p * 128, (lane >> 3) * 32768 + (lane & 7) * 16, u32x4{xq[0], xq[1], st, tag});
    }
  }
  TS(41);
  if (VP == 2) ldv();
  u32x4 o0, o1;
  auto retile = [&]() NOPK {
    wait_vm<VP == 2 ? 0 : 2>();
#pragma unroll
    for (int i = 0; i < 8; i++) { tie(v[i]); tie(t2[i]); }
    u32x4 o2;
#pragma unroll
    for (int j = 0; j < 4; j++) {
      o0[j] = __builtin_amdgcn_perm(v[2 * j + 1], v[2 * j], 0x05040100u);
      o1[j] = __builtin_amdgcn_perm(v[2 * j + 1], v[2 * j], 0x07060302u);
      o2[j] = __builtin_amdgcn_perm(t2[2 * j + 1], t2[2 * j], 0x05040100u);
    }
    st16x2_56(ws + WS_W1F + (size_t)(fb * 7 * 32 + (d0 >> 4)) * 1024 + (w & 1) * 512 + mat * 128, (lane >> 3) * 32768 + (lane & 7) * 16, o0, o1);
    st16(ws + WS_W2F + w * 229376 + (b2 >> 2) * 4096 + (b2 & 3) * 256, g * 1024 + r * 16, o2);
  };
  auto gemv = [&]() NOPK
  {
    wait_vm<18>(); tie(pf); tie(wm0); tie(wm1); tie(c0); tie(c1);
    const float cx0 = bflo(c0), cx1 = bflo(c1);
    const u32 sp = pk(cx0 * __builtin_amdgcn_rcpf(1.f + __builtin_amdgcn_exp2f(cx0 * -LOG2E)),
                      cx1 * __builtin_amdgcn_rcpf(1.f + __builtin_amdgcn_exp2f(cx1 * -LOG2E)));
    float acc[8];
#pragma unroll
    for (int j = 0; j < 4; j++) {
      acc[2 * j] = dot2(__builtin_amdgcn_perm(wm1[j], wm0[j], 0x05040100u), sp, 0.f);
      acc[2 * j + 1] = dot2(__builtin_amdgcn_perm(wm1[j], wm0[j], 0x07060302u), sp, 0.f);
    }
    const float q0 = dpp_add<0x128>(rs16(rs32(acc[0], acc[4]), rs32(acc[2], acc[6])));
    const float q1 = dpp_add<0x128>(rs16(rs32(acc[1], acc[5]), rs32(acc[3], acc[7])));
    *(float*)(smem + A_GV + (tid & 448) * 4 + (lane & 7) * 32 + (lane >> 3) * 4) = (lane & 8) ? q1 : q0;
    if (arrive_last(smem + L_CNT, 8) && idx < 24) {
      float s = 0.f;
#pragma unroll
      for (int i = 0; i < 8; i++) s += *(const float*)(smem + A_GV + i * 256 + lane * 4);
      st8(ws + WS_MG + (cg & 3) * 24576 + (cg >> 2) * 512, lane * 8, u32x2{f2u(s), tag});
    }
  };
  TS(4);
  if (GP == 0) { gemv(); TS(44); }
  int wa = w;
  if (GP == 2 || RP == 2 || RP == 3) {
    asm volatile("" : "+s"(wa));
    if (wa >= 4) {
      asm volatile("; ROLEH");
      if (GP == 2) { gemv(); TS(44); }
      if (RP == 2) { retile(); TS(55); }
    }
    if (RP == 3) { retile(); TS(55); }
  }
  {
    asm volatile("" ::: "memory");
    const float mF = (float)(16 * r), mX = (float)gx;
    const char* tp = tab + g * 8 * 48;
    const char* tq = tp + (r < 8 ? 16 : 32);
    f32x4 acc[3];
#pragma unroll
    for (int s = 0; s < 3; s++) acc[s] = f32x4{0.f, 0.f, 0.f, 0.f};
#pragma unroll
    for (int ks = 0; ks < 2; ks++) {
      u32x4 Ff, Gf[3];
#pragma unroll
      for (int i = 0; i < 4; i++) {
        const f32x4 s0 = *(const f32x4*)(tp + (ks * 4 + i) * 48), s1 = *(const f32x4*)(tq + (ks * 4 + i) * 48);
        const float e0 = __builtin_amdgcn_exp2f(s0[0] * mF), p0 = __builtin_amdgcn_fractf(s0[1] * mF);
        const float zx = e0 * __builtin_amdgcn_cosf(p0), zy = e0 * __builtin_amdgcn_sinf(p0);
        Ff[i] = pk(s0[2] * zx - s0[3] * zy, s0[2] * zy + s0[3] * zx);
        const float e1 = __builtin_amdgcn_exp2f(s0[0] * mX), p1 = __builtin_amdgcn_fractf(s0[1] * -mX);
        const float xx = e1 * __builtin_amdgcn_cosf(p1), xy = e1 * __builtin_amdgcn_sinf(p1);
        Gf[0][i] = pk(xx, xy);
        Gf[1][i] = pk(xx * s1[0] - xy * s1[1], xx * s1[1] + xy * s1[0]);
        Gf[2][i] = pk(xx * s1[2] - xy * s1[3], xx * s1[3] + xy * s1[2]);
      }
#pragma unroll
      for (int s = 0; s < 3; s++) acc[s] = mfma16(Ff, Gf[s], acc[s]);
      if (ks == 0) {
        TS(45);
        if (GP == 1) { gemv(); TS(44); }
        if (RP == 0) { retile(); TS(55); }
        if (GP == 2 || RP == 2) {
          asm volatile("" : "+s"(wa));
          if (wa < 4) {
            asm volatile("; ROLEL");
            if (GP == 2) { gemv(); TS(44); }
            if (RP == 2) { retile(); TS(55); }
          }
          asm volatile("; ROLEJ");
        }
      }
    }
#pragma unroll
    for (int s = 0; s < 3; s++) *(f32x4*)(smem + A_P + w * 3072 + s * 1024 + lane * 16) = acc[s];
  }
  if (RP == 1) retile();
  TS(5);
  u32x4 xg4[4];
  u32x2 mg, mh;
  const char* xgb = ws + WS_XG + (size_t)(p >> 1) * 32768;
  const u32 xgo = (tid >> 3) * 512 + (tid & 7) * 16;
  u32 mga = (4 - ((lane >> 3) & 1)) * 512 + d0 + (lane & 7), mgb = ((0x45210 >> ((lane & 14) * 2)) & 7) * 512 + 2 * p + (lane & 1);
  tie(mga); tie(mgb);
  const u32 mgo = ((lane & 16) ? mgb : mga) * 8 + (lane >> 5) * 24576;
  auto lookx = [&]() NOPK {
    ld16<0>(xg4[0], xgb, xgo); ld16<128>(xg4[1], xgb, xgo); ld16<256>(xg4[2], xgb, xgo); ld16<384>(xg4[3], xgb, xgo);
  };
  auto lookm = [&]() NOPK {
    ld8(mg, ws + WS_MG, mgo); ld8(mh, ws + WS_MG + 49152, mgo);
  };
  if (LX == 0) lookx();
  if (FWP == 1) wait_vm<LX == 0 ? 4 : 0>();
  lds_barrier();
  TS(6);
  if (FWP == 1 && w == 7) st4one(ws + WS_FW + (fb * 16 + mat * 8 + dq) * 128, tag);
  if (LM == 0) lookm();
  TSF(0, 6); TSF(41, 41); TSF(44, 45); TSF(55, 56); TSF(58, 59);
  if ((w & 3) < 3) {
    const char* pp = smem + A_P + kch * 12288 + (w & 3) * 1024 + lane * 16;
    const f32x4 q = (*(const f32x4*)(pp) + *(const f32x4*)(pp + 3072)) + (*(const f32x4*)(pp + 6144) + *(const f32x4*)(pp + 9216));
    const u32 k0 = pk(q[0] + dadd, q[1]), k1 = pk(q[2], q[3]);
    *(LDS_AS u16*)(size_t)kwa[0] = (u16)k0; *(LDS_AS u16*)(size_t)kwa[1] = (u16)(k0 >> 16);
    *(LDS_AS u16*)(size_t)kwa[2] = (u16)k1; *(LDS_AS u16*)(size_t)kwa[3] = (u16)(k1 >> 16);
  }
  if (LX == 1) lookx();
  if (LM == 1) lookm();
  if (FWP == 0) wait_vm<(LX == 2 ? 0 : 4) + (LM == 2 ? 0 : 2)>();
  lds_barrier();
  TS(7);
  if (FWP == 0 && w == 7) st4one(ws + WS_FW + (fb * 16 + mat * 8 + dq) * 128, tag);
  const int tt = (xg & 1) * 8 + (idx & 7), fs = (xg >> 1) * 4 + (idx >> 3);
  const u32 fwo = r * 128;
  const char* fwb = ws + WS_FW + fs * 2048;
  u32 fw;
  if (LX == 2) lookx();
  if (LM == 2) lookm();
  {
    u32 e[11];
#pragma unroll
    for (int j = 0; j < 11; j++) e[j] = *(const LDS_AS u16*)(size_t)kra[j];
    char* kc = smem + B_KC + kch * B_KCH + (tid & 255) * 16;
#pragma unroll
    for (int j = 0; j < 4; j++) {
      u32x4 o;
#pragma unroll
      for (int i = 0; i < 4; i++) asm volatile("v_lshl_or_b32 %0, %1, 16, %2" : "=v"(o[i]) : "v"(e[j + 2 * i + 1]), "v"(e[j + 2 * i]));
      *(u32x4*)(kc + j * B_KCS) = o;
    }
  }
  TS(8);
  const int ch = w & 1, nq = w >> 1;
  u32 pa_ = lds0 + B_KC + ch * B_KCH + (3 - (r & 3)) * B_KCS + 2 * (2040 - 8 * (r >> 2) + 8 * g) - 512 * 7 - 64 * (2 * nq + 1);
  u32 pb_ = lds0 + B_U + ch * B_UCH + ((r & 1) ? 0 : B_US) + 2 * (512 + 32 * (r >> 1) + 8 * g) - 64 * (2 * nq + 1);
  u32 yo = w * 4096 + 128 * (r >> 1) + 32 * (g ^ ((r & 2) | ((r >> 2) & 1))) + 16 * ((r ^ ch) & 1);
  u32 pu_ = lds0 + B_U + 1024 + tid * 8, ba = (r & 11) == g ? (r & 4) * 8 : 64;
  tie(pa_); tie(pb_); tie(yo); tie(pu_); tie(ba);
  wait_vm<2>(); tie(xg4[0]); tie(xg4[1]); tie(xg4[2]); tie(xg4[3]);
  TS(47);
#pragma unroll 1
  for (int it = 0; it < SPIN_CAP && !all_lanes(dif4(xg4[0][3], xg4[1][3], xg4[2][3], xg4[3][3], tag) == 0); it++) {
    lookx();
    wait_vm<0>(); tie(xg4[0]); tie(xg4[1]); tie(xg4[2]); tie(xg4[3]);
  }
  float h0[4], h1[4];
  {
    u32x4 xq;
    u32 olo = 0x3f80u, ohi = 0x3f800000u;
    asm("" : "+s"(olo), "+s"(ohi));
#pragma unroll
    for (int i = 0; i < 4; i++) {
      u32 xa = xg4[i][0], xb = xg4[i][1];
      asm("" : "+v"(xb));
      xq[i] = (p & 1) ? xb : xa;
      const float m = bflo(xg4[i][2]), rs = __builtin_amdgcn_rcpf(bfhi(xg4[i][2]));
      h0[i] = dot2(olo, xq[i], m) * rs;
      h1[i] = dot2(ohi, xq[i], m) * rs;
    }
    *(u32x4*)(smem + B_XS + tid * 16) = xq;
  }
#pragma unroll
  for (int i = 0; i < 4; i++) { tie(h0[i]); tie(h1[i]); }
  TS(48);
  wait_vm<0>(); tie(mg); tie(mh);
#pragma unroll 1
  for (int it = 0; it < SPIN_CAP && !all_lanes(dif(dif(0, mg[1], tag), mh[1], tag) == 0); it++) {
    lookm();
    wait_vm<0>(); tie(mg); tie(mh);
  }
  TS(9);
  const int h = lane >> 5, c = lane & 31;
  const int tt2 = (~xg & 1) * 8 + (xg & 6) + (idx >> 4), sel = (idx >> 3) & 1, dt = idx & 7;
  const int rot = R1 == 0 ? 0 : R1 == 1 ? (2 * (idx >> 3) + idx) & 7 : xg;
  const int ft = fs * 7 + w - 1;
  u32x4 A[32];
  const u32 ao = lane * 16;
  auto lda = [&](int i) NOPK {
    const char* wq = ws + WS_W1F + (size_t)ft * 32768 + (((i >> 2) + rot) & 7) * 4096;
    asm volatile("s_cmp_eq_u32 %4, 0\n\ts_cbranch_scc1 .Lna%=\n\tglobal_load_dwordx4 %0, %1, %2 offset:%3 sc1\n.Lna%=:"
                 : "=v"(A[i]) : "v"(ao), "s"(wq), "n"((i & 3) * 1024), "s"(w) : "memory", "scc");
  };
  auto needw = [&](unsigned long long mask) NOPK {
#pragma unroll 1
    for (int it = 0; it < SPIN_CAP && (__builtin_amdgcn_ballot_w64(fw != tag) & mask) != 0; it++) {
      __builtin_amdgcn_s_sleep(1);
      ld4(fw, fwb, fwo); wait_vm<0>(); tie(fw);
    }
  };
  auto ldb = [&](int i) NOPK { ld16k(A[i], ws + WS_W1F + (size_t)ft * 32768 + (((i >> 2) + rot) & 7) * 4096, i & 3, ao); };
  ld4(fw, fwb, fwo);
  const int g8 = tid >> 1, cc = tid & 1;
  const float mq = u2f(mg[0]) + u2f(mh[0]);
  const float mv = rbf(rs32(mq, mq)), mv1 = rbf(1.f + mv);
  {
    const float gm = u2f(__builtin_amdgcn_update_dpp(0, f2i(mv), 0x104, 0xf, 0xf, true)) * mv;
    const float gs = u2f(__builtin_amdgcn_update_dpp(0, f2i(mv), 0x102, 0xf, 0xf, true)) * mv1;
    const float sh0 = rdl(gm, 16), sh1 = rdl(gm, 17), sc0 = rdl(gs, 18), sc1 = rdl(gs, 19);
#pragma unroll
    for (int i = 0; i < 4; i++) { h0[i] = fmaf(h0[i], sc0, sh0); h1[i] = fmaf(h1[i], sc1, sh1); }
    char* u = (char*)(LDS_AS char*)(size_t)pu_;
    const u32x2 u0 = {pk(h0[0], h0[1]), pk(h0[2], h0[3])}, u1 = {pk(h1[0], h1[1]), pk(h1[2], h1[3])};
    *(u32x2*)(u) = u0; *(u32x2*)(u + B_US + 8) = u0;
    *(u32x2*)(u + B_UCH) = u1; *(u32x2*)(u + B_UCH + B_US + 8) = u1;
  }
  float kb;
  TS(10);
  lds_barrier();
  TS(11);
  auto epi = [&](int yb) NOPK {
    const int gl = g8 & 127;
    const int yo = cc * 4112 + (gl >> 2) * 128 + 32 * ((gl & 3) ^ (((gl >> 1) & 2) | ((gl >> 3) & 1)));
    const char *Y = smem + yb + yo, *Z = smem + yb + (yo ^ 16);
    const f32x4 ya = (*(const f32x4*)Y + *(const f32x4*)(Y + 8192)) + (*(const f32x4*)(Y + 16384) + *(const f32x4*)(Y + 24576));
    const f32x4 yb4 = (*(const f32x4*)Z + *(const f32x4*)(Z + 8192)) + (*(const f32x4*)(Z + 16384) + *(const f32x4*)(Z + 24576));
    const u32x4 xa = *(const u32x4*)(smem + B_XS + g8 * 32), xb = *(const u32x4*)(smem + B_XS + g8 * 32 + 16);
    const u32 one = cc ? 0x3f800000u : 0x3f80u;
    u32x4 o, os;
#pragma unroll
    for (int j = 0; j < 4; j++) {
      const float y0 = j < 2 ? dot2(xa[2 * j], one, ya[2 * j]) : dot2(xb[2 * j - 4], one, yb4[2 * j - 4]);
      const float y1 = j < 2 ? dot2(xa[2 * j + 1], one, ya[2 * j + 1]) : dot2(xb[2 * j - 3], one, yb4[2 * j - 3]);
      o[j] = pk(y0, y1);
      os[j] = pk(y0 * kb, y1 * kb);
    }
    const u32 xo = ((g8 >> 2) & 15) * 131072 + cc * 64 + (g8 & 3) * 16;
    st16(ws + WS_XS + p * 128 + (w >> 1) * 32768, xo, os);
    st16(ws + WS_X1 + p * 128 + (w >> 1) * 32768, xo, o);
  };
  u32x4 W2x[12];
  const int rt2 = R2 == 0 ? 0 : R2 == 1 ? dt + 2 * (idx >> 3) : xg, rot2 = rt2 < 7 ? rt2 : rt2 - 7;
#define kof(j) ((j) + rot2 < 7 ? (j) + rot2 : (j) + rot2 - 7)
  const char* w2b = ws + WS_W2F + dt * 229376 + w * 28672;
  auto ldw2 = [&](int j) NOPK {
    const char* q = w2b + kof(j >> 2) * 4096;
    if (j < 12) ld16k(W2x[j], q, j & 3, ao); else ld16k(A[j - 12], q, j & 3, ao);
  };
  constexpr int LB = LBA + LBC, NL2 = (51 / SCR + 1 < 32 - LB) ? 51 / SCR + 1 : 32 - LB;
  u32x2 cdv[8];
  const u32 co = ((c >> 3) & 1) * 2048 + ((w - 1) * 8 + (c & 7)) * 32 + h * 16 + (c >> 4) * 8;
  auto ldcd = [&]() NOPK {
    const char* cb = ws + WS_CD + fs * 4096;
    asm volatile("" : "+s"(cb));
#pragma unroll
    for (int i = 0; i < 8; i++) ld8(cdv[i], cb + i * 65536, co);
  };
  {
    const char* pa = (const char*)(LDS_AS const char*)(size_t)pa_;
    const char* pb = (const char*)(LDS_AS const char*)(size_t)pb_;
    f32x4 acc[4];
    u32x4 la[2][4], lb[2][4], hf[4];
    auto ldhi = [&](int q) NOPK {
      const int e = 4 + ((q & 7) >> 1);
      hf[q & 3] = *(const u32x4*)(((q & 1) ? pa + 512 * (7 - e) : pb + 512 * e) + 64 * (1 - (q >> 3)));
    };
#pragma unroll
    for (int vv = 0; vv < 2; vv++)
#pragma unroll
      for (int n = 0; n < 8; n++) {
        const int e = (0x30212310 >> (4 * n)) & 3;
        if ((0xb1 >> n) & 1) la[vv][e] = *(const u32x4*)(pa + 512 * (7 - e) + 64 * (1 - vv));
        else lb[vv][e] = *(const u32x4*)(pb + 512 * e + 64 * (1 - vv));
      }
    __builtin_amdgcn_sched_barrier(0);
    u32x4 Ac;
    {
      const u32 val = lane < 8 ? f2u(mv1) : lane < 16 ? f2u(mv) : 0u;
      const u32 pr = __builtin_amdgcn_perm(swap1(val), val, 0x07060302u);
#pragma unroll
      for (int j = 0; j < 4; j++) Ac[j] = (u32)__builtin_amdgcn_ds_bpermute(ba + 8 * j, (int)pr);
      kb = sel2(cc, rdl(mv1, 24), rdl(mv1, 25));
    }
    __builtin_amdgcn_sched_barrier(0);
    auto lda0 = [&]() NOPK {
      wait_vm<0>(); tie(fw);
      needw(0xffffffffull);
      asm volatile("s_cmp_eq_u32 %6, 0\n\ts_cbranch_scc1 .Lna%=\n\tglobal_load_dwordx4 %0, %4, %5 sc1\n\tglobal_load_dwordx4 %1, %4, %5 offset:1024 sc1\n\t"
                   "global_load_dwordx4 %2, %4, %5 offset:2048 sc1\n\tglobal_load_dwordx4 %3, %4, %5 offset:3072 sc1\n.Lna%=:"
                   : "=&v"(A[0]), "=&v"(A[1]), "=&v"(A[2]), "=&v"(A[3]) : "v"(ao), "s"(ws + WS_W1F + (size_t)ft * 32768 + rot * 4096), "s"(w) : "memory", "scc");
#pragma unroll
      for (int i = 4; i < LBA; i++) lda(i);
      TS(54);
    };
    if (FWN == 0) lda0();
#pragma unroll
    for (int i = 0; i < 4; i++) acc[i] = f32x4{0.f, 0.f, 0.f, 0.f};
    f32x4 cs0, cs1;
    TS(46);
    __builtin_amdgcn_sched_barrier(0);
#pragma unroll
    for (int n = 0; n < 20; n++) {
      const int vv = n / 10, k = n % 10, e = (0x0231211000ll >> (4 * k)) & 3, u = (0x0000112231ll >> (4 * k)) & 3;
      acc[e + u] = mfma16(la[vv][e], lb[vv][u], acc[e + u]);
      if (FWN == 0 && n % 3 == 0 && n / 3 < LBC) lda(LBA + n / 3);
      if (FWN > 0 && n == FWN - 1) { __builtin_amdgcn_sched_barrier(0); lda0(); }
      if (FWN > 0 && n >= FWN && n - FWN < LBC) lda(LBA + n - FWN);
      if (n >= 12 && n < 15) ldhi(n - 12);
      if (n == 11) { const f32x4 z = {0.f, 0.f, 0.f, 0.f}; cs0 = mfma16(Ac, o0, z); cs1 = mfma16(Ac, o1, z); }
      if (n == 15) { *(f32x4*)(smem + B_RED + w * 2048 + lane * 16) = cs0; *(f32x4*)(smem + B_RED + w * 2048 + 1024 + lane * 16) = cs1; }
      __builtin_amdgcn_sched_barrier(0);
    }
    TS(12);
#pragma unroll
    for (int i = 0; i < 4; i++) *(f32x4*)(smem + B_Y1 + yo + 1024 * i) = acc[i];
    lds_barrier();
    TS(13);
    TSF(7, 13); TSF(46, 48); TSF(54, 54);
    auto part2 = [&](auto hi) NOPK {
      TS(14);
#pragma unroll
      for (int n = 0; n < 52; n++) {
        const int vv = n / 26, k = n % 26;
        const int e = (k < 13 ? 0x2210144443210ll >> (4 * k) : 0x7036631023555ll >> (4 * (k - 13))) & 7;
        const int u = (k < 13 ? 0x2555332104444ll >> (4 * k) : 0x0731026631210ll >> (4 * (k - 13))) & 7;
        const int q = vv * 8 + (e > 3 ? 2 * e - 7 : 2 * u - 8);
        if (k == 0 || k == 4 || k == 8 || k == 12 || k == 16 || k == 20 || k == 23 || k == 25) {
          const int qn = vv * 8 + (k < 23 ? k / 4 : k == 23 ? 6 : 7) + 3;
          if (qn < 16) ldhi(qn);
        }
        __builtin_amdgcn_sched_barrier(0);
        acc[e + u - 4] = mfma16(e > 3 ? hf[q & 3] : la[vv][e], u > 3 ? hf[q & 3] : lb[vv][u], n < 4 ? f32x4{0.f, 0.f, 0.f, 0.f} : acc[e + u - 4]);
        if (decltype(hi)::v && n % SCR == 0 && n / SCR < NL2) ldb(LB + n / SCR);
        __builtin_amdgcn_sched_barrier(0);
      }
      TS(16);
#pragma unroll
      for (int i = 0; i < 4; i++) *(f32x4*)(smem + B_Y2 + yo + 1024 * i) = acc[i];
    };
    int w_ = w;
    asm volatile("" : "+s"(w_));
    if (w_ < 4) {
      asm volatile("; ROLEL");
      __builtin_amdgcn_s_setprio(3);
      epi(B_Y1);
      __builtin_amdgcn_s_setprio(0);
      part2(IC<0>{});
      if (F1P == 1) {
        wait_vm<0>();
        if (arrive_last(smem + L_CNT + 8, 4)) st4one(ws + WS_FX + (b & 63) * 128 + (b >> 6) * 4, tag);
        TS(15);
#pragma unroll
        for (int i = LB; i < LB + NL1; i++) lda(i);
      }
    }
    asm volatile("" : "+s"(w_));
    if (w_ >= 4) {
      asm volatile("; ROLEH");
      {
        float s = 0.f;
#pragma unroll
        for (int q = 0; q < 8; q++) s += *(const float*)(smem + B_RED + q * 2048 + h * 1024 + c * 16 + (w - 4) * 4);
        st8(ws + WS_CD + ((dq * 16 + fb) * 2 + mat) * 2048 + (w - 4) * 512, r * 32 + (g & 1) * 16 + h * 8, u32x2{f2u(s), tag});
      }
      part2(IC<1>{});
      if (w_ == 7) {
        st8m<1>(ws + WS_GB + p * 8, 0, u32x2{f2u(rdl(mv, 22)), f2u(rdl(mv, 23))});
      }
    }
    asm volatile("; ROLEJ");
    lds_barrier();
    TS(42);
    asm volatile("" : "+s"(w_));
    if (w_ < 4) {
      asm volatile("; ROLEL");
      if (F1P == 0) {
        wait_vm<0>();
        if (arrive_last(smem + L_CNT + 8, 4)) st4one(ws + WS_FX + (b & 63) * 128 + (b >> 6) * 4, tag);
        TS(15);
      }
      if (w_ != 0) {
        asm volatile("; ROLEG");
        if (SLP > 0) __builtin_amdgcn_s_sleep(SLP);
#pragma unroll
        for (int i = LB + NL1; i < 32; i++) { if (WIN > 0) wait_vm<(WIN > 0 ? WIN - 1 : 0)>(); ldb(i); }
        ldcd();
      }
    }
    asm volatile("" : "+s"(w_));
    if (w_ >= 4) {
      asm volatile("; ROLEH");
      __builtin_amdgcn_s_setprio(3);
      epi(B_Y2);
      __builtin_amdgcn_s_setprio(0);
      TS(43);
#pragma unroll
      for (int i = LB + NL2; i < 32; i++) ldb(i);
      ldcd();
    }
    asm volatile("; ROLEJ");
  }
  u32 fh;
  const u32 lpc = lds0 + w * 4096;
  const char* fhb = ws + WS_FH + tt2 * 2048 + w * 256;
  const u32 fho = ((lane >> 3) & 1) * 128 + g * 32 + (lane & 7) * 4;
  const char* fpb = ws + WS_FH + (tt * 16 + fs) * 128 + w * 4;
  const char* hq = ws + WS_HF + (size_t)((tt2 * 8 + sel) * 56 + w * 7) * 1024;
  u32x4 H[3][7];
  auto need = [&](int q) NOPK {
#pragma unroll 1
    for (int it = 0; it < SPIN_CAP && (__builtin_amdgcn_ballot_w64(fh != tag) & (0xfefeull << (16 * q))) != 0; it++) {
      __builtin_amdgcn_s_sleep(2);
      ld4(fh, fhb, fho); wait_vm<0>(); tie(fh);
    }
  };
  auto ldh = [&](int q) NOPK {
#pragma unroll
    for (int j = 0; j < 7; j++) ld16(H[q % 3][j], hq + q * 114688 + kof(j) * 1024, ao);
  };
  asm volatile("; ROLEC");
  u32x4 fx;
  auto look_x = [&](int part, unsigned long long mask) NOPK {
    asm volatile("s_mov_b64 exec, %3\n\tglobal_load_dwordx4 %0, %1, %2 sc1\n\ts_mov_b64 exec, -1"
                 : "+v"(fx) : "v"(lane * 128), "s"(ws + WS_FX + part * 8192), "s"(mask) : "memory");
  };
  auto miss_x = [&]() NOPK { return __builtin_amdgcn_ballot_w64(dif4(fx[0], fx[1], fx[2], fx[3], tag) != 0); };
  if (w == 0) {
    asm volatile("; ROLE0");
    __builtin_amdgcn_s_setprio(3);
    u32x4 sx[4], gs[8];
    auto dma = [&](int t, int q0 = 0, int q1 = 8) NOPK {
#pragma unroll
      for (int q = q0; q < q1; q++) glds4k(ws + WS_XS + tt * 131072 + t * 32768 + ((q + rot) & 7) * 4096, ao, lds0 + t * 32768 + q * 4096);
    };
    auto st_ld = [&](int half) NOPK {
      const char* q = ws + WS_X1 + tt * 131072 + half * 65536 + fs * 4096;
      ld16<0>(sx[0], q, ao); ld16<1024>(sx[1], q, ao); ld16<2048>(sx[2], q, ao); ld16<3072>(sx[3], q, ao);
    };
    auto st_pub = [&](int half) NOPK {
      float S[8], Q[8], U[4];
#pragma unroll
      for (int i = 0; i < 8; i++) { tie(sx[i & 3]); S[i] = 0.f; Q[i] = 0.f; }
#pragma unroll
      for (int i = 0; i < 2; i++)
#pragma unroll
        for (int j = 0; j < 4; j++) {
          const u32 lo = __builtin_amdgcn_perm(sx[2 * i + 1][j], sx[2 * i][j], 0x05040100u), hi = __builtin_amdgcn_perm(sx[2 * i + 1][j], sx[2 * i][j], 0x07060302u);
          S[2 * j] = dot2(lo, 0x3f803f80u, S[2 * j]); Q[2 * j] = dot2(lo, lo, Q[2 * j]);
          S[2 * j + 1] = dot2(hi, 0x3f803f80u, S[2 * j + 1]); Q[2 * j + 1] = dot2(hi, hi, Q[2 * j + 1]);
        }
#pragma unroll
      for (int t = 0; t < 4; t++) U[t] = dpp_add<0x128>(dpp_add<0x124>(rs16(rs32(S[t], Q[t]), rs32(S[t + 4], Q[t + 4]))));
      const float val = (lane & 8) ? ((lane & 4) ? U[3] : U[2]) : ((lane & 4) ? U[1] : U[0]);
      st8(ws + WS_ST + tt * 16384 + half * 8192 + fs * 512, (lane & 3) * 128 + (g & 1) * 64 + (lane & 12) * 4 + h * 8, u32x2{f2u(val), tag});
    };
    const u32 go = h * 4096 + c * 16;
    auto st_look = [&](int half) NOPK {
      const char* q = ws + WS_ST + tt * 16384 + half * 8192;
      ld16<0>(gs[0], q, go); ld16<512>(gs[1], q, go); ld16<1024>(gs[2], q, go); ld16<1536>(gs[3], q, go);
      ld16<2048>(gs[4], q, go); ld16<2560>(gs[5], q, go); ld16<3072>(gs[6], q, go); ld16<3584>(gs[7], q, go);
    };
    auto st_ok = [&]() NOPK {
      u32 d[4] = {0, 0, 0, 0};
#pragma unroll
      for (int i = 0; i < 8; i++) tie(gs[i]);
#pragma unroll
      for (int i = 0; i < 16; i++) d[i & 3] = dif(d[i & 3], gs[i >> 1][1 + 2 * (i & 1)], tag);
      return all_lanes(((d[0] | d[1]) | (d[2] | d[3])) == 0);
    };
    auto st_get = [&](int half, bool ok) NOPK {
#pragma unroll 1
      for (int it = 0; it < SPIN_CAP && !ok; it++) { st_look(half); wait_vm<0>(); ok = st_ok(); }
      float a[2];
#pragma unroll
      for (int k = 0; k < 2; k++)
        a[k] = (((u2f(gs[0][2 * k]) + u2f(gs[1][2 * k])) + (u2f(gs[2][2 * k]) + u2f(gs[3][2 * k]))) +
                ((u2f(gs[4][2 * k]) + u2f(gs[5][2 * k])) + (u2f(gs[6][2 * k]) + u2f(gs[7][2 * k])))) * (k ? 1.f / D : -1.f / D);
      const float v = fmaxf(fmaf(-a[0], a[0], a[1]), 0.f) + 1e-5f, rs = __builtin_amdgcn_rsqf(v);
      *(u32x4*)(smem + C_RAW + half * 1024 + lane * 16) = u32x4{split(a[0]), split(v * rs), f2u(v), f2u(rs * -LOG2E)};
    };
    auto stage = [&](u32 k) NOPK {
      asm volatile("" ::: "memory");
      *(volatile LDS_AS u32*)(smem + L_CNT + 16) = k;
      asm volatile("" ::: "memory");
    };
    unsigned long long mx = ~0ull;
    fx = u32x4{0, 0, 0, 0};
#pragma unroll 1
    for (int it = 0; it < SPIN_CAP && mx != 0; it++) {
      look_x(0, mx); wait_vm<0>(); tie(fx);
      mx = miss_x();
    }
    TS(17);
    if (SO == 1) st_ld(0);
    if (HF > 0) {
#pragma unroll 1
      for (int it = 0; it < SPIN_CAP * 16 && *(volatile LDS_AS u32*)(smem + L_CNT + 12) < 4; it++) {}
      __builtin_amdgcn_s_sleep(HF - 1);
    }
    TS(16);
    if (SO == 0) { dma(0); st_ld(0); dma(1, 0, D1); wait_vm<4 + 4 * D1>(); stage(1); TS(18); wait_vm<4 * D1>(); st_pub(0); TS(19); }
    else if (D1 >= 0) { dma(0); dma(1, 0, D1); wait_vm<32 + 4 * D1>(); st_pub(0); TS(19); if (D2 > D1) dma(1, D1, D2); wait_vm<1 + 4 * (D2 > D1 ? D2 : D1)>(); stage(1); TS(18); }
    else { dma(0, 0, 8 + D1); wait_vm<32 + 4 * D1>(); st_pub(0); TS(19); dma(0, 8 + D1, 8); dma(1, 0, D2); wait_vm<4 * D2>(); stage(1); TS(18); }
    TC(20);
    dma(1, D2 > D1 ? D2 : D1, 8);
    wait_vm<0>();
    TSF(14, 20); TSF(42, 43); TSF(53, 53);
    stage(2);
    TS(49);
    bool got = false;
    mx = ~0ull;
#pragma unroll 1
    for (int it = 0; it < SPIN_CAP && mx != 0; it++) {
      look_x(1, mx);
      if (!got) st_look(0);
      wait_vm<0>(); tie(fx);
      if (it == 0) { TS(21); }
      if (!got && st_ok()) { st_get(0, true); stage(3); got = true; }
      mx = miss_x();
    }
    TS(22);
    if (SO == 1) st_ld(1);
    if (!got) {
      st_look(0);
      dma(2);
      wait_vm<32>();
      st_get(0, st_ok()); stage(3);
    } else dma(2);
    if (SO == 0) { st_ld(1); dma(3, 0, D1); wait_vm<4 + 4 * D1>(); stage(4); TS(50); wait_vm<4 * D1>(); st_pub(1); }
    else { dma(3, 0, D3); TS(61); wait_vm<32 + 4 * D3>(); TS(62); st_pub(1); TS(63); wait_vm<1 + 4 * D3>(); stage(4); TS(50); }
    dma(3, D3, 8);
    TS(23);
    if (CB == 0) bar();
    TS(24);
#pragma unroll
    for (int q = 0; q < 4; q++) {
      const char* xr_ = ws + WS_X1 + (tt2 * 4 + q) * 32768 + dt * 4096 + sel * 32;
      const u32 xo = r * 256 + h * 64 + (g & 1) * 16, la = lds0 + D_RES + q * 2048;
      glds1k<0>(xr_, xo, la); glds1k<0>(xr_ + 128, xo, la + 1024);
    }
    asm volatile("s_mov_b32 m0, %2\n\ts_nop 0\n\tglobal_load_lds_dword %0, %1 sc1"
                 ::"v"(lane * 4), "s"(ws + WS_GB + dt * 256), "s"(lds0 + D_GB) : "memory");
    st_look(1);
    wait_vm<17>();
    stage(5);
    TS(25);
    wait_vm<0>();
    st_get(1, st_ok());
    stage(6);
    if (b == 0) st4one(ws + WS_E, tag);
#pragma unroll
    for (int i = 0; i < 28; i++) { ldw2(i); if (WT > 0) wait_vm<(WT > 0 ? WT : 0)>(); }
    __builtin_amdgcn_s_setprio(0);
    TS(26); TS(27); TC(28);
    TSF(21, 28); TSF(49, 50); TSF(61, 63);
    ld4(fh, fhb, fho);
    wait_vm<0>(); tie(fh);
#pragma unroll 1
    for (int it = 0; CB == 1 && it < SPIN_CAP * 16 && *(volatile LDS_AS u32*)(smem + L_CNT + 20) < 7; it++) {}
#pragma unroll
    for (int i = 0; i < 28; i++) { if (i < 12) tie(W2x[i]); else tie(A[i - 12]); }
    TS(29);
  }
  int w_ = w;
  asm volatile("" : "+s"(w_));
  if (w_ != 0) {
    asm volatile("; ROLEG");
    const char* rb = smem + h * 512 + ((lane >> 2) & 3) * 64 + ((lane >> 4) & 1) * 32 + (lane & 3) * 8;
    const char* rb2 = far(rb + 65536);
    const char* hb = ws + WS_HF + (size_t)(tt * 8 * 56 + (ft >> 1)) * 1024 + (ft & 1) * 512;
    u32 ho = (c >> 4) * 57344 + h * 256 + r * 16;
    u32 pwa = lds0 + L_CNT + 16;
    const char* f2p = ws + WS_FX + 8192 + (b & 63) * 128 + (b >> 6) * 4;
    tie(pwa);
    asm volatile("" ::"s"(hb), "v"(ho), "s"(f2p), "s"(fpb), "s"(fhb), "s"(w2b), "v"((u32)(size_t)(LDS_AS const char*)rb), "v"((u32)(size_t)(LDS_AS const char*)rb2));
    u32x4 Ax;
    {
      if (w_ >= 4) {
        asm volatile("; ROLEH");
        wait_vm<32 - LB - NL2 + 8>();
        TS(15);
        if (arrive_last(smem + L_CNT + 12, 4)) { st4one(f2p, tag); wait_vm<1>(); } else wait_vm<0>();
      } else wait_vm<0>();
      TS(53);
#pragma unroll 1
      for (int it = 0; it < SPIN_CAP; it++) {
#pragma unroll
        for (int i = 0; i < 8; i++) tie(cdv[i]);
        if (all_lanes((dif4(cdv[0][1], cdv[1][1], cdv[2][1], cdv[3][1], tag) | dif4(cdv[4][1], cdv[5][1], cdv[6][1], cdv[7][1], tag)) == 0)) break;
        ldcd();
        wait_vm<0>();
      }
      const float cd = ((u2f(cdv[0][0]) + u2f(cdv[1][0])) + (u2f(cdv[2][0]) + u2f(cdv[3][0]))) + ((u2f(cdv[4][0]) + u2f(cdv[5][0])) + (u2f(cdv[6][0]) + u2f(cdv[7][0])));
      const float chi = rbf(cd);
      Ax = u32x4{pk(chi, chi), pk(cd - chi, cd - chi), 0, 0};
      tie(Ax);
    }
    f32x16 acc[2], zero;
#pragma unroll
    for (int i = 0; i < 16; i++) zero[i] = 0.f;
#pragma unroll
    for (int i = 0; i < 32; i++) tie(A[i]);
    u32x4 Bq[3], op;
    u32 be, pw;
    Ep q[2];
    const char* sp = smem + C_RAW + c * 16;
    float ev, gu, oa;
#define stat_wait(need) stat_wait2(need, 0)
#define stat_wait2(need, off) do { u32 t0_, t1_;     \
      asm volatile("s_mov_b32 %2, %5\n\ts_nop 0\n\tv_readfirstlane_b32 %1, %0\n.Lw%=:\n\ts_cmp_ge_u32 %1, %4\n\ts_cbranch_scc1 .Lx%=\n\t" \
                   "s_sleep %7\n\tds_read_b32 %0, %3 offset:%6\n\ts_waitcnt lgkmcnt(0)\n\tv_readfirstlane_b32 %1, %0\n\ts_sub_u32 %2, %2, 1\n\ts_cbranch_scc0 .Lw%=\n.Lx%=:" \
                   : "+v"(pw), "=&s"(t0_), "=&s"(t1_) : "v"(pwa), "n"(need), "n"(SPIN_CAP * 16), "n"(off), "n"(SQ) : "memory", "scc"); } while (0)
    TS(17);
    pw = 0;
    stat_wait(1);
    TS(18);
    TS(19); TC(20);
    TSF(14, 20); TSF(42, 43); TSF(53, 53);
    __builtin_amdgcn_s_setprio(3);
    Bq[0] = tr8(rb); Bq[1] = tr8(rb + 1024);
#define W2C(n) (((n) < NB0 ? (n) : NB0) + ((n) <= W2S ? 0 : ((n) - W2S + W2I - 1) / W2I < 12 - NB0 ? ((n) - W2S + W2I - 1) / W2I : 12 - NB0))
    static_assert(W2C(87 + DS) == 12 && W2S >= NB0, "w2 loads");
    auto tile = [&](auto tc) NOPK {
    constexpr int T = decltype(tc)::v, E = T - 1, SH = T == 1 ? SH1 : 0;
#pragma unroll
    for (int ks = 0; ks < 32; ks++) {
      const int n = T * 32 + ks, m = n + 2;
      if (n == 30) {
        TS(21);
        stat_wait(2);
        TS(22);
      } else if (n == 62) {
        TS(23);
        stat_wait(4);
        TS(24);
      }
      if (CB == 0 && n == 65 + DS) { TS(61); bar(); TS(62); }
      if (CB == 1 && n == 64)
        asm volatile("s_mov_b64 exec, 1\n\tds_add_u32 %0, %1 offset:4\n\ts_mov_b64 exec, -1" ::"v"(pwa), "v"(1) : "memory");
      if (CB == 1 && n == 63 + DS) pw = *(volatile LDS_AS u32*)(smem + L_CNT + 20);
      if (CB == 1 && n == 65 + DS) { TS(61); stat_wait2(7, 4); TS(62); }
      if (n == 30 || n == 62) __builtin_amdgcn_s_setprio(3);
      if (n == 14 || n == 46 || n == 100) __builtin_amdgcn_s_setprio(2);
      if (n == 22 || n == 54 || n == 118) __builtin_amdgcn_s_setprio(1);
      if (n == 27 || n == 59) __builtin_amdgcn_s_setprio(0);
      if (n == 32 + SH1) { TS(49); stat_wait(3); TS(50); }
      if (n == 94) { TS(25); stat_wait(5); }
      if (n == 96) { stat_wait(6); TS(51); }
      if (n == 118) tie(acc[1]);
      if (n == 28 || n == 30 + SH1 || n == 60 || n == 92 || n == 94) pw = *(volatile LDS_AS u32*)(smem + L_CNT + 16);
      if (n == 120) wait_vm<10>();
      if (m < 128) Bq[m % 3] = tr8((m < 64 ? rb : rb2) + (m & 63) * 1024);
      if ((T > 0 && ks == SH) || n == 118) {
        const int X = n == 118 ? 3 : E;
        be = *(const u32*)(sp + X * 512 + h * 4);
        const f32x2 vq = *(const f32x2*)(sp + X * 512 + 8);
        q[X & 1].v = vq[0]; q[X & 1].q0 = vq[1];
      }
      if (n >= 120) A[n - 112] = *(const u32x4*)(smem + ((n >> 2) & 1) * 32768 + w * 4096 + (n & 3) * 1024 + lane * 16);
      __builtin_amdgcn_sched_barrier(0);
      acc[T & 1] = mfma32(A[ks], Bq[n % 3], ks == 0 ? zero : acc[T & 1]);
      if ((T > 0 && ks == SH + 2) || n == 120) {
        const int X = (n == 120 ? 3 : E) & 1;
        acc[X] = mfma32(Ax, u32x4{be, be, 0, 0}, acc[X]);
      }
      if (T == 3 && E2 == 2) {
        if (ks >= 5 && ks < 13) {
          const int k = ks - 5;
          const float o = gu * __builtin_amdgcn_rcpf(ev);
          if (k & 1) op[k >> 1] = pk(oa, o); else oa = o;
        }
        if (ks >= 4 && ks < 12) {
          const int k = ks - 4, i = (k >> 1) + 4 * (k & 1);
          ev = fmaf(__builtin_amdgcn_exp2f(accg(acc[E & 1], i) * q[E & 1].q0), q[E & 1].v, q[E & 1].v);
          gu = accg(acc[E & 1], i) * accu(acc[E & 1], i);
        }
      } else if (T > 0 && ks >= SH + 4 && ks < SH + 20) {
        const int k = (ks - SH - 4) >> 1, i = (k >> 1) + 4 * (k & 1);
        if (((ks - SH) & 1) == 0) {
          ev = fmaf(__builtin_amdgcn_exp2f(accg(acc[E & 1], i) * q[E & 1].q0), q[E & 1].v, q[E & 1].v);
          gu = accg(acc[E & 1], i) * accu(acc[E & 1], i);
        } else {
          const float o = gu * __builtin_amdgcn_rcpf(ev);
          if (k & 1) op[k >> 1] = pk(oa, o); else oa = o;
        }
      }
      __builtin_amdgcn_sched_barrier(0);
      if (W2C(n + 1) > W2C(n)) ldw2(W2C(n));
      if (T > 1 && ks == (T == 2 ? 9 + SH1 : 9)) { wait_vm<T == 2 ? (9 + SH1 + 1 - DS) / 3 + W2C(74 + SH1) - W2C(53 + SH1) : 4 + DS / 3 + 12 - W2C(85)>(); st4one(fpb + (T - 2) * 32, tag); }
      if (T > 0 && ks == (T == 3 && E2 == 2 ? 13 : SH + 20)) st16(hb + E * 114688, ho, op);
      if (T > 1 && ks % 3 == 1 && ks >= (T == 2 ? DS : 0) && ks < (T == 2 ? 24 + DS : 24)) {
        const int i = (ks - (T == 2 ? DS : 0)) / 3;
        const char* q = w2b + kof(5 + (i >> 2)) * 4096;
        const u32 la = lpc + (i >> 2) * 32768;
        if (T == 3) ldw2(12 + i);
        else if ((i & 3) == 0) glds1k<0>(q, ao, la); else if ((i & 3) == 1) glds1k<1024>(q, ao, la);
        else if ((i & 3) == 2) glds1k<2048>(q, ao, la); else glds1k<3072>(q, ao, la);
      }
      if (E2 == 2 && n == 96 + F2N) { wait_vm<4 + (F2N > 26)>(); st4one(fpb + 64, tag); }
      if (n == 122) ld4(fh, fhb, fho);
    }
    };
    tile(IC<0>{}); tile(IC<1>{}); tile(IC<2>{}); tile(IC<3>{});
    if (E2 == 1) { wait_vm<2>(); st4one(fpb + 64, tag); }
    static_assert(F2N >= 24, "order");
    TS(26); TS(27); TC(28);
    TSF(21, 28); TSF(49, 51); TSF(61, 62);
#pragma unroll
    for (int i = 0; i < 4; i++) op[i] = pk(swi(acc[1], i, q[1]), swi(acc[1], i + 4, q[1]));
    st16(hb + 3 * 114688, ho, op);
    __builtin_amdgcn_s_setprio(0);
    wait_vm<(E2 == 1 || (E2 == 2 && F2N > 26) ? 2 : 1)>(); tie(fh);
    if (E2 == 0) st4one(fpb + 64, tag);
#pragma unroll
    for (int i = 0; i < 20; i++) { if (i < 12) tie(W2x[i]); else tie(A[i - 12]); }
    TS(29);
  }
  {
    asm volatile("; ROLED");
    u32 f2;
    auto ldh3 = [&]() NOPK {
#pragma unroll
      for (int j = 0; j < 7; j++)
        asm volatile("global_load_dwordx4 %0, %1, %2 sc1" : "+v"(H[0][j]) : "v"(ao), "s"(hq + 3 * 114688 + kof(j) * 1024) : "memory");
    };
    char* wr = smem + w * 4096 + r * 256;
    auto quarter = [&](int q, const char* nx) NOPK {
      f32x4 acc[4];
#pragma unroll
      for (int j = 0; j < 7; j++) {
        __builtin_amdgcn_sched_barrier(0);
        if (q < 2) ld16(H[q + 1][j], nx + kof(j) * 1024, ao);
        if (q == 2) asm volatile("global_load_dwordx4 %0, %1, %2 sc1" : "+v"(H[0][j]) : "v"(ao), "s"(nx + kof(j) * 1024) : "memory");
        if (q == 1 && j == LP) ld4(f2, fhb, fho);
        if (q == 0 && j == 0) wait_vm<3>();
        if (q == 1) { if (j < LP) wait_vm<7>(); else wait_vm<8>(); }
        if (q == 2) { if (j <= LP) wait_vm<9>(); else wait_vm<8>(); }
        if (q == 3) wait_vmn(6 - j + (FN == 2));
        tie(H[q % 3][j]);
#pragma unroll
        for (int ds = 0; ds < 4; ds++) {
          const int f = 4 * j + ds;
          acc[ds] = mfma16(f < 12 ? W2x[f] : A[f - 12], H[q % 3][j], j == 0 ? f32x4{0.f, 0.f, 0.f, 0.f} : acc[ds]);
        }
      }
      __builtin_amdgcn_sched_barrier(0);
#pragma unroll
      for (int ds = 0; ds < 4; ds++) *(f32x4*)(wr + q * 32768 + (((ds * 4 + g) ^ r) * 16)) = acc[ds];
    };
    auto lwait = [&](int off, u32 n) NOPK {
#pragma unroll 1
      for (int it = 0; it < SPIN_CAP * 16 && *(volatile LDS_AS u32*)(smem + L_CNT + off) < n; it++) __builtin_amdgcn_s_sleep(SQ2);
      asm volatile("" ::: "memory");
    };
    auto ladd = [&](int off) NOPK {
      asm volatile("s_mov_b64 exec, 1\n\tds_add_u32 %0, %1\n\ts_mov_b64 exec, -1" ::"v"(lds0 + L_CNT + off), "v"(1) : "memory");
    };
    const int tk = (tid >> 4) & 15, qh = w >> 2;
    u32 xq[3][4];
    f32x4 gbv;
    auto finish = [&](int qp, int q) NOPK {
      const char* rp = smem + q * 32768 + tk * 256 + ((r ^ tk) * 16);
      f32x4 s = *(const f32x4*)rp;
#pragma unroll
      for (int i = 1; i < 8; i++) s += *(const f32x4*)(rp + i * 4096);
      float y[4];
#pragma unroll
      for (int j = 0; j < 4; j++) y[j] = fmaf(gbv[j], s[j], bflo(xq[qp][j]));
      asm volatile("global_store_dwordx2 %0, %1, %2 sc1" ::"v"((tk * D + r * 4) * 2), "v"(u32x2{pk(y[0], y[1]), pk(y[2], y[3])}),
                   "s"(out + (size_t)(q * 512 + tt2 * 32 + sel * 16) * D + dt * 64) : "memory");
    };
    need(0);
    ldh(0);
    TS(30);
    {
      const char* xp = smem + D_RES + (tk >> 3) * 256 + r * 16 + (tk & 7) * 2;
#pragma unroll
      for (int i = 0; i < (FN == 1 ? 12 : 8); i++)
        xq[i >> 2][i & 3] = *(const u16*)(xp + (FN == 1 ? (i < 4 ? qh * 6144 : (i >> 2) * 2048) : qh * 2048 + (i >> 2) * 4096) + (i & 3) * 512);
      gbv = *(const f32x4*)(smem + D_GB + r * 16);
    }
    wait_vm<(E2 ? 7 : 8)>();
    st4one(fpb + 96, tag);
    if (w != 0) ladd(20);
    TS(31);
    need(1);
    if (GT == 1) lwait(20, 14);
    TS(56);
    ld4(fh, fhb, fho);
    quarter(0, hq + 114688);
    TS(58);
    if (LP < 0) ld4(f2, fhb, fho);
    wait_vm<(LP < 0 ? 8 : 7)>(); tie(fh);
    TS(32);
    need(2);
    TS(33);
    quarter(1, hq + 2 * 114688);
    if (FN == 2) ladd(32);
    TS(57);
    wait_vm<6 - LP>(); tie(f2);
    TS(59);
    const bool ok3 = (__builtin_amdgcn_ballot_w64(f2 != tag) & (0xfefeull << 48)) == 0;
    ld4(fh, fhb, fho);
    if (GT == 2) lwait(20, 14);
    quarter(2, ok3 ? hq + 3 * 114688 : w2b);
    if (FN) ladd(24);
    TS(35);
    if (!ok3) {
      wait_vm<0>(); tie(fh);
      need(3);
      ldh3();
    }
    TS(36);
    if (FN == 2) { lwait(32, 8); finish(0, qh); TS(34); }
    TSF(29, 36); TSF(56, 59);
    quarter(3, nullptr);
    tie(fh);
    TS(38);
    if (FN) {
      ladd(28);
      lwait(24 + 4 * qh, 8);
      if (FN == 2) finish(1, 2 + qh);
      else {
        finish(0, 3 * qh);
        if (w < 4) { finish(1, 1); finish(2, 2); }
      }
    } else {
      lds_barrier();
      finish(0, qh);
      finish(1, 2 + qh);
    }
    asm volatile(".Lpf1:");
    TS(39);
    TS(40);
    TSF(37, 40);
    TSEND();
  }
}

extern "C" void kernel_launch(void* const* d_in, const int* in_sizes, int n_in, void* d_out, int out_size, void* d_ws,
                              size_t ws_size, hipStream_t stream) {
  const u16* const* in = (const u16* const*)d_in;
  fused<<<256, 512, 0, stream>>>(in[0], in[1], in[2], in[3], in[4], in[5], in[6], in[7], in[8], in[10], in[11], in[12], in[13], in[14],
                                 (char*)d_ws, (u16*)d_out);
}
```
